```python
import torch

def _softmax(x, axis=-1):
    return torch.softmax(x, dim=axis)
def _gelu(x):
    return torch.nn.functional.gelu(x, approximate="tanh")
def _silu(x):
    return torch.nn.functional.silu(x)
def _sigmoid(x):
    return torch.sigmoid(x)
def _layernorm(x, axis=-1, eps=1e-5):
    m = x.mean(dim=axis, keepdim=True)
    v = x.var(dim=axis, keepdim=True, unbiased=False)
    return (x - m) / torch.sqrt(v + eps)
def _scatter_add(tgt, idx, src, axis=0):
    return tgt.clone().index_add_(axis, idx, src)
def _segment_sum(src, seg, n):
    out = torch.zeros((n,) + src.shape[1:], dtype=src.dtype)
    return out.index_add_(0, seg, src)
def _pad(x, pw):
    flat = tuple(v for pair in reversed(pw) for v in pair)
    return torch.nn.functional.pad(x, flat)

kernel_name = 'gated_mix__ssm_conv_s4__moe_arch_mod__prompt'

def setup_inputs():

    _w = (lambda a: (lambda t: t.to(torch.bfloat16) if t.is_floating_point() else t)(torch.as_tensor(a)))
    def _A_setup():
        B, L, D, N = 1, 2048, 576, 128
        A_re = -(lambda _x: torch.exp(torch.as_tensor(_x)))((lambda *s: torch.randn(*s, dtype=torch.bfloat16) * 3.2)(D, N))
        A_im = torch.pi * (lambda *s: torch.randn(*s, dtype=torch.bfloat16) * 0.05)(D, N)
        B_re = (lambda *s: torch.randn(*s, dtype=torch.bfloat16) * 0.2)(D, N)
        B_im = (lambda *s: torch.randn(*s, dtype=torch.bfloat16) * 0.2)(D, N)
        C_re = (lambda *s: torch.randn(*s, dtype=torch.bfloat16) * 0.2)(D, N)
        C_im = (lambda *s: torch.randn(*s, dtype=torch.bfloat16) * 0.2)(D, N)
        log_dt = (lambda *s: torch.randn(*s, dtype=torch.bfloat16) * 0.05)(D) * 0.5 - 4.0
        Dskip = (lambda *s: torch.randn(*s, dtype=torch.bfloat16) * 0.1)(D)
        return {
            "u": _w((lambda *s: torch.randn(*s, dtype=torch.bfloat16) * 0.05)(B, L, D)),
            "A_re": _w(A_re),
            "A_im": _w(A_im),
            "B_re": _w(B_re),
            "B_im": _w(B_im),
            "C_re": _w(C_re),
            "C_im": _w(C_im),
            "log_dt": _w(log_dt),
            "Dskip": _w(Dskip),
        }
    _A_in = _A_setup()

    def _B_setup():
        B, S, D, F, C = 1, 2048, 576, 4608, 8
        return {
            "x": _w((lambda *s: torch.randn(*s, dtype=torch.bfloat16) * 0.05)(B, S, D)),
            "w_r": _w((lambda *s: torch.randn(*s, dtype=torch.bfloat16) * 3.2)(D)),
            "w_gate": _w((lambda *s: torch.randn(*s, dtype=torch.bfloat16) * 0.1)(D, F)),
            "w_up": _w((lambda *s: torch.randn(*s, dtype=torch.bfloat16) * 0.1)(D, F)),
            "w_down": _w((lambda *s: torch.randn(*s, dtype=torch.bfloat16) * 0.1)(F, D)),
        }
    _B_in = _B_setup()

    return {
        "A_u": _A_in["u"],
        "A_A_re": _A_in["A_re"],
        "A_A_im": _A_in["A_im"],
        "A_B_re": _A_in["B_re"],
        "A_B_im": _A_in["B_im"],
        "A_C_re": _A_in["C_re"],
        "A_C_im": _A_in["C_im"],
        "A_log_dt": _A_in["log_dt"],
        "A_Dskip": _A_in["Dskip"],
        "B_x": _B_in["x"],
        "B_w_r": _B_in["w_r"],
        "B_w_gate": _B_in["w_gate"],
        "B_w_up": _B_in["w_up"],
        "B_w_down": _B_in["w_down"],
        "gate_w": _w((lambda *s: torch.randn(*s, dtype=torch.bfloat16) * 0.05)(576) * 16.0),
    }

def reference(A_u, A_A_re, A_A_im, A_B_re, A_B_im, A_C_re, A_C_im, A_log_dt, A_Dskip, B_x, B_w_r, B_w_gate, B_w_up, B_w_down, gate_w):
    def _A_ref(u, A_re, A_im, B_re, B_im, C_re, C_im, log_dt, Dskip):
        Bsz, L, D = u.shape
        dt = torch.exp(log_dt)[:, None]
        A = A_re + 1j * A_im
        Bc = B_re + 1j * B_im
        Cc = C_re + 1j * C_im
        logdA = dt * A
        dB = dt * Bc
        l = (lambda x, dt: x.to(dt))(torch.arange(L), A_re.dtype)
        P = torch.exp(logdA[:, :, None] * l[None, None, :])
        K = torch.einsum("dn,dnl,dn->dl", Cc, P, dB).real
        L2 = 2 * L
        Kp = _pad(K, ((0, 0), (0, L)))
        up = (lambda x, *d: x.permute(*d))(u, 0, 2, 1)
        up = _pad(up, ((0, 0), (0, 0), (0, L)))
        Kf = (lambda x, axes=(-2,-1): torch.fft.rfft2(x.float(), dim=axes))(Kp[:, None, :], axes=(-2, -1))
        Uf = (lambda x, axes=(-2,-1): torch.fft.rfft2(x.float(), dim=axes))(up[:, :, None, :], axes=(-2, -1))
        Yf = Uf * Kf[None, :, :, :]
        y = (lambda x, s, axes=(-2,-1): torch.fft.irfft2(x, s=s, dim=axes).to(torch.bfloat16))(Yf, (1, L2), axes=(-2, -1))[:, :, 0, :L]
        y = (lambda x, *d: x.permute(*d))(y, 0, 2, 1) + u * Dskip[None, None, :]
        return y

    def _B_ref(x, w_r, w_gate, w_up, w_down):
        B, S, D = x.shape
        C = 8
        r = x @ w_r
        idx = torch.argsort(-r, axis=-1)[:, :C]
        idx_e = (lambda x, n, axis: torch.repeat_interleave(x, n, dim=axis))(idx.reshape(B, C, 1), D, axis=2)
        x_sel = (lambda x, i, axis: torch.take_along_dim(x, i, dim=axis))(x, idx_e, axis=1)
        g = _silu(x_sel @ w_gate)
        u = x_sel @ w_up
        y = (g * u) @ w_down
        r_sel = _sigmoid((lambda x, i, axis: torch.take_along_dim(x, i, dim=axis))(r, idx, axis=1))
        y = y * r_sel.reshape(B, C, 1)
        b_off = (torch.arange(B) * S).reshape(B, 1)
        flat_idx = (idx + b_off).reshape(B * C)
        zero = (x * 0.0).reshape(B * S, D)
        upd = _scatter_add(zero, flat_idx, y.reshape(B * C, D), axis=0)
        return x + upd.reshape(B, S, D)

    x = A_u
    g = _sigmoid((x * gate_w).sum(-1, keepdims=True))
    a = _A_ref(x, A_A_re, A_A_im, A_B_re, A_B_im, A_C_re, A_C_im, A_log_dt, A_Dskip)
    b = _B_ref(x, B_w_r, B_w_gate, B_w_up, B_w_down)
    return g * a + (1 - g) * b
```

```cpp
#include <hip/hip_runtime.h>
#include <stdint.h>

typedef unsigned short u16;
typedef unsigned int u32;
typedef unsigned long long u64;
typedef __attribute__((ext_vector_type(4))) u32 v4u;
typedef __attribute__((ext_vector_type(2))) u32 v2u;
typedef __attribute__((ext_vector_type(2))) float v2f;
typedef __attribute__((ext_vector_type(4))) float v4f;
typedef __attribute__((ext_vector_type(16))) float v16f;
typedef __attribute__((ext_vector_type(8))) short bf16x8;
typedef __attribute__((ext_vector_type(4))) short s16x4;
typedef __attribute__((ext_vector_type(2))) __bf16 bf16x2_t;
typedef __attribute__((ext_vector_type(2))) _Float16 f16x2_t;
typedef __attribute__((ext_vector_type(8))) _Float16 f16x8;
#define LAS __attribute__((address_space(3)))
#define GAS __attribute__((address_space(1)))
#define RLX_AGENT __ATOMIC_RELAXED, __HIP_MEMORY_SCOPE_AGENT
#define SBAR() __builtin_amdgcn_sched_barrier(0)
#define BAR() asm volatile("s_waitcnt lgkmcnt(0)\n\ts_barrier" ::: "memory")
#define WSR() __builtin_amdgcn_make_buffer_rsrc(p.ws, 0, 0x7fffffff, 0x00020000)
#define REREAD() asm volatile("" ::: "memory")

#define WNT " sc1"
#define CODE_KB 20
#define NB1 3
#define REPG 2
#define PFS0 24
#define FDELAY 0
#define K1 2
#define K2 3
#define PFWD 12
#define XPF 0

constexpr int L = 2048, D = 576, NST = 128, F = 4608;
constexpr int NFFN = 72;
constexpr u32 OFF_RG = 4096;
constexpr u32 OFF_G = OFF_RG + L * 8;
constexpr u32 OFF_SEL = OFF_G + REPG * 8192;
constexpr u32 OFF_CI = OFF_SEL + 16 * 256;
constexpr u32 OFF_PART = OFF_CI + 4096;
constexpr u32 SPIN_CAP = 1u << 19;

#define STAMP(i)
#define STAMP_FLUSH()

struct Params {
    const u16 *x, *Are, *Aim, *Bre, *Bim, *Cre, *Cim, *logdt, *dskip, *wr, *wg, *wu, *wd, *gw;
    u16* out;
    char* ws;
};

__device__ __forceinline__ float bflo(u32 w) { return __uint_as_float(w << 16); }
__device__ __forceinline__ float bfhi(u32 w) { return __uint_as_float(w & 0xffff0000u); }
__device__ __forceinline__ float bf2f(u16 h) { return __uint_as_float((u32)h << 16); }
__device__ __forceinline__ u32 cvtpk(float lo, float hi) { v2f v = {lo, hi}; bf16x2_t r = __builtin_convertvector(v, bf16x2_t); return __builtin_bit_cast(u32, r); }
__device__ __forceinline__ u32 cvth(v2f v) { return __builtin_bit_cast(u32, __builtin_convertvector(v, f16x2_t)); }
__device__ __forceinline__ float rbf(float f) { return bflo(cvtpk(f, f)); }
__device__ __forceinline__ float sigmoidf_(float s) { return __builtin_amdgcn_rcpf(1.f + __expf(-s)); }

template <int CTRL, int RM> __device__ __forceinline__ float dpp_add(float v) {
    return v + __builtin_bit_cast(float, __builtin_amdgcn_update_dpp(0, __builtin_bit_cast(int, v), CTRL, RM, 0xf, true));
}
__device__ __forceinline__ float wave_sum63(float v) {
    v = dpp_add<0x111, 0xf>(v); v = dpp_add<0x112, 0xf>(v); v = dpp_add<0x114, 0xf>(v); v = dpp_add<0x118, 0xf>(v);
    v = dpp_add<0x142, 0xa>(v); v = dpp_add<0x143, 0xc>(v);
    return v;
}
template <int CTRL, int RM> __device__ __forceinline__ u32 dpp_umax(u32 v) {
    u32 o = (u32)__builtin_amdgcn_update_dpp(0, (int)v, CTRL, RM, 0xf, true);
    return v > o ? v : o;
}

typedef v2f cf;
__device__ __forceinline__ cf cmul(cf a, cf b) { cf t = a.yy * (cf){-b.y, b.x}; return __builtin_elementwise_fma(a.xx, b, t); }
__device__ __forceinline__ cf cexp_(float a2, float brev, float rho) {
    float m = __builtin_amdgcn_exp2f(a2 * rho);
    float ph = __builtin_amdgcn_fractf(brev * rho);
    cf c; c.x = m * __builtin_amdgcn_cosf(ph); c.y = m * __builtin_amdgcn_sinf(ph); return c;
}

struct cf2 { cf a, b; };
__device__ __forceinline__ cf2 cmul2(cf2 x, cf2 y) { return {cmul(x.a, y.a), cmul(x.b, y.b)}; }
__device__ __forceinline__ cf2 cexp2(v2f a2, v2f brev, float rho) { return {cexp_(a2.x, brev.x, rho), cexp_(a2.y, brev.y, rho)}; }

__device__ __forceinline__ u32 make_payload(float r, float g) {
    const u32 w = cvtpk(g, r);
    return w ^ (((u32)((int)w >> 31) & 0x7fff0000u) | 0x80000000u);
}
__device__ __forceinline__ u32 make_key(u32 pay, u32 t) { return (pay & 0xffff0000u) | (0xffffu - t); }
__device__ __forceinline__ float key_to_r(u32 k) {
    u32 hi = k & 0xffff0000u;
    return __uint_as_float((hi & 0x80000000u) ? (hi ^ 0x80000000u) : (~hi & 0xffff0000u));
}

constexpr int STG_ROW = 528;
constexpr int NA = 21;
constexpr int KCH = (NA + 32) * STG_ROW;
constexpr int KCOPY = 1344;
constexpr int CH_Y = 4 * KCOPY;
constexpr int LDS_U = 4 * KCH;
constexpr int LDS_TAP = LDS_U + 4 * 4608;
constexpr int LDS_CTL = LDS_TAP + 4 * 2304;
constexpr int C_TAG = 0, C_STG = 4, C_PUB = 8, C_ACK = 12, C_Y = 16;
constexpr int LDS_RW = LDS_CTL + 1024;
constexpr int LDS_DUMMY = LDS_RW + 4 * 3072;
constexpr int LDS_TOTAL = LDS_DUMMY + 1024;
static_assert(CH_Y + 8 * 1152 <= KCH && LDS_TOTAL <= 160 * 1024, "lds");
constexpr int WDP = 1184;
constexpr int FL_WD = 0;
constexpr int FL_RED = 78 * 1024;
constexpr int FL_H = FL_RED + 24576;
constexpr int XSP = 1168;
constexpr int FL_XS = FL_H + 8 * 144;
constexpr int FL_TAG = FL_XS + 8 * XSP;

__device__ __forceinline__ int sum_rows(int v) {
    auto a = __builtin_amdgcn_permlane16_swap((u32)v, (u32)v, false, false);
    v = (int)(a[0] + a[1]);
    auto b = __builtin_amdgcn_permlane32_swap((u32)v, (u32)v, false, false);
    return (int)(b[0] + b[1]);
}
__device__ __forceinline__ float sum_rows_f(float v) {
    auto a = __builtin_amdgcn_permlane16_swap(__float_as_uint(v), __float_as_uint(v), false, false);
    v = __uint_as_float(a[0]) + __uint_as_float(a[1]);
    auto b = __builtin_amdgcn_permlane32_swap(__float_as_uint(v), __float_as_uint(v), false, false);
    return __uint_as_float(b[0]) + __uint_as_float(b[1]);
}
__device__ __forceinline__ void top8_abc(LAS char* t8, const u32 (&key)[4], int tid, int lane, int wave) {
    LAS u32* gmax = (LAS u32*)t8;
    LAS u32* cnt = (LAS u32*)(t8 + 64);
    LAS u32* cand = (LAS u32*)(t8 + 256);
    u32 m = max(max(key[0], key[1]), max(key[2], key[3]));
    { auto a = __builtin_amdgcn_permlane16_swap(m, m, false, false); m = max(a[0], a[1]); }
    { auto a = __builtin_amdgcn_permlane32_swap(m, m, false, false); m = max(a[0], a[1]); }
    if (lane < 16) asm volatile("ds_max_u32 %0, %1" :: "v"((u32)(uintptr_t)(gmax + lane)), "v"(m) : "memory");
    BAR();
    const u32 v = gmax[lane & 15];
    const v4u q = *(LAS v4u*)(gmax + 4 * (lane >> 4));
    int rank = (q.x > v) + (q.y > v) + (q.z > v) + (q.w > v);
    rank = sum_rows(rank);
    const u64 bal = __ballot(rank == 7);
    const u32 tau = (u32)__builtin_amdgcn_readlane((int)v, (int)__builtin_ctzll(bal));
    u64 mk[4]; int tot = 0;
#pragma unroll
    for (int j = 0; j < 4; ++j) { mk[j] = __ballot(key[j] >= tau); tot += __builtin_popcountll(mk[j]); }
    if (tot) {
        u32 base = 0;
        if (lane == 0) asm volatile("ds_add_rtn_u32 %0, %1, %2\n\ts_waitcnt lgkmcnt(0)" : "=v"(base) : "v"((u32)(uintptr_t)cnt), "v"((u32)tot) : "memory");
        base = __builtin_amdgcn_readfirstlane(base);
#pragma unroll
        for (int j = 0; j < 4; ++j) {
            const u32 pos = __builtin_amdgcn_mbcnt_hi((u32)(mk[j] >> 32), __builtin_amdgcn_mbcnt_lo((u32)mk[j], base));
            if (key[j] >= tau) cand[pos] = key[j];
            base += __builtin_popcountll(mk[j]);
        }
    }
    BAR();
}
__device__ __forceinline__ u32 top8_d(LAS char* t8, int lane, int n) {
    LAS u32* cand = (LAS u32*)(t8 + 256);
    u32 res = 0;
    _Pragma("unroll 1") for (int i0 = 0; i0 < n; i0 += 64) {
        const int i = i0 + lane;
        const u32 k = i < n ? cand[i] : 0u;
        int rk = 0;
        _Pragma("unroll 1") for (int j0 = 0; j0 < n; j0 += 16) {
            v4u c[4];
#pragma unroll
            for (int e = 0; e < 4; ++e) c[e] = *(LAS v4u*)(cand + j0 + 4 * e);
#pragma unroll
            for (int e = 0; e < 16; ++e) rk += (c[e >> 2][e & 3] > k) & (j0 + e < n);
        }
        const bool hit = i < n && rk < 8;
        const u32 got = (u32)__builtin_amdgcn_ds_permute((hit ? rk : 63) * 4, (int)(hit ? k : 0u));
        if (lane < 8) res |= got;
    }
    return res;
}

__device__ __forceinline__ u32 lds_peek(LAS char* word) { return __hip_atomic_load((LAS u32*)word, __ATOMIC_RELAXED, __HIP_MEMORY_SCOPE_WORKGROUP); }
#define LWAIT_BODY "1:\n\tds_read_b32 %0, %3 offset:%4\n\ts_waitcnt lgkmcnt(0)\n\tv_readfirstlane_b32 %1, %0\n\ts_cmp_ge_u32 %1, %5\n\ts_cbranch_scc1 2f\n\ts_sleep 1\n\ts_sub_u32 %2, %2, 1\n\ts_cbranch_scc0 1b\n2:"
#define LWAIT(addr, off, want) do { u32 t_, s_, c_; asm volatile("s_mov_b32 %2, 0x400000\n" LWAIT_BODY : "=&v"(t_), "=&s"(s_), "=&s"(c_) : "v"(addr), "n"(off), "n"(want) : "memory", "scc"); } while (0)
#define LWAIT_PEEK(peek, addr, off, want) do { u32 t_, s_, c_; asm volatile("s_nop 0\n\tv_readfirstlane_b32 %1, %6\n\ts_mov_b32 %2, 0x400000\n\ts_cmp_ge_u32 %1, %5\n\ts_cbranch_scc1 2f\n" LWAIT_BODY : "=&v"(t_), "=&s"(s_), "=&s"(c_) : "v"(addr), "n"(off), "n"(want), "v"(peek) : "memory", "scc"); } while (0)
#define LDS_ADD1(addr, one, off) asm volatile("s_mov_b64 exec, 1\n\tds_add_u32 %0, %1 offset:%2\n\ts_mov_b64 exec, -1" :: "v"(addr), "v"(one), "n"(off) : "memory")
#define LDS_SETB(addr, one, off) asm volatile("s_mov_b64 exec, 1\n\tds_write_b8 %0, %1 offset:%2\n\ts_mov_b64 exec, -1" :: "v"(addr), "v"(one), "n"(off) : "memory")
__device__ __forceinline__ void glds16(const void* g, u32 lds_dst, bool nt) {
    u32 keep;
    if (nt) asm volatile("s_mov_b32 %0, m0\n\ts_mov_b32 m0, %2\n\ts_nop 0\n\tglobal_load_lds_dwordx4 %1, off" WNT "\n\ts_mov_b32 m0, %0" : "=&s"(keep) : "v"(g), "s"(lds_dst) : "memory");
    else asm volatile("s_mov_b32 %0, m0\n\ts_mov_b32 m0, %2\n\ts_nop 0\n\tglobal_load_lds_dwordx4 %1, off\n\ts_mov_b32 m0, %0" : "=&s"(keep) : "v"(g), "s"(lds_dst) : "memory");
}
__device__ __forceinline__ bf16x8 band(u32 bits, u32 val) {
    v4u r;
#pragma unroll
    for (int j = 0; j < 4; ++j) r[j] = __builtin_amdgcn_perm((u32)__builtin_amdgcn_sbfe((int)bits, 2 * j + 1, 1), (u32)__builtin_amdgcn_sbfe((int)bits, 2 * j, 1), 0x05040100u) & val;
    return __builtin_bit_cast(bf16x8, r);
}

typedef __attribute__((address_space(4))) const Params* KArg;
#define GET_PARAMS() KArg kp = (KArg)__builtin_amdgcn_kernarg_segment_ptr(); asm volatile("" : "+s"(kp)); const Params p = {kp->x, kp->Are, kp->Aim, kp->Bre, kp->Bim, kp->Cre, kp->Cim, kp->logdt, kp->dskip, kp->wr, kp->wg, kp->wu, kp->wd, kp->gw, kp->out, kp->ws}; \
                     asm volatile("s_waitcnt lgkmcnt(0)" :: "s"(kw0), "s"(kw1))
#define ROLE_END() do { STAMP_FLUSH(); __builtin_amdgcn_s_waitcnt(0x0070); asm volatile("s_endpgm" ::: "memory"); } while (0)
#define MF(A_, B_, C_) __builtin_amdgcn_mfma_f32_16x16x32_bf16(A_, B_, C_, 0, 0, 0)
#define GRP(mask, n) __builtin_amdgcn_sched_group_barrier(mask, n, 0)

__global__ __launch_bounds__(512, 2) void fused_kernel(Params p_unused) {
    STAMP(0);
    u32 kw0, kw1;
    u64 pc0;
    asm volatile("s_getpc_b64 %0" : "=s"(pc0));
    pc0 &= ~(u64)255;
    asm volatile("s_load_dword %0, %2, 0x0\n\ts_load_dword %1, %2, 0x40" : "=&s"(kw0), "=&s"(kw1) : "s"(__builtin_amdgcn_kernarg_segment_ptr()));
    extern __shared__ __attribute__((aligned(16))) char smem_[];
    LAS char* smem = (LAS char*)smem_;
    const int tid = threadIdx.x, lane = tid & 63;
    const int wave = __builtin_amdgcn_readfirstlane(tid >> 6);
    const int b = blockIdx.x, xcd = b & 7, slot = b >> 3;

    if (slot < 4) glds16((const char*)(GAS const char*)pc0 + min(slot * 8 + wave, CODE_KB - 1) * 1024 + lane * 16, (u32)(uintptr_t)(smem + LDS_DUMMY), false);
    const int cw = slot < 16 ? xcd * 16 + slot : 128 + xcd * 2 + (slot - 16);
    const int chbase = cw * 4;
    const int w = xcd * 9 + (slot - 18);
#define ACT() do { \
            LAS float* rr = (LAS float*)(smem + FL_RED) + wave * 128 + lane; \
            float a = 0.f, u = 0.f; \
            _Pragma("unroll") for (int ww = 0; ww < 6; ++ww) { a += rr[ww * 1024]; u += rr[ww * 1024 + 64]; } \
            a = rbf(a); u = rbf(u); \
            const float sl = rbf(a * sigmoidf_(a)); \
            *(LAS u16*)(smem + FL_H + wave * 144 + ((lane & 35) | ((lane & 12) << 1) | ((lane & 16) >> 2)) * 2) = (u16)(cvtpk(sl * u, 0.f) & 0xffffu); } while (0)
    if (slot >= 18) {
    if (slot >= 27 || wave >= 6) {
    if (slot < 28) {
    if (slot == 27)
    {
        GET_PARAMS();
        u32 tag = __hip_atomic_load((GAS u32*)p.ws, RLX_AGENT) + 1u;
        u32 key[4], g01, g23;
        if (wave == 1) ((LAS u32*)(smem + 256))[lane] = 0;
        if (tid < 17) ((LAS u32*)smem)[tid] = 0;
        BAR();
        const u32 base = OFF_RG + tid * 16;
        for (u32 spins = 0;; ++spins) {
            REREAD();
            v4u q0 = __builtin_amdgcn_raw_buffer_load_b128(WSR(), base, 0, 16);
            v4u q1 = __builtin_amdgcn_raw_buffer_load_b128(WSR(), base + 8192, 0, 16);
            bool ok = (q0.y == tag) & (q0.w == tag) & (q1.y == tag) & (q1.w == tag);
            g01 = __builtin_amdgcn_perm(q0.z, q0.x, 0x05040100u); g23 = __builtin_amdgcn_perm(q1.z, q1.x, 0x05040100u);
            key[0] = make_key(q0.x, 2 * tid); key[1] = make_key(q0.z, 2 * tid + 1);
            key[2] = make_key(q1.x, 1024 + 2 * tid); key[3] = make_key(q1.z, 1025 + 2 * tid);
            if (__all(ok) || spins > SPIN_CAP) break;
        }
        STAMP(2);
        *(LAS u32*)(smem + 16384 + tid * 4) = g01; *(LAS u32*)(smem + 16384 + 2048 + tid * 4) = g23;
        top8_abc(smem, key, tid, lane, wave);
        if (wave == 0) {
            LAS u32* cand = (LAS u32*)(smem + 256);
            u32 k16 = cand[lane & 15], nv = *(LAS u32*)(smem + 64);
            v4u q = *(LAS v4u*)(cand + 4 * (lane >> 4));
            asm volatile("" : "+v"(nv), "+v"(k16), "+v"(q));
            int rk = sum_rows((q.x > k16) + (q.y > k16) + (q.z > k16) + (q.w > k16));
            const int nc = __builtin_amdgcn_readfirstlane((int)nv);
            if (__builtin_expect(nc > 16, 0)) {
                k16 = (u32)__shfl((int)top8_d(smem, lane, nc), lane & 7);
                rk = lane & 15;
            }
            if (rk < 8 && lane < 16) {
                __hip_atomic_store((GAS u64*)(p.ws + OFF_SEL + xcd * 256) + rk, ((u64)tag << 32) | k16, RLX_AGENT);
                const u32 gs = *(LAS u16*)(smem + 16384 + ((0xffffu - (k16 & 0xffffu)) & 2047) * 2);
                __builtin_amdgcn_raw_buffer_store_b128((v4u){k16, tag, gs, tag}, WSR(), OFF_SEL + (8 + xcd) * 256 + rk * 16, 0, 16);
            }
            if (lane == 32) __hip_atomic_store((GAS u64*)(p.ws + OFF_CI) + 272 + xcd, ((u64)tag << 32) | tag, RLX_AGENT);
            const u64 selm = __ballot(rk < 8 && lane < 16);
            u64 mm = selm;
#pragma unroll
            for (int j = 0; j < 8; j += 4) {
                int tt[4];
#pragma unroll
                for (int i = 0; i < 4; ++i) { const int ln = __builtin_ctzll(mm | (1ull << 63)); mm &= mm - 1; tt[i] = (int)(0xffffu - ((u32)__builtin_amdgcn_readlane((int)k16, ln) & 0xffffu)) & 2047; }
                const int tq = (lane >> 4) == 0 ? tt[0] : ((lane >> 4) == 1 ? tt[1] : ((lane >> 4) == 2 ? tt[2] : tt[3]));
                glds16(p.x + (size_t)tq * D + min(lane & 15, 8) * 64, (u32)(uintptr_t)(smem + 8192), false);
            }
        }
        STAMP(3);
        if (xcd == 0) {
            const u32 cio = OFF_CI + (tid < 140 ? tid : 0) * 16;
            for (u32 spins = 0;; ++spins) {
                REREAD();
                const v4u q2 = __builtin_amdgcn_raw_buffer_load_b128(WSR(), cio, 0, 16);
                if (__all((q2.y == tag) & (q2.w == tag)) || spins > SPIN_CAP) break;
                __builtin_amdgcn_s_sleep(16);
            }
            BAR();
            if (tid == 0) __hip_atomic_store((GAS u32*)p.ws, tag, RLX_AGENT);
        }
        ROLE_END();
    }
    else
        {
            GET_PARAMS();
            const u32 tg = __builtin_amdgcn_readfirstlane(__hip_atomic_load((GAS u32*)p.ws, RLX_AGENT)) + 1u;
            *(LAS u32*)(smem + FL_TAG) = tg;
            asm volatile("s_mov_b64 exec, 1\n\tglobal_store_dwordx2 %0, %1, %2 sc1\n\ts_mov_b64 exec, -1" :: "v"(OFF_CI + (128 + 2 * w + (wave - 6)) * 8), "v"((v2u){tg, tg}), "s"(p.ws) : "memory");
            v2u sel;
            u32 c_;
            const u32 l16 = lane * 16;
            asm volatile("s_mov_b32 %[c], 0x80000\n1:\n\tglobal_load_dwordx2 v[10:11], %[so], %[ws] sc1\n\ts_waitcnt vmcnt(0)\n\tv_cmp_ne_u32 vcc, %[tag], v11\n\ts_cbranch_vccz 2f\n\ts_sleep 1\n\ts_sub_u32 %[c], %[c], 1\n\ts_cbranch_scc0 1b\n2:"
                         : "=&{v[10:11]}"(sel), [c] "=&s"(c_) : [so] "v"(OFF_SEL + xcd * 256 + (4 * (wave - 6) + (lane & 3)) * 8), [ws] "s"(p.ws), [tag] "s"(tg) : "memory", "scc", "vcc");
            STAMP(3);
#pragma unroll
            for (int k = 0; k < 4; ++k) {
                const u32 t = ~(u32)__builtin_amdgcn_readlane((int)sel.x, k) & 2047u;
                const char* xr = (const char*)p.x + t * (D * 2);
                const u32 dst = (u32)(uintptr_t)(smem + FL_XS) + ((wave - 6) * 4 + k) * XSP;
                u32 keep;
                asm volatile("s_mov_b32 %0, m0\n\ts_mov_b32 m0, %3\n\ts_nop 0\n\tglobal_load_lds_dwordx4 %1, %2\n\ts_mov_b64 exec, 0xff\n\tglobal_load_lds_dwordx4 %1, %2 offset:1024\n\ts_mov_b64 exec, -1\n\ts_mov_b32 m0, %0"
                             : "=&s"(keep) : "v"(l16), "s"(xr), "s"(dst) : "memory");
            }
            BAR();
            asm volatile("s_waitcnt vmcnt(0)\n\ts_barrier" ::: "memory");
            STAMP(10);
            BAR();
            ACT();
            BAR();
            ROLE_END();
        }
    } else
    {
        GET_PARAMS();
        const int gw = (slot - 28) + 4 * wave;
        const u32 dst = (u32)(uintptr_t)(smem + wave * 1024);
        if (FDELAY) __builtin_amdgcn_s_sleep(FDELAY);
        {
            const int wa = xcd * 9 + ((3 - (int)(((uintptr_t)p.wg >> 7) & 7) - xcd * 9) & 7);
            const u16* sb = ((lane & 8) ? p.wu : p.wg) + (size_t)(8 * (lane >> 4)) * F + 64 * wa + 8 * (lane & 7);
            const int two = wa == xcd * 9;
            _Pragma("unroll 1") for (int e = gw; e < (144 << two); e += 32) {
                const int r = e >> two, stp = r / 6, v = r - 6 * stp;
                glds16(sb + (size_t)(96 * v + 32 * (stp >> 3) + (stp & 7)) * F + (e & two) * 512, dst, false);
            }
        }
        _Pragma("unroll 1") for (int e = gw + (12 - PFWD) * 54; e < 12 * 54; e += 32) {
            const int j = e / 9, r = e - 9 * j;
            glds16(p.wd + (size_t)(xcd * 648 + r * 72 + j) * 512 + lane * 8, dst, false);
        }
        if (PFS0 < 24 && gw < 27) {
            const u16* src[2];
#pragma unroll
            for (int k = 0; k < 2; ++k) {
                const int ln = (2 * gw + k) * 8 + (lane >> 3), seg = ln / 9, rr = seg >> 1;
                src[k] = ((seg & 1) ? p.wu : p.wg) + (size_t)(96 * (rr >> 2) + 8 * (rr & 3)) * F + xcd * 576 + (ln - 9 * seg) * 64 + (lane & 7) * 8;
            }
            _Pragma("unroll 1") for (int stp = PFS0; stp < 24; ++stp) {
                const size_t ro = (size_t)(32 * (stp >> 3) + (stp & 7)) * F;
                glds16(src[0] + ro, dst, false);
                glds16(src[1] + ro, dst, false);
            }
        }
        STAMP(11);
        asm volatile("s_waitcnt vmcnt(0)" ::: "memory");
        STAMP(6);
        STAMP_FLUSH();
    }
    } else
        {
            const int n = lane & 15, q = lane >> 4;
            GET_PARAMS();
            v4u gu[3][8];
            if (FDELAY) __builtin_amdgcn_s_sleep(FDELAY);
            const char* dsrc = (const char*)(p.wd + (size_t)(64 * w) * D);
            const u32 ldst = (u32)(uintptr_t)(smem + FL_WD) + wave * 1024;
            u32 dof[13];
#define LD_WD(j) do { u32 keep; asm volatile("s_mov_b32 %0, m0\n\ts_mov_b32 m0, %3\n\ts_nop 0\n\tglobal_load_lds_dwordx4 %1, %2" WNT "\n\ts_mov_b32 m0, %0" : "=&s"(keep) : "v"(dof[j]), "s"(dsrc), "s"(ldst + (j) * 6144) : "memory"); } while (0)
            const u16* wsrc = (n < 8 ? p.wg : p.wu) + 64 * w + 8 * (n & 7) + (size_t)(96 * wave + 8 * q) * F;
#define LD_GU(s) _Pragma("unroll") for (int j = 0; j < 8; ++j) asm volatile("global_load_dwordx4 %0, %1, off" WNT : "=v"(gu[s][j]) : "v"(wsrc + (size_t)(32 * s + j) * F))
            LD_GU(0);
            LD_GU(1);
            LD_GU(2);
#pragma unroll
            for (int j = 0; j < 13; ++j) {
                const u32 id = min((u32)((6 * j + wave) * 64 + lane), 4735u), row = id / 74u;
                dof[j] = row * 1152u + min(id - 74u * row, 71u) * 16u;
                asm volatile("" : "+v"(dof[j]));
            }
#pragma unroll
            for (int j = 0; j < K1; ++j) LD_WD(j);
            STAMP(11);
            bf16x8 guf[8];
            auto tr88 = [&](const v4u (&in)[8], bf16x8 (&o)[8]) __attribute__((always_inline)) {
#pragma unroll
                for (int pp = 0; pp < 4; ++pp) {
                    v4u lo, hi;
#pragma unroll
                    for (int jj = 0; jj < 4; ++jj) {
                        lo[jj] = __builtin_amdgcn_perm(in[2 * jj + 1][pp], in[2 * jj][pp], 0x05040100u);
                        hi[jj] = __builtin_amdgcn_perm(in[2 * jj + 1][pp], in[2 * jj][pp], 0x07060302u);
                    }
                    o[2 * pp] = __builtin_bit_cast(bf16x8, lo); o[2 * pp + 1] = __builtin_bit_cast(bf16x8, hi);
                }
#pragma unroll
                for (int i = 0; i < 8; ++i) asm volatile("" : "+v"(o[i]));
            };
#define WAIT_GU(s, N) asm volatile("s_waitcnt vmcnt(%8)" : "+v"(gu[s][0]), "+v"(gu[s][1]), "+v"(gu[s][2]), "+v"(gu[s][3]), "+v"(gu[s][4]), "+v"(gu[s][5]), "+v"(gu[s][6]), "+v"(gu[s][7]) : "n"(N + K1))
            LAS char* xp = smem + FL_XS + (n & 7) * XSP + (96 * wave + 8 * q) * 2;
            LAS char* ta = smem + FL_WD + (lane >> 2) * WDP + (lane & 3) * 8 + wave * 64;
            LAS char* rp = smem + FL_RED + ((wave * 8 + 4 * (q & 1)) * 128 + 8 * n) * 4 + (q >> 1) * 16;
            const u32 obase = OFF_PART + ((4 * (2 * wave + (n >> 3)) + q) * NFFN + w) * 128 + (n & 7) * 16;
            u32 obase_ = obase;
            LAS char* hp = smem + FL_H + (n & 7) * 144 + q * 16;
            asm volatile("" : "+v"(xp), "+v"(ta), "+v"(rp), "+v"(obase_), "+v"(hp));
            bf16x8 g0[8], g1[8];
            WAIT_GU(0, 16);
            STAMP(5);
            tr88(gu[0], g0);
            WAIT_GU(1, 8);
            tr88(gu[1], g1);
            STAMP(12);
            WAIT_GU(2, 0);
            STAMP(6);
            tr88(gu[2], guf);
            SBAR();
            __builtin_amdgcn_s_barrier();
            SBAR();
#pragma unroll
            for (int j = K1; j < K1 + K2; ++j) LD_WD(j);
            asm volatile("s_barrier" ::: "memory");
            bf16x8 xf[3];
#pragma unroll
            for (int s = 0; s < 3; ++s) xf[s] = *(LAS bf16x8*)(xp + s * 64);
            v4f ha[8];
#pragma unroll
            for (int i = 0; i < 24; ++i) {
                ha[i & 7] = MF(xf[i >> 3], i < 8 ? g0[i & 7] : (i < 16 ? g1[i & 7] : guf[i & 7]), i < 8 ? ((v4f){0.f, 0.f, 0.f, 0.f}) : ha[i & 7]);
                if ((i & 1) && K1 + K2 + (i >> 1) < 13) { SBAR(); LD_WD(K1 + K2 + (i >> 1)); SBAR(); }
            }
#pragma unroll
            for (int r = 0; r < 4; ++r) {
                const v4f lo = {ha[0][r], ha[1][r], ha[2][r], ha[3][r]}, hi = {ha[4][r], ha[5][r], ha[6][r], ha[7][r]};
                *(LAS v4f*)(rp + r * 512) = q < 2 ? lo : hi;
            }
            const u32 tag = *(LAS u32*)(smem + FL_TAG);
            STAMP(7);
            BAR();
            ACT();
            STAMP(8);
            asm volatile("s_waitcnt vmcnt(0) lgkmcnt(0)\n\ts_barrier" ::: "memory");
            STAMP(13);
            const bf16x8 h0 = *(LAS bf16x8*)(hp);
            const bf16x8 h1 = *(LAS bf16x8*)(hp + 64);
            s16x4 a[6][4];
#pragma unroll
            for (int k = 0; k < 6; ++k) {
                const int ko = (k >> 1) * 384 + (k & 1) * 32;
                a[k][0] = __builtin_amdgcn_ds_read_tr16_b64_v4i16((LAS s16x4*)(ta + ko));
                a[k][1] = __builtin_amdgcn_ds_read_tr16_b64_v4i16((LAS s16x4*)(ta + ko + 16 * WDP));
            }
#pragma unroll
            for (int k = 0; k < 6; ++k) {
                const int ko = (k >> 1) * 384 + (k & 1) * 32;
                a[k][2] = __builtin_amdgcn_ds_read_tr16_b64_v4i16((LAS s16x4*)(ta + ko + 32 * WDP));
                a[k][3] = __builtin_amdgcn_ds_read_tr16_b64_v4i16((LAS s16x4*)(ta + ko + 48 * WDP));
            }
            SBAR();
            v4f ya[6];
#pragma unroll
            for (int k = 0; k < 6; ++k) ya[k] = __builtin_amdgcn_mfma_f32_16x16x32_bf16(__builtin_shufflevector(a[k][0], a[k][1], 0, 1, 2, 3, 4, 5, 6, 7), h0, (v4f){0.f, 0.f, 0.f, 0.f}, 0, 0, 0);
#pragma unroll
            for (int k = 0; k < 6; ++k) ya[k] = __builtin_amdgcn_mfma_f32_16x16x32_bf16(__builtin_shufflevector(a[k][2], a[k][3], 0, 1, 2, 3, 4, 5, 6, 7), h1, ya[k], 0, 0, 0);
#pragma unroll
            for (int k = 0; k < 3; ++k) {
                const v4f v = n < 8 ? ya[2 * k] : ya[2 * k + 1];
                __builtin_amdgcn_raw_buffer_store_b128((v4u){cvtpk(v[0], v[1]), tag, cvtpk(v[2], v[3]), tag}, WSR(), obase_ + k * (48 * NFFN * 128), 0, 16);
            }
            STAMP(9);
            ROLE_END();
        }
    } else {
    if (wave >= 4)
    {
        const int hw = wave - 4, ht = tid - 256;
        const v4u z4 = {0, 0, 0, 0};
        GET_PARAMS();
        u32 tagv;
        asm volatile("global_load_dword %0, %1, %2 sc1" : "=v"(tagv) : "v"(0u), "s"(p.ws) : "memory");
        const int sub = lane & 15;
        v4u xr[5];
        const int rrow = 16 * (cw & 127) + 4 * hw;
        {
            const u32 dst = (u32)(uintptr_t)(smem + LDS_RW) + hw * 3072;
            glds16(p.wr + lane * 8, dst, false);
            glds16(lane < 8 ? p.wr + 512 + lane * 8 : p.gw + (lane - 8) * 8, dst + 1024, false);
            glds16(p.gw + 448 + (lane & 15) * 8, dst + 2048, false);
            const u32 ro = (u32)((rrow + (lane >> 4)) * D + sub * 8) * 2, ro4 = ro - (sub & 8) * 16;
#pragma unroll
            for (int j = 0; j < 4; ++j) asm volatile("global_load_dwordx4 %0, %1, %2 offset:%3" : "=v"(xr[j]) : "v"(ro), "s"(p.x), "n"(j * 256));
            asm volatile("global_load_dwordx4 %0, %1, %2 offset:1024" : "=v"(xr[4]) : "v"(ro4), "s"(p.x));
        }
        __builtin_amdgcn_s_barrier();
        STAMP(1);
        if (XPF) {
            const int id = min((slot * 4 + hw) * 64 + lane, 4095);
            u32 keep;
            asm volatile("s_mov_b32 %0, m0\n\ts_mov_b32 m0, %3\n\ts_nop 0\n\tglobal_load_lds_dword %1, %2\n\ts_mov_b32 m0, %0" : "=&s"(keep) : "v"((u32)((id >> 1) * 1152 + ((id & 1) ? 1024 : xcd * 128))), "s"(p.x), "s"((u32)(uintptr_t)(smem + LDS_DUMMY)) : "memory");
        }
        v2u uu[8];
        const u32 uo0 = (u32)((256 * hw + 4 * lane) * D + chbase) * 2, uo1 = uo0 + 1024 * D * 2;
#define LD_U(i) asm volatile("global_load_dwordx2 %0, %1, %2 offset:%3" : "=v"(uu[i]) : "v"((i) < 4 ? uo0 : uo1), "s"(p.x), "n"(((i) & 3) * D * 2))
#pragma unroll
        for (int i = 0; i < NB1; ++i) LD_U(i);
        *(LAS v4u*)(smem + LDS_TAP + ht * 16) = z4; *(LAS v4u*)(smem + LDS_TAP + 4096 + ht * 16) = z4;
        *(LAS v4u*)(smem + (ht < 128 ? LDS_TAP + 8192 + ht * 16 : LDS_U + ((ht >> 5) & 3) * 4608 + (ht & 31) * 16)) = z4;
        u32 ctl = (u32)(uintptr_t)(smem + LDS_CTL), one = 1u, cty = ctl + 4 * hw;
        u32 po = cw >= 128 ? OFF_CI + ((cw - 128) * 4 + hw) * 16 : (lane < 2 ? OFF_RG + rrow * 8 + lane * 16 : OFF_G + (lane - 2) * 8192 + rrow * 4);
        const u64 pmask = cw >= 128 ? 1ull : (1ull << (2 + REPG)) - 1;
        u32 go = OFF_G + (cw % REPG) * 8192 + 2048 + (64 * hw + lane) * 16;
        LAS char* up = smem + LDS_U + (256 + 256 * hw + 4 * lane) * 2;
        LAS char* yp = smem + CH_Y + hw * 1152 + (lane + (lane >> 3)) * 16;
        LAS char* rw = smem + LDS_RW + hw * 3072 + sub * 16;
        asm volatile("" : "+v"(ctl), "+v"(one), "+v"(cty), "+v"(po), "+v"(go), "+v"(up), "+v"(yp), "+v"(rw));
        BAR();
        __builtin_amdgcn_s_setprio(3);
        u32 tag;
        {
            asm volatile("s_waitcnt vmcnt(%6)" : "+v"(xr[0]), "+v"(xr[1]), "+v"(xr[2]), "+v"(xr[3]), "+v"(xr[4]), "+v"(tagv) : "n"(NB1 + XPF) : "memory");
            STAMP(2);
            tag = __builtin_amdgcn_readfirstlane(tagv) + 1u;
            if (sub >= 8) xr[4] = z4;
            float r = 0.f, s = 0.f;
#pragma unroll
            for (int j = 0; j < 5; ++j) {
                const v4u a = *(LAS v4u*)(rw + j * 256), g = *(LAS v4u*)(rw + 1152 + j * 256);
#pragma unroll
                for (int e = 0; e < 4; ++e) {
                    const u32 xe = xr[j][e], ae = a[e], ge = g[e];
                    r = __builtin_amdgcn_fdot2_f32_bf16(__builtin_bit_cast(bf16x2_t, xe), __builtin_bit_cast(bf16x2_t, ae), r, false);
                    s = __builtin_amdgcn_fdot2_f32_bf16(__builtin_bit_cast(bf16x2_t, xe), __builtin_bit_cast(bf16x2_t, ge), s, false);
                }
            }
            r = dpp_add<0x128, 0xf>(r); s = dpp_add<0x128, 0xf>(s); r = dpp_add<0x124, 0xf>(r); s = dpp_add<0x124, 0xf>(s);
            r = dpp_add<0x122, 0xf>(r); s = dpp_add<0x122, 0xf>(s); r = dpp_add<0x121, 0xf>(r); s = dpp_add<0x121, 0xf>(s);
            const u32 pay = make_payload(r, sigmoidf_(rbf(s)));
            const u32 p0 = (u32)__builtin_amdgcn_readlane((int)pay, 0), p1 = (u32)__builtin_amdgcn_readlane((int)pay, 16), p2 = (u32)__builtin_amdgcn_readlane((int)pay, 32), p3 = (u32)__builtin_amdgcn_readlane((int)pay, 48);
            const u32 g01 = (p0 & 0xffffu) | (p1 << 16), g23 = (p2 & 0xffffu) | (p3 << 16);
            const v4u pv = {lane == 0 ? p0 : (lane == 1 ? p2 : g01), tag, lane == 0 ? p1 : (lane == 1 ? p3 : g23), tag};
            asm volatile("s_mov_b64 exec, %3\n\tglobal_store_dwordx4 %0, %1, %2 sc1\n\ts_mov_b64 exec, -1\n\ts_nop 0" :: "v"(po), "v"(pv), "s"(p.ws), "s"(pmask) : "memory");
        }
        STAMP(3);
        __builtin_amdgcn_s_setprio(0);
        *(LAS u32*)(smem + LDS_CTL + C_TAG) = tag;
        LDS_ADD1(ctl, one, C_PUB);
        LWAIT(ctl, C_PUB, 4);
#pragma unroll
        for (int i = NB1; i < 8; ++i) LD_U(i);
        v4u gq[2];
#define POLL_G "global_load_dwordx4 v[100:103], %[go], %[ws] offset:-2048 sc1\n\tglobal_load_dwordx4 v[104:107], %[go], %[ws] offset:2048 sc1\n\t"
        asm volatile(POLL_G : "={v[100:103]}"(gq[0]), "={v[104:107]}"(gq[1]) : [go] "v"(go), [ws] "s"(p.ws) : "memory");
        STAMP(4);
        auto stage = [&](int g) __attribute__((always_inline)) {
            const v2u* u4 = uu + 4 * g;
            LAS char* d = up + g * 2048;
            *(LAS v2u*)(d) = (v2u){__builtin_amdgcn_perm(u4[1].x, u4[0].x, 0x05040100u), __builtin_amdgcn_perm(u4[3].x, u4[2].x, 0x05040100u)};
            *(LAS v2u*)(d + 4608) = (v2u){__builtin_amdgcn_perm(u4[1].x, u4[0].x, 0x07060302u), __builtin_amdgcn_perm(u4[3].x, u4[2].x, 0x07060302u)};
            *(LAS v2u*)(d + 2 * 4608) = (v2u){__builtin_amdgcn_perm(u4[1].y, u4[0].y, 0x05040100u), __builtin_amdgcn_perm(u4[3].y, u4[2].y, 0x05040100u)};
            *(LAS v2u*)(d + 3 * 4608) = (v2u){__builtin_amdgcn_perm(u4[1].y, u4[0].y, 0x07060302u), __builtin_amdgcn_perm(u4[3].y, u4[2].y, 0x07060302u)};
            LDS_ADD1(ctl, one, C_STG);
        };
        asm volatile("s_waitcnt vmcnt(6)" : "+v"(uu[0]), "+v"(uu[1]), "+v"(uu[2]), "+v"(uu[3]));
        stage(0);
        STAMP(5);
        asm volatile("s_waitcnt vmcnt(2)" : "+v"(uu[4]), "+v"(uu[5]), "+v"(uu[6]), "+v"(uu[7]));
        LWAIT(ctl, C_STG, 4);
        stage(1);
        STAMP(6);
        {
            u32 c_;
            asm volatile("s_mov_b32 %[c], 0x80000\n1:\n\ts_waitcnt vmcnt(0)\n\tv_cmpx_eq_u32 %[tag], v101\n\tv_cmpx_eq_u32 %[tag], v103\n\tv_cmpx_eq_u32 %[tag], v105\n\tv_cmpx_eq_u32 %[tag], v107\n\t"
                         "s_cmp_eq_u64 exec, -1\n\ts_mov_b64 exec, -1\n\ts_cbranch_scc1 2f\n\ts_sleep 4\n\t" POLL_G "s_sub_u32 %[c], %[c], 1\n\ts_cbranch_scc0 1b\n\ts_waitcnt vmcnt(0)\n2:\n\ts_nop 4"
                         : "+{v[100:103]}"(gq[0]), "+{v[104:107]}"(gq[1]), [c] "=&s"(c_) : [go] "v"(go), [ws] "s"(p.ws), [tag] "s"(tag) : "memory", "scc", "vcc");
        }
        STAMP(7);
#pragma unroll
        for (int g = 0; g < 2; ++g) {
            const v2u* u4 = uu + 4 * g;
            const v2f g01 = {bflo(gq[g].x), bfhi(gq[g].x)}, g23 = {bflo(gq[g].z), bfhi(gq[g].z)};
            v2f xa[4], xb[4];
            xa[0] = (v2f){bflo(u4[0].x), bflo(u4[1].x)}; xa[1] = (v2f){bfhi(u4[0].x), bfhi(u4[1].x)}; xa[2] = (v2f){bflo(u4[0].y), bflo(u4[1].y)}; xa[3] = (v2f){bfhi(u4[0].y), bfhi(u4[1].y)};
            xb[0] = (v2f){bflo(u4[2].x), bflo(u4[3].x)}; xb[1] = (v2f){bfhi(u4[2].x), bfhi(u4[3].x)}; xb[2] = (v2f){bflo(u4[2].y), bflo(u4[3].y)}; xb[3] = (v2f){bfhi(u4[2].y), bfhi(u4[3].y)};
#pragma unroll
            for (int c = 0; c < 4; ++c) asm volatile("" : "+v"(xa[c]), "+v"(xb[c]));
            if (g) LWAIT(cty, C_Y + 16, 0x01010101); else LWAIT(cty, C_Y, 0x01010101);
            v2f oa[4], ob[4];
#pragma unroll
            for (int c = 0; c < 4; ++c) {
                const v4f y = *(LAS v4f*)(yp + c * KCH + g * 4608);
                oa[c] = __builtin_elementwise_fma(g01, (v2f){y[0], y[1]} - xa[c], xa[c]);
                ob[c] = __builtin_elementwise_fma(g23, (v2f){y[2], y[3]} - xb[c], xb[c]);
            }
#define ST_O(i, v) asm volatile("global_store_dwordx2 %0, %1, %2 offset:%3" :: "v"(g ? uo1 : uo0), "v"(v), "s"(p.out), "n"((i) * D * 2) : "memory")
            ST_O(0, ((v2u){cvtpk(oa[0].x, oa[1].x), cvtpk(oa[2].x, oa[3].x)}));
            ST_O(1, ((v2u){cvtpk(oa[0].y, oa[1].y), cvtpk(oa[2].y, oa[3].y)}));
            ST_O(2, ((v2u){cvtpk(ob[0].x, ob[1].x), cvtpk(ob[2].x, ob[3].x)}));
            ST_O(3, ((v2u){cvtpk(ob[0].y, ob[1].y), cvtpk(ob[2].y, ob[3].y)}));
            STAMP(8 + g);
        }
        asm volatile("s_waitcnt vmcnt(0)" ::: "memory");
        LDS_ADD1(ctl, one, C_ACK);
        STAMP(10);
        ROLE_END();
    }
    else
    {
        GET_PARAMS();
        const int ch = wave;
        const int dch = chbase + ch, pi = dch * NST + lane * 2;
        const u32 are = __builtin_nontemporal_load((const u32*)(p.Are + pi)), aim = __builtin_nontemporal_load((const u32*)(p.Aim + pi)), bre = __builtin_nontemporal_load((const u32*)(p.Bre + pi));
        const u32 bim = __builtin_nontemporal_load((const u32*)(p.Bim + pi)), cre = __builtin_nontemporal_load((const u32*)(p.Cre + pi)), cim = __builtin_nontemporal_load((const u32*)(p.Cim + pi));
        const u16 ldt = p.logdt[dch], dsk = p.dskip[dch];
        SBAR();
        __builtin_amdgcn_s_barrier();
        SBAR();
        LAS char* kb = smem + ch * KCH;
        const int r32 = lane & 31, h = lane >> 5, q = lane >> 4, n = lane & 15;
        const int ai = 4 * (r32 >> 3) + (r32 & 3), hh = (r32 >> 2) & 1;
        const u64 t0lo = 0x0d0a090803020100ull, t0hi = 0x000000000013110full;
        const u64 t1lo = 0x0e0c0b0a07060504ull, t1hi = 0x0000000000141210ull;
        const u64 tsel = ai < 8 ? (hh ? t1lo : t0lo) : (hh ? t1hi : t0hi);
        const int o = (int)((tsel >> (8 * (ai & 7))) & 0xff);
        LAS char* ap = kb + o * STG_ROW + h * 16;
        LAS char* bp = kb + (NA + r32) * STG_ROW + h * 16;
        LAS v2u* sg = (LAS v2u*)(kb + lane * 8);
        bf16x8 b5 = band(((0x1fu << 6) << n) >> (8 * q), 0x3f803f80u), b3 = band(((0x7u << 11) << n) >> (8 * q), 0x3f803f80u);
        bf16x8 b9 = band(((0x1ffu << 4) << n) >> (8 * q), 0x3f803f80u), b7 = band(((0x7fu << 5) << n) >> (8 * q), 0x3f803f80u);
        bf16x8 pat = band(((0x3u << 12) << n) >> (8 * q), 0xffffffffu);
        LAS char* k0 = kb + 32 + lane * 16;
        LAS char* kt = kb + (lane & 3) * KCOPY + (lane < 16 ? 32 + 1024 + (lane >> 2) * 16 : ((lane >> 2) & 1) * 16);
        LAS char* a0 = kb + ((lane >> 2) & 3) * KCOPY + (28 - 8 * q + 4 * (lane & 3)) * 2;
        LAS char* bb = smem + LDS_U + ch * 4608 + (16 * n + 8 * q - 16) * 2;
        LAS char* yb = kb + CH_Y + (4 * n + q + (n >> 1)) * 16;
        LAS char* tp = smem + LDS_TAP + ch * 2304 + ((n & 1) ? 13 + n - 4 * q : 96 + 14 + n - 4 * q) * 4;
        LAS char* ta = smem + LDS_TAP + ch * 2304 + (h ? 0 : 768) + 64 + 2 * r32;
        LAS char* tb = smem + LDS_TAP + ch * 2304 + 1600 + 64 * h + 2 * r32;
        u32 ctl = (u32)(uintptr_t)(smem + LDS_CTL), ctc = ctl + ch, one = 1u;
        u32 m01 = lane < 2 ? ~0u : 0u, m2 = lane == 2 ? ~0u : 0u, m3 = lane == 3 ? ~0u : 0u, m0_ = lane == 0 ? ~0u : 0u;
        asm volatile("" : "+v"(b5), "+v"(b3), "+v"(b9), "+v"(b7), "+v"(pat), "+v"(ap), "+v"(bp), "+v"(sg), "+v"(m01), "+v"(m2), "+v"(m3), "+v"(m0_));
        asm volatile("" : "+v"(k0), "+v"(kt), "+v"(a0), "+v"(bb), "+v"(yb), "+v"(tp), "+v"(ta), "+v"(tb), "+v"(ctl), "+v"(ctc), "+v"(one));
        __builtin_amdgcn_s_barrier();
        SBAR();
        const float dt = rbf(expf(bf2f(ldt)));
        STAMP(1);
        v16f acc = {};
        float kscale;
        {
            const v2f a2 = (v2f){bflo(are), bfhi(are)} * dt * 1.44269504088896341f;
            const v2f brev = (v2f){bflo(aim), bfhi(aim)} * dt * 0.15915494309189535f;
            const cf2 Bc = {{dt * bflo(bre), dt * bflo(bim)}, {dt * bfhi(bre), dt * bfhi(bim)}};
            const cf2 Cc = {{bflo(cre), bflo(cim)}, {bfhi(cre), bfhi(cim)}};
            cf2 W = cmul2(Cc, Bc);
            u32 mx = max(max(__float_as_uint(W.a.x) & 0x7f800000u, __float_as_uint(W.a.y) & 0x7f800000u), max(__float_as_uint(W.b.x) & 0x7f800000u, __float_as_uint(W.b.y) & 0x7f800000u));
            mx = dpp_umax<0x111, 0xf>(mx); mx = dpp_umax<0x112, 0xf>(mx); mx = dpp_umax<0x114, 0xf>(mx); mx = dpp_umax<0x118, 0xf>(mx);
            mx = dpp_umax<0x142, 0xa>(mx); mx = dpp_umax<0x143, 0xc>(mx);
            mx = min(max((u32)__builtin_amdgcn_readlane((int)mx, 63), 0x00800000u), 0x7e800000u);
            kscale = __uint_as_float(mx);
            const float inv = __uint_as_float(0x7f000000u - mx);
            W.a *= inv; W.b *= inv;
            auto put = [&](int oo, cf2 v) __attribute__((always_inline)) { sg[oo * (STG_ROW / 8)] = (v2u){cvth(v.a), cvth(v.b)}; };
            auto putc = [&](int c, cf2 v) __attribute__((always_inline)) { put(NA + c, v); };
            const cf2 E1 = cexp2(a2, brev, 1.f), E8 = cexp2(a2, brev, 8.f), E256 = cexp2(a2, brev, 256.f);
            const cf2 c1 = {{E8.a.x, -E8.a.y}, {E8.b.x, -E8.b.y}};
            const cf2 c2 = cmul2(c1, c1), E2 = cmul2(E1, E1), Y256 = cmul2(W, E256), X1 = cmul2(W, E1);
            put(0, W); putc(0, (cf2){{1.f, 0.f}, {1.f, 0.f}}); putc(1, c1);
            const cf2 c4 = cmul2(c2, c2), c3 = cmul2(c2, c1), E4 = cmul2(E2, E2), Y264 = cmul2(Y256, E8), Y258 = cmul2(Y256, E2), X2 = cmul2(W, E2), X3 = cmul2(X1, E2);
            putc(2, c2); put(8, Y256); put(1, X1);
            const cf2 c8 = cmul2(c4, c4), c5 = cmul2(c4, c1), c6 = cmul2(c4, c2), c7 = cmul2(c4, c3), Y520 = cmul2(Y264, E256), Y260 = cmul2(Y256, E4);
            putc(4, c4); putc(3, c3); put(12, Y264); put(9, Y258); put(2, X2); put(3, X3);
            const cf2 c16 = cmul2(c8, c8), Y776 = cmul2(Y520, E256), Y516 = cmul2(Y260, E256);
            const cf2 c9 = cmul2(c8, c1), c10 = cmul2(c8, c2), c11 = cmul2(c8, c3), c12 = cmul2(c8, c4);
            putc(8, c8); putc(5, c5); putc(6, c6); putc(7, c7); put(14, Y520); put(10, Y260);
            const cf2 Y1032 = cmul2(Y776, E256), c13 = cmul2(c8, c5), c14 = cmul2(c8, c6), c15 = cmul2(c8, c7);
            put(4, cmul2(W, E4)); put(5, cmul2(X1, E4)); put(6, cmul2(X2, E4)); put(7, cmul2(X3, E4)); put(11, cmul2(Y258, E4)); put(15, cmul2(Y516, E256));
            putc(16, c16); put(16, Y776); put(13, Y516); putc(9, c9); putc(10, c10); putc(11, c11); putc(12, c12);
            const cf2 Y1288 = cmul2(Y1032, E256);
            putc(17, cmul2(c16, c1)); putc(18, cmul2(c16, c2)); putc(19, cmul2(c16, c3)); putc(20, cmul2(c16, c4));
            put(17, Y1032); putc(13, c13); putc(14, c14); putc(15, c15);
            const cf2 Y1544 = cmul2(Y1288, E256);
            putc(21, cmul2(c16, c5)); putc(22, cmul2(c16, c6)); putc(23, cmul2(c16, c7)); putc(24, cmul2(c16, c8)); put(18, Y1288);
            put(20, cmul2(Y1544, E256)); put(19, Y1544);
            putc(25, cmul2(c16, c9)); putc(26, cmul2(c16, c10)); putc(27, cmul2(c16, c11)); putc(28, cmul2(c16, c12));
            putc(29, cmul2(c16, c13)); putc(30, cmul2(c16, c14)); putc(31, cmul2(c16, c15));
            STAMP(2);
            asm volatile("" ::: "memory");
            f16x8 fa[16], fb[16];
#pragma unroll
            for (int ks = 0; ks < 16; ++ks) { fa[ks] = *(LAS f16x8*)(ap + ks * 32); fb[ks] = *(LAS f16x8*)(bp + ks * 32); }
            SBAR();
            asm volatile("" ::: "memory");
#pragma unroll
            for (int ks = 0; ks < 16; ++ks) acc = __builtin_amdgcn_mfma_f32_32x32x16_f16(fa[ks], fb[ks], acc, 0, 0, 0);
            acc *= kscale;
        }
        STAMP(3);
        acc[0] += __uint_as_float(((u32)dsk << 16) & m0_);
        u32 t78 = cvtpk(acc[7], acc[8]), t9a = cvtpk(acc[9], acc[10]);
        {
            const auto s0 = __builtin_amdgcn_permlane32_swap(cvtpk(acc[0], acc[1]), cvtpk(acc[4], acc[4]), false, false);
            const auto s1 = __builtin_amdgcn_permlane32_swap(cvtpk(acc[2], acc[3]), cvtpk(acc[5], acc[6]), false, false);
            const v4u ww = {s0[0], s1[0], s0[1], s1[1]};
            const u32 pz = (u32)__builtin_amdgcn_update_dpp(0, (int)ww.z, 0x138, 0xf, 0xf, true), pw = (u32)__builtin_amdgcn_update_dpp(0, (int)ww.w, 0x138, 0xf, 0xf, true);
            const v4u c1 = {__builtin_amdgcn_alignbit(ww.x, pw, 16), __builtin_amdgcn_alignbit(ww.y, ww.x, 16), __builtin_amdgcn_alignbit(ww.z, ww.y, 16), __builtin_amdgcn_alignbit(ww.w, ww.z, 16)};
            *(LAS v4u*)(k0) = ww;
            *(LAS v4u*)(k0 + KCOPY) = c1;
            *(LAS v4u*)(k0 + 2 * KCOPY) = (v4u){pw, ww.x, ww.y, ww.z};
            *(LAS v4u*)(k0 + 3 * KCOPY) = (v4u){__builtin_amdgcn_alignbit(pw, pz, 16), c1.x, c1.y, c1.z};
            const u32 w0 = (u32)__builtin_amdgcn_readlane((int)ww.z, 63), w1 = (u32)__builtin_amdgcn_readlane((int)ww.w, 63);
            const u32 w3 = w1 >> 16, w2 = w3 * 0x10001u, w10 = (w0 >> 16) | (w1 << 16);
            *(LAS v4u*)kt = (v4u){(w2 & m01) | (w1 & m2) | (w10 & m3), (w3 & m0_) | (w2 & ((m01 ^ m0_) | m2 | m3)), (w3 & m2) | (w2 & m3), 0u};
        }
        asm volatile("" ::: "memory");
        u32 stg = lds_peek(smem + LDS_CTL + C_STG);
        bf16x8 A[17];
#pragma unroll
        for (int x = 0; x < 17; ++x) {
            s16x4 lo = __builtin_amdgcn_ds_read_tr16_b64_v4i16((LAS s16x4*)(a0 + x * 64 + 8));
            s16x4 hi = __builtin_amdgcn_ds_read_tr16_b64_v4i16((LAS s16x4*)(a0 + x * 64));
            A[x] = __builtin_shufflevector(lo, hi, 0, 1, 2, 3, 4, 5, 6, 7);
        }
        SBAR();
        {
            *(LAS u16*)(ta) = (u16)t78; *(LAS u16*)(ta + 64) = (u16)(t78 >> 16); *(LAS u16*)(ta + 388) = (u16)t78; *(LAS u16*)(ta + 452) = (u16)(t78 >> 16);
            *(LAS u16*)(tb) = (u16)t9a; *(LAS u16*)(tb + 128) = (u16)(t9a >> 16); *(LAS u16*)(tb + 388) = (u16)t9a; *(LAS u16*)(tb + 516) = (u16)(t9a >> 16);
            const u32 k1024 = ((u32)__builtin_amdgcn_readlane((int)t78, 63) >> 16) * 0x10001u;
            pat = __builtin_bit_cast(bf16x8, __builtin_bit_cast(v4u, pat) & k1024);
        }
        SBAR();
        STAMP(4);
        LWAIT_PEEK(stg, ctl, C_STG, 4);
        STAMP(5);
        auto ldu = [&](int z) __attribute__((always_inline)) { return *(LAS bf16x8*)(bb + (z + 8) * 64); };
        auto ldt2 = [&](int off) __attribute__((always_inline)) {
            const v2u lo = *(volatile LAS v2u*)(tp + off), hi = *(volatile LAS v2u*)(tp + off + 8);
            return __builtin_bit_cast(bf16x8, (v4u){lo.x, lo.y, hi.x, hi.y});
        };
        auto pack = [&](const v4f& lo, const v4f& hi) __attribute__((always_inline)) {
            return __builtin_bit_cast(bf16x8, (v4u){cvtpk(lo[3], hi[3]), cvtpk(lo[2], hi[2]), cvtpk(lo[1], hi[1]), cvtpk(lo[0], hi[0])});
        };
#define POLL_SEL "global_load_dwordx4 v[240:243], %[so], %[ws] sc1\n\t"
        v4u sq;
        const u32 so = OFF_SEL + (8 + xcd) * 256 + (lane & 7) * 16;
        v4f ac[8];
#pragma unroll
        for (int T = 0; T < 8; ++T) ac[T] = (v4f){0.f, 0.f, 0.f, 0.f};
        const v4f zero4 = {0.f, 0.f, 0.f, 0.f};
        bf16x8 G0[4], G1[4], Wa, T5[3], T3[3], T97[4];
#pragma unroll
        for (int j = 0; j < 4; ++j) G0[j] = ldu(j - 7);
        SBAR();
#pragma unroll
        for (int bk = 0; bk < 8; ++bk) {
#pragma unroll
            for (int j = 0; j < 4; ++j) G1[j] = ldu(8 * bk - 3 + j);
            if (bk < 6) Wa = *(LAS bf16x8*)(bb + (8 * bk + 8) * 64 - 16);
            if (bk == 0) {
#pragma unroll
                for (int d = 0; d < 3; ++d) { T5[d] = ldt2(d * 64); T3[d] = ldt2(768 + d * 64); }
            }
#pragma unroll
            for (int j = 0; j < 4; ++j) {
                ac[bk] = MF(A[7 - j], G0[j], ac[bk]);
                if (bk < 7) ac[bk + 1] = MF(A[15 - j], G0[j], ac[bk + 1]);
            }
#pragma unroll
            for (int g = 0; g < 8; ++g) { GRP(0x8, 1); if (bk == 0) GRP(0x100, 3); else GRP(0x100, 1); }
            SBAR();
            if (bk == 2) stg = lds_peek(smem + LDS_CTL + C_STG);
            if (bk == 6) asm volatile(POLL_SEL : "={v[240:243]}"(sq) : [so] "v"(so), [ws] "s"(p.ws) : "memory");
            if (bk == 3) {
                LWAIT_PEEK(stg, ctl, C_STG, 8);
                SBAR();
            }
            if (bk < 7) {
#pragma unroll
                for (int j = 0; j < 4; ++j) G0[j] = ldu(8 * bk + 1 + j);
            }
            if (bk == 0) {
#pragma unroll
                for (int d = 0; d < 4; ++d) T97[d] = ldt2(1536 + d * 64);
            }
#pragma unroll
            for (int j = 0; j < 4; ++j) {
                ac[bk] = MF(A[3 - j], G1[j], ac[bk]);
                if (bk < 7) ac[bk + 1] = MF(A[11 - j], G1[j], ac[bk + 1]);
            }
            if (bk < 6) ac[bk + 2] = MF(A[16], G1[3], ac[bk + 2]);
#pragma unroll
            for (int g = 0; g < 9; ++g) { GRP(0x8, 1); if (bk == 0) GRP(0x100, 2); else GRP(0x100, 1); }
            SBAR();
            if (bk < 6) {
                const v4f d5a = MF(Wa, b5, zero4), d5b = MF(G1[3], b5, zero4), d3a = MF(Wa, b3, zero4), d3b = MF(G1[3], b3, zero4);
                SBAR();
                *(LAS v4f*)(yb + bk * 1152) = ac[bk];
                LDS_SETB(ctc, one, C_Y + 4 * bk);
                SBAR();
                v4f d9, d7;
                if (bk < 4) { d9 = MF(Wa, b9, zero4); d7 = MF(G1[3], b7, zero4); ac[bk + 4] = MF(pat, G1[3], ac[bk + 4]); }
                const bf16x8 p5 = pack(d5b, d5a), p3 = pack(d3b, d3a);
#pragma unroll
                for (int d = 0; d < 3; ++d) if (bk + 2 + d < 8) ac[bk + 2 + d] = MF(p5, T5[d], ac[bk + 2 + d]);
#pragma unroll
                for (int d = 0; d < 3; ++d) if (bk + 2 + d < 8) ac[bk + 2 + d] = MF(p3, T3[d], ac[bk + 2 + d]);
                if (bk < 4) {
                    const bf16x8 p97 = pack(d7, d9);
#pragma unroll
                    for (int d = 0; d < 4; ++d) if (bk + 4 + d < 8) ac[bk + 4 + d] = MF(p97, T97[d], ac[bk + 4 + d]);
                }
#pragma unroll
                for (int g = 0; g < 13; ++g) { GRP(0x8, 1); GRP(0x2, 2); }
                SBAR();
            } else {
                *(LAS v4f*)(yb + bk * 1152) = ac[bk];
                LDS_SETB(ctc, one, C_Y + 4 * bk);
                SBAR();
            }
            if (bk == 0) STAMP(6);
            if (bk == 3) STAMP(7);
            if (bk == 6) STAMP(8);
        }
        STAMP(9);
        STAMP_FLUSH();
        asm volatile("s_cmp_eq_u32 %0, 0\n\ts_cbranch_scc1 1f\n\ts_endpgm\n1:" :: "s"(wave) : "memory", "scc");
        {
            const u32 tag = __builtin_amdgcn_readfirstlane(*(LAS u32*)(smem + LDS_CTL + C_TAG));
            u32 c_;
            asm volatile("s_mov_b32 %[c], 0x80000\n1:\n\ts_waitcnt vmcnt(0)\n\tv_cmpx_eq_u32 %[tag], v241\n\tv_cmpx_eq_u32 %[tag], v243\n\t"
                         "s_cmp_eq_u64 exec, -1\n\ts_mov_b64 exec, -1\n\ts_cbranch_scc1 2f\n\ts_sleep 4\n\t" POLL_SEL "s_sub_u32 %[c], %[c], 1\n\ts_cbranch_scc0 1b\n\ts_waitcnt vmcnt(0)\n2:\n\ts_nop 4"
                         : "+{v[240:243]}"(sq), [c] "=&s"(c_) : [so] "v"(so), [ws] "s"(p.ws), [tag] "s"(tag) : "memory", "scc", "vcc");
            STAMP(10);
            const u32 pb = OFF_PART + cw * (NFFN * 128) + 4096 + lane * 16, pb2 = pb + 4096;
            v4u pq[9];
#define POLL_P "global_load_dwordx4 v[64:67], %[pb], %[ws] offset:-4096 sc1\n\tglobal_load_dwordx4 v[68:71], %[pb], %[ws] offset:-3072 sc1\n\tglobal_load_dwordx4 v[72:75], %[pb], %[ws] offset:-2048 sc1\n\t" \
               "global_load_dwordx4 v[76:79], %[pb], %[ws] offset:-1024 sc1\n\tglobal_load_dwordx4 v[80:83], %[pb], %[ws] sc1\n\tglobal_load_dwordx4 v[84:87], %[pb], %[ws] offset:1024 sc1\n\t" \
               "global_load_dwordx4 v[88:91], %[pb], %[ws] offset:2048 sc1\n\tglobal_load_dwordx4 v[92:95], %[pb], %[ws] offset:3072 sc1\n\tglobal_load_dwordx4 v[96:99], %[pb2], %[ws] sc1\n\t"
            asm volatile(POLL_P : "={v[64:67]}"(pq[0]), "={v[68:71]}"(pq[1]), "={v[72:75]}"(pq[2]), "={v[76:79]}"(pq[3]), "={v[80:83]}"(pq[4]), "={v[84:87]}"(pq[5]), "={v[88:91]}"(pq[6]), "={v[92:95]}"(pq[7]), "={v[96:99]}"(pq[8])
                         : [pb] "v"(pb), [pb2] "v"(pb2), [ws] "s"(p.ws) : "memory");
            const int t = (int)(0xffffu - (sq.x & 0xffffu)) & 2047;
            const float rs = rbf(sigmoidf_(key_to_r(sq.x))), g = bflo(sq.z);
            LWAIT(ctl, C_Y + 28, 0x01010101);
            const int uq = (t & 255) >> 2;
            float xv[4], av[4];
#pragma unroll
            for (int c = 0; c < 4; ++c) {
                xv[c] = bf2f(*(LAS u16*)(smem + LDS_U + c * 4608 + (256 + t) * 2));
                av[c] = g * *(LAS float*)(smem + c * KCH + CH_Y + (t >> 8) * 1152 + (uq + (uq >> 3)) * 16 + (t & 3) * 4);
            }
            const u32 e1 = n < 4 ? ((n & 1) ? 0x3f800000u : 0x00003f80u) : 0u;
            bf16x8 sel4 = __builtin_bit_cast(bf16x8, (n & 2) ? (v4u){0u, e1, 0u, e1} : (v4u){e1, 0u, e1, 0u});
            u32 oo = (u32)(t * D + chbase) * 2;
            float omg = 1.f - g;
            asm volatile("" : "+v"(sel4), "+v"(oo), "+v"(omg), "+v"(xv[0]), "+v"(xv[1]), "+v"(xv[2]), "+v"(xv[3]), "+v"(av[0]), "+v"(av[1]), "+v"(av[2]), "+v"(av[3]));
            LWAIT(ctl, C_ACK, 4);
            asm volatile("s_mov_b32 %[c], 0x80000\n1:\n\ts_waitcnt vmcnt(0)\n\t"
                         "v_cmpx_eq_u32 %[tag], v65\n\tv_cmpx_eq_u32 %[tag], v67\n\tv_cmpx_eq_u32 %[tag], v69\n\tv_cmpx_eq_u32 %[tag], v71\n\tv_cmpx_eq_u32 %[tag], v73\n\tv_cmpx_eq_u32 %[tag], v75\n\t"
                         "v_cmpx_eq_u32 %[tag], v77\n\tv_cmpx_eq_u32 %[tag], v79\n\tv_cmpx_eq_u32 %[tag], v81\n\tv_cmpx_eq_u32 %[tag], v83\n\tv_cmpx_eq_u32 %[tag], v85\n\tv_cmpx_eq_u32 %[tag], v87\n\t"
                         "v_cmpx_eq_u32 %[tag], v89\n\tv_cmpx_eq_u32 %[tag], v91\n\tv_cmpx_eq_u32 %[tag], v93\n\tv_cmpx_eq_u32 %[tag], v95\n\tv_cmpx_eq_u32 %[tag], v97\n\tv_cmpx_eq_u32 %[tag], v99\n\t"
                         "s_cmp_eq_u64 exec, -1\n\ts_mov_b64 exec, -1\n\ts_cbranch_scc1 2f\n\t" POLL_P "s_sub_u32 %[c], %[c], 1\n\ts_cbranch_scc0 1b\n\ts_waitcnt vmcnt(0)\n2:\n\ts_nop 4"
                         : "+{v[64:67]}"(pq[0]), "+{v[68:71]}"(pq[1]), "+{v[72:75]}"(pq[2]), "+{v[76:79]}"(pq[3]), "+{v[80:83]}"(pq[4]), "+{v[84:87]}"(pq[5]), "+{v[88:91]}"(pq[6]), "+{v[92:95]}"(pq[7]), "+{v[96:99]}"(pq[8]), [c] "=&s"(c_)
                         : [pb] "v"(pb), [pb2] "v"(pb2), [ws] "s"(p.ws), [tag] "s"(tag) : "memory", "scc", "vcc");
            STAMP(11);
            v4f s0 = MF(sel4, __builtin_bit_cast(bf16x8, ((v4u){pq[0].x, pq[0].z, pq[1].x, pq[1].z})), zero4);
            v4f s1 = MF(sel4, __builtin_bit_cast(bf16x8, ((v4u){pq[2].x, pq[2].z, pq[3].x, pq[3].z})), zero4);
            s0 = MF(sel4, __builtin_bit_cast(bf16x8, ((v4u){pq[4].x, pq[4].z, pq[5].x, pq[5].z})), s0);
            s1 = MF(sel4, __builtin_bit_cast(bf16x8, ((v4u){pq[6].x, pq[6].z, pq[7].x, pq[7].z})), s1);
            s0 = MF(sel4, __builtin_bit_cast(bf16x8, ((v4u){pq[8].x, pq[8].z, 0u, 0u})), s0);
            float o[4];
#pragma unroll
            for (int c = 0; c < 4; ++c) o[c] = fmaf(omg, xv[c] + rbf(rbf(dpp_add<0x128, 0xf>(s0[c] + s1[c])) * rs), av[c]);
            asm volatile("s_mov_b64 exec, 0xff\n\tglobal_store_dwordx2 %0, %1, %2\n\ts_mov_b64 exec, -1" :: "v"(oo), "v"((v2u){cvtpk(o[0], o[1]), cvtpk(o[2], o[3])}), "s"(p.out) : "memory");
        }
        STAMP(12);
        ROLE_END();
    }
    }
}

extern "C" void kernel_launch(void* const* d_in, const int* in_sizes, int n_in,
                              void* d_out, int out_size, void* d_ws, size_t ws_size,
                              hipStream_t stream) {
    Params p;
    p.x = (const u16*)d_in[0]; p.Are = (const u16*)d_in[1]; p.Aim = (const u16*)d_in[2]; p.Bre = (const u16*)d_in[3];
    p.Bim = (const u16*)d_in[4]; p.Cre = (const u16*)d_in[5]; p.Cim = (const u16*)d_in[6]; p.logdt = (const u16*)d_in[7];
    p.dskip = (const u16*)d_in[8]; p.wr = (const u16*)d_in[10]; p.wg = (const u16*)d_in[11]; p.wu = (const u16*)d_in[12];
    p.wd = (const u16*)d_in[13]; p.gw = (const u16*)d_in[14]; p.out = (u16*)d_out; p.ws = (char*)d_ws;
    (void)hipFuncSetAttribute((const void*)fused_kernel, hipFuncAttributeMaxDynamicSharedMemorySize, LDS_TOTAL);
    fused_kernel<<<256, 512, LDS_TOTAL, stream>>>(p);
}
```

```cpp
#include <hip/hip_runtime.h>
#include <stdint.h>

typedef unsigned short u16;
typedef unsigned int u32;
typedef unsigned long long u64;
typedef __attribute__((ext_vector_type(4))) u32 v4u;
typedef __attribute__((ext_vector_type(2))) u32 v2u;
typedef __attribute__((ext_vector_type(2))) float v2f;
typedef __attribute__((ext_vector_type(4))) float v4f;
typedef __attribute__((ext_vector_type(16))) float v16f;
typedef __attribute__((ext_vector_type(8))) short bf16x8;
typedef __attribute__((ext_vector_type(4))) short s16x4;
typedef __attribute__((ext_vector_type(2))) __bf16 bf16x2_t;
typedef __attribute__((ext_vector_type(2))) _Float16 f16x2_t;
typedef __attribute__((ext_vector_type(8))) _Float16 f16x8;
#define LAS __attribute__((address_space(3)))
#define GAS __attribute__((address_space(1)))
#define RLX_AGENT __ATOMIC_RELAXED, __HIP_MEMORY_SCOPE_AGENT
#define SBAR() __builtin_amdgcn_sched_barrier(0)
#define BAR() asm volatile("s_waitcnt lgkmcnt(0)\n\ts_barrier" ::: "memory")
#define WSR() __builtin_amdgcn_make_buffer_rsrc(p.ws, 0, 0x7fffffff, 0x00020000)
#define REREAD() asm volatile("" ::: "memory")

#define WNT " sc1"
#define CODE_KB 20
#define NB1 2
#define REPG 2
#define PFG0 400
#define FDELAY 0
#define PFR0 16
#define SELPF 0
#define SELWAIT 50
#define MPRIO 0
#define YSLEEP 1
#define HPRIO 0
#define K2 10
#define WT 0
#define XPF 0

constexpr int L = 2048, D = 576, NST = 128, F = 4608;
constexpr int NFFN = 72;
constexpr u32 OFF_RG = 4096;
constexpr u32 OFF_G = OFF_RG + L * 8;
constexpr u32 OFF_SEL = OFF_G + REPG * 8192;
constexpr u32 OFF_CI = OFF_SEL + 16 * 256;
constexpr u32 OFF_PART = OFF_CI + 4096;
constexpr u32 SPIN_CAP = 1u << 19;

#define STAMP(i)
#define STAMP0_FIX()
#define STAMP_FLUSH()

struct Params {
    const u16 *x, *Are, *Aim, *Bre, *Bim, *Cre, *Cim, *logdt, *dskip, *wr, *wg, *wu, *wd, *gw;
    u16* out;
    char* ws;
};

__device__ __forceinline__ float bflo(u32 w) { return __uint_as_float(w << 16); }
__device__ __forceinline__ float bfhi(u32 w) { return __uint_as_float(w & 0xffff0000u); }
__device__ __forceinline__ float bf2f(u16 h) { return __uint_as_float((u32)h << 16); }
__device__ __forceinline__ u32 cvtpk(float lo, float hi) { v2f v = {lo, hi}; bf16x2_t r = __builtin_convertvector(v, bf16x2_t); return __builtin_bit_cast(u32, r); }
__device__ __forceinline__ u32 cvth(v2f v) { return __builtin_bit_cast(u32, __builtin_convertvector(v, f16x2_t)); }
__device__ __forceinline__ float rbf(float f) { return bflo(cvtpk(f, f)); }
__device__ __forceinline__ float sigmoidf_(float s) { return __builtin_amdgcn_rcpf(1.f + __expf(-s)); }

template <int CTRL, int RM> __device__ __forceinline__ float dpp_add(float v) {
    return v + __builtin_bit_cast(float, __builtin_amdgcn_update_dpp(0, __builtin_bit_cast(int, v), CTRL, RM, 0xf, true));
}
__device__ __forceinline__ float wave_sum63(float v) {
    v = dpp_add<0x111, 0xf>(v); v = dpp_add<0x112, 0xf>(v); v = dpp_add<0x114, 0xf>(v); v = dpp_add<0x118, 0xf>(v);
    v = dpp_add<0x142, 0xa>(v); v = dpp_add<0x143, 0xc>(v);
    return v;
}
template <int CTRL, int RM> __device__ __forceinline__ u32 dpp_umax(u32 v) {
    u32 o = (u32)__builtin_amdgcn_update_dpp(0, (int)v, CTRL, RM, 0xf, true);
    return v > o ? v : o;
}

typedef v2f cf;
__device__ __forceinline__ cf cmul(cf a, cf b) { cf t = a.yy * (cf){-b.y, b.x}; return __builtin_elementwise_fma(a.xx, b, t); }
__device__ __forceinline__ cf cexp_(float a2, float brev, float rho) {
    float m = __builtin_amdgcn_exp2f(a2 * rho);
    float ph = __builtin_amdgcn_fractf(brev * rho);
    cf c; c.x = m * __builtin_amdgcn_cosf(ph); c.y = m * __builtin_amdgcn_sinf(ph); return c;
}

struct cf2 { cf a, b; };
__device__ __forceinline__ cf2 cmul2(cf2 x, cf2 y) { return {cmul(x.a, y.a), cmul(x.b, y.b)}; }
__device__ __forceinline__ cf2 cexp2(v2f a2, v2f brev, float rho) { return {cexp_(a2.x, brev.x, rho), cexp_(a2.y, brev.y, rho)}; }

__device__ __forceinline__ u32 make_payload(float r, float g) {
    const u32 w = cvtpk(g, r);
    return w ^ (((u32)((int)w >> 31) & 0x7fff0000u) | 0x80000000u);
}
__device__ __forceinline__ u32 make_key(u32 pay, u32 t) { return (pay & 0xffff0000u) | (0xffffu - t); }
__device__ __forceinline__ float key_to_r(u32 k) {
    u32 hi = k & 0xffff0000u;
    return __uint_as_float((hi & 0x80000000u) ? (hi ^ 0x80000000u) : (~hi & 0xffff0000u));
}

constexpr int STG_ROW = 528;
constexpr int NA = 21;
constexpr int KCH = (NA + 32) * STG_ROW;
constexpr int KCOPY = 1344;
constexpr int CH_Y = 4 * KCOPY;
constexpr int LDS_U = 4 * KCH;
constexpr int LDS_TAP = LDS_U + 4 * 4608;
constexpr int LDS_CTL = LDS_TAP + 4 * 2304;
constexpr int C_TAG = 0, C_STG = 4, C_PUB = 8, C_ACK = 12, C_Y = 64;
constexpr int LDS_RW = LDS_CTL + 1024;
constexpr int LDS_G = LDS_RW;
constexpr int LDS_DUMMY = LDS_RW + 4 * 3072;
constexpr int LDS_TOTAL = LDS_DUMMY + 1024;
static_assert(CH_Y + 8 * 1152 <= KCH && LDS_TOTAL <= 160 * 1024, "lds");
constexpr int WDP = 1184;
constexpr int FL_WD = 0;
constexpr int FL_RED = 74 * 1024;
constexpr int FL_H = FL_RED + 24576;
constexpr int XSP = 1168;
constexpr int FL_XS = FL_H + 8 * 144;
constexpr int FL_Z = FL_XS + 8 * XSP;
constexpr int FL_TAG = FL_Z + 256;

__device__ __forceinline__ int sum_rows(int v) {
    auto a = __builtin_amdgcn_permlane16_swap((u32)v, (u32)v, false, false);
    v = (int)(a[0] + a[1]);
    auto b = __builtin_amdgcn_permlane32_swap((u32)v, (u32)v, false, false);
    return (int)(b[0] + b[1]);
}
__device__ __forceinline__ float sum_rows_f(float v) {
    auto a = __builtin_amdgcn_permlane16_swap(__float_as_uint(v), __float_as_uint(v), false, false);
    v = __uint_as_float(a[0]) + __uint_as_float(a[1]);
    auto b = __builtin_amdgcn_permlane32_swap(__float_as_uint(v), __float_as_uint(v), false, false);
    return __uint_as_float(b[0]) + __uint_as_float(b[1]);
}
__device__ __forceinline__ u32 top8_d(LAS char* t8, int lane, int n) {
    LAS u32* cand = (LAS u32*)(t8 + 256);
    u32 res = 0;
    _Pragma("unroll 1") for (int i0 = 0; i0 < n; i0 += 64) {
        const int i = i0 + lane;
        const u32 k = i < n ? cand[i] : 0u;
        int rk = 0;
        _Pragma("unroll 1") for (int j0 = 0; j0 < n; j0 += 16) {
            v4u c[4];
#pragma unroll
            for (int e = 0; e < 4; ++e) c[e] = *(LAS v4u*)(cand + j0 + 4 * e);
#pragma unroll
            for (int e = 0; e < 16; ++e) rk += (c[e >> 2][e & 3] > k) & (j0 + e < n);
        }
        const bool hit = i < n && rk < 8;
        const u32 got = (u32)__builtin_amdgcn_ds_permute((hit ? rk : 63) * 4, (int)(hit ? k : 0u));
        if (lane < 8) res |= got;
    }
    return res;
}

__device__ __forceinline__ u32 lds_peek(LAS char* word) { return __hip_atomic_load((LAS u32*)word, __ATOMIC_RELAXED, __HIP_MEMORY_SCOPE_WORKGROUP); }
#define LWAIT_BODY "1:\n\tds_read_b32 %0, %3 offset:%4\n\ts_waitcnt lgkmcnt(0)\n\tv_readfirstlane_b32 %1, %0\n\ts_cmp_ge_u32 %1, %5\n\ts_cbranch_scc1 2f\n\ts_sleep 1\n\ts_sub_u32 %2, %2, 1\n\ts_cbranch_scc0 1b\n2:"
#define LSPIN(addr, off, want) do { u32 t_, s_, c_; asm volatile("s_mov_b32 %2, 0x800000\n1:\n\tds_read_b32 %0, %3 offset:%4\n\ts_waitcnt lgkmcnt(0)\n\tv_readfirstlane_b32 %1, %0\n\ts_cmp_ge_u32 %1, %5\n\ts_cbranch_scc1 2f\n\ts_sub_u32 %2, %2, 1\n\ts_cbranch_scc0 1b\n2:" : "=&v"(t_), "=&s"(s_), "=&s"(c_) : "v"(addr), "n"(off), "n"(want) : "memory", "scc"); } while (0)
#define LWAIT(addr, off, want) do { u32 t_, s_, c_; asm volatile("s_mov_b32 %2, 0x400000\n" LWAIT_BODY : "=&v"(t_), "=&s"(s_), "=&s"(c_) : "v"(addr), "n"(off), "n"(want) : "memory", "scc"); } while (0)
#define LWAIT_PEEK(peek, addr, off, want) do { u32 t_, s_, c_; asm volatile("s_nop 0\n\tv_readfirstlane_b32 %1, %6\n\ts_mov_b32 %2, 0x400000\n\ts_cmp_ge_u32 %1, %5\n\ts_cbranch_scc1 2f\n" LWAIT_BODY : "=&v"(t_), "=&s"(s_), "=&s"(c_) : "v"(addr), "n"(off), "n"(want), "v"(peek) : "memory", "scc"); } while (0)
#define LDS_ADD1(addr, one, off) asm volatile("s_mov_b64 exec, 1\n\tds_add_u32 %0, %1 offset:%2\n\ts_mov_b64 exec, -1" :: "v"(addr), "v"(one), "n"(off) : "memory")
#define LDS_SETB(addr, one, off) asm volatile("s_mov_b64 exec, 1\n\tds_write_b8 %0, %1 offset:%2\n\ts_mov_b64 exec, -1" :: "v"(addr), "v"(one), "n"(off) : "memory")
__device__ __forceinline__ void glds16(const void* g, u32 lds_dst, bool nt) {
    u32 keep;
    if (nt) asm volatile("s_mov_b32 %0, m0\n\ts_mov_b32 m0, %2\n\ts_nop 0\n\tglobal_load_lds_dwordx4 %1, off" WNT "\n\ts_mov_b32 m0, %0" : "=&s"(keep) : "v"(g), "s"(lds_dst) : "memory");
    else asm volatile("s_mov_b32 %0, m0\n\ts_mov_b32 m0, %2\n\ts_nop 0\n\tglobal_load_lds_dwordx4 %1, off\n\ts_mov_b32 m0, %0" : "=&s"(keep) : "v"(g), "s"(lds_dst) : "memory");
}
__device__ __forceinline__ bf16x8 band(u32 bits, u32 val) {
    v4u r;
#pragma unroll
    for (int j = 0; j < 4; ++j) r[j] = __builtin_amdgcn_perm((u32)__builtin_amdgcn_sbfe((int)bits, 2 * j + 1, 1), (u32)__builtin_amdgcn_sbfe((int)bits, 2 * j, 1), 0x05040100u) & val;
    return __builtin_bit_cast(bf16x8, r);
}

template <int LIST> __device__ __forceinline__ bool touch(const Params& p, int xcd, int e, int lane, u32 dst) {
    const int wa = xcd * 9 + ((3 - (int)(((uintptr_t)p.wg >> 7) & 7) - xcd * 9) & 7), two = wa == xcd * 9;
    const int na = LIST ? 0 : (PFG0 * 2) << two, nb = LIST ? 0 : (576 - PFG0) * 18, nc = LIST ? 64 * 81 : 0;
    if (!LIST && e * 64 >= na + nb) return false;
    const u32 i = min(e * 64 + lane, na + nb + nc - 1);
    const char* a;
    if (i < (u32)(na + nb)) {
        u32 seg, col;
        if (i < (u32)na) { seg = i >> two; col = (wa + 8 * (i & two)) * 128u; }
        else { const u32 k = (i - na) / 9u; seg = k + 2 * PFG0; col = xcd * 1152u + (i - na - 9u * k) * 128u; }
        a = (const char*)((seg & 1) ? p.wu : p.wg) + (seg >> 1) * (F * 2) + col;
    } else {
        const u32 c = i - na - nb, k = c / 9u, rw = k / 9u;
        a = (const char*)p.wd + ((xcd * 9u + k - 9u * rw) * 64u + (rw >> 1) + 32u * (rw & 1)) * 1152u + (c - 9u * k) * 128u;
    }
    asm volatile("s_mov_b32 m0, %1\n\ts_nop 0\n\tglobal_load_lds_dword %0, off" :: "v"(a), "s"(dst) : "memory");
    return true;
}

#define KGEN_ASM ".p2align 3\n\tv_mov_b32 v64, 0x3c00\n\tv_mov_b32 v65, 0x3c00\n\tv_pk_mul_f32 v[66:67], v[56:57], v[56:57] op_sel:[1,1] op_sel_hi:[1,0] neg_lo:[0,1]\n\tv_pk_mul_f32 v[68:69], v[58:59], v[58:59] op_sel:[1,1] op_sel_hi:[1,0] neg_lo:[0,1]\n\tv_pk_mul_f32 v[70:71], v[52:53], v[52:53] op_sel:[1,1] op_sel_hi:[1,0] neg_lo:[0,1]\n\tv_pk_mul_f32 v[72:73], v[54:55], v[54:55] op_sel:[1,1] op_sel_hi:[1,0] neg_lo:[0,1]\n\tv_pk_fma_f32 v[66:67], v[56:57], v[56:57], v[66:67] op_sel:[0,0,0] op_sel_hi:[0,1,1]\n\tv_pk_fma_f32 v[68:69], v[58:59], v[58:59], v[68:69] op_sel:[0,0,0] op_sel_hi:[0,1,1]\n\tv_pk_fma_f32 v[70:71], v[52:53], v[52:53], v[70:71] op_sel:[0,0,0] op_sel_hi:[0,1,1]\n\tv_pk_fma_f32 v[72:73], v[54:55], v[54:55], v[72:73] op_sel:[0,0,0] op_sel_hi:[0,1,1]\n\tv_cvt_pk_f16_f32 v74, v56, -v57\n\tv_cvt_pk_f16_f32 v75, v58, -v59\n\tv_cvt_pk_f16_f32 v76, v48, v49\n\tv_cvt_pk_f16_f32 v77, v50, v51\n\tv_pk_mul_f32 v[78:79], v[48:49], v[60:61] op_sel:[1,1] op_sel_hi:[1,0] neg_lo:[0,1]\n\tv_pk_mul_f32 v[80:81], v[50:51], v[62:63] op_sel:[1,1] op_sel_hi:[1,0] neg_lo:[0,1]\n\tv_pk_mul_f32 v[82:83], v[48:49], v[52:53] op_sel:[1,1] op_sel_hi:[1,0] neg_lo:[0,1]\n\tv_pk_mul_f32 v[84:85], v[50:51], v[54:55] op_sel:[1,1] op_sel_hi:[1,0] neg_lo:[0,1]\n\tds_write2_b64 %[b0], v[64:65], v[74:75] offset0:0 offset1:66\n\tv_pk_fma_f32 v[78:79], v[48:49], v[60:61], v[78:79] op_sel:[0,0,0] op_sel_hi:[0,1,1]\n\tv_pk_fma_f32 v[80:81], v[50:51], v[62:63], v[80:81] op_sel:[0,0,0] op_sel_hi:[0,1,1]\n\tv_pk_fma_f32 v[82:83], v[48:49], v[52:53], v[82:83] op_sel:[0,0,0] op_sel_hi:[0,1,1]\n\tv_pk_fma_f32 v[84:85], v[50:51], v[54:55], v[84:85] op_sel:[0,0,0] op_sel_hi:[0,1,1]\n\tv_cvt_pk_f16_f32 v86, v66, -v67\n\tv_cvt_pk_f16_f32 v87, v68, -v69\n\tv_pk_mul_f32 v[88:89], v[66:67], v[66:67] op_sel:[1,1] op_sel_hi:[1,0] neg_lo:[0,1]\n\tv_pk_mul_f32 v[90:91], v[68:69], v[68:69] op_sel:[1,1] op_sel_hi:[1,0] neg_lo:[0,1]\n\tv_pk_mul_f32 v[92:93], v[66:67], v[56:57] op_sel:[1,1] op_sel_hi:[1,0] neg_lo:[0,1]\n\tv_pk_mul_f32 v[94:95], v[68:69], v[58:59] op_sel:[1,1] op_sel_hi:[1,0] neg_lo:[0,1]\n\tv_pk_fma_f32 v[88:89], v[66:67], v[66:67], v[88:89] op_sel:[0,0,0] op_sel_hi:[0,1,1]\n\tv_pk_fma_f32 v[90:91], v[68:69], v[68:69], v[90:91] op_sel:[0,0,0] op_sel_hi:[0,1,1]\n\tv_pk_fma_f32 v[92:93], v[66:67], v[56:57], v[92:93] op_sel:[0,0,0] op_sel_hi:[0,1,1]\n\tv_pk_fma_f32 v[94:95], v[68:69], v[58:59], v[94:95] op_sel:[0,0,0] op_sel_hi:[0,1,1]\n\tv_cvt_pk_f16_f32 v64, v78, v79\n\tv_cvt_pk_f16_f32 v65, v80, v81\n\tv_cvt_pk_f16_f32 v96, v82, v83\n\tv_cvt_pk_f16_f32 v97, v84, v85\n\tv_pk_mul_f32 v[98:99], v[70:71], v[70:71] op_sel:[1,1] op_sel_hi:[1,0] neg_lo:[0,1]\n\tv_pk_mul_f32 v[100:101], v[72:73], v[72:73] op_sel:[1,1] op_sel_hi:[1,0] neg_lo:[0,1]\n\tv_pk_mul_f32 v[102:103], v[78:79], v[56:57] op_sel:[1,1] op_sel_hi:[1,0] neg_lo:[0,1]\n\tv_pk_mul_f32 v[104:105], v[80:81], v[58:59] op_sel:[1,1] op_sel_hi:[1,0] neg_lo:[0,1]\n\tds_write2_b64 %[b8], v[76:77], v[96:97] offset0:0 offset1:66\n\tv_pk_fma_f32 v[98:99], v[70:71], v[70:71], v[98:99] op_sel:[0,0,0] op_sel_hi:[0,1,1]\n\tv_pk_fma_f32 v[100:101], v[72:73], v[72:73], v[100:101] op_sel:[0,0,0] op_sel_hi:[0,1,1]\n\tv_pk_fma_f32 v[102:103], v[78:79], v[56:57], v[102:103] op_sel:[0,0,0] op_sel_hi:[0,1,1]\n\tv_pk_fma_f32 v[104:105], v[80:81], v[58:59], v[104:105] op_sel:[0,0,0] op_sel_hi:[0,1,1]\n\tv_cvt_pk_f16_f32 v106, v88, -v89\n\tv_cvt_pk_f16_f32 v107, v90, -v91\n\tv_cvt_pk_f16_f32 v108, v92, -v93\n\tv_cvt_pk_f16_f32 v109, v94, -v95\n\tv_pk_mul_f32 v[110:111], v[78:79], v[70:71] op_sel:[1,1] op_sel_hi:[1,0] neg_lo:[0,1]\n\tv_pk_mul_f32 v[112:113], v[80:81], v[72:73] op_sel:[1,1] op_sel_hi:[1,0] neg_lo:[0,1]\n\tv_pk_mul_f32 v[114:115], v[48:49], v[70:71] op_sel:[1,1] op_sel_hi:[1,0] neg_lo:[0,1]\n\tv_pk_mul_f32 v[116:117], v[50:51], v[72:73] op_sel:[1,1] op_sel_hi:[1,0] neg_lo:[0,1]\n\tds_write2_b64 %[b0], v[86:87], v[108:109] offset0:132 offset1:198\n\tv_pk_fma_f32 v[110:111], v[78:79], v[70:71], v[110:111] op_sel:[0,0,0] op_sel_hi:[0,1,1]\n\tv_pk_fma_f32 v[112:113], v[80:81], v[72:73], v[112:113] op_sel:[0,0,0] op_sel_hi:[0,1,1]\n\tv_pk_fma_f32 v[114:115], v[48:49], v[70:71], v[114:115] op_sel:[0,0,0] op_sel_hi:[0,1,1]\n\tv_pk_fma_f32 v[116:117], v[50:51], v[72:73], v[116:117] op_sel:[0,0,0] op_sel_hi:[0,1,1]\n\tv_cvt_pk_f16_f32 v76, v102, v103\n\tv_cvt_pk_f16_f32 v77, v104, v105\n\tv_pk_mul_f32 v[96:97], v[82:83], v[70:71] op_sel:[1,1] op_sel_hi:[1,0] neg_lo:[0,1]\n\tv_pk_mul_f32 v[118:119], v[84:85], v[72:73] op_sel:[1,1] op_sel_hi:[1,0] neg_lo:[0,1]\n\tv_pk_mul_f32 v[120:121], v[88:89], v[88:89] op_sel:[1,1] op_sel_hi:[1,0] neg_lo:[0,1]\n\tv_pk_mul_f32 v[122:123], v[90:91], v[90:91] op_sel:[1,1] op_sel_hi:[1,0] neg_lo:[0,1]\n\tv_pk_fma_f32 v[96:97], v[82:83], v[70:71], v[96:97] op_sel:[0,0,0] op_sel_hi:[0,1,1]\n\tv_pk_fma_f32 v[118:119], v[84:85], v[72:73], v[118:119] op_sel:[0,0,0] op_sel_hi:[0,1,1]\n\tv_pk_fma_f32 v[120:121], v[88:89], v[88:89], v[120:121] op_sel:[0,0,0] op_sel_hi:[0,1,1]\n\tv_pk_fma_f32 v[122:123], v[90:91], v[90:91], v[122:123] op_sel:[0,0,0] op_sel_hi:[0,1,1]\n\tv_cvt_pk_f16_f32 v124, v110, v111\n\tv_cvt_pk_f16_f32 v125, v112, v113\n\tv_cvt_pk_f16_f32 v126, v114, v115\n\tv_cvt_pk_f16_f32 v127, v116, v117\n\tv_pk_mul_f32 v[70:71], v[88:89], v[56:57] op_sel:[1,1] op_sel_hi:[1,0] neg_lo:[0,1]\n\tv_pk_mul_f32 v[72:73], v[90:91], v[58:59] op_sel:[1,1] op_sel_hi:[1,0] neg_lo:[0,1]\n\tv_pk_mul_f32 v[128:129], v[102:103], v[60:61] op_sel:[1,1] op_sel_hi:[1,0] neg_lo:[0,1]\n\tv_pk_mul_f32 v[130:131], v[104:105], v[62:63] op_sel:[1,1] op_sel_hi:[1,0] neg_lo:[0,1]\n\tds_write2_b64 %[b11], v[64:65], v[124:125] offset0:0 offset1:66\n\tv_pk_fma_f32 v[70:71], v[88:89], v[56:57], v[70:71] op_sel:[0,0,0] op_sel_hi:[0,1,1]\n\tv_pk_fma_f32 v[72:73], v[90:91], v[58:59], v[72:73] op_sel:[0,0,0] op_sel_hi:[0,1,1]\n\tv_pk_fma_f32 v[128:129], v[102:103], v[60:61], v[128:129] op_sel:[0,0,0] op_sel_hi:[0,1,1]\n\tv_pk_fma_f32 v[130:131], v[104:105], v[62:63], v[130:131] op_sel:[0,0,0] op_sel_hi:[0,1,1]\n\tv_cvt_pk_f16_f32 v132, v96, v97\n\tv_cvt_pk_f16_f32 v133, v118, v119\n\tv_cvt_pk_f16_f32 v134, v120, -v121\n\tv_cvt_pk_f16_f32 v135, v122, -v123\n\tv_pk_mul_f32 v[102:103], v[78:79], v[98:99] op_sel:[1,1] op_sel_hi:[1,0] neg_lo:[0,1]\n\tv_pk_mul_f32 v[104:105], v[80:81], v[100:101] op_sel:[1,1] op_sel_hi:[1,0] neg_lo:[0,1]\n\tv_pk_mul_f32 v[136:137], v[88:89], v[66:67] op_sel:[1,1] op_sel_hi:[1,0] neg_lo:[0,1]\n\tv_pk_mul_f32 v[138:139], v[90:91], v[68:69] op_sel:[1,1] op_sel_hi:[1,0] neg_lo:[0,1]\n\tds_write2_b64 %[b8], v[126:127], v[132:133] offset0:132 offset1:198\n\tv_pk_fma_f32 v[102:103], v[78:79], v[98:99], v[102:103] op_sel:[0,0,0] op_sel_hi:[0,1,1]\n\tv_pk_fma_f32 v[104:105], v[80:81], v[100:101], v[104:105] op_sel:[0,0,0] op_sel_hi:[0,1,1]\n\tv_pk_fma_f32 v[136:137], v[88:89], v[66:67], v[136:137] op_sel:[0,0,0] op_sel_hi:[0,1,1]\n\tv_pk_fma_f32 v[138:139], v[90:91], v[68:69], v[138:139] op_sel:[0,0,0] op_sel_hi:[0,1,1]\n\tv_cvt_pk_f16_f32 v64, v70, -v71\n\tv_cvt_pk_f16_f32 v65, v72, -v73\n\tv_cvt_pk_f16_f32 v124, v128, v129\n\tv_cvt_pk_f16_f32 v125, v130, v131\n\tv_pk_mul_f32 v[78:79], v[88:89], v[92:93] op_sel:[1,1] op_sel_hi:[1,0] neg_lo:[0,1]\n\tv_pk_mul_f32 v[80:81], v[90:91], v[94:95] op_sel:[1,1] op_sel_hi:[1,0] neg_lo:[0,1]\n\tv_pk_mul_f32 v[140:141], v[120:121], v[120:121] op_sel:[1,1] op_sel_hi:[1,0] neg_lo:[0,1]\n\tv_pk_mul_f32 v[142:143], v[122:123], v[122:123] op_sel:[1,1] op_sel_hi:[1,0] neg_lo:[0,1]\n\tds_write2_b64 %[b1], v[106:107], v[64:65] offset0:0 offset1:66\n\tv_pk_fma_f32 v[78:79], v[88:89], v[92:93], v[78:79] op_sel:[0,0,0] op_sel_hi:[0,1,1]\n\tv_pk_fma_f32 v[80:81], v[90:91], v[94:95], v[80:81] op_sel:[0,0,0] op_sel_hi:[0,1,1]\n\tv_pk_fma_f32 v[140:141], v[120:121], v[120:121], v[140:141] op_sel:[0,0,0] op_sel_hi:[0,1,1]\n\tv_pk_fma_f32 v[142:143], v[122:123], v[122:123], v[142:143] op_sel:[0,0,0] op_sel_hi:[0,1,1]\n\tv_cvt_pk_f16_f32 v126, v102, v103\n\tv_cvt_pk_f16_f32 v127, v104, v105\n\tv_cvt_pk_f16_f32 v132, v136, -v137\n\tv_cvt_pk_f16_f32 v133, v138, -v139\n\tv_pk_mul_f32 v[144:145], v[120:121], v[56:57] op_sel:[1,1] op_sel_hi:[1,0] neg_lo:[0,1]\n\tv_pk_mul_f32 v[146:147], v[122:123], v[58:59] op_sel:[1,1] op_sel_hi:[1,0] neg_lo:[0,1]\n\tv_pk_mul_f32 v[148:149], v[128:129], v[60:61] op_sel:[1,1] op_sel_hi:[1,0] neg_lo:[0,1]\n\tv_pk_mul_f32 v[150:151], v[130:131], v[62:63] op_sel:[1,1] op_sel_hi:[1,0] neg_lo:[0,1]\n\tds_write2_b64 %[b9], v[76:77], v[124:125] offset0:0 offset1:66\n\tv_pk_fma_f32 v[144:145], v[120:121], v[56:57], v[144:145] op_sel:[0,0,0] op_sel_hi:[0,1,1]\n\tv_pk_fma_f32 v[146:147], v[122:123], v[58:59], v[146:147] op_sel:[0,0,0] op_sel_hi:[0,1,1]\n\tv_pk_fma_f32 v[148:149], v[128:129], v[60:61], v[148:149] op_sel:[0,0,0] op_sel_hi:[0,1,1]\n\tv_pk_fma_f32 v[150:151], v[130:131], v[62:63], v[150:151] op_sel:[0,0,0] op_sel_hi:[0,1,1]\n\tv_cvt_pk_f16_f32 v152, v78, -v79\n\tv_cvt_pk_f16_f32 v153, v80, -v81\n\tv_cvt_pk_f16_f32 v154, v140, -v141\n\tv_cvt_pk_f16_f32 v155, v142, -v143\n\tv_pk_mul_f32 v[128:129], v[102:103], v[60:61] op_sel:[1,1] op_sel_hi:[1,0] neg_lo:[0,1]\n\tv_pk_mul_f32 v[130:131], v[104:105], v[62:63] op_sel:[1,1] op_sel_hi:[1,0] neg_lo:[0,1]\n\tv_pk_mul_f32 v[140:141], v[110:111], v[98:99] op_sel:[1,1] op_sel_hi:[1,0] neg_lo:[0,1]\n\tv_pk_mul_f32 v[142:143], v[112:113], v[100:101] op_sel:[1,1] op_sel_hi:[1,0] neg_lo:[0,1]\n\tds_write2_b64 %[b1], v[132:133], v[152:153] offset0:132 offset1:198\n\tv_pk_fma_f32 v[128:129], v[102:103], v[60:61], v[128:129] op_sel:[0,0,0] op_sel_hi:[0,1,1]\n\tv_pk_fma_f32 v[130:131], v[104:105], v[62:63], v[130:131] op_sel:[0,0,0] op_sel_hi:[0,1,1]\n\tv_pk_fma_f32 v[140:141], v[110:111], v[98:99], v[140:141] op_sel:[0,0,0] op_sel_hi:[0,1,1]\n\tv_pk_fma_f32 v[142:143], v[112:113], v[100:101], v[142:143] op_sel:[0,0,0] op_sel_hi:[0,1,1]\n\tv_cvt_pk_f16_f32 v76, v144, -v145\n\tv_cvt_pk_f16_f32 v77, v146, -v147\n\tv_cvt_pk_f16_f32 v124, v148, v149\n\tv_cvt_pk_f16_f32 v125, v150, v151\n\tv_pk_mul_f32 v[102:103], v[120:121], v[66:67] op_sel:[1,1] op_sel_hi:[1,0] neg_lo:[0,1]\n\tv_pk_mul_f32 v[104:105], v[122:123], v[68:69] op_sel:[1,1] op_sel_hi:[1,0] neg_lo:[0,1]\n\tv_pk_mul_f32 v[110:111], v[120:121], v[92:93] op_sel:[1,1] op_sel_hi:[1,0] neg_lo:[0,1]\n\tv_pk_mul_f32 v[112:113], v[122:123], v[94:95] op_sel:[1,1] op_sel_hi:[1,0] neg_lo:[0,1]\n\tds_write2_b64 %[b2], v[134:135], v[76:77] offset0:0 offset1:66\n\tv_pk_fma_f32 v[102:103], v[120:121], v[66:67], v[102:103] op_sel:[0,0,0] op_sel_hi:[0,1,1]\n\tv_pk_fma_f32 v[104:105], v[122:123], v[68:69], v[104:105] op_sel:[0,0,0] op_sel_hi:[0,1,1]\n\tv_pk_fma_f32 v[110:111], v[120:121], v[92:93], v[110:111] op_sel:[0,0,0] op_sel_hi:[0,1,1]\n\tv_pk_fma_f32 v[112:113], v[122:123], v[94:95], v[112:113] op_sel:[0,0,0] op_sel_hi:[0,1,1]\n\tv_cvt_pk_f16_f32 v144, v128, v129\n\tv_cvt_pk_f16_f32 v145, v130, v131\n\tv_cvt_pk_f16_f32 v146, v140, v141\n\tv_cvt_pk_f16_f32 v147, v142, v143\n\tv_pk_mul_f32 v[66:67], v[148:149], v[60:61] op_sel:[1,1] op_sel_hi:[1,0] neg_lo:[0,1]\n\tv_pk_mul_f32 v[68:69], v[150:151], v[62:63] op_sel:[1,1] op_sel_hi:[1,0] neg_lo:[0,1]\n\tv_pk_mul_f32 v[92:93], v[128:129], v[60:61] op_sel:[1,1] op_sel_hi:[1,0] neg_lo:[0,1]\n\tv_pk_mul_f32 v[94:95], v[130:131], v[62:63] op_sel:[1,1] op_sel_hi:[1,0] neg_lo:[0,1]\n\tds_write2_b64 %[b9], v[124:125], v[146:147] offset0:132 offset1:198\n\tv_pk_fma_f32 v[66:67], v[148:149], v[60:61], v[66:67] op_sel:[0,0,0] op_sel_hi:[0,1,1]\n\tv_pk_fma_f32 v[68:69], v[150:151], v[62:63], v[68:69] op_sel:[0,0,0] op_sel_hi:[0,1,1]\n\tv_pk_fma_f32 v[92:93], v[128:129], v[60:61], v[92:93] op_sel:[0,0,0] op_sel_hi:[0,1,1]\n\tv_pk_fma_f32 v[94:95], v[130:131], v[62:63], v[94:95] op_sel:[0,0,0] op_sel_hi:[0,1,1]\n\tv_cvt_pk_f16_f32 v140, v102, -v103\n\tv_cvt_pk_f16_f32 v141, v104, -v105\n\tv_cvt_pk_f16_f32 v142, v110, -v111\n\tv_cvt_pk_f16_f32 v143, v112, -v113\n\tv_pk_mul_f32 v[102:103], v[120:121], v[88:89] op_sel:[1,1] op_sel_hi:[1,0] neg_lo:[0,1]\n\tv_pk_mul_f32 v[104:105], v[122:123], v[90:91] op_sel:[1,1] op_sel_hi:[1,0] neg_lo:[0,1]\n\tv_pk_mul_f32 v[110:111], v[120:121], v[70:71] op_sel:[1,1] op_sel_hi:[1,0] neg_lo:[0,1]\n\tv_pk_mul_f32 v[112:113], v[122:123], v[72:73] op_sel:[1,1] op_sel_hi:[1,0] neg_lo:[0,1]\n\tds_write2_b64 %[b2], v[140:141], v[142:143] offset0:132 offset1:198\n\tv_pk_fma_f32 v[102:103], v[120:121], v[88:89], v[102:103] op_sel:[0,0,0] op_sel_hi:[0,1,1]\n\tv_pk_fma_f32 v[104:105], v[122:123], v[90:91], v[104:105] op_sel:[0,0,0] op_sel_hi:[0,1,1]\n\tv_pk_fma_f32 v[110:111], v[120:121], v[70:71], v[110:111] op_sel:[0,0,0] op_sel_hi:[0,1,1]\n\tv_pk_fma_f32 v[112:113], v[122:123], v[72:73], v[112:113] op_sel:[0,0,0] op_sel_hi:[0,1,1]\n\tv_cvt_pk_f16_f32 v124, v66, v67\n\tv_cvt_pk_f16_f32 v125, v68, v69\n\tv_cvt_pk_f16_f32 v128, v92, v93\n\tv_cvt_pk_f16_f32 v129, v94, v95\n\tv_pk_mul_f32 v[70:71], v[48:49], v[98:99] op_sel:[1,1] op_sel_hi:[1,0] neg_lo:[0,1]\n\tv_pk_mul_f32 v[72:73], v[50:51], v[100:101] op_sel:[1,1] op_sel_hi:[1,0] neg_lo:[0,1]\n\tv_pk_mul_f32 v[88:89], v[82:83], v[98:99] op_sel:[1,1] op_sel_hi:[1,0] neg_lo:[0,1]\n\tv_pk_mul_f32 v[90:91], v[84:85], v[100:101] op_sel:[1,1] op_sel_hi:[1,0] neg_lo:[0,1]\n\tds_write2_b64 %[b10], v[126:127], v[144:145] offset0:132 offset1:198\n\tv_pk_fma_f32 v[70:71], v[48:49], v[98:99], v[70:71] op_sel:[0,0,0] op_sel_hi:[0,1,1]\n\tv_pk_fma_f32 v[72:73], v[50:51], v[100:101], v[72:73] op_sel:[0,0,0] op_sel_hi:[0,1,1]\n\tv_pk_fma_f32 v[88:89], v[82:83], v[98:99], v[88:89] op_sel:[0,0,0] op_sel_hi:[0,1,1]\n\tv_pk_fma_f32 v[90:91], v[84:85], v[100:101], v[90:91] op_sel:[0,0,0] op_sel_hi:[0,1,1]\n\tv_cvt_pk_f16_f32 v92, v102, -v103\n\tv_cvt_pk_f16_f32 v93, v104, -v105\n\tv_cvt_pk_f16_f32 v94, v110, -v111\n\tv_cvt_pk_f16_f32 v95, v112, -v113\n\tv_pk_mul_f32 v[82:83], v[66:67], v[60:61] op_sel:[1,1] op_sel_hi:[1,0] neg_lo:[0,1]\n\tv_pk_mul_f32 v[84:85], v[68:69], v[62:63] op_sel:[1,1] op_sel_hi:[1,0] neg_lo:[0,1]\n\tv_pk_mul_f32 v[102:103], v[120:121], v[136:137] op_sel:[1,1] op_sel_hi:[1,0] neg_lo:[0,1]\n\tv_pk_mul_f32 v[104:105], v[122:123], v[138:139] op_sel:[1,1] op_sel_hi:[1,0] neg_lo:[0,1]\n\tds_write2_b64 %[b3], v[92:93], v[94:95] offset0:0 offset1:66\n\tv_pk_fma_f32 v[82:83], v[66:67], v[60:61], v[82:83] op_sel:[0,0,0] op_sel_hi:[0,1,1]\n\tv_pk_fma_f32 v[84:85], v[68:69], v[62:63], v[84:85] op_sel:[0,0,0] op_sel_hi:[0,1,1]\n\tv_pk_fma_f32 v[102:103], v[120:121], v[136:137], v[102:103] op_sel:[0,0,0] op_sel_hi:[0,1,1]\n\tv_pk_fma_f32 v[104:105], v[122:123], v[138:139], v[104:105] op_sel:[0,0,0] op_sel_hi:[0,1,1]\n\tv_cvt_pk_f16_f32 v110, v70, v71\n\tv_cvt_pk_f16_f32 v111, v72, v73\n\tv_cvt_pk_f16_f32 v112, v88, v89\n\tv_cvt_pk_f16_f32 v113, v90, v91\n\tv_pk_mul_f32 v[66:67], v[120:121], v[78:79] op_sel:[1,1] op_sel_hi:[1,0] neg_lo:[0,1]\n\tv_pk_mul_f32 v[68:69], v[122:123], v[80:81] op_sel:[1,1] op_sel_hi:[1,0] neg_lo:[0,1]\n\tv_pk_mul_f32 v[70:71], v[114:115], v[98:99] op_sel:[1,1] op_sel_hi:[1,0] neg_lo:[0,1]\n\tv_pk_mul_f32 v[72:73], v[116:117], v[100:101] op_sel:[1,1] op_sel_hi:[1,0] neg_lo:[0,1]\n\tds_write2_b64 %[b11], v[110:111], v[112:113] offset0:132 offset1:198\n\tv_pk_fma_f32 v[66:67], v[120:121], v[78:79], v[66:67] op_sel:[0,0,0] op_sel_hi:[0,1,1]\n\tv_pk_fma_f32 v[68:69], v[122:123], v[80:81], v[68:69] op_sel:[0,0,0] op_sel_hi:[0,1,1]\n\tv_pk_fma_f32 v[70:71], v[114:115], v[98:99], v[70:71] op_sel:[0,0,0] op_sel_hi:[0,1,1]\n\tv_pk_fma_f32 v[72:73], v[116:117], v[100:101], v[72:73] op_sel:[0,0,0] op_sel_hi:[0,1,1]\n\tv_cvt_pk_f16_f32 v88, v82, v83\n\tv_cvt_pk_f16_f32 v89, v84, v85\n\tv_cvt_pk_f16_f32 v90, v102, -v103\n\tv_cvt_pk_f16_f32 v91, v104, -v105\n\tv_pk_mul_f32 v[78:79], v[96:97], v[98:99] op_sel:[1,1] op_sel_hi:[1,0] neg_lo:[0,1]\n\tv_pk_mul_f32 v[80:81], v[118:119], v[100:101] op_sel:[1,1] op_sel_hi:[1,0] neg_lo:[0,1]\n\tv_pk_mul_f32 v[102:103], v[82:83], v[60:61] op_sel:[1,1] op_sel_hi:[1,0] neg_lo:[0,1]\n\tv_pk_mul_f32 v[104:105], v[84:85], v[62:63] op_sel:[1,1] op_sel_hi:[1,0] neg_lo:[0,1]\n\tds_write2_b64 %[b12], v[128:129], v[124:125] offset0:132 offset1:198\n\tv_pk_fma_f32 v[78:79], v[96:97], v[98:99], v[78:79] op_sel:[0,0,0] op_sel_hi:[0,1,1]\n\tv_pk_fma_f32 v[80:81], v[118:119], v[100:101], v[80:81] op_sel:[0,0,0] op_sel_hi:[0,1,1]\n\tv_pk_fma_f32 v[102:103], v[82:83], v[60:61], v[102:103] op_sel:[0,0,0] op_sel_hi:[0,1,1]\n\tv_pk_fma_f32 v[104:105], v[84:85], v[62:63], v[104:105] op_sel:[0,0,0] op_sel_hi:[0,1,1]\n\tv_cvt_pk_f16_f32 v110, v66, -v67\n\tv_cvt_pk_f16_f32 v111, v68, -v69\n\tv_cvt_pk_f16_f32 v112, v70, v71\n\tv_cvt_pk_f16_f32 v113, v72, v73\n\tv_pk_mul_f16 v66, v154, v74 op_sel:[1,1] op_sel_hi:[1,0] neg_lo:[0,1]\n\tv_pk_mul_f16 v67, v155, v75 op_sel:[1,1] op_sel_hi:[1,0] neg_lo:[0,1]\n\tv_pk_mul_f16 v68, v154, v86 op_sel:[1,1] op_sel_hi:[1,0] neg_lo:[0,1]\n\tv_pk_mul_f16 v69, v155, v87 op_sel:[1,1] op_sel_hi:[1,0] neg_lo:[0,1]\n\tds_write2_b64 %[b3], v[90:91], v[110:111] offset0:132 offset1:198\n\tv_pk_fma_f16 v66, v154, v74, v66 op_sel:[0,0,0] op_sel_hi:[0,1,1]\n\tv_pk_fma_f16 v67, v155, v75, v67 op_sel:[0,0,0] op_sel_hi:[0,1,1]\n\tv_pk_fma_f16 v68, v154, v86, v68 op_sel:[0,0,0] op_sel_hi:[0,1,1]\n\tv_pk_fma_f16 v69, v155, v87, v69 op_sel:[0,0,0] op_sel_hi:[0,1,1]\n\tv_cvt_pk_f16_f32 v70, v78, v79\n\tv_cvt_pk_f16_f32 v71, v80, v81\n\tv_cvt_pk_f16_f32 v72, v102, v103\n\tv_cvt_pk_f16_f32 v73, v104, v105\n\tv_pk_mul_f16 v78, v154, v108 op_sel:[1,1] op_sel_hi:[1,0] neg_lo:[0,1]\n\tv_pk_mul_f16 v79, v155, v109 op_sel:[1,1] op_sel_hi:[1,0] neg_lo:[0,1]\n\tv_pk_mul_f32 v[80:81], v[102:103], v[60:61] op_sel:[1,1] op_sel_hi:[1,0] neg_lo:[0,1]\n\tv_pk_mul_f32 v[82:83], v[104:105], v[62:63] op_sel:[1,1] op_sel_hi:[1,0] neg_lo:[0,1]\n\tds_write2_b64 %[b4], v[154:155], v[66:67] offset0:0 offset1:66\n\tv_pk_fma_f16 v78, v154, v108, v78 op_sel:[0,0,0] op_sel_hi:[0,1,1]\n\tv_pk_fma_f16 v79, v155, v109, v79 op_sel:[0,0,0] op_sel_hi:[0,1,1]\n\tv_pk_fma_f32 v[80:81], v[102:103], v[60:61], v[80:81] op_sel:[0,0,0] op_sel_hi:[0,1,1]\n\tv_pk_fma_f32 v[82:83], v[104:105], v[62:63], v[82:83] op_sel:[0,0,0] op_sel_hi:[0,1,1]\n\tv_pk_mul_f16 v74, v154, v106 op_sel:[1,1] op_sel_hi:[1,0] neg_lo:[0,1]\n\tv_pk_mul_f16 v75, v155, v107 op_sel:[1,1] op_sel_hi:[1,0] neg_lo:[0,1]\n\tv_pk_mul_f16 v84, v154, v64 op_sel:[1,1] op_sel_hi:[1,0] neg_lo:[0,1]\n\tv_pk_mul_f16 v85, v155, v65 op_sel:[1,1] op_sel_hi:[1,0] neg_lo:[0,1]\n\tds_write2_b64 %[b4], v[68:69], v[78:79] offset0:132 offset1:198\n\tv_pk_fma_f16 v74, v154, v106, v74 op_sel:[0,0,0] op_sel_hi:[0,1,1]\n\tv_pk_fma_f16 v75, v155, v107, v75 op_sel:[0,0,0] op_sel_hi:[0,1,1]\n\tv_pk_fma_f16 v84, v154, v64, v84 op_sel:[0,0,0] op_sel_hi:[0,1,1]\n\tv_pk_fma_f16 v85, v155, v65, v85 op_sel:[0,0,0] op_sel_hi:[0,1,1]\n\tv_cvt_pk_f16_f32 v66, v80, v81\n\tv_cvt_pk_f16_f32 v67, v82, v83\n\tv_pk_mul_f16 v80, v154, v132 op_sel:[1,1] op_sel_hi:[1,0] neg_lo:[0,1]\n\tv_pk_mul_f16 v81, v155, v133 op_sel:[1,1] op_sel_hi:[1,0] neg_lo:[0,1]\n\tv_pk_mul_f16 v82, v154, v152 op_sel:[1,1] op_sel_hi:[1,0] neg_lo:[0,1]\n\tv_pk_mul_f16 v83, v155, v153 op_sel:[1,1] op_sel_hi:[1,0] neg_lo:[0,1]\n\tds_write2_b64 %[b5], v[74:75], v[84:85] offset0:0 offset1:66\n\tv_pk_fma_f16 v80, v154, v132, v80 op_sel:[0,0,0] op_sel_hi:[0,1,1]\n\tv_pk_fma_f16 v81, v155, v133, v81 op_sel:[0,0,0] op_sel_hi:[0,1,1]\n\tv_pk_fma_f16 v82, v154, v152, v82 op_sel:[0,0,0] op_sel_hi:[0,1,1]\n\tv_pk_fma_f16 v83, v155, v153, v83 op_sel:[0,0,0] op_sel_hi:[0,1,1]\n\tv_pk_mul_f16 v64, v154, v134 op_sel:[1,1] op_sel_hi:[1,0] neg_lo:[0,1]\n\tv_pk_mul_f16 v65, v155, v135 op_sel:[1,1] op_sel_hi:[1,0] neg_lo:[0,1]\n\tv_pk_mul_f16 v68, v154, v76 op_sel:[1,1] op_sel_hi:[1,0] neg_lo:[0,1]\n\tv_pk_mul_f16 v69, v155, v77 op_sel:[1,1] op_sel_hi:[1,0] neg_lo:[0,1]\n\tds_write2_b64 %[b5], v[80:81], v[82:83] offset0:132 offset1:198\n\tv_pk_fma_f16 v64, v154, v134, v64 op_sel:[0,0,0] op_sel_hi:[0,1,1]\n\tv_pk_fma_f16 v65, v155, v135, v65 op_sel:[0,0,0] op_sel_hi:[0,1,1]\n\tv_pk_fma_f16 v68, v154, v76, v68 op_sel:[0,0,0] op_sel_hi:[0,1,1]\n\tv_pk_fma_f16 v69, v155, v77, v69 op_sel:[0,0,0] op_sel_hi:[0,1,1]\n\tv_pk_mul_f16 v74, v154, v140 op_sel:[1,1] op_sel_hi:[1,0] neg_lo:[0,1]\n\tv_pk_mul_f16 v75, v155, v141 op_sel:[1,1] op_sel_hi:[1,0] neg_lo:[0,1]\n\tv_pk_mul_f16 v78, v154, v142 op_sel:[1,1] op_sel_hi:[1,0] neg_lo:[0,1]\n\tv_pk_mul_f16 v79, v155, v143 op_sel:[1,1] op_sel_hi:[1,0] neg_lo:[0,1]\n\tds_write2_b64 %[b6], v[64:65], v[68:69] offset0:0 offset1:66\n\tv_pk_fma_f16 v74, v154, v140, v74 op_sel:[0,0,0] op_sel_hi:[0,1,1]\n\tv_pk_fma_f16 v75, v155, v141, v75 op_sel:[0,0,0] op_sel_hi:[0,1,1]\n\tv_pk_fma_f16 v78, v154, v142, v78 op_sel:[0,0,0] op_sel_hi:[0,1,1]\n\tv_pk_fma_f16 v79, v155, v143, v79 op_sel:[0,0,0] op_sel_hi:[0,1,1]\n\tv_pk_mul_f16 v76, v154, v92 op_sel:[1,1] op_sel_hi:[1,0] neg_lo:[0,1]\n\tv_pk_mul_f16 v77, v155, v93 op_sel:[1,1] op_sel_hi:[1,0] neg_lo:[0,1]\n\tv_pk_mul_f16 v80, v154, v94 op_sel:[1,1] op_sel_hi:[1,0] neg_lo:[0,1]\n\tv_pk_mul_f16 v81, v155, v95 op_sel:[1,1] op_sel_hi:[1,0] neg_lo:[0,1]\n\tds_write2_b64 %[b6], v[74:75], v[78:79] offset0:132 offset1:198\n\tv_pk_fma_f16 v76, v154, v92, v76 op_sel:[0,0,0] op_sel_hi:[0,1,1]\n\tv_pk_fma_f16 v77, v155, v93, v77 op_sel:[0,0,0] op_sel_hi:[0,1,1]\n\tv_pk_fma_f16 v80, v154, v94, v80 op_sel:[0,0,0] op_sel_hi:[0,1,1]\n\tv_pk_fma_f16 v81, v155, v95, v81 op_sel:[0,0,0] op_sel_hi:[0,1,1]\n\tv_pk_mul_f16 v64, v154, v90 op_sel:[1,1] op_sel_hi:[1,0] neg_lo:[0,1]\n\tv_pk_mul_f16 v65, v155, v91 op_sel:[1,1] op_sel_hi:[1,0] neg_lo:[0,1]\n\tv_pk_mul_f16 v68, v154, v110 op_sel:[1,1] op_sel_hi:[1,0] neg_lo:[0,1]\n\tv_pk_mul_f16 v69, v155, v111 op_sel:[1,1] op_sel_hi:[1,0] neg_lo:[0,1]\n\tds_write2_b64 %[b7], v[76:77], v[80:81] offset0:0 offset1:66\n\tv_pk_fma_f16 v64, v154, v90, v64 op_sel:[0,0,0] op_sel_hi:[0,1,1]\n\tv_pk_fma_f16 v65, v155, v91, v65 op_sel:[0,0,0] op_sel_hi:[0,1,1]\n\tv_pk_fma_f16 v68, v154, v110, v68 op_sel:[0,0,0] op_sel_hi:[0,1,1]\n\tv_pk_fma_f16 v69, v155, v111, v69 op_sel:[0,0,0] op_sel_hi:[0,1,1]\n\ts_nop 0\n\tds_write2_b64 %[b7], v[64:65], v[68:69] offset0:132 offset1:198\n\tds_write2_b64 %[b10], v[88:89], v[66:67] offset0:0 offset1:66\n\tds_write2_b64 %[b12], v[112:113], v[70:71] offset0:0 offset1:66\n\tds_write_b64 %[b13], v[72:73] offset:0"
#define KGEN_CLOBBER "v64", "v65", "v66", "v67", "v68", "v69", "v70", "v71", "v72", "v73", "v74", "v75", "v76", "v77", "v78", "v79", "v80", "v81", "v82", "v83", "v84", "v85", "v86", "v87", "v88", "v89", "v90", "v91", "v92", "v93", "v94", "v95", "v96", "v97", "v98", "v99", "v100", "v101", "v102", "v103", "v104", "v105", "v106", "v107", "v108", "v109", "v110", "v111", "v112", "v113", "v114", "v115", "v116", "v117", "v118", "v119", "v120", "v121", "v122", "v123", "v124", "v125", "v126", "v127", "v128", "v129", "v130", "v131", "v132", "v133", "v134", "v135", "v136", "v137", "v138", "v139", "v140", "v141", "v142", "v143", "v144", "v145", "v146", "v147", "v148", "v149", "v150", "v151", "v152", "v153", "v154", "v155"
#define KROW_0LO 0x2b2a2d2c23222120ull
#define KROW_0HI 0x202020202b343332ull
#define KROW_1LO 0x2524272a31302f2eull
#define KROW_1HI 0x2b2b2b2b20292826ull
#define KMFMA_ASM "ds_read_b128 v[64:67], %[ap] offset:0\n\tds_read_b128 v[68:71], %[bp] offset:0\n\tds_read_b128 v[72:75], %[ap] offset:32\n\tds_read_b128 v[76:79], %[bp] offset:32\n\tds_read_b128 v[80:83], %[ap] offset:64\n\tds_read_b128 v[84:87], %[bp] offset:64\n\tds_read_b128 v[88:91], %[ap] offset:96\n\tds_read_b128 v[92:95], %[bp] offset:96\n\ts_waitcnt lgkmcnt(6)\n\tv_mfma_f32_32x32x16_f16 v[48:63], v[64:67], v[68:71], 0\n\tds_read_b128 v[96:99], %[ap] offset:128\n\tds_read_b128 v[100:103], %[bp] offset:128\n\ts_waitcnt lgkmcnt(6)\n\tv_mfma_f32_32x32x16_f16 v[48:63], v[72:75], v[76:79], v[48:63]\n\tds_read_b128 v[104:107], %[ap] offset:160\n\tds_read_b128 v[108:111], %[bp] offset:160\n\ts_waitcnt lgkmcnt(6)\n\tv_mfma_f32_32x32x16_f16 v[48:63], v[80:83], v[84:87], v[48:63]\n\tds_read_b128 v[64:67], %[ap] offset:192\n\tds_read_b128 v[68:71], %[bp] offset:192\n\ts_waitcnt lgkmcnt(6)\n\tv_mfma_f32_32x32x16_f16 v[48:63], v[88:91], v[92:95], v[48:63]\n\tds_read_b128 v[72:75], %[ap] offset:224\n\tds_read_b128 v[76:79], %[bp] offset:224\n\ts_waitcnt lgkmcnt(6)\n\tv_mfma_f32_32x32x16_f16 v[48:63], v[96:99], v[100:103], v[48:63]\n\tds_read_b128 v[80:83], %[ap] offset:256\n\tds_read_b128 v[84:87], %[bp] offset:256\n\ts_waitcnt lgkmcnt(6)\n\tv_mfma_f32_32x32x16_f16 v[48:63], v[104:107], v[108:111], v[48:63]\n\tds_read_b128 v[88:91], %[ap] offset:288\n\tds_read_b128 v[92:95], %[bp] offset:288\n\ts_waitcnt lgkmcnt(6)\n\tv_mfma_f32_32x32x16_f16 v[48:63], v[64:67], v[68:71], v[48:63]\n\tds_read_b128 v[96:99], %[ap] offset:320\n\tds_read_b128 v[100:103], %[bp] offset:320\n\ts_waitcnt lgkmcnt(6)\n\tv_mfma_f32_32x32x16_f16 v[48:63], v[72:75], v[76:79], v[48:63]\n\tds_read_b128 v[104:107], %[ap] offset:352\n\tds_read_b128 v[108:111], %[bp] offset:352\n\ts_waitcnt lgkmcnt(6)\n\tv_mfma_f32_32x32x16_f16 v[48:63], v[80:83], v[84:87], v[48:63]\n\tds_read_b128 v[64:67], %[ap] offset:384\n\tds_read_b128 v[68:71], %[bp] offset:384\n\ts_waitcnt lgkmcnt(6)\n\tv_mfma_f32_32x32x16_f16 v[48:63], v[88:91], v[92:95], v[48:63]\n\tds_read_b128 v[72:75], %[ap] offset:416\n\tds_read_b128 v[76:79], %[bp] offset:416\n\ts_waitcnt lgkmcnt(6)\n\tv_mfma_f32_32x32x16_f16 v[48:63], v[96:99], v[100:103], v[48:63]\n\tds_read_b128 v[80:83], %[ap] offset:448\n\tds_read_b128 v[84:87], %[bp] offset:448\n\ts_waitcnt lgkmcnt(6)\n\tv_mfma_f32_32x32x16_f16 v[48:63], v[104:107], v[108:111], v[48:63]\n\tds_read_b128 v[88:91], %[ap] offset:480\n\tds_read_b128 v[92:95], %[bp] offset:480\n\ts_waitcnt lgkmcnt(6)\n\tv_mfma_f32_32x32x16_f16 v[48:63], v[64:67], v[68:71], v[48:63]\n\tds_read_b32 %[stg], %[ctl] offset:4\n\ts_waitcnt lgkmcnt(4)\n\tv_mfma_f32_32x32x16_f16 v[48:63], v[72:75], v[76:79], v[48:63]\n\ts_waitcnt lgkmcnt(2)\n\tv_mfma_f32_32x32x16_f16 v[48:63], v[80:83], v[84:87], v[48:63]\n\ts_waitcnt lgkmcnt(0)\n\tv_mfma_f32_32x32x16_f16 v[48:63], v[88:91], v[92:95], v[48:63]\n\ts_nop 7\n\ts_nop 3\n\ts_waitcnt lgkmcnt(0)"
#define KMFMA_CLOBBER "v64", "v65", "v66", "v67", "v68", "v69", "v70", "v71", "v72", "v73", "v74", "v75", "v76", "v77", "v78", "v79", "v80", "v81", "v82", "v83", "v84", "v85", "v86", "v87", "v88", "v89", "v90", "v91", "v92", "v93", "v94", "v95", "v96", "v97", "v98", "v99", "v100", "v101", "v102", "v103", "v104", "v105", "v106", "v107", "v108", "v109", "v110", "v111"
#define CONV_ASM ".p2align 3\n\tds_read_b64_tr_b16 v[52:53], %[a0] offset:456\n\tds_read_b64_tr_b16 v[54:55], %[a0] offset:448\n\tds_read_b64_tr_b16 v[84:85], %[a0] offset:968\n\tds_read_b64_tr_b16 v[86:87], %[a0] offset:960\n\tds_read_b64_tr_b16 v[48:49], %[a0] offset:392\n\tds_read_b64_tr_b16 v[50:51], %[a0] offset:384\n\tds_read_b64_tr_b16 v[80:81], %[a0] offset:904\n\tds_read_b64_tr_b16 v[82:83], %[a0] offset:896\n\tds_read_b64_tr_b16 v[44:45], %[a0] offset:328\n\tds_read_b64_tr_b16 v[46:47], %[a0] offset:320\n\tds_read_b64_tr_b16 v[76:77], %[a0] offset:840\n\tds_read_b64_tr_b16 v[78:79], %[a0] offset:832\n\tv_readfirstlane_b32 %[s], %[stg]\n\ts_mov_b32 %[c], 0x400000\n\ts_cmp_ge_u32 %[s], 4\n\ts_cbranch_scc1 2f\n\t1:\n\tds_read_b32 v216, %[ctl] offset:4\n\ts_waitcnt lgkmcnt(0)\n\tv_readfirstlane_b32 %[s], v216\n\ts_cmp_ge_u32 %[s], 4\n\ts_cbranch_scc1 2f\n\ts_sleep 1\n\ts_sub_u32 %[c], %[c], 1\n\ts_cbranch_scc0 1b\n\t.p2align 3\n\t2:\n\tds_read_b128 v[164:167], %[bb] offset:64\n\tds_read_b128 v[168:171], %[bb] offset:128\n\tds_read_b128 v[172:175], %[bb] offset:192\n\tds_read_b64_tr_b16 v[40:41], %[a0] offset:264\n\tds_read_b64_tr_b16 v[42:43], %[a0] offset:256\n\tds_read_b128 v[176:179], %[bb] offset:256\n\ts_waitcnt lgkmcnt(5)\n\ts_nop 0\n\tv_mfma_f32_16x16x32_bf16 v[132:135], v[52:55], v[164:167], 0\n\tds_read_b64_tr_b16 v[72:73], %[a0] offset:776\n\tds_read_b64_tr_b16 v[74:75], %[a0] offset:768\n\tds_read_b64_tr_b16 v[36:37], %[a0] offset:200\n\tv_mfma_f32_16x16x32_bf16 v[136:139], v[84:87], v[164:167], 0\n\tds_read_b64_tr_b16 v[38:39], %[a0] offset:192\n\ts_waitcnt lgkmcnt(8)\n\ts_nop 0\n\tv_mfma_f32_16x16x32_bf16 v[132:135], v[48:51], v[168:171], v[132:135]\n\tds_read_b128 v[180:183], %[bb] offset:320\n\tds_read_b64_tr_b16 v[68:69], %[a0] offset:712\n\tds_read_b64_tr_b16 v[70:71], %[a0] offset:704\n\tv_mfma_f32_16x16x32_bf16 v[136:139], v[80:83], v[168:171], v[136:139]\n\tds_read_b64_tr_b16 v[32:33], %[a0] offset:136\n\ts_waitcnt lgkmcnt(8)\n\ts_nop 0\n\tv_mfma_f32_16x16x32_bf16 v[132:135], v[44:47], v[172:175], v[132:135]\n\tds_read_b64_tr_b16 v[34:35], %[a0] offset:128\n\tds_read_b128 v[184:187], %[bb] offset:384\n\tds_read_b64_tr_b16 v[64:65], %[a0] offset:648\n\tv_mfma_f32_16x16x32_bf16 v[136:139], v[76:79], v[172:175], v[136:139]\n\tds_read_b64_tr_b16 v[66:67], %[a0] offset:640\n\tds_read_b64_tr_b16 v[28:29], %[a0] offset:72\n\tds_read_b64_tr_b16 v[30:31], %[a0] offset:64\n\tv_mfma_f32_16x16x32_bf16 v[132:135], v[40:43], v[176:179], v[132:135]\n\tds_read_b128 v[164:167], %[bb] offset:448\n\ts_waitcnt lgkmcnt(8)\n\ts_nop 0\n\tv_mfma_f32_16x16x32_bf16 v[136:139], v[72:75], v[176:179], v[136:139]\n\tds_read_b64_tr_b16 v[60:61], %[a0] offset:584\n\tds_read_b64_tr_b16 v[62:63], %[a0] offset:576\n\tds_read_b64_tr_b16 v[24:25], %[a0] offset:8\n\tv_mfma_f32_16x16x32_bf16 v[132:135], v[36:39], v[180:183], v[132:135]\n\tds_read_b64_tr_b16 v[26:27], %[a0] offset:0\n\tds_read_b128 v[168:171], %[bb] offset:512\n\tds_read_b64_tr_b16 v[56:57], %[a0] offset:520\n\tv_mfma_f32_16x16x32_bf16 v[136:139], v[68:71], v[180:183], v[136:139]\n\tds_read_b64_tr_b16 v[58:59], %[a0] offset:512\n\ts_waitcnt lgkmcnt(8)\n\ts_nop 0\n\tv_mfma_f32_16x16x32_bf16 v[132:135], v[32:35], v[184:187], v[132:135]\n\tds_read_b64_tr_b16 v[88:89], %[a0] offset:1032\n\tds_read_b64_tr_b16 v[90:91], %[a0] offset:1024\n\tds_read_b128 v[188:191], %[bb] offset:496\n\tv_mfma_f32_16x16x32_bf16 v[136:139], v[64:67], v[184:187], v[136:139]\n\tds_read_b128 v[172:175], %[bb] offset:576\n\ts_waitcnt lgkmcnt(8)\n\ts_nop 0\n\tv_mfma_f32_16x16x32_bf16 v[132:135], v[28:31], v[164:167], v[132:135]\n\tds_read_b128 v[176:179], %[bb] offset:640\n\tv_mfma_f32_16x16x32_bf16 v[136:139], v[60:63], v[164:167], v[136:139]\n\ts_waitcnt lgkmcnt(5)\n\ts_nop 0\n\tv_mfma_f32_16x16x32_bf16 v[132:135], v[24:27], v[168:171], v[132:135]\n\tds_write_b16 %[ta], %[t78]\n\tds_read_b128 v[180:183], %[bb] offset:704\n\tv_mfma_f32_16x16x32_bf16 v[136:139], v[56:59], v[168:171], v[136:139]\n\ts_waitcnt lgkmcnt(3)\n\ts_nop 0\n\tv_mfma_f32_16x16x32_bf16 v[140:143], v[88:91], v[168:171], 0\n\tds_write_b16_d16_hi %[ta], %[t78] offset:64\n\tds_read_b128 v[184:187], %[bb] offset:768\n\tv_mfma_f32_16x16x32_bf16 v[204:207], v[188:191], v[4:7], 0\n\tds_write_b128 %[yb], v[132:135] offset:0\n\tv_mfma_f32_16x16x32_bf16 v[218:221], v[168:171], v[4:7], 0\n\tds_write_b16 %[ta], %[t78] offset:388\n\tds_read_b128 v[164:167], %[bb] offset:832\n\tv_mfma_f32_16x16x32_bf16 v[208:211], v[188:191], v[8:11], 0\n\tds_write_b8 %[ctc], %[one] offset:0\n\tds_write_b16_d16_hi %[ta], %[t78] offset:452\n\tv_mfma_f32_16x16x32_bf16 v[222:225], v[168:171], v[8:11], 0\n\tds_write_b16 %[tb], %[t9a]\n\tv_mfma_f32_16x16x32_bf16 v[212:215], v[188:191], v[12:15], 0\n\tds_write_b16_d16_hi %[tb], %[t9a] offset:128\n\tv_cvt_pk_bf16_f32 v192, v221, v207\n\tv_mfma_f32_16x16x32_bf16 v[226:229], v[168:171], v[16:19], 0\n\tds_write_b16 %[tb], %[t9a] offset:388\n\tv_cvt_pk_bf16_f32 v193, v220, v206\n\tv_mfma_f32_16x16x32_bf16 v[148:151], v[20:23], v[168:171], 0\n\tds_write_b16_d16_hi %[tb], %[t9a] offset:516\n\tds_read_b128 v[168:171], %[bb] offset:896\n\tv_mfma_f32_16x16x32_bf16 v[136:139], v[52:55], v[172:175], v[136:139]\n\tds_read_b64 v[92:93], %[tp] offset:0\n\tds_read_b64 v[94:95], %[tp] offset:8\n\tds_read_b64 v[96:97], %[tp] offset:64\n\tv_mfma_f32_16x16x32_bf16 v[140:143], v[84:87], v[172:175], v[140:143]\n\tds_read_b128 v[172:175], %[bb] offset:960\n\ts_waitcnt lgkmcnt(7)\n\ts_nop 0\n\tv_mfma_f32_16x16x32_bf16 v[136:139], v[48:51], v[176:179], v[136:139]\n\tds_read_b64 v[98:99], %[tp] offset:72\n\tds_read_b64 v[100:101], %[tp] offset:128\n\tds_read_b64 v[102:103], %[tp] offset:136\n\tv_mfma_f32_16x16x32_bf16 v[140:143], v[80:83], v[176:179], v[140:143]\n\tds_read_b128 v[176:179], %[bb] offset:1024\n\tds_read_b64 v[104:105], %[tp] offset:768\n\tds_read_b64 v[106:107], %[tp] offset:776\n\tv_mfma_f32_16x16x32_bf16 v[136:139], v[44:47], v[180:183], v[136:139]\n\tds_read_b64 v[108:109], %[tp] offset:832\n\tds_read_b64 v[110:111], %[tp] offset:840\n\tds_read_b64 v[112:113], %[tp] offset:896\n\tv_mfma_f32_16x16x32_bf16 v[140:143], v[76:79], v[180:183], v[140:143]\n\tds_read_b64 v[114:115], %[tp] offset:904\n\tds_read_b64 v[116:117], %[tp] offset:1536\n\tds_read_b64 v[118:119], %[tp] offset:1544\n\tv_mfma_f32_16x16x32_bf16 v[136:139], v[40:43], v[184:187], v[136:139]\n\tds_read_b64 v[120:121], %[tp] offset:1600\n\tds_read_b64 v[122:123], %[tp] offset:1608\n\tv_cvt_pk_bf16_f32 v194, v219, v205\n\tv_mfma_f32_16x16x32_bf16 v[140:143], v[72:75], v[184:187], v[140:143]\n\tv_cvt_pk_bf16_f32 v195, v218, v204\n\tds_read_b64 v[124:125], %[tp] offset:1664\n\tds_read_b64 v[126:127], %[tp] offset:1672\n\tv_mfma_f32_16x16x32_bf16 v[136:139], v[36:39], v[164:167], v[136:139]\n\tds_read_b64 v[128:129], %[tp] offset:1728\n\tds_read_b64 v[130:131], %[tp] offset:1736\n\tv_cvt_pk_bf16_f32 v196, v225, v211\n\tv_mfma_f32_16x16x32_bf16 v[140:143], v[68:71], v[164:167], v[140:143]\n\tv_cvt_pk_bf16_f32 v197, v224, v210\n\ts_waitcnt lgkmcnt(6)\n\ts_nop 0\n\tv_mfma_f32_16x16x32_bf16 v[136:139], v[32:35], v[168:171], v[136:139]\n\tv_cvt_pk_bf16_f32 v198, v223, v209\n\tv_mfma_f32_16x16x32_bf16 v[140:143], v[64:67], v[168:171], v[140:143]\n\tv_cvt_pk_bf16_f32 v199, v222, v208\n\tv_mfma_f32_16x16x32_bf16 v[136:139], v[28:31], v[172:175], v[136:139]\n\tv_cvt_pk_bf16_f32 v200, v229, v215\n\tv_mfma_f32_16x16x32_bf16 v[140:143], v[60:63], v[172:175], v[140:143]\n\tds_read_b128 v[188:191], %[bb] offset:1008\n\tv_cvt_pk_bf16_f32 v201, v228, v214\n\tv_mfma_f32_16x16x32_bf16 v[136:139], v[24:27], v[176:179], v[136:139]\n\tv_cvt_pk_bf16_f32 v202, v227, v213\n\tv_mfma_f32_16x16x32_bf16 v[140:143], v[56:59], v[176:179], v[140:143]\n\tv_cvt_pk_bf16_f32 v203, v226, v212\n\tv_mfma_f32_16x16x32_bf16 v[144:147], v[88:91], v[176:179], 0\n\tv_mfma_f32_16x16x32_bf16 v[140:143], v[192:195], v[92:95], v[140:143]\n\tv_mfma_f32_16x16x32_bf16 v[144:147], v[192:195], v[96:99], v[144:147]\n\tv_mfma_f32_16x16x32_bf16 v[148:151], v[192:195], v[100:103], v[148:151]\n\tv_mfma_f32_16x16x32_bf16 v[140:143], v[196:199], v[104:107], v[140:143]\n\tds_write_b128 %[yb], v[136:139] offset:1152\n\tds_read_b128 v[180:183], %[bb] offset:1088\n\tv_mfma_f32_16x16x32_bf16 v[144:147], v[196:199], v[108:111], v[144:147]\n\tv_mfma_f32_16x16x32_bf16 v[148:151], v[196:199], v[112:115], v[148:151]\n\tds_write_b8 %[ctc], %[one] offset:64\n\tds_read_b128 v[184:187], %[bb] offset:1152\n\tv_mfma_f32_16x16x32_bf16 v[148:151], v[200:203], v[116:119], v[148:151]\n\ts_waitcnt lgkmcnt(4)\n\ts_nop 0\n\tv_mfma_f32_16x16x32_bf16 v[152:155], v[200:203], v[120:123], 0\n\tds_read_b128 v[164:167], %[bb] offset:1216\n\tv_mfma_f32_16x16x32_bf16 v[156:159], v[200:203], v[124:127], 0\n\tv_mfma_f32_16x16x32_bf16 v[160:163], v[200:203], v[128:131], 0\n\tds_read_b128 v[168:171], %[bb] offset:1280\n\tv_mfma_f32_16x16x32_bf16 v[204:207], v[188:191], v[4:7], 0\n\tv_mfma_f32_16x16x32_bf16 v[218:221], v[176:179], v[4:7], 0\n\tds_read_b128 v[172:175], %[bb] offset:1344\n\tv_mfma_f32_16x16x32_bf16 v[208:211], v[188:191], v[8:11], 0\n\tv_mfma_f32_16x16x32_bf16 v[222:225], v[176:179], v[8:11], 0\n\tv_mfma_f32_16x16x32_bf16 v[212:215], v[188:191], v[12:15], 0\n\tv_mfma_f32_16x16x32_bf16 v[226:229], v[176:179], v[16:19], 0\n\tv_mfma_f32_16x16x32_bf16 v[152:155], v[20:23], v[176:179], v[152:155]\n\ts_waitcnt lgkmcnt(0)\n\ts_nop 0\n\tv_mfma_f32_16x16x32_bf16 v[140:143], v[52:55], v[180:183], v[140:143]\n\tds_read_b128 v[176:179], %[bb] offset:1408\n\tv_cvt_pk_bf16_f32 v192, v221, v207\n\tv_mfma_f32_16x16x32_bf16 v[144:147], v[84:87], v[180:183], v[144:147]\n\tds_read_b128 v[180:183], %[bb] offset:1472\n\tv_cvt_pk_bf16_f32 v193, v220, v206\n\tv_mfma_f32_16x16x32_bf16 v[140:143], v[48:51], v[184:187], v[140:143]\n\tv_cvt_pk_bf16_f32 v194, v219, v205\n\tv_mfma_f32_16x16x32_bf16 v[144:147], v[80:83], v[184:187], v[144:147]\n\tds_read_b128 v[184:187], %[bb] offset:1536\n\tv_cvt_pk_bf16_f32 v195, v218, v204\n\tv_mfma_f32_16x16x32_bf16 v[140:143], v[44:47], v[164:167], v[140:143]\n\tv_cvt_pk_bf16_f32 v196, v225, v211\n\tv_mfma_f32_16x16x32_bf16 v[144:147], v[76:79], v[164:167], v[144:147]\n\tv_cvt_pk_bf16_f32 v197, v224, v210\n\tv_mfma_f32_16x16x32_bf16 v[140:143], v[40:43], v[168:171], v[140:143]\n\tv_cvt_pk_bf16_f32 v198, v223, v209\n\tv_mfma_f32_16x16x32_bf16 v[144:147], v[72:75], v[168:171], v[144:147]\n\tv_cvt_pk_bf16_f32 v199, v222, v208\n\tv_mfma_f32_16x16x32_bf16 v[140:143], v[36:39], v[172:175], v[140:143]\n\tv_cvt_pk_bf16_f32 v200, v229, v215\n\tv_mfma_f32_16x16x32_bf16 v[144:147], v[68:71], v[172:175], v[144:147]\n\ts_waitcnt lgkmcnt(0)\n\ts_nop 0\n\tv_mfma_f32_16x16x32_bf16 v[140:143], v[32:35], v[176:179], v[140:143]\n\tv_cvt_pk_bf16_f32 v201, v228, v214\n\tv_mfma_f32_16x16x32_bf16 v[144:147], v[64:67], v[176:179], v[144:147]\n\tv_cvt_pk_bf16_f32 v202, v227, v213\n\tv_mfma_f32_16x16x32_bf16 v[140:143], v[28:31], v[180:183], v[140:143]\n\tds_read_b128 v[188:191], %[bb] offset:1520\n\tv_cvt_pk_bf16_f32 v203, v226, v212\n\tv_mfma_f32_16x16x32_bf16 v[144:147], v[60:63], v[180:183], v[144:147]\n\tv_mfma_f32_16x16x32_bf16 v[140:143], v[24:27], v[184:187], v[140:143]\n\tv_mfma_f32_16x16x32_bf16 v[144:147], v[56:59], v[184:187], v[144:147]\n\tv_mfma_f32_16x16x32_bf16 v[148:151], v[88:91], v[184:187], v[148:151]\n\tv_mfma_f32_16x16x32_bf16 v[144:147], v[192:195], v[92:95], v[144:147]\n\tv_mfma_f32_16x16x32_bf16 v[148:151], v[192:195], v[96:99], v[148:151]\n\tv_mfma_f32_16x16x32_bf16 v[152:155], v[192:195], v[100:103], v[152:155]\n\tds_read_b128 v[164:167], %[bb] offset:1600\n\tv_mfma_f32_16x16x32_bf16 v[144:147], v[196:199], v[104:107], v[144:147]\n\tv_mfma_f32_16x16x32_bf16 v[148:151], v[196:199], v[108:111], v[148:151]\n\tds_write_b128 %[yb], v[140:143] offset:2304\n\tv_mfma_f32_16x16x32_bf16 v[152:155], v[196:199], v[112:115], v[152:155]\n\tds_read_b128 v[168:171], %[bb] offset:1664\n\tv_mfma_f32_16x16x32_bf16 v[152:155], v[200:203], v[116:119], v[152:155]\n\tds_write_b8 %[ctc], %[one] offset:128\n\tds_read_b128 v[172:175], %[bb] offset:1728\n\tv_mfma_f32_16x16x32_bf16 v[156:159], v[200:203], v[120:123], v[156:159]\n\tv_mfma_f32_16x16x32_bf16 v[160:163], v[200:203], v[124:127], v[160:163]\n\ts_waitcnt lgkmcnt(3)\n\ts_nop 0\n\tv_mfma_f32_16x16x32_bf16 v[204:207], v[188:191], v[4:7], 0\n\tds_read_b128 v[176:179], %[bb] offset:1792\n\tv_mfma_f32_16x16x32_bf16 v[218:221], v[184:187], v[4:7], 0\n\tds_read_b128 v[180:183], %[bb] offset:1856\n\tv_mfma_f32_16x16x32_bf16 v[208:211], v[188:191], v[8:11], 0\n\tv_mfma_f32_16x16x32_bf16 v[222:225], v[184:187], v[8:11], 0\n\tv_mfma_f32_16x16x32_bf16 v[212:215], v[188:191], v[12:15], 0\n\tv_mfma_f32_16x16x32_bf16 v[226:229], v[184:187], v[16:19], 0\n\tds_read_b32 v216, %[ctl] offset:4\n\tv_mfma_f32_16x16x32_bf16 v[156:159], v[20:23], v[184:187], v[156:159]\n\tds_read_b128 v[184:187], %[bb] offset:1920\n\tv_cvt_pk_bf16_f32 v192, v221, v207\n\tv_mfma_f32_16x16x32_bf16 v[144:147], v[52:55], v[164:167], v[144:147]\n\tv_cvt_pk_bf16_f32 v193, v220, v206\n\tv_mfma_f32_16x16x32_bf16 v[148:151], v[84:87], v[164:167], v[148:151]\n\ts_waitcnt lgkmcnt(2)\n\ts_nop 0\n\tv_mfma_f32_16x16x32_bf16 v[144:147], v[48:51], v[168:171], v[144:147]\n\tds_read_b128 v[164:167], %[bb] offset:1984\n\tv_cvt_pk_bf16_f32 v194, v219, v205\n\tv_mfma_f32_16x16x32_bf16 v[148:151], v[80:83], v[168:171], v[148:151]\n\tds_read_b128 v[168:171], %[bb] offset:2048\n\tv_cvt_pk_bf16_f32 v195, v218, v204\n\tv_mfma_f32_16x16x32_bf16 v[144:147], v[44:47], v[172:175], v[144:147]\n\tv_cvt_pk_bf16_f32 v196, v225, v211\n\tv_mfma_f32_16x16x32_bf16 v[148:151], v[76:79], v[172:175], v[148:151]\n\tv_cvt_pk_bf16_f32 v197, v224, v210\n\tv_mfma_f32_16x16x32_bf16 v[144:147], v[40:43], v[176:179], v[144:147]\n\ts_waitcnt lgkmcnt(2)\n\ts_nop 0\n\tv_readfirstlane_b32 %[s], v216\n\ts_mov_b32 %[c], 0x400000\n\ts_cmp_ge_u32 %[s], 8\n\ts_cbranch_scc1 2f\n\t1:\n\tds_read_b32 v216, %[ctl] offset:4\n\ts_waitcnt lgkmcnt(0)\n\tv_readfirstlane_b32 %[s], v216\n\ts_cmp_ge_u32 %[s], 8\n\ts_cbranch_scc1 2f\n\ts_sleep 1\n\ts_sub_u32 %[c], %[c], 1\n\ts_cbranch_scc0 1b\n\t.p2align 3\n\t2:\n\tv_mfma_f32_16x16x32_bf16 v[148:151], v[72:75], v[176:179], v[148:151]\n\tv_cvt_pk_bf16_f32 v198, v223, v209\n\tv_mfma_f32_16x16x32_bf16 v[144:147], v[36:39], v[180:183], v[144:147]\n\tv_cvt_pk_bf16_f32 v199, v222, v208\n\tv_mfma_f32_16x16x32_bf16 v[148:151], v[68:71], v[180:183], v[148:151]\n\tv_cvt_pk_bf16_f32 v200, v229, v215\n\tv_mfma_f32_16x16x32_bf16 v[144:147], v[32:35], v[184:187], v[144:147]\n\tv_cvt_pk_bf16_f32 v201, v228, v214\n\tv_mfma_f32_16x16x32_bf16 v[148:151], v[64:67], v[184:187], v[148:151]\n\ts_waitcnt lgkmcnt(0)\n\ts_nop 0\n\tv_mfma_f32_16x16x32_bf16 v[144:147], v[28:31], v[164:167], v[144:147]\n\tds_read_b128 v[188:191], %[bb] offset:2032\n\tv_cvt_pk_bf16_f32 v202, v227, v213\n\tv_mfma_f32_16x16x32_bf16 v[148:151], v[60:63], v[164:167], v[148:151]\n\tv_cvt_pk_bf16_f32 v203, v226, v212\n\tv_mfma_f32_16x16x32_bf16 v[144:147], v[24:27], v[168:171], v[144:147]\n\tv_mfma_f32_16x16x32_bf16 v[148:151], v[56:59], v[168:171], v[148:151]\n\tv_mfma_f32_16x16x32_bf16 v[152:155], v[88:91], v[168:171], v[152:155]\n\tv_mfma_f32_16x16x32_bf16 v[148:151], v[192:195], v[92:95], v[148:151]\n\tv_mfma_f32_16x16x32_bf16 v[152:155], v[192:195], v[96:99], v[152:155]\n\tds_read_b128 v[172:175], %[bb] offset:2112\n\tv_mfma_f32_16x16x32_bf16 v[156:159], v[192:195], v[100:103], v[156:159]\n\tv_mfma_f32_16x16x32_bf16 v[148:151], v[196:199], v[104:107], v[148:151]\n\tds_read_b128 v[176:179], %[bb] offset:2176\n\tv_mfma_f32_16x16x32_bf16 v[152:155], v[196:199], v[108:111], v[152:155]\n\tds_write_b128 %[yb], v[144:147] offset:3456\n\tv_mfma_f32_16x16x32_bf16 v[156:159], v[196:199], v[112:115], v[156:159]\n\tds_read_b128 v[180:183], %[bb] offset:2240\n\tv_mfma_f32_16x16x32_bf16 v[156:159], v[200:203], v[116:119], v[156:159]\n\tds_write_b8 %[ctc], %[one] offset:192\n\tv_mfma_f32_16x16x32_bf16 v[160:163], v[200:203], v[120:123], v[160:163]\n\ts_waitcnt lgkmcnt(3)\n\ts_nop 0\n\tv_mfma_f32_16x16x32_bf16 v[204:207], v[188:191], v[4:7], 0\n\tds_read_b128 v[184:187], %[bb] offset:2304\n\tv_mfma_f32_16x16x32_bf16 v[218:221], v[168:171], v[4:7], 0\n\tds_read_b128 v[164:167], %[bb] offset:2368\n\tv_mfma_f32_16x16x32_bf16 v[208:211], v[188:191], v[8:11], 0\n\tv_mfma_f32_16x16x32_bf16 v[222:225], v[168:171], v[8:11], 0\n\tv_mfma_f32_16x16x32_bf16 v[212:215], v[188:191], v[12:15], 0\n\tv_mfma_f32_16x16x32_bf16 v[226:229], v[168:171], v[16:19], 0\n\tv_mfma_f32_16x16x32_bf16 v[160:163], v[20:23], v[168:171], v[160:163]\n\tds_read_b128 v[168:171], %[bb] offset:2432\n\tv_mfma_f32_16x16x32_bf16 v[148:151], v[52:55], v[172:175], v[148:151]\n\tv_cvt_pk_bf16_f32 v192, v221, v207\n\tv_mfma_f32_16x16x32_bf16 v[152:155], v[84:87], v[172:175], v[152:155]\n\tds_read_b128 v[172:175], %[bb] offset:2496\n\tv_cvt_pk_bf16_f32 v193, v220, v206\n\tv_mfma_f32_16x16x32_bf16 v[148:151], v[48:51], v[176:179], v[148:151]\n\tv_cvt_pk_bf16_f32 v194, v219, v205\n\tv_mfma_f32_16x16x32_bf16 v[152:155], v[80:83], v[176:179], v[152:155]\n\ts_waitcnt lgkmcnt(1)\n\ts_nop 0\n\tv_mfma_f32_16x16x32_bf16 v[148:151], v[44:47], v[180:183], v[148:151]\n\tds_read_b128 v[176:179], %[bb] offset:2560\n\tv_cvt_pk_bf16_f32 v195, v218, v204\n\tv_mfma_f32_16x16x32_bf16 v[152:155], v[76:79], v[180:183], v[152:155]\n\tv_cvt_pk_bf16_f32 v196, v225, v211\n\tv_mfma_f32_16x16x32_bf16 v[148:151], v[40:43], v[184:187], v[148:151]\n\tv_cvt_pk_bf16_f32 v197, v224, v210\n\tv_mfma_f32_16x16x32_bf16 v[152:155], v[72:75], v[184:187], v[152:155]\n\tv_cvt_pk_bf16_f32 v198, v223, v209\n\tv_mfma_f32_16x16x32_bf16 v[148:151], v[36:39], v[164:167], v[148:151]\n\tv_cvt_pk_bf16_f32 v199, v222, v208\n\tv_mfma_f32_16x16x32_bf16 v[152:155], v[68:71], v[164:167], v[152:155]\n\tv_cvt_pk_bf16_f32 v200, v229, v215\n\tv_mfma_f32_16x16x32_bf16 v[148:151], v[32:35], v[168:171], v[148:151]\n\tds_read_b128 v[188:191], %[bb] offset:2544\n\tv_cvt_pk_bf16_f32 v201, v228, v214\n\tv_mfma_f32_16x16x32_bf16 v[152:155], v[64:67], v[168:171], v[152:155]\n\ts_waitcnt lgkmcnt(1)\n\ts_nop 0\n\tv_mfma_f32_16x16x32_bf16 v[148:151], v[28:31], v[172:175], v[148:151]\n\tv_cvt_pk_bf16_f32 v202, v227, v213\n\tv_mfma_f32_16x16x32_bf16 v[152:155], v[60:63], v[172:175], v[152:155]\n\tv_cvt_pk_bf16_f32 v203, v226, v212\n\tv_mfma_f32_16x16x32_bf16 v[148:151], v[24:27], v[176:179], v[148:151]\n\tds_read_b128 v[180:183], %[bb] offset:2624\n\tv_mfma_f32_16x16x32_bf16 v[152:155], v[56:59], v[176:179], v[152:155]\n\tv_mfma_f32_16x16x32_bf16 v[156:159], v[88:91], v[176:179], v[156:159]\n\tds_read_b128 v[184:187], %[bb] offset:2688\n\tv_mfma_f32_16x16x32_bf16 v[152:155], v[192:195], v[92:95], v[152:155]\n\tv_mfma_f32_16x16x32_bf16 v[156:159], v[192:195], v[96:99], v[156:159]\n\tds_read_b128 v[164:167], %[bb] offset:2752\n\tv_mfma_f32_16x16x32_bf16 v[160:163], v[192:195], v[100:103], v[160:163]\n\tds_write_b128 %[yb], v[148:151] offset:4608\n\tv_mfma_f32_16x16x32_bf16 v[152:155], v[196:199], v[104:107], v[152:155]\n\tds_read_b128 v[168:171], %[bb] offset:2816\n\tv_mfma_f32_16x16x32_bf16 v[156:159], v[196:199], v[108:111], v[156:159]\n\tds_write_b8 %[ctc], %[one] offset:256\n\tv_mfma_f32_16x16x32_bf16 v[160:163], v[196:199], v[112:115], v[160:163]\n\tds_read_b128 v[172:175], %[bb] offset:2880\n\tv_mfma_f32_16x16x32_bf16 v[160:163], v[200:203], v[116:119], v[160:163]\n\ts_waitcnt lgkmcnt(3)\n\ts_nop 0\n\tv_mfma_f32_16x16x32_bf16 v[204:207], v[188:191], v[4:7], 0\n\tv_mfma_f32_16x16x32_bf16 v[218:221], v[176:179], v[4:7], 0\n\tv_mfma_f32_16x16x32_bf16 v[208:211], v[188:191], v[8:11], 0\n\tv_mfma_f32_16x16x32_bf16 v[222:225], v[176:179], v[8:11], 0\n\tds_read_b128 v[176:179], %[bb] offset:2944\n\tv_mfma_f32_16x16x32_bf16 v[152:155], v[52:55], v[180:183], v[152:155]\n\tv_mfma_f32_16x16x32_bf16 v[156:159], v[84:87], v[180:183], v[156:159]\n\tds_read_b128 v[180:183], %[bb] offset:3008\n\tv_mfma_f32_16x16x32_bf16 v[152:155], v[48:51], v[184:187], v[152:155]\n\tv_mfma_f32_16x16x32_bf16 v[156:159], v[80:83], v[184:187], v[156:159]\n\tds_read_b128 v[184:187], %[bb] offset:3072\n\tv_cvt_pk_bf16_f32 v192, v221, v207\n\tv_mfma_f32_16x16x32_bf16 v[152:155], v[44:47], v[164:167], v[152:155]\n\tv_cvt_pk_bf16_f32 v193, v220, v206\n\tv_mfma_f32_16x16x32_bf16 v[156:159], v[76:79], v[164:167], v[156:159]\n\ts_waitcnt lgkmcnt(1)\n\ts_nop 0\n\tv_mfma_f32_16x16x32_bf16 v[152:155], v[40:43], v[168:171], v[152:155]\n\tv_cvt_pk_bf16_f32 v194, v219, v205\n\tv_mfma_f32_16x16x32_bf16 v[156:159], v[72:75], v[168:171], v[156:159]\n\tds_read_b128 v[188:191], %[bb] offset:3056\n\tv_cvt_pk_bf16_f32 v195, v218, v204\n\tv_mfma_f32_16x16x32_bf16 v[152:155], v[36:39], v[172:175], v[152:155]\n\tv_cvt_pk_bf16_f32 v196, v225, v211\n\tv_mfma_f32_16x16x32_bf16 v[156:159], v[68:71], v[172:175], v[156:159]\n\tv_cvt_pk_bf16_f32 v197, v224, v210\n\tv_mfma_f32_16x16x32_bf16 v[152:155], v[32:35], v[176:179], v[152:155]\n\tv_cvt_pk_bf16_f32 v198, v223, v209\n\tv_mfma_f32_16x16x32_bf16 v[156:159], v[64:67], v[176:179], v[156:159]\n\tds_read_b128 v[164:167], %[bb] offset:3136\n\tv_cvt_pk_bf16_f32 v199, v222, v208\n\tv_mfma_f32_16x16x32_bf16 v[152:155], v[28:31], v[180:183], v[152:155]\n\tv_mfma_f32_16x16x32_bf16 v[156:159], v[60:63], v[180:183], v[156:159]\n\ts_waitcnt lgkmcnt(1)\n\ts_nop 0\n\tv_mfma_f32_16x16x32_bf16 v[152:155], v[24:27], v[184:187], v[152:155]\n\tds_read_b128 v[168:171], %[bb] offset:3200\n\tv_mfma_f32_16x16x32_bf16 v[156:159], v[56:59], v[184:187], v[156:159]\n\tds_read_b128 v[172:175], %[bb] offset:3264\n\tv_mfma_f32_16x16x32_bf16 v[160:163], v[88:91], v[184:187], v[160:163]\n\tv_mfma_f32_16x16x32_bf16 v[156:159], v[192:195], v[92:95], v[156:159]\n\tds_read_b128 v[176:179], %[bb] offset:3328\n\tv_mfma_f32_16x16x32_bf16 v[160:163], v[192:195], v[96:99], v[160:163]\n\tv_mfma_f32_16x16x32_bf16 v[156:159], v[196:199], v[104:107], v[156:159]\n\tds_write_b128 %[yb], v[152:155] offset:5760\n\tv_mfma_f32_16x16x32_bf16 v[160:163], v[196:199], v[108:111], v[160:163]\n\tds_read_b128 v[180:183], %[bb] offset:3392\n\tv_mfma_f32_16x16x32_bf16 v[204:207], v[188:191], v[4:7], 0\n\tds_write_b8 %[ctc], %[one] offset:320\n\tv_mfma_f32_16x16x32_bf16 v[218:221], v[184:187], v[4:7], 0\n\tv_mfma_f32_16x16x32_bf16 v[208:211], v[188:191], v[8:11], 0\n\tv_mfma_f32_16x16x32_bf16 v[222:225], v[184:187], v[8:11], 0\n\ts_waitcnt lgkmcnt(1)\n\ts_nop 0\n\tv_mfma_f32_16x16x32_bf16 v[156:159], v[52:55], v[164:167], v[156:159]\n\tds_read_b128 v[184:187], %[bb] offset:3456\n\tv_mfma_f32_16x16x32_bf16 v[160:163], v[84:87], v[164:167], v[160:163]\n\tds_read_b128 v[164:167], %[bb] offset:3520\n\tv_cvt_pk_bf16_f32 v192, v221, v207\n\tv_mfma_f32_16x16x32_bf16 v[156:159], v[48:51], v[168:171], v[156:159]\n\tv_cvt_pk_bf16_f32 v193, v220, v206\n\tv_mfma_f32_16x16x32_bf16 v[160:163], v[80:83], v[168:171], v[160:163]\n\tds_read_b128 v[168:171], %[bb] offset:3584\n\tv_cvt_pk_bf16_f32 v194, v219, v205\n\tv_mfma_f32_16x16x32_bf16 v[156:159], v[44:47], v[172:175], v[156:159]\n\tv_cvt_pk_bf16_f32 v195, v218, v204\n\tv_mfma_f32_16x16x32_bf16 v[160:163], v[76:79], v[172:175], v[160:163]\n\tds_read_b128 v[172:175], %[bb] offset:3648\n\tv_cvt_pk_bf16_f32 v196, v225, v211\n\tv_mfma_f32_16x16x32_bf16 v[156:159], v[40:43], v[176:179], v[156:159]\n\tglobal_load_dwordx2 v[240:241], %[so], %[ws] sc1\n\tv_cvt_pk_bf16_f32 v197, v224, v210\n\tv_mfma_f32_16x16x32_bf16 v[160:163], v[72:75], v[176:179], v[160:163]\n\tds_read_b128 v[176:179], %[bb] offset:3712\n\tv_cvt_pk_bf16_f32 v198, v223, v209\n\tv_mfma_f32_16x16x32_bf16 v[156:159], v[36:39], v[180:183], v[156:159]\n\tv_cvt_pk_bf16_f32 v199, v222, v208\n\tv_mfma_f32_16x16x32_bf16 v[160:163], v[68:71], v[180:183], v[160:163]\n\ts_waitcnt lgkmcnt(1)\n\ts_nop 0\n\tv_mfma_f32_16x16x32_bf16 v[156:159], v[32:35], v[184:187], v[156:159]\n\tds_read_b128 v[180:183], %[bb] offset:3776\n\tv_mfma_f32_16x16x32_bf16 v[160:163], v[64:67], v[184:187], v[160:163]\n\tds_read_b128 v[184:187], %[bb] offset:3840\n\tv_mfma_f32_16x16x32_bf16 v[156:159], v[28:31], v[164:167], v[156:159]\n\tv_mfma_f32_16x16x32_bf16 v[160:163], v[60:63], v[164:167], v[160:163]\n\tds_read_b128 v[164:167], %[bb] offset:3904\n\tv_mfma_f32_16x16x32_bf16 v[156:159], v[24:27], v[168:171], v[156:159]\n\tv_mfma_f32_16x16x32_bf16 v[160:163], v[56:59], v[168:171], v[160:163]\n\tds_read_b128 v[168:171], %[bb] offset:3968\n\tv_mfma_f32_16x16x32_bf16 v[160:163], v[192:195], v[92:95], v[160:163]\n\tv_mfma_f32_16x16x32_bf16 v[160:163], v[196:199], v[104:107], v[160:163]\n\tv_mfma_f32_16x16x32_bf16 v[160:163], v[52:55], v[172:175], v[160:163]\n\ts_waitcnt lgkmcnt(1)\n\ts_nop 0\n\tv_mfma_f32_16x16x32_bf16 v[160:163], v[48:51], v[176:179], v[160:163]\n\tds_write_b128 %[yb], v[156:159] offset:6912\n\tv_mfma_f32_16x16x32_bf16 v[160:163], v[44:47], v[180:183], v[160:163]\n\tds_read_b128 v[172:175], %[bb] offset:4032\n\tv_mfma_f32_16x16x32_bf16 v[160:163], v[40:43], v[184:187], v[160:163]\n\tds_write_b8 %[ctc], %[one] offset:384\n\tds_read_b128 v[176:179], %[bb] offset:4096\n\tv_mfma_f32_16x16x32_bf16 v[160:163], v[36:39], v[164:167], v[160:163]\n\ts_waitcnt lgkmcnt(4)\n\ts_nop 0\n\tv_mfma_f32_16x16x32_bf16 v[160:163], v[32:35], v[168:171], v[160:163]\n\ts_waitcnt lgkmcnt(2)\n\ts_nop 0\n\tv_mfma_f32_16x16x32_bf16 v[160:163], v[28:31], v[172:175], v[160:163]\n\ts_waitcnt lgkmcnt(0)\n\ts_nop 0\n\tv_mfma_f32_16x16x32_bf16 v[160:163], v[24:27], v[176:179], v[160:163]\n\ts_nop 7\n\tds_write_b128 %[yb], v[160:163] offset:8064\n\tds_write_b8 %[ctc], %[one] offset:448"
#define CONV_CLOBBER "v24", "v25", "v26", "v27", "v28", "v29", "v30", "v31", "v32", "v33", "v34", "v35", "v36", "v37", "v38", "v39", "v40", "v41", "v42", "v43", "v44", "v45", "v46", "v47", "v48", "v49", "v50", "v51", "v52", "v53", "v54", "v55", "v56", "v57", "v58", "v59", "v60", "v61", "v62", "v63", "v64", "v65", "v66", "v67", "v68", "v69", "v70", "v71", "v72", "v73", "v74", "v75", "v76", "v77", "v78", "v79", "v80", "v81", "v82", "v83", "v84", "v85", "v86", "v87", "v88", "v89", "v90", "v91", "v92", "v93", "v94", "v95", "v96", "v97", "v98", "v99", "v100", "v101", "v102", "v103", "v104", "v105", "v106", "v107", "v108", "v109", "v110", "v111", "v112", "v113", "v114", "v115", "v116", "v117", "v118", "v119", "v120", "v121", "v122", "v123", "v124", "v125", "v126", "v127", "v128", "v129", "v130", "v131", "v132", "v133", "v134", "v135", "v136", "v137", "v138", "v139", "v140", "v141", "v142", "v143", "v144", "v145", "v146", "v147", "v148", "v149", "v150", "v151", "v152", "v153", "v154", "v155", "v156", "v157", "v158", "v159", "v160", "v161", "v162", "v163", "v164", "v165", "v166", "v167", "v168", "v169", "v170", "v171", "v172", "v173", "v174", "v175", "v176", "v177", "v178", "v179", "v180", "v181", "v182", "v183", "v184", "v185", "v186", "v187", "v188", "v189", "v190", "v191", "v192", "v193", "v194", "v195", "v196", "v197", "v198", "v199", "v200", "v201", "v202", "v203", "v204", "v205", "v206", "v207", "v208", "v209", "v210", "v211", "v212", "v213", "v214", "v215", "v216", "v217", "v218", "v219", "v220", "v221", "v222", "v223", "v224", "v225", "v226", "v227", "v228", "v229"

typedef __attribute__((address_space(4))) const Params* KArg;
#define GET_PARAMS() KArg kp = (KArg)__builtin_amdgcn_kernarg_segment_ptr(); asm volatile("" : "+s"(kp)); const Params p = {kp->x, kp->Are, kp->Aim, kp->Bre, kp->Bim, kp->Cre, kp->Cim, kp->logdt, kp->dskip, kp->wr, kp->wg, kp->wu, kp->wd, kp->gw, kp->out, kp->ws}; \
                     asm volatile("s_waitcnt lgkmcnt(0)" :: "s"(kw0), "s"(kw1)); STAMP0_FIX()
#define ROLE_END() do { STAMP_FLUSH(); __builtin_amdgcn_s_waitcnt(0x0070); asm volatile("s_endpgm" ::: "memory"); } while (0)
#define MF(A_, B_, C_) __builtin_amdgcn_mfma_f32_16x16x32_bf16(A_, B_, C_, 0, 0, 0)
#define GRP(mask, n) __builtin_amdgcn_sched_group_barrier(mask, n, 0)

__global__ __launch_bounds__(512, 2) void fused_kernel(Params p_unused) {
    u32 kw0, kw1;
    u64 pc0;
    asm volatile("s_getpc_b64 %0" : "=s"(pc0));
    pc0 &= ~(u64)255;
    asm volatile("s_load_dword %0, %2, 0x0\n\ts_load_dword %1, %2, 0x40" : "=&s"(kw0), "=&s"(kw1) : "s"(__builtin_amdgcn_kernarg_segment_ptr()));
    extern __shared__ __attribute__((aligned(16))) char smem_[];
    LAS char* smem = (LAS char*)smem_;
    const int tid = threadIdx.x, lane = tid & 63;
    const int wave = __builtin_amdgcn_readfirstlane(tid >> 6);
    const int b = blockIdx.x, xcd = b & 7, slot = b >> 3;

    if (slot < 4) glds16((const char*)(GAS const char*)pc0 + min(slot * 8 + wave, CODE_KB - 1) * 1024 + lane * 16, (u32)(uintptr_t)(smem + LDS_DUMMY), false);
    const int cw = slot < 16 ? xcd * 16 + slot : 128 + xcd * 2 + (slot - 16);
    const int chbase = cw * 4;
    const int w = xcd * 9 + (slot - 18);
    if (slot >= 18) {
    if (slot >= 27 || wave >= 6) {
    if (slot < 28) {
    if (slot == 27)
    {
        GET_PARAMS();
        const u32 tag = __builtin_amdgcn_readfirstlane(__hip_atomic_load((GAS u32*)p.ws, RLX_AGENT)) + 1u;
        if (tid < 128) ((LAS u32*)smem)[tid] = 0;
        u32 ga = (u32)(uintptr_t)(smem + (lane & 15) * 4), gv = (u32)(uintptr_t)(smem + (lane >> 2) * 4), gq_ = (u32)(uintptr_t)(smem + (lane & 3) * 16), zero = (u32)(uintptr_t)smem;
        u32 ro = OFF_RG + 4096 + tid * 16 , so = OFF_SEL + ((lane & 1) * 8 + xcd) * 256, xo = (lane & 3) * 128;
        asm volatile("" : "+v"(ga), "+v"(gv), "+v"(gq_), "+v"(zero), "+v"(ro), "+v"(so), "+v"(xo));
        BAR();
        __builtin_amdgcn_s_sleep(SELWAIT);
        v4u qa, qb;
        u32 c_;
#define POLL_A "global_load_dwordx4 v[20:23], %[ro], %[ws] offset:-4096 sc1\n\tglobal_load_dwordx4 v[24:27], %[r2], %[ws] sc1\n\t"
#define POLL_B "global_load_dwordx4 v[28:31], %[ro], %[ws] offset:-4096 sc1\n\tglobal_load_dwordx4 v[32:35], %[r2], %[ws] sc1\n\t"
        asm volatile("s_mov_b32 %[c], 0x40000\n\t" POLL_A "s_sleep 8\n\t" POLL_B
                     "1:\n\ts_waitcnt vmcnt(2)\n\tv_cmpx_eq_u32 %[tag], v21\n\tv_cmpx_eq_u32 %[tag], v23\n\tv_cmpx_eq_u32 %[tag], v25\n\tv_cmpx_eq_u32 %[tag], v27\n\t"
                     "s_cmp_eq_u64 exec, -1\n\ts_mov_b64 exec, -1\n\ts_cbranch_scc1 3f\n\t" POLL_A
                     "s_waitcnt vmcnt(2)\n\tv_cmpx_eq_u32 %[tag], v29\n\tv_cmpx_eq_u32 %[tag], v31\n\tv_cmpx_eq_u32 %[tag], v33\n\tv_cmpx_eq_u32 %[tag], v35\n\t"
                     "s_cmp_eq_u64 exec, -1\n\ts_mov_b64 exec, -1\n\ts_cbranch_scc1 2f\n\t" POLL_B
                     "s_sub_u32 %[c], %[c], 1\n\ts_cbranch_scc0 1b\n\ts_waitcnt vmcnt(0)\n"
                     "2:\n\tv_mov_b32 v36, v28\n\tv_mov_b32 v37, v30\n\tv_mov_b32 v38, v32\n\tv_mov_b32 v39, v34\n\ts_branch 4f\n"
                     "3:\n\tv_mov_b32 v36, v20\n\tv_mov_b32 v37, v22\n\tv_mov_b32 v38, v24\n\tv_mov_b32 v39, v26\n"
                     "4:"
                     : "=&{v[36:39]}"(qa), [c] "=&s"(c_) : [ro] "v"(ro), [r2] "v"(ro + 4096), [ws] "s"(p.ws), [tag] "s"(tag)
                     : "memory", "scc", "vcc", "v20", "v21", "v22", "v23", "v24", "v25", "v26", "v27", "v28", "v29", "v30", "v31", "v32", "v33", "v34", "v35");
        asm volatile("" : "=&{v[20:23]}"(qb));
        v4u qc, qd, qe;
        asm volatile("" : "=&{v[24:27]}"(qc), "=&{v[28:31]}"(qd), "=&{v[32:35]}"(qe));
        STAMP(2);
        u32 rank, tau, cnt;
        u64 m0_, m1_, m2_, m3_;
        {
            u32 v, nv; v4u q4;
            asm volatile("ds_max_u32 %0, %1\n\ts_waitcnt lgkmcnt(0)\n\ts_barrier" :: "v"(ga), "v"(max(max(qa.x, qa.y), max(qa.z, qa.w))) : "memory");
#define RANK16(off) asm volatile("ds_read_b32 %[v], %[gv] offset:" #off "\n\tds_read_b128 v[40:43], %[gq] offset:" #off "\n\tds_read_b32 %[n], %[z] offset:64\n\tv_mov_b32 %[r], 0\n\ts_waitcnt lgkmcnt(0)\n\t" \
                         "v_cmp_gt_u32 %[a], v40, %[v]\n\tv_cmp_gt_u32 %[b], v41, %[v]\n\tv_cmp_gt_u32 %[c], v42, %[v]\n\tv_cmp_gt_u32 %[d], v43, %[v]\n\t" \
                         "v_addc_co_u32 %[r], vcc, 0, %[r], %[a]\n\tv_addc_co_u32 %[r], vcc, 0, %[r], %[b]\n\tv_addc_co_u32 %[r], vcc, 0, %[r], %[c]\n\tv_addc_co_u32 %[r], vcc, 0, %[r], %[d]\n\ts_nop 1\n\t" \
                         "v_add_u32_dpp %[r], %[r], %[r] quad_perm:[1,0,3,2] row_mask:0xf bank_mask:0xf\n\ts_nop 1\n\tv_add_u32_dpp %[r], %[r], %[r] quad_perm:[2,3,0,1] row_mask:0xf bank_mask:0xf\n\ts_nop 0" \
                         : [v] "=&v"(v), [r] "=&v"(rank), [n] "=&v"(nv), "=&{v[40:43]}"(q4), [a] "=&s"(m0_), [b] "=&s"(m1_), [c] "=&s"(m2_), [d] "=&s"(m3_) : [gv] "v"(gv), [gq] "v"(gq_), [z] "v"(zero) : "memory", "vcc")
            RANK16(0);
            tau = (u32)__builtin_amdgcn_readlane((int)v, (int)__builtin_ctzll(__ballot(rank == 7)));
            u32 pos;
            asm volatile("s_nop 1\n\tv_mov_b32 %[n], 0\n\tv_cmp_le_u32 %[a], %[t], v36\n\tv_cmp_le_u32 %[b], %[t], v37\n\tv_cmp_le_u32 %[c], %[t], v38\n\tv_cmp_le_u32 %[d], %[t], v39\n\t"
                         "v_addc_co_u32 %[n], vcc, 0, %[n], %[a]\n\tv_addc_co_u32 %[n], vcc, 0, %[n], %[b]\n\tv_addc_co_u32 %[n], vcc, 0, %[n], %[c]\n\tv_addc_co_u32 %[n], vcc, 0, %[n], %[d]\n\t"
                         "v_lshlrev_b32 %[n], 2, %[n]\n\tv_cmpx_ne_u32 0, %[n]\n\tds_add_rtn_u32 %[p], %[z], %[n] offset:64\n\ts_waitcnt lgkmcnt(0)\n\t"
                         "s_mov_b64 exec, %[a]\n\tds_write_b32 %[p], v36 offset:256\n\tv_add_u32 %[p], 4, %[p]\n\t"
                         "s_mov_b64 exec, %[b]\n\tds_write_b32 %[p], v37 offset:256\n\tv_add_u32 %[p], 4, %[p]\n\t"
                         "s_mov_b64 exec, %[c]\n\tds_write_b32 %[p], v38 offset:256\n\tv_add_u32 %[p], 4, %[p]\n\t"
                         "s_mov_b64 exec, %[d]\n\tds_write_b32 %[p], v39 offset:256\n\t"
                         "s_mov_b64 exec, -1\n\ts_waitcnt lgkmcnt(0)\n\ts_barrier"
                         : [n] "=&v"(cnt), [p] "=&v"(pos), [a] "=&s"(m0_), [b] "=&s"(m1_), [c] "=&s"(m2_), [d] "=&s"(m3_) : [t] "s"(tau), [z] "v"(zero), "{v[36:39]}"(qa) : "memory", "vcc");
            if (wave == 0) {
                RANK16(256);
                const int nc = __builtin_amdgcn_readfirstlane((int)nv) >> 2;
                if (__builtin_expect(nc > 16, 0)) {
                    v = (u32)__shfl((int)top8_d(smem, lane, nc), (lane >> 2) & 7);
                    rank = lane >> 2;
                }
                const u64 m8 = __ballot(rank < 8);
                asm volatile("s_and_b64 exec, %[m], %[k2]\n\tglobal_store_dwordx2 %[so], %[d], %[ws] sc1\n\ts_mov_b64 exec, %[m]\n\ts_mov_b32 m0, 0x8000\n\ts_nop 0\n\t"
                             "global_load_lds_dword %[xo], %[x]\n\tglobal_load_lds_dword %[xo], %[x] offset:512\n\ts_and_b64 exec, %[m], %[k1]\n\tglobal_load_lds_dword %[xo], %[x] offset:1024\n\ts_mov_b64 exec, -1"
                             :: [m] "s"(m8), [k2] "s"(0x3333333333333333ull), [k1] "s"(0x1111111111111111ull), [so] "v"(so + rank * 8), [d] "v"((v2u){v, tag}), [ws] "s"(p.ws),
                                [xo] "v"(xo + (~v & 2047u) * 1152u), [x] "s"(p.x) : "memory", "scc");
                asm volatile("s_mov_b64 exec, 1\n\tglobal_store_dwordx2 %0, %1, %2 sc1\n\ts_mov_b64 exec, -1" :: "v"(OFF_CI + (272 + xcd) * 8), "v"((v2u){tag, tag}), "s"(p.ws) : "memory");
            }
        }
        STAMP(3);
        asm volatile("s_waitcnt vmcnt(0)" :: "v"(qb), "v"(qc), "v"(qd), "v"(qe) : "memory");
        if (xcd == 0) {
            const u32 cio = OFF_CI + (tid < 140 ? tid : 0) * 16;
            for (u32 spins = 0;; ++spins) {
                REREAD();
                const v4u q2 = __builtin_amdgcn_raw_buffer_load_b128(WSR(), cio, 0, 16);
                if (__all((q2.y == tag) & (q2.w == tag)) || spins > SPIN_CAP) break;
                __builtin_amdgcn_s_sleep(16);
            }
            BAR();
            if (tid == 0) __hip_atomic_store((GAS u32*)p.ws, tag, RLX_AGENT);
        }
        ROLE_END();
    }
    else
        {
            GET_PARAMS();
            const u32 l16 = lane * 16;
            const char* dsrc = (const char*)(p.wd + (size_t)(64 * w + 32 * (wave - 6)) * D);
            const u32 ddst = (u32)(uintptr_t)(smem + FL_WD) + (wave - 6) * 32 * WDP;
            u32 tagv;
            asm volatile("global_load_dword %0, %1, %2 sc1" : "=v"(tagv) : "v"(0u), "s"(p.ws) : "memory");
            u32 dof[37];
#pragma unroll
            for (int j = 0; j < 37; ++j) {
                const u32 r0 = (64 * j) / 74, c = 64 * j - 74 * r0 + lane;
                dof[j] = c >= 74u ? (r0 + 1) * 1152u + (c - 74u) * 16u : r0 * 1152u + min(c, 71u) * 16u;
                asm volatile("" : "+v"(dof[j]));
            }
#define LD_WD(j) asm volatile("s_mov_b32 m0, %2\n\ts_nop 0\n\tglobal_load_lds_dwordx4 %0, %1" WNT :: "v"(dof[j]), "s"(dsrc), "s"(ddst + (j) * 1024) : "memory")
            asm volatile("s_waitcnt vmcnt(0)" : "+v"(tagv));
            const u32 tg = __builtin_amdgcn_readfirstlane(tagv) + 1u;
            *(LAS v4u*)(smem + FL_Z + min(l16, 256u)) = (v4u){lane >= 16 ? tg : 0u, 0u, 0u, 0u};
            asm volatile("s_mov_b64 exec, 1\n\tglobal_store_dwordx2 %0, %1, %2 sc1\n\ts_mov_b64 exec, -1" :: "v"(OFF_CI + (128 + 2 * w + (wave - 6)) * 8), "v"((v2u){tg, tg}), "s"(p.ws) : "memory");
            if (FDELAY) __builtin_amdgcn_s_sleep(FDELAY);
            v2u sel;
            u32 c_;
            asm volatile("s_mov_b32 %[c], 0x80000\n1:\n\tglobal_load_dwordx2 v[10:11], %[so], %[ws] sc1\n\ts_waitcnt vmcnt(0)\n\tv_cmp_ne_u32 vcc, %[tag], v11\n\ts_cbranch_vccz 2f\n\ts_sub_u32 %[c], %[c], 1\n\ts_cbranch_scc0 1b\n2:"
                         : "=&{v[10:11]}"(sel), [c] "=&s"(c_) : [so] "v"(OFF_SEL + xcd * 256 + (4 * (wave - 6) + (lane & 3)) * 8), [ws] "s"(p.ws), [tag] "s"(tg) : "memory", "scc", "vcc");
            STAMP(3);
#pragma unroll
            for (int k = 0; k < 4; ++k) {
                const u32 t = ~(u32)__builtin_amdgcn_readlane((int)sel.x, k) & 2047u;
                const char* xr = (const char*)p.x + t * (D * 2);
                asm volatile("s_mov_b32 m0, %2\n\ts_nop 0\n\tglobal_load_lds_dwordx4 %0, %1\n\ts_mov_b64 exec, 0xff\n\tglobal_load_lds_dwordx4 %0, %1 offset:1024\n\ts_mov_b64 exec, -1"
                             :: "v"(l16), "s"(xr), "s"((u32)(uintptr_t)(smem + FL_XS) + ((wave - 6) * 4 + k) * XSP) : "memory");
            }
#pragma unroll
            for (int j = 0; j < K2; ++j) LD_WD(j);
            asm volatile("s_waitcnt vmcnt(%0) lgkmcnt(0)\n\ts_barrier" :: "n"(K2) : "memory");
            STAMP(10);
#pragma unroll
            for (int j = K2; j < 37; ++j) LD_WD(j);
            STAMP(11);
            asm volatile("s_waitcnt vmcnt(0)" ::: "memory");
            STAMP(13);
            __builtin_amdgcn_s_barrier();
            ROLE_END();
        }
    } else
    {
        GET_PARAMS();
        if (FDELAY) __builtin_amdgcn_s_sleep(FDELAY);
        _Pragma("unroll 1") for (int e = (slot - 28) + 4 * wave; touch<0>(p, xcd, e, lane, (u32)(uintptr_t)(smem + wave * 256)); e += 32) {}
        STAMP(11);
        asm volatile("s_waitcnt vmcnt(0)" ::: "memory");
        STAMP(6);
        STAMP_FLUSH();
    }
    } else
        {
            const int n = lane & 15, q = lane >> 4;
            GET_PARAMS();
            v4u gu[3][8];
            if (FDELAY) __builtin_amdgcn_s_sleep(FDELAY);
            const u16* wsrc = (n < 8 ? p.wg : p.wu) + 64 * w + 8 * (n & 7) + (size_t)(96 * wave + 8 * q) * F;
#define LD_GU(s) _Pragma("unroll") for (int j = 0; j < 8; ++j) asm volatile("global_load_dwordx4 %0, %1, off" WNT : "=v"(gu[s][j]) : "v"(wsrc + (size_t)(32 * s + j) * F))
            LD_GU(0);
            LD_GU(1);
            LD_GU(2);
            STAMP(11);
            bf16x8 guf[8];
            auto tr88 = [&](const v4u (&in)[8], bf16x8 (&o)[8]) __attribute__((always_inline)) {
#pragma unroll
                for (int pp = 0; pp < 4; ++pp) {
                    v4u lo, hi;
#pragma unroll
                    for (int jj = 0; jj < 4; ++jj) {
                        lo[jj] = __builtin_amdgcn_perm(in[2 * jj + 1][pp], in[2 * jj][pp], 0x05040100u);
                        hi[jj] = __builtin_amdgcn_perm(in[2 * jj + 1][pp], in[2 * jj][pp], 0x07060302u);
                    }
                    o[2 * pp] = __builtin_bit_cast(bf16x8, lo); o[2 * pp + 1] = __builtin_bit_cast(bf16x8, hi);
                }
#pragma unroll
                for (int i = 0; i < 8; ++i) asm volatile("" : "+v"(o[i]));
            };
#define WAIT_GU(s, N) asm volatile("s_waitcnt vmcnt(%8)" : "+v"(gu[s][0]), "+v"(gu[s][1]), "+v"(gu[s][2]), "+v"(gu[s][3]), "+v"(gu[s][4]), "+v"(gu[s][5]), "+v"(gu[s][6]), "+v"(gu[s][7]) : "n"(N))
            LAS char* xrow = smem + FL_XS + (n & 7) * XSP + (96 * wave + 8 * q) * 2;
            LAS char* xa = n < 8 ? xrow : smem + FL_Z + q * 16;
            LAS char* xb = n < 8 ? smem + FL_Z + q * 16 : xrow;
            LAS char* ta = smem + FL_WD + (lane >> 2) * WDP + (lane & 3) * 8 + wave * 64;
            LAS char* rp = smem + FL_RED + wave * 4096 + (q & 1) * 2048 + (q >> 1) * 512 + n * 8;
            LAS char* rp2 = smem + FL_RED + wave * 4096 + (q & 1) * 2048 + (q >> 1) * 512 + (n ^ 8) * 8;
            const int sl_ = (lane >> 3) * 16 + ((lane & 7) ^ ((lane >> 1) & 8)), hc = 8 * (lane & 7) + (lane >> 3);
            LAS char* ra = smem + FL_RED + (wave & 3) * 1024 + sl_ * 8;
            LAS char* ru = smem + FL_RED + (wave & 3) * 1024 + (sl_ ^ 8) * 8;
            LAS char* hw_ = smem + FL_H + (wave & 3) * 288 + ((hc & 35) | ((hc & 12) << 1) | ((hc & 16) >> 2)) * 2;
            const u32 obase = OFF_PART + ((4 * (2 * wave + (n >> 3)) + q) * NFFN + w) * 128 + (n & 7) * 16;
            u32 obase_ = obase;
            LAS char* hp = smem + FL_H + (n & 7) * 144 + q * 16;
            asm volatile("" : "+v"(xa), "+v"(xb), "+v"(ta), "+v"(rp), "+v"(obase_), "+v"(hp), "+v"(ra), "+v"(hw_), "+v"(rp2), "+v"(ru));
            bf16x8 g0[8], g1[8];
            WAIT_GU(0, 16);
            STAMP(5);
            tr88(gu[0], g0);
            WAIT_GU(1, 8);
            tr88(gu[1], g1);
            STAMP(12);
            WAIT_GU(2, 0);
            STAMP(6);
            tr88(gu[2], guf);
            asm volatile("s_barrier" ::: "memory");
            bf16x8 xf[6];
#pragma unroll
            for (int s = 0; s < 3; ++s) { xf[2 * s] = *(LAS bf16x8*)(xa + s * 64); xf[2 * s + 1] = *(LAS bf16x8*)(xb + s * 64); }
            const u32 tag = *(LAS u32*)(smem + FL_TAG);
            SBAR();
            STAMP(1);
            STAMP(2);
            v4f ha[4];
#pragma unroll
            for (int i = 0; i < 24; ++i) {
                const int f = (i & 3) + 4 * ((i >> 2) & 1);
                ha[i & 3] = MF(xf[i >> 2], i < 8 ? g0[f] : (i < 16 ? g1[f] : guf[f]), i < 4 ? ((v4f){0.f, 0.f, 0.f, 0.f}) : ha[i & 3]);
            }
            STAMP(4);
#pragma unroll
            for (int r = 0; r < 2; ++r)
#pragma unroll
                for (int i = 0; i < 4; ++i) *(LAS v2f*)((i & 2 ? rp2 : rp) + r * 1024 + i * 128) = (v2f){ha[i][2 * r], ha[i][2 * r + 1]};
            STAMP(7);
            BAR();
            s16x4 a[6][4];
            if (wave < 4) {
                v2f pa[6], pu[6];
#pragma unroll
                for (int ww = 0; ww < 6; ++ww) pa[ww] = *(volatile LAS v2f*)(ra + ww * 4096);
#pragma unroll
                for (int ww = 0; ww < 6; ++ww) pu[ww] = *(volatile LAS v2f*)(ru + ww * 4096);
                SBAR();
                const v2f av = ((pa[0] + pa[1]) + (pa[2] + pa[3])) + (pa[4] + pa[5]), uv = ((pu[0] + pu[1]) + (pu[2] + pu[3])) + (pu[4] + pu[5]);
                const u32 ab = cvtpk(av.x, av.y), ub = cvtpk(uv.x, uv.y);
                const u32 sl = cvtpk(bflo(ab) * sigmoidf_(bflo(ab)), bfhi(ab) * sigmoidf_(bfhi(ab)));
                const u32 hh = cvtpk(bflo(sl) * bflo(ub), bfhi(sl) * bfhi(ub));
                *(LAS u16*)(hw_) = (u16)hh; *(LAS u16*)(hw_ + 144) = (u16)(hh >> 16);
                asm volatile("s_waitcnt lgkmcnt(0)" ::: "memory");
            }
            STAMP(8);
#pragma unroll
            for (int k = 0; k < 6; ++k) {
                const int ko = (k >> 1) * 384 + (k & 1) * 32;
                a[k][0] = __builtin_amdgcn_ds_read_tr16_b64_v4i16((LAS s16x4*)(ta + ko));
                a[k][1] = __builtin_amdgcn_ds_read_tr16_b64_v4i16((LAS s16x4*)(ta + ko + 16 * WDP));
                a[k][2] = __builtin_amdgcn_ds_read_tr16_b64_v4i16((LAS s16x4*)(ta + ko + 32 * WDP));
                a[k][3] = __builtin_amdgcn_ds_read_tr16_b64_v4i16((LAS s16x4*)(ta + ko + 48 * WDP));
            }
            asm volatile("s_barrier" ::: "memory");
            STAMP(13);
            const bf16x8 h0 = *(LAS bf16x8*)(hp);
            const bf16x8 h1 = *(LAS bf16x8*)(hp + 64);
            v4f ya[6];
#pragma unroll
            for (int k = 0; k < 6; ++k) ya[k] = __builtin_amdgcn_mfma_f32_16x16x32_bf16(__builtin_shufflevector(a[k][0], a[k][1], 0, 1, 2, 3, 4, 5, 6, 7), h0, (v4f){0.f, 0.f, 0.f, 0.f}, 0, 0, 0);
#pragma unroll
            for (int k = 0; k < 6; ++k) ya[k] = __builtin_amdgcn_mfma_f32_16x16x32_bf16(__builtin_shufflevector(a[k][2], a[k][3], 0, 1, 2, 3, 4, 5, 6, 7), h1, ya[k], 0, 0, 0);
#pragma unroll
            for (int k = 0; k < 3; ++k) {
                const v4f v = n < 8 ? ya[2 * k] : ya[2 * k + 1];
                __builtin_amdgcn_raw_buffer_store_b128((v4u){cvtpk(v[0], v[1]), tag, cvtpk(v[2], v[3]), tag}, WSR(), obase_ + k * (48 * NFFN * 128), 0, 16);
            }
            STAMP(9);
            ROLE_END();
        }
    } else {
    if (wave >= 4)
    {
        const int hw = wave - 4, ht = tid - 256;
        const v4u z4 = {0, 0, 0, 0};
        GET_PARAMS();
        u32 tagv;
        asm volatile("global_load_dword %0, %1, %2 sc1" : "=v"(tagv) : "v"(0u), "s"(p.ws) : "memory");
        const int sub = lane & 15;
        v4u xr[5];
        const int rrow = 16 * (cw & 127) + 4 * hw;
        {
            const u32 dst = (u32)(uintptr_t)(smem + LDS_RW) + hw * 3072;
            glds16(p.wr + lane * 8, dst, false);
            glds16(lane < 8 ? p.wr + 512 + lane * 8 : p.gw + (lane - 8) * 8, dst + 1024, false);
            glds16(p.gw + 448 + (lane & 15) * 8, dst + 2048, false);
            const u32 ro = (u32)((rrow + (lane >> 4)) * D + sub * 8) * 2, ro4 = ro - (sub & 8) * 16;
#pragma unroll
            for (int j = 0; j < 4; ++j) asm volatile("global_load_dwordx4 %0, %1, %2 offset:%3" : "=v"(xr[j]) : "v"(ro), "s"(p.x), "n"(j * 256));
            asm volatile("global_load_dwordx4 %0, %1, %2 offset:1024" : "=v"(xr[4]) : "v"(ro4), "s"(p.x));
        }
        __builtin_amdgcn_s_barrier();
        STAMP(1);
        if (XPF) {
            const int id = min((slot * 4 + hw) * 64 + lane, 4095);
            u32 keep;
            asm volatile("s_mov_b32 %0, m0\n\ts_mov_b32 m0, %3\n\ts_nop 0\n\tglobal_load_lds_dword %1, %2\n\ts_mov_b32 m0, %0" : "=&s"(keep) : "v"((u32)((id >> 1) * 1152 + ((id & 1) ? 1024 : xcd * 128))), "s"(p.x), "s"((u32)(uintptr_t)(smem + LDS_DUMMY)) : "memory");
        }
        v2u uu[8];
        const u32 uo0 = (u32)((256 * hw + 4 * lane) * D + chbase) * 2, uo1 = uo0 + 1024 * D * 2;
#define LD_U(i) asm volatile("global_load_dwordx2 %0, %1, %2 offset:%3" : "=v"(uu[i]) : "v"((i) < 4 ? uo0 : uo1), "s"(p.x), "n"(((i) & 3) * D * 2))
#pragma unroll
        for (int i = 0; i < NB1; ++i) LD_U(i);
        *(LAS v4u*)(smem + LDS_TAP + ht * 16) = z4; *(LAS v4u*)(smem + LDS_TAP + 4096 + ht * 16) = z4;
        *(LAS v4u*)(smem + (ht < 128 ? LDS_TAP + 8192 + ht * 16 : LDS_U + ((ht >> 5) & 3) * 4608 + (ht & 31) * 16)) = z4;
        u32 ctl = (u32)(uintptr_t)(smem + LDS_CTL), one = 1u, cty = ctl + 64 * hw + (lane & 3) * 16;
        u32 po = cw >= 128 ? OFF_CI + ((cw - 128) * 4 + hw) * 16 : (sub == 0 ? OFF_RG + rrow * 8 + (lane >> 1) : OFF_G + (lane - 1) * 8192 + rrow * 4);
        const u64 pmask = cw >= 128 ? 1ull : ((1ull << (1 + REPG)) - 1) | (1ull << 32);
        u32 nrow = 0xffffu - (rrow + (lane >> 4));
        u32 go = OFF_G + (cw % REPG) * 8192 + 2048 + (64 * hw + lane) * 16;
        LAS char* up = smem + LDS_U + (256 + 256 * hw + 4 * lane) * 2;
        LAS char* yp = smem + CH_Y + hw * 1152 + (lane + (lane >> 3)) * 16;
        LAS char* rw = smem + LDS_RW + hw * 3072 + sub * 16;
        asm volatile("" : "+v"(ctl), "+v"(one), "+v"(cty), "+v"(po), "+v"(go), "+v"(up), "+v"(yp), "+v"(rw), "+v"(nrow));
        BAR();
        __builtin_amdgcn_s_setprio(3);
        u32 tag;
        {
            asm volatile("s_waitcnt vmcnt(%6)" : "+v"(xr[0]), "+v"(xr[1]), "+v"(xr[2]), "+v"(xr[3]), "+v"(xr[4]), "+v"(tagv) : "n"(NB1 + XPF) : "memory");
            STAMP(2);
            tag = __builtin_amdgcn_readfirstlane(tagv) + 1u;
            if (sub >= 8) xr[4] = z4;
            float r = 0.f, s = 0.f;
#pragma unroll
            for (int j = 0; j < 5; ++j) {
                const v4u a = *(LAS v4u*)(rw + j * 256), g = *(LAS v4u*)(rw + 1152 + j * 256);
#pragma unroll
                for (int e = 0; e < 4; ++e) {
                    const u32 xe = xr[j][e], ae = a[e], ge = g[e];
                    r = __builtin_amdgcn_fdot2_f32_bf16(__builtin_bit_cast(bf16x2_t, xe), __builtin_bit_cast(bf16x2_t, ae), r, false);
                    s = __builtin_amdgcn_fdot2_f32_bf16(__builtin_bit_cast(bf16x2_t, xe), __builtin_bit_cast(bf16x2_t, ge), s, false);
                }
            }
            r = dpp_add<0x128, 0xf>(r); s = dpp_add<0x128, 0xf>(s); r = dpp_add<0x124, 0xf>(r); s = dpp_add<0x124, 0xf>(s);
            r = dpp_add<0x122, 0xf>(r); s = dpp_add<0x122, 0xf>(s); r = dpp_add<0x121, 0xf>(r); s = dpp_add<0x121, 0xf>(s);
            const u32 pay = make_payload(r, sigmoidf_(rbf(s))), kk = (pay & 0xffff0000u) | nrow;
            const u32 p0 = (u32)__builtin_amdgcn_readlane((int)pay, 0), p1 = (u32)__builtin_amdgcn_readlane((int)pay, 16), p2 = (u32)__builtin_amdgcn_readlane((int)pay, 32), p3 = (u32)__builtin_amdgcn_readlane((int)pay, 48);
            const auto sw = __builtin_amdgcn_permlane16_swap(kk, kk, false, false);
            const u32 g01 = (p0 & 0xffffu) | (p1 << 16), g23 = (p2 & 0xffffu) | (p3 << 16);
            const v4u pv = {sub ? g01 : sw[0], tag, sub ? g23 : sw[1], tag};
            asm volatile("s_mov_b64 exec, %3\n\tglobal_store_dwordx4 %0, %1, %2 sc1\n\ts_mov_b64 exec, -1\n\ts_nop 0" :: "v"(po), "v"(pv), "s"(p.ws), "s"(pmask) : "memory");
        }
        STAMP(3);
        __builtin_amdgcn_s_setprio(HPRIO);
        *(LAS u32*)(smem + LDS_CTL + C_TAG) = tag;
        LDS_ADD1(ctl, one, C_PUB);
        LWAIT(ctl, C_PUB, 4);
#pragma unroll
        for (int i = NB1; i < 4; ++i) LD_U(i);
        LDS_ADD1(ctl, one, C_PUB);
        LWAIT(ctl, C_PUB, 8);
#pragma unroll
        for (int i = 4; i < 8; ++i) LD_U(i);
        v4u gq[2];
#define POLL_G "global_load_dwordx4 v[100:103], %[go], %[ws] offset:-2048 sc1\n\tglobal_load_dwordx4 v[104:107], %[go], %[ws] offset:2048 sc1\n\t"
        asm volatile(POLL_G : "={v[100:103]}"(gq[0]), "={v[104:107]}"(gq[1]) : [go] "v"(go), [ws] "s"(p.ws) : "memory");
        STAMP(4);
        auto stage = [&](int g) __attribute__((always_inline)) {
            const v2u* u4 = uu + 4 * g;
            LAS char* d = up + g * 2048;
            *(LAS v2u*)(d) = (v2u){__builtin_amdgcn_perm(u4[1].x, u4[0].x, 0x05040100u), __builtin_amdgcn_perm(u4[3].x, u4[2].x, 0x05040100u)};
            *(LAS v2u*)(d + 4608) = (v2u){__builtin_amdgcn_perm(u4[1].x, u4[0].x, 0x07060302u), __builtin_amdgcn_perm(u4[3].x, u4[2].x, 0x07060302u)};
            *(LAS v2u*)(d + 2 * 4608) = (v2u){__builtin_amdgcn_perm(u4[1].y, u4[0].y, 0x05040100u), __builtin_amdgcn_perm(u4[3].y, u4[2].y, 0x05040100u)};
            *(LAS v2u*)(d + 3 * 4608) = (v2u){__builtin_amdgcn_perm(u4[1].y, u4[0].y, 0x07060302u), __builtin_amdgcn_perm(u4[3].y, u4[2].y, 0x07060302u)};
            LDS_ADD1(ctl, one, C_STG);
        };
        asm volatile("s_waitcnt vmcnt(6)" : "+v"(uu[0]), "+v"(uu[1]), "+v"(uu[2]), "+v"(uu[3]));
        stage(0);
        STAMP(5);
        touch<1>(p, xcd, slot * 4 + hw, lane, (u32)(uintptr_t)(smem + LDS_DUMMY));
        touch<1>(p, xcd, 72 + ((slot * 4 + hw) % 20), lane, (u32)(uintptr_t)(smem + LDS_DUMMY));
        asm volatile("s_waitcnt vmcnt(4)" : "+v"(uu[4]), "+v"(uu[5]), "+v"(uu[6]), "+v"(uu[7]));
        LWAIT(ctl, C_STG, 4);
        stage(1);
        STAMP(6);
        {
            u32 c_;
            asm volatile("s_mov_b32 %[c], 0x80000\n\ts_waitcnt vmcnt(2)\n1:\n\tv_cmpx_eq_u32 %[tag], v101\n\tv_cmpx_eq_u32 %[tag], v103\n\tv_cmpx_eq_u32 %[tag], v105\n\tv_cmpx_eq_u32 %[tag], v107\n\t"
                         "s_cmp_eq_u64 exec, -1\n\ts_mov_b64 exec, -1\n\ts_cbranch_scc1 2f\n\ts_sleep 4\n\t" POLL_G "s_waitcnt vmcnt(0)\n\ts_sub_u32 %[c], %[c], 1\n\ts_cbranch_scc0 1b\n2:\n\ts_nop 4"
                         : "+{v[100:103]}"(gq[0]), "+{v[104:107]}"(gq[1]), [c] "=&s"(c_) : [go] "v"(go), [ws] "s"(p.ws), [tag] "s"(tag) : "memory", "scc", "vcc");
        }
        STAMP(7);
        *(LAS v2u*)(smem + LDS_G + hw * 3072 + lane * 8) = (v2u){gq[0].x, gq[0].z}; *(LAS v2u*)(smem + LDS_G + hw * 3072 + 512 + lane * 8) = (v2u){gq[1].x, gq[1].z};
#pragma unroll
        for (int g = 0; g < 2; ++g) {
            const v2u* u4 = uu + 4 * g;
            const float gr[4] = {bflo(gq[g].x), bfhi(gq[g].x), bflo(gq[g].z), bfhi(gq[g].z)};
            float o[4][4];
#pragma unroll
            for (int r = 0; r < 4; ++r) {
                const float xv[4] = {bflo(u4[r].x), bfhi(u4[r].x), bflo(u4[r].y), bfhi(u4[r].y)};
#pragma unroll
                for (int c = 0; c < 4; ++c) asm("v_fma_f32 %0, -%1, %2, %2" : "=v"(o[r][c]) : "v"(gr[r]), "v"(xv[c]));
                asm volatile("" : "+v"(o[r][0]), "+v"(o[r][1]), "+v"(o[r][2]), "+v"(o[r][3]));
            }
            {
                u32 t_, c_;
                asm volatile("s_mov_b32 %1, 0x400000\n1:\n\tds_read_b32 %0, %2 offset:%3\n\ts_waitcnt lgkmcnt(0)\n\tv_cmp_eq_u32 vcc, 0, %0\n\ts_cmp_eq_u64 vcc, 0\n\ts_cbranch_scc1 2f\n\ts_sleep %4\n\ts_sub_u32 %1, %1, 1\n\ts_cbranch_scc0 1b\n2:"
                             : "=&v"(t_), "=&s"(c_) : "v"(cty), "n"(C_Y + 256 * g), "n"(YSLEEP) : "memory", "scc", "vcc");
            }
            if (g == 0) STAMP(11);
            v4f y[4];
#pragma unroll
            for (int c = 0; c < 4; ++c) y[c] = *(LAS v4f*)(yp + c * KCH + g * 4608);
            SBAR();
#define ST_O(i, v) asm volatile("global_store_dwordx2 %0, %1, %2 offset:%3" :: "v"(g ? uo1 : uo0), "v"(v), "s"(p.out), "n"((i) * D * 2) : "memory")
#pragma unroll
            for (int r = 0; r < 4; ++r) {
#pragma unroll
                for (int c = 0; c < 4; ++c) { const float yv = y[c][r]; asm("v_fmac_f32 %0, %1, %2" : "+v"(o[r][c]) : "v"(gr[r]), "v"(yv)); }
                ST_O(r, ((v2u){cvtpk(o[r][0], o[r][1]), cvtpk(o[r][2], o[r][3])}));
            }
            STAMP(8 + g);
        }
        asm volatile("s_waitcnt vmcnt(0)" ::: "memory");
        LDS_ADD1(ctl, one, C_ACK);
        STAMP(10);
        ROLE_END();
    }
    else
    {
        GET_PARAMS();
        const int ch = wave;
        const int dch = chbase + ch, pi = dch * NST + lane * 2;
        const u32 are = __builtin_nontemporal_load((const u32*)(p.Are + pi)), aim = __builtin_nontemporal_load((const u32*)(p.Aim + pi)), bre = __builtin_nontemporal_load((const u32*)(p.Bre + pi));
        const u32 bim = __builtin_nontemporal_load((const u32*)(p.Bim + pi)), cre = __builtin_nontemporal_load((const u32*)(p.Cre + pi)), cim = __builtin_nontemporal_load((const u32*)(p.Cim + pi));
        const u16 ldt = p.logdt[dch], dsk = p.dskip[dch];
        SBAR();
        __builtin_amdgcn_s_barrier();
        SBAR();
        LAS char* kb = smem + ch * KCH;
        const int r32 = lane & 31, h = lane >> 5, q = lane >> 4, n = lane & 15;
        const int ai = 4 * (r32 >> 3) + (r32 & 3), hh = (r32 >> 2) & 1;
        const u64 tsel = ai < 8 ? (hh ? KROW_1LO : KROW_0LO) : (hh ? KROW_1HI : KROW_0HI);
        const int o = (int)((tsel >> (8 * (ai & 7))) & 0xff);
        LAS char* ap = kb + o * STG_ROW + h * 16;
        LAS char* bp = kb + r32 * STG_ROW + h * 16;
        u32 sg[14];
#pragma unroll
        for (int k = 0; k < 14; ++k) { sg[k] = (u32)(uintptr_t)(kb + lane * 8 + k * 4 * STG_ROW); asm volatile("" : "+v"(sg[k])); }
        bf16x8 b5 = band(((0x1fu << 6) << n) >> (8 * q), 0x3f803f80u), b3 = band(((0x7u << 11) << n) >> (8 * q), 0x3f803f80u);
        bf16x8 b9 = band(((0x1ffu << 4) << n) >> (8 * q), 0x3f803f80u), b7 = band(((0x7fu << 5) << n) >> (8 * q), 0x3f803f80u);
        bf16x8 pat = band(((0x3u << 12) << n) >> (8 * q), 0xffffffffu);
        LAS char* k0 = kb + 32 + lane * 16;
        LAS char* kt = kb + (lane & 3) * KCOPY + (lane < 16 ? 32 + 1024 + (lane >> 2) * 16 : ((lane >> 2) & 1) * 16);
        LAS char* a0 = kb + ((lane >> 2) & 3) * KCOPY + (28 - 8 * q + 4 * (lane & 3)) * 2;
        LAS char* bb = smem + LDS_U + ch * 4608 + (16 * n + 8 * q - 16) * 2;
        LAS char* yb = kb + CH_Y + (4 * n + q + (n >> 1)) * 16;
        LAS char* tp = smem + LDS_TAP + ch * 2304 + ((n & 1) ? 13 + n - 4 * q : 96 + 14 + n - 4 * q) * 4;
        LAS char* ta = smem + LDS_TAP + ch * 2304 + (h ? 0 : 768) + 64 + 2 * r32;
        LAS char* tb = smem + LDS_TAP + ch * 2304 + 1600 + 64 * h + 2 * r32;
        u32 ctl = (u32)(uintptr_t)(smem + LDS_CTL), ctc = ctl + C_Y + ch * 16 + (lane & 15), one = 1u, so = OFF_SEL + (8 + xcd) * 256 + (lane & 7) * 8;
        u32 m01 = lane < 2 ? ~0u : 0u, m2 = lane == 2 ? ~0u : 0u, m3 = lane == 3 ? ~0u : 0u, m0_ = lane == 0 ? ~0u : 0u;
        asm volatile("" : "+v"(b5), "+v"(b3), "+v"(b9), "+v"(b7), "+v"(pat), "+v"(ap), "+v"(bp), "+v"(m01), "+v"(m2), "+v"(m3), "+v"(m0_));
        asm volatile("" : "+v"(k0), "+v"(kt), "+v"(a0), "+v"(bb), "+v"(yb), "+v"(tp), "+v"(ta), "+v"(tb), "+v"(ctl), "+v"(ctc), "+v"(one), "+v"(so));
        __builtin_amdgcn_s_barrier();
        SBAR();
        const float dt = rbf(expf(bf2f(ldt)));
        STAMP(1);
        v16f acc;
        u32 stg;
        float kscale;
        {
            const v2f a2 = (v2f){bflo(are), bfhi(are)} * dt * 1.44269504088896341f;
            const v2f brev = (v2f){bflo(aim), bfhi(aim)} * dt * 0.15915494309189535f;
            const cf2 Bc = {{dt * bflo(bre), dt * bflo(bim)}, {dt * bfhi(bre), dt * bfhi(bim)}};
            const cf2 Cc = {{bflo(cre), bflo(cim)}, {bfhi(cre), bfhi(cim)}};
            cf2 W = cmul2(Cc, Bc);
            u32 mx = max(max(__float_as_uint(W.a.x) & 0x7f800000u, __float_as_uint(W.a.y) & 0x7f800000u), max(__float_as_uint(W.b.x) & 0x7f800000u, __float_as_uint(W.b.y) & 0x7f800000u));
            mx = dpp_umax<0x111, 0xf>(mx); mx = dpp_umax<0x112, 0xf>(mx); mx = dpp_umax<0x114, 0xf>(mx); mx = dpp_umax<0x118, 0xf>(mx);
            mx = dpp_umax<0x142, 0xa>(mx); mx = dpp_umax<0x143, 0xc>(mx);
            mx = min(max((u32)__builtin_amdgcn_readlane((int)mx, 63), 0x00800000u), 0x7e800000u);
            kscale = __uint_as_float(mx);
            const float inv = __uint_as_float(0x7f000000u - mx);
            W.a *= inv; W.b *= inv;
            const cf2 E1 = cexp2(a2, brev, 1.f), E8 = cexp2(a2, brev, 8.f), E256 = cexp2(a2, brev, 256.f);
            asm volatile("s_nop 0\n\t" KGEN_ASM :: "{v[48:49]}"(W.a), "{v[50:51]}"(W.b), "{v[52:53]}"(E1.a), "{v[54:55]}"(E1.b), "{v[56:57]}"(E8.a), "{v[58:59]}"(E8.b), "{v[60:61]}"(E256.a), "{v[62:63]}"(E256.b),
                         [b0] "v"(sg[0]), [b1] "v"(sg[1]), [b2] "v"(sg[2]), [b3] "v"(sg[3]), [b4] "v"(sg[4]), [b5] "v"(sg[5]), [b6] "v"(sg[6]), [b7] "v"(sg[7]), [b8] "v"(sg[8]), [b9] "v"(sg[9]),
                         [b10] "v"(sg[10]), [b11] "v"(sg[11]), [b12] "v"(sg[12]), [b13] "v"(sg[13]) : "memory", KGEN_CLOBBER);
            STAMP(2);
            asm volatile(KMFMA_ASM : "=&{v[48:63]}"(acc), [stg] "=&v"(stg) : [ap] "v"(ap), [bp] "v"(bp), [ctl] "v"(ctl) : "memory", KMFMA_CLOBBER);
            acc *= kscale;
        }
        STAMP(3);
        acc[0] += __uint_as_float(((u32)dsk << 16) & m0_);
        u32 t78 = cvtpk(acc[7], acc[8]), t9a = cvtpk(acc[9], acc[10]);
        {
            const auto s0 = __builtin_amdgcn_permlane32_swap(cvtpk(acc[0], acc[1]), cvtpk(acc[4], acc[4]), false, false);
            const auto s1 = __builtin_amdgcn_permlane32_swap(cvtpk(acc[2], acc[3]), cvtpk(acc[5], acc[6]), false, false);
            const v4u ww = {s0[0], s1[0], s0[1], s1[1]};
            const u32 pz = (u32)__builtin_amdgcn_update_dpp(0, (int)ww.z, 0x138, 0xf, 0xf, true), pw = (u32)__builtin_amdgcn_update_dpp(0, (int)ww.w, 0x138, 0xf, 0xf, true);
            const v4u c1 = {__builtin_amdgcn_alignbit(ww.x, pw, 16), __builtin_amdgcn_alignbit(ww.y, ww.x, 16), __builtin_amdgcn_alignbit(ww.z, ww.y, 16), __builtin_amdgcn_alignbit(ww.w, ww.z, 16)};
            *(LAS v4u*)(k0) = ww;
            *(LAS v4u*)(k0 + KCOPY) = c1;
            *(LAS v4u*)(k0 + 2 * KCOPY) = (v4u){pw, ww.x, ww.y, ww.z};
            *(LAS v4u*)(k0 + 3 * KCOPY) = (v4u){__builtin_amdgcn_alignbit(pw, pz, 16), c1.x, c1.y, c1.z};
            const u32 w0 = (u32)__builtin_amdgcn_readlane((int)ww.z, 63), w1 = (u32)__builtin_amdgcn_readlane((int)ww.w, 63);
            const u32 w3 = w1 >> 16, w2 = w3 * 0x10001u, w10 = (w0 >> 16) | (w1 << 16);
            *(LAS v4u*)kt = (v4u){(w2 & m01) | (w1 & m2) | (w10 & m3), (w3 & m0_) | (w2 & ((m01 ^ m0_) | m2 | m3)), (w3 & m2) | (w2 & m3), 0u};
        }
        asm volatile("" ::: "memory");
        pat = __builtin_bit_cast(bf16x8, __builtin_bit_cast(v4u, pat) & (((u32)__builtin_amdgcn_readlane((int)t78, 63) >> 16) * 0x10001u));
#define POLL_SEL "global_load_dwordx2 v[240:241], %[so], %[ws] sc1\n\t"
        v2u sq;
        const v4f zero4 = {0.f, 0.f, 0.f, 0.f};
        if (MPRIO) __builtin_amdgcn_s_setprio(MPRIO);
        STAMP(4);
        {
            u32 s_, c_;
            asm volatile(CONV_ASM : "=&{v[240:241]}"(sq), [s] "=&s"(s_), [c] "=&s"(c_)
                         : "{v[4:7]}"(b5), "{v[8:11]}"(b3), "{v[12:15]}"(b9), "{v[16:19]}"(b7), "{v[20:23]}"(pat), [a0] "v"(a0), [bb] "v"(bb), [yb] "v"(yb), [tp] "v"(tp), [ta] "v"(ta), [tb] "v"(tb),
                           [ctl] "v"(ctl), [ctc] "v"(ctc), [one] "v"(one), [so] "v"(so), [t78] "v"(t78), [t9a] "v"(t9a), [stg] "v"(stg), [ws] "s"(p.ws) : "memory", "scc", CONV_CLOBBER);
        }
        STAMP(9);
        asm volatile("s_cmp_eq_u32 %0, 0\n\ts_cbranch_scc1 1f\n\ts_endpgm\n1:" :: "s"(wave) : "memory", "scc");
        {
            const u32 tag = __builtin_amdgcn_readfirstlane(*(LAS u32*)(smem + LDS_CTL + C_TAG));
            u32 c_;
            asm volatile("s_mov_b32 %[c], 0x80000\n1:\n\ts_waitcnt vmcnt(0)\n\tv_cmpx_eq_u32 %[tag], v241\n\t"
                         "s_cmp_eq_u64 exec, -1\n\ts_mov_b64 exec, -1\n\ts_cbranch_scc1 2f\n\ts_sleep 4\n\t" POLL_SEL "s_sub_u32 %[c], %[c], 1\n\ts_cbranch_scc0 1b\n\ts_waitcnt vmcnt(0)\n2:\n\ts_nop 4"
                         : "+{v[240:241]}"(sq), [c] "=&s"(c_) : [so] "v"(so), [ws] "s"(p.ws), [tag] "s"(tag) : "memory", "scc", "vcc");
            STAMP(10);
            const u32 pb = OFF_PART + cw * (NFFN * 128) + 4096 + lane * 16, pb2 = pb + 4096;
            v4u pq[9];
#define POLL_P "global_load_dwordx4 v[64:67], %[pb], %[ws] offset:-4096 sc1\n\tglobal_load_dwordx4 v[68:71], %[pb], %[ws] offset:-3072 sc1\n\tglobal_load_dwordx4 v[72:75], %[pb], %[ws] offset:-2048 sc1\n\t" \
               "global_load_dwordx4 v[76:79], %[pb], %[ws] offset:-1024 sc1\n\tglobal_load_dwordx4 v[80:83], %[pb], %[ws] sc1\n\tglobal_load_dwordx4 v[84:87], %[pb], %[ws] offset:1024 sc1\n\t" \
               "global_load_dwordx4 v[88:91], %[pb], %[ws] offset:2048 sc1\n\tglobal_load_dwordx4 v[92:95], %[pb], %[ws] offset:3072 sc1\n\tglobal_load_dwordx4 v[96:99], %[pb2], %[ws] sc1\n\t"
            asm volatile("s_mov_b32 %[c], 0x80000\n1:\n\tglobal_load_dwordx2 v[64:65], %[ph], %[ws] sc1\n\ts_waitcnt vmcnt(0)\n\tv_cmp_ne_u32 vcc, %[tag], v65\n\ts_cbranch_vccz 2f\n\ts_sub_u32 %[c], %[c], 1\n\ts_cbranch_scc0 1b\n2:\n\t" POLL_P
                         : "=&{v[64:67]}"(pq[0]), "=&{v[68:71]}"(pq[1]), "=&{v[72:75]}"(pq[2]), "=&{v[76:79]}"(pq[3]), "=&{v[80:83]}"(pq[4]), "=&{v[84:87]}"(pq[5]), "=&{v[88:91]}"(pq[6]), "=&{v[92:95]}"(pq[7]), "=&{v[96:99]}"(pq[8]), [c] "=&s"(c_)
                         : [pb] "v"(pb), [pb2] "v"(pb2), [ph] "v"(OFF_PART + (cw * NFFN + (cw >> 1)) * 128), [ws] "s"(p.ws), [tag] "s"(tag) : "memory", "scc", "vcc");
            const int t = (int)(0xffffu - (sq.x & 0xffffu)) & 2047;
            const float rs = rbf(sigmoidf_(key_to_r(sq.x)));
            LWAIT(ctl, C_ACK, 4);
            const float g = bf2f(*(LAS u16*)(smem + LDS_G + ((t >> 8) & 3) * 3072 + (t >> 10) * 512 + (t & 255) * 2));
            const int uq = (t & 255) >> 2;
            float xv[4], av[4];
#pragma unroll
            for (int c = 0; c < 4; ++c) {
                xv[c] = bf2f(*(LAS u16*)(smem + LDS_U + c * 4608 + (256 + t) * 2));
                av[c] = g * *(LAS float*)(smem + c * KCH + CH_Y + (t >> 8) * 1152 + (uq + (uq >> 3)) * 16 + (t & 3) * 4);
            }
            const u32 e1 = n < 4 ? ((n & 1) ? 0x3f800000u : 0x00003f80u) : 0u;
            bf16x8 sel4 = __builtin_bit_cast(bf16x8, (n & 2) ? (v4u){0u, e1, 0u, e1} : (v4u){e1, 0u, e1, 0u});
            u32 oo = (u32)(t * D + chbase) * 2;
            float omg = 1.f - g;
            asm volatile("" : "+v"(sel4), "+v"(oo), "+v"(omg), "+v"(xv[0]), "+v"(xv[1]), "+v"(xv[2]), "+v"(xv[3]), "+v"(av[0]), "+v"(av[1]), "+v"(av[2]), "+v"(av[3]));
            asm volatile("s_mov_b32 %[c], 0x80000\n1:\n\ts_waitcnt vmcnt(0)\n\t"
                         "v_cmpx_eq_u32 %[tag], v65\n\tv_cmpx_eq_u32 %[tag], v67\n\tv_cmpx_eq_u32 %[tag], v69\n\tv_cmpx_eq_u32 %[tag], v71\n\tv_cmpx_eq_u32 %[tag], v73\n\tv_cmpx_eq_u32 %[tag], v75\n\t"
                         "v_cmpx_eq_u32 %[tag], v77\n\tv_cmpx_eq_u32 %[tag], v79\n\tv_cmpx_eq_u32 %[tag], v81\n\tv_cmpx_eq_u32 %[tag], v83\n\tv_cmpx_eq_u32 %[tag], v85\n\tv_cmpx_eq_u32 %[tag], v87\n\t"
                         "v_cmpx_eq_u32 %[tag], v89\n\tv_cmpx_eq_u32 %[tag], v91\n\tv_cmpx_eq_u32 %[tag], v93\n\tv_cmpx_eq_u32 %[tag], v95\n\tv_cmpx_eq_u32 %[tag], v97\n\tv_cmpx_eq_u32 %[tag], v99\n\t"
                         "s_cmp_eq_u64 exec, -1\n\ts_mov_b64 exec, -1\n\ts_cbranch_scc1 2f\n\t" POLL_P "s_sub_u32 %[c], %[c], 1\n\ts_cbranch_scc0 1b\n\ts_waitcnt vmcnt(0)\n2:\n\ts_nop 4"
                         : "+{v[64:67]}"(pq[0]), "+{v[68:71]}"(pq[1]), "+{v[72:75]}"(pq[2]), "+{v[76:79]}"(pq[3]), "+{v[80:83]}"(pq[4]), "+{v[84:87]}"(pq[5]), "+{v[88:91]}"(pq[6]), "+{v[92:95]}"(pq[7]), "+{v[96:99]}"(pq[8]), [c] "=&s"(c_)
                         : [pb] "v"(pb), [pb2] "v"(pb2), [ws] "s"(p.ws), [tag] "s"(tag) : "memory", "scc", "vcc");
            STAMP(11);
            v4f s0 = MF(sel4, __builtin_bit_cast(bf16x8, ((v4u){pq[0].x, pq[0].z, pq[1].x, pq[1].z})), zero4);
            v4f s1 = MF(sel4, __builtin_bit_cast(bf16x8, ((v4u){pq[2].x, pq[2].z, pq[3].x, pq[3].z})), zero4);
            s0 = MF(sel4, __builtin_bit_cast(bf16x8, ((v4u){pq[4].x, pq[4].z, pq[5].x, pq[5].z})), s0);
            s1 = MF(sel4, __builtin_bit_cast(bf16x8, ((v4u){pq[6].x, pq[6].z, pq[7].x, pq[7].z})), s1);
            s0 = MF(sel4, __builtin_bit_cast(bf16x8, ((v4u){pq[8].x, pq[8].z, 0u, 0u})), s0);
            float o[4];
#pragma unroll
            for (int c = 0; c < 4; ++c) o[c] = fmaf(omg, xv[c] + rbf(rbf(dpp_add<0x128, 0xf>(s0[c] + s1[c])) * rs), av[c]);
            asm volatile("s_mov_b64 exec, 0xff\n\tglobal_store_dwordx2 %0, %1, %2\n\ts_mov_b64 exec, -1" :: "v"(oo), "v"((v2u){cvtpk(o[0], o[1]), cvtpk(o[2], o[3])}), "s"(p.out) : "memory");
        }
        STAMP(12);
        ROLE_END();
    }
    }
}

extern "C" void kernel_launch(void* const* d_in, const int* in_sizes, int n_in,
                              void* d_out, int out_size, void* d_ws, size_t ws_size,
                              hipStream_t stream) {
    Params p;
    p.x = (const u16*)d_in[0]; p.Are = (const u16*)d_in[1]; p.Aim = (const u16*)d_in[2]; p.Bre = (const u16*)d_in[3];
    p.Bim = (const u16*)d_in[4]; p.Cre = (const u16*)d_in[5]; p.Cim = (const u16*)d_in[6]; p.logdt = (const u16*)d_in[7];
    p.dskip = (const u16*)d_in[8]; p.wr = (const u16*)d_in[10]; p.wg = (const u16*)d_in[11]; p.wu = (const u16*)d_in[12];
    p.wd = (const u16*)d_in[13]; p.gw = (const u16*)d_in[14]; p.out = (u16*)d_out; p.ws = (char*)d_ws;
    (void)hipFuncSetAttribute((const void*)fused_kernel, hipFuncAttributeMaxDynamicSharedMemorySize, LDS_TOTAL);
    fused_kernel<<<256, 512, LDS_TOTAL, stream>>>(p);
}
```

```cpp
#define FDELAY 8
#include <hip/hip_runtime.h>
#include <stdint.h>

typedef unsigned short u16;
typedef unsigned int u32;
typedef unsigned long long u64;
typedef __attribute__((ext_vector_type(4))) u32 v4u;
typedef __attribute__((ext_vector_type(2))) u32 v2u;
typedef __attribute__((ext_vector_type(2))) float v2f;
typedef __attribute__((ext_vector_type(4))) float v4f;
typedef __attribute__((ext_vector_type(16))) float v16f;
typedef __attribute__((ext_vector_type(8))) short bf16x8;
typedef __attribute__((ext_vector_type(4))) short s16x4;
typedef __attribute__((ext_vector_type(2))) __bf16 bf16x2_t;
typedef __attribute__((ext_vector_type(2))) _Float16 f16x2_t;
typedef __attribute__((ext_vector_type(8))) _Float16 f16x8;
#define LAS __attribute__((address_space(3)))
#define GAS __attribute__((address_space(1)))
#define RLX_AGENT __ATOMIC_RELAXED, __HIP_MEMORY_SCOPE_AGENT
#define SBAR() __builtin_amdgcn_sched_barrier(0)
#define BAR() asm volatile("s_waitcnt lgkmcnt(0)\n\ts_barrier" ::: "memory")
#define WSR() __builtin_amdgcn_make_buffer_rsrc(p.ws, 0, 0x7fffffff, 0x00020000)
#define REREAD() asm volatile("" ::: "memory")

#define WNT " sc1"
#define CODE_KB 20
#define NB1 2
#define REPG 2
#define PFG0 400
#define PFR0 16
#define SELPF 0
#define SELWAIT 50
#define MPRIO 0
#define YSLEEP 1
#define HPRIO 0
#define K2 10
#define TOUCH 0
#define WT 0
#define XPF 0

constexpr int L = 2048, D = 576, NST = 128, F = 4608;
constexpr int NFFN = 72;
constexpr u32 OFF_RG = 4096;
constexpr u32 OFF_G = OFF_RG + L * 8;
constexpr u32 OFF_SEL = OFF_G + REPG * 8192;
constexpr u32 OFF_CI = OFF_SEL + 16 * 256;
constexpr u32 OFF_PART = OFF_CI + 4096;
constexpr u32 SPIN_CAP = 1u << 19;

#define STAMP(i)
#define STAMP0_FIX()
#define STAMP_FLUSH()

struct Params {
    const u16 *x, *Are, *Aim, *Bre, *Bim, *Cre, *Cim, *logdt, *dskip, *wr, *wg, *wu, *wd, *gw;
    u16* out;
    char* ws;
};

__device__ __forceinline__ float bflo(u32 w) { return __uint_as_float(w << 16); }
__device__ __forceinline__ float bfhi(u32 w) { return __uint_as_float(w & 0xffff0000u); }
__device__ __forceinline__ float bf2f(u16 h) { return __uint_as_float((u32)h << 16); }
__device__ __forceinline__ u32 cvtpk(float lo, float hi) { v2f v = {lo, hi}; bf16x2_t r = __builtin_convertvector(v, bf16x2_t); return __builtin_bit_cast(u32, r); }
__device__ __forceinline__ u32 cvth(v2f v) { return __builtin_bit_cast(u32, __builtin_convertvector(v, f16x2_t)); }
__device__ __forceinline__ float rbf(float f) { return bflo(cvtpk(f, f)); }
__device__ __forceinline__ float sigmoidf_(float s) { return __builtin_amdgcn_rcpf(1.f + __expf(-s)); }

template <int CTRL, int RM> __device__ __forceinline__ float dpp_add(float v) {
    return v + __builtin_bit_cast(float, __builtin_amdgcn_update_dpp(0, __builtin_bit_cast(int, v), CTRL, RM, 0xf, true));
}
__device__ __forceinline__ float wave_sum63(float v) {
    v = dpp_add<0x111, 0xf>(v); v = dpp_add<0x112, 0xf>(v); v = dpp_add<0x114, 0xf>(v); v = dpp_add<0x118, 0xf>(v);
    v = dpp_add<0x142, 0xa>(v); v = dpp_add<0x143, 0xc>(v);
    return v;
}
template <int CTRL, int RM> __device__ __forceinline__ u32 dpp_umax(u32 v) {
    u32 o = (u32)__builtin_amdgcn_update_dpp(0, (int)v, CTRL, RM, 0xf, true);
    return v > o ? v : o;
}

typedef v2f cf;
__device__ __forceinline__ cf cmul(cf a, cf b) { cf t = a.yy * (cf){-b.y, b.x}; return __builtin_elementwise_fma(a.xx, b, t); }
__device__ __forceinline__ cf cexp_(float a2, float brev, float rho) {
    float m = __builtin_amdgcn_exp2f(a2 * rho);
    float ph = __builtin_amdgcn_fractf(brev * rho);
    cf c; c.x = m * __builtin_amdgcn_cosf(ph); c.y = m * __builtin_amdgcn_sinf(ph); return c;
}

struct cf2 { cf a, b; };
__device__ __forceinline__ cf2 cmul2(cf2 x, cf2 y) { return {cmul(x.a, y.a), cmul(x.b, y.b)}; }
__device__ __forceinline__ cf2 cexp2(v2f a2, v2f brev, float rho) { return {cexp_(a2.x, brev.x, rho), cexp_(a2.y, brev.y, rho)}; }

__device__ __forceinline__ u32 make_payload(float r, float g) {
    const u32 w = cvtpk(g, r);
    return w ^ (((u32)((int)w >> 31) & 0x7fff0000u) | 0x80000000u);
}
__device__ __forceinline__ u32 make_key(u32 pay, u32 t) { return (pay & 0xffff0000u) | (0xffffu - t); }
__device__ __forceinline__ float key_to_r(u32 k) {
    u32 hi = k & 0xffff0000u;
    return __uint_as_float((hi & 0x80000000u) ? (hi ^ 0x80000000u) : (~hi & 0xffff0000u));
}

constexpr int STG_ROW = 528;
constexpr int NA = 21;
constexpr int KCH = (NA + 32) * STG_ROW;
constexpr int KCOPY = 1344;
constexpr int CH_Y = 4 * KCOPY;
constexpr int LDS_U = 4 * KCH;
constexpr int LDS_TAP = LDS_U + 4 * 4608;
constexpr int LDS_CTL = LDS_TAP + 4 * 2304;
constexpr int C_TAG = 0, C_STG = 4, C_PUB = 8, C_ACK = 12, C_Y = 64;
constexpr int LDS_RW = LDS_CTL + 1024;
constexpr int LDS_G = LDS_RW;
constexpr int LDS_DUMMY = LDS_RW + 4 * 3072;
constexpr int LDS_TOTAL = LDS_DUMMY + 1024;
static_assert(CH_Y + 8 * 1152 <= KCH && LDS_TOTAL <= 160 * 1024, "lds");
constexpr int WDP = 1184;
constexpr int FL_WD = 0;
constexpr int FL_RED = 74 * 1024;
constexpr int FL_H = FL_RED + 24576;
constexpr int XSP = 1168;
constexpr int FL_XS = FL_H + 8 * 144;
constexpr int FL_Z = FL_XS + 8 * XSP;
constexpr int FL_TAG = FL_Z + 256;

__device__ __forceinline__ int sum_rows(int v) {
    auto a = __builtin_amdgcn_permlane16_swap((u32)v, (u32)v, false, false);
    v = (int)(a[0] + a[1]);
    auto b = __builtin_amdgcn_permlane32_swap((u32)v, (u32)v, false, false);
    return (int)(b[0] + b[1]);
}
__device__ __forceinline__ float sum_rows_f(float v) {
    auto a = __builtin_amdgcn_permlane16_swap(__float_as_uint(v), __float_as_uint(v), false, false);
    v = __uint_as_float(a[0]) + __uint_as_float(a[1]);
    auto b = __builtin_amdgcn_permlane32_swap(__float_as_uint(v), __float_as_uint(v), false, false);
    return __uint_as_float(b[0]) + __uint_as_float(b[1]);
}
__device__ __forceinline__ u32 top8_d(LAS char* t8, int lane, int n) {
    LAS u32* cand = (LAS u32*)(t8 + 256);
    u32 res = 0;
    _Pragma("unroll 1") for (int i0 = 0; i0 < n; i0 += 64) {
        const int i = i0 + lane;
        const u32 k = i < n ? cand[i] : 0u;
        int rk = 0;
        _Pragma("unroll 1") for (int j0 = 0; j0 < n; j0 += 16) {
            v4u c[4];
#pragma unroll
            for (int e = 0; e < 4; ++e) c[e] = *(LAS v4u*)(cand + j0 + 4 * e);
#pragma unroll
            for (int e = 0; e < 16; ++e) rk += (c[e >> 2][e & 3] > k) & (j0 + e < n);
        }
        const bool hit = i < n && rk < 8;
        const u32 got = (u32)__builtin_amdgcn_ds_permute((hit ? rk : 63) * 4, (int)(hit ? k : 0u));
        if (lane < 8) res |= got;
    }
    return res;
}

__device__ __forceinline__ u32 lds_peek(LAS char* word) { return __hip_atomic_load((LAS u32*)word, __ATOMIC_RELAXED, __HIP_MEMORY_SCOPE_WORKGROUP); }
#define LWAIT_BODY "1:\n\tds_read_b32 %0, %3 offset:%4\n\ts_waitcnt lgkmcnt(0)\n\tv_readfirstlane_b32 %1, %0\n\ts_cmp_ge_u32 %1, %5\n\ts_cbranch_scc1 2f\n\ts_sleep 1\n\ts_sub_u32 %2, %2, 1\n\ts_cbranch_scc0 1b\n2:"
#define LSPIN(addr, off, want) do { u32 t_, s_, c_; asm volatile("s_mov_b32 %2, 0x800000\n1:\n\tds_read_b32 %0, %3 offset:%4\n\ts_waitcnt lgkmcnt(0)\n\tv_readfirstlane_b32 %1, %0\n\ts_cmp_ge_u32 %1, %5\n\ts_cbranch_scc1 2f\n\ts_sub_u32 %2, %2, 1\n\ts_cbranch_scc0 1b\n2:" : "=&v"(t_), "=&s"(s_), "=&s"(c_) : "v"(addr), "n"(off), "n"(want) : "memory", "scc"); } while (0)
#define LWAIT(addr, off, want) do { u32 t_, s_, c_; asm volatile("s_mov_b32 %2, 0x400000\n" LWAIT_BODY : "=&v"(t_), "=&s"(s_), "=&s"(c_) : "v"(addr), "n"(off), "n"(want) : "memory", "scc"); } while (0)
#define LWAIT_PEEK(peek, addr, off, want) do { u32 t_, s_, c_; asm volatile("s_nop 0\n\tv_readfirstlane_b32 %1, %6\n\ts_mov_b32 %2, 0x400000\n\ts_cmp_ge_u32 %1, %5\n\ts_cbranch_scc1 2f\n" LWAIT_BODY : "=&v"(t_), "=&s"(s_), "=&s"(c_) : "v"(addr), "n"(off), "n"(want), "v"(peek) : "memory", "scc"); } while (0)
#define LDS_ADD1(addr, one, off) asm volatile("s_mov_b64 exec, 1\n\tds_add_u32 %0, %1 offset:%2\n\ts_mov_b64 exec, -1" :: "v"(addr), "v"(one), "n"(off) : "memory")
#define LDS_SETB(addr, one, off) asm volatile("s_mov_b64 exec, 1\n\tds_write_b8 %0, %1 offset:%2\n\ts_mov_b64 exec, -1" :: "v"(addr), "v"(one), "n"(off) : "memory")
__device__ __forceinline__ void glds16(const void* g, u32 lds_dst, bool nt) {
    u32 keep;
    if (nt) asm volatile("s_mov_b32 %0, m0\n\ts_mov_b32 m0, %2\n\ts_nop 0\n\tglobal_load_lds_dwordx4 %1, off" WNT "\n\ts_mov_b32 m0, %0" : "=&s"(keep) : "v"(g), "s"(lds_dst) : "memory");
    else asm volatile("s_mov_b32 %0, m0\n\ts_mov_b32 m0, %2\n\ts_nop 0\n\tglobal_load_lds_dwordx4 %1, off\n\ts_mov_b32 m0, %0" : "=&s"(keep) : "v"(g), "s"(lds_dst) : "memory");
}
__device__ __forceinline__ bf16x8 band(u32 bits, u32 val) {
    v4u r;
#pragma unroll
    for (int j = 0; j < 4; ++j) r[j] = __builtin_amdgcn_perm((u32)__builtin_amdgcn_sbfe((int)bits, 2 * j + 1, 1), (u32)__builtin_amdgcn_sbfe((int)bits, 2 * j, 1), 0x05040100u) & val;
    return __builtin_bit_cast(bf16x8, r);
}

template <int LIST> __device__ __forceinline__ bool touch(const Params& p, int xcd, int e, int lane, u32 dst) {
    const int wa = xcd * 9 + ((3 - (int)(((uintptr_t)p.wg >> 7) & 7) - xcd * 9) & 7), two = wa == xcd * 9;
    const int na = LIST ? 0 : (PFG0 * 2) << two, nb = LIST ? 0 : (576 - PFG0) * 18, nc = LIST ? 64 * 81 : 0;
    if (!LIST && e * 64 >= na + nb) return false;
    const u32 i = min(e * 64 + lane, na + nb + nc - 1);
    const char* a;
    if (i < (u32)(na + nb)) {
        u32 seg, col;
        if (i < (u32)na) { seg = i >> two; col = (wa + 8 * (i & two)) * 128u; }
        else { const u32 k = (i - na) / 9u; seg = k + 2 * PFG0; col = xcd * 1152u + (i - na - 9u * k) * 128u; }
        a = (const char*)((seg & 1) ? p.wu : p.wg) + (seg >> 1) * (F * 2) + col;
    } else {
        const u32 c = i - na - nb, k = c / 9u, rw = k / 9u;
        a = (const char*)p.wd + ((xcd * 9u + k - 9u * rw) * 64u + (rw >> 1) + 32u * (rw & 1)) * 1152u + (c - 9u * k) * 128u;
    }
    asm volatile("s_mov_b32 m0, %1\n\ts_nop 0\n\tglobal_load_lds_dword %0, off" :: "v"(a), "s"(dst) : "memory");
    return true;
}

#define KGEN_ASM ".p2align 3\n\tv_mov_b32 v64, 0x3c00\n\tv_mov_b32 v65, 0x3c00\n\tv_pk_mul_f32 v[66:67], v[56:57], v[56:57] op_sel:[1,1] op_sel_hi:[1,0] neg_lo:[0,1]\n\tv_pk_mul_f32 v[68:69], v[58:59], v[58:59] op_sel:[1,1] op_sel_hi:[1,0] neg_lo:[0,1]\n\tv_pk_mul_f32 v[70:71], v[52:53], v[52:53] op_sel:[1,1] op_sel_hi:[1,0] neg_lo:[0,1]\n\tv_pk_mul_f32 v[72:73], v[54:55], v[54:55] op_sel:[1,1] op_sel_hi:[1,0] neg_lo:[0,1]\n\tv_pk_fma_f32 v[66:67], v[56:57], v[56:57], v[66:67] op_sel:[0,0,0] op_sel_hi:[0,1,1]\n\tv_pk_fma_f32 v[68:69], v[58:59], v[58:59], v[68:69] op_sel:[0,0,0] op_sel_hi:[0,1,1]\n\tv_pk_fma_f32 v[70:71], v[52:53], v[52:53], v[70:71] op_sel:[0,0,0] op_sel_hi:[0,1,1]\n\tv_pk_fma_f32 v[72:73], v[54:55], v[54:55], v[72:73] op_sel:[0,0,0] op_sel_hi:[0,1,1]\n\tv_cvt_pk_f16_f32 v74, v56, -v57\n\tv_cvt_pk_f16_f32 v75, v58, -v59\n\tv_cvt_pk_f16_f32 v76, v48, v49\n\tv_cvt_pk_f16_f32 v77, v50, v51\n\tv_pk_mul_f32 v[78:79], v[48:49], v[60:61] op_sel:[1,1] op_sel_hi:[1,0] neg_lo:[0,1]\n\tv_pk_mul_f32 v[80:81], v[50:51], v[62:63] op_sel:[1,1] op_sel_hi:[1,0] neg_lo:[0,1]\n\tv_pk_mul_f32 v[82:83], v[48:49], v[52:53] op_sel:[1,1] op_sel_hi:[1,0] neg_lo:[0,1]\n\tv_pk_mul_f32 v[84:85], v[50:51], v[54:55] op_sel:[1,1] op_sel_hi:[1,0] neg_lo:[0,1]\n\tds_write2_b64 %[b0], v[64:65], v[74:75] offset0:0 offset1:66\n\tv_pk_fma_f32 v[78:79], v[48:49], v[60:61], v[78:79] op_sel:[0,0,0] op_sel_hi:[0,1,1]\n\tv_pk_fma_f32 v[80:81], v[50:51], v[62:63], v[80:81] op_sel:[0,0,0] op_sel_hi:[0,1,1]\n\tv_pk_fma_f32 v[82:83], v[48:49], v[52:53], v[82:83] op_sel:[0,0,0] op_sel_hi:[0,1,1]\n\tv_pk_fma_f32 v[84:85], v[50:51], v[54:55], v[84:85] op_sel:[0,0,0] op_sel_hi:[0,1,1]\n\tv_cvt_pk_f16_f32 v86, v66, -v67\n\tv_cvt_pk_f16_f32 v87, v68, -v69\n\tv_pk_mul_f32 v[88:89], v[66:67], v[66:67] op_sel:[1,1] op_sel_hi:[1,0] neg_lo:[0,1]\n\tv_pk_mul_f32 v[90:91], v[68:69], v[68:69] op_sel:[1,1] op_sel_hi:[1,0] neg_lo:[0,1]\n\tv_pk_mul_f32 v[92:93], v[66:67], v[56:57] op_sel:[1,1] op_sel_hi:[1,0] neg_lo:[0,1]\n\tv_pk_mul_f32 v[94:95], v[68:69], v[58:59] op_sel:[1,1] op_sel_hi:[1,0] neg_lo:[0,1]\n\tv_pk_fma_f32 v[88:89], v[66:67], v[66:67], v[88:89] op_sel:[0,0,0] op_sel_hi:[0,1,1]\n\tv_pk_fma_f32 v[90:91], v[68:69], v[68:69], v[90:91] op_sel:[0,0,0] op_sel_hi:[0,1,1]\n\tv_pk_fma_f32 v[92:93], v[66:67], v[56:57], v[92:93] op_sel:[0,0,0] op_sel_hi:[0,1,1]\n\tv_pk_fma_f32 v[94:95], v[68:69], v[58:59], v[94:95] op_sel:[0,0,0] op_sel_hi:[0,1,1]\n\tv_cvt_pk_f16_f32 v64, v78, v79\n\tv_cvt_pk_f16_f32 v65, v80, v81\n\tv_cvt_pk_f16_f32 v96, v82, v83\n\tv_cvt_pk_f16_f32 v97, v84, v85\n\tv_pk_mul_f32 v[98:99], v[70:71], v[70:71] op_sel:[1,1] op_sel_hi:[1,0] neg_lo:[0,1]\n\tv_pk_mul_f32 v[100:101], v[72:73], v[72:73] op_sel:[1,1] op_sel_hi:[1,0] neg_lo:[0,1]\n\tv_pk_mul_f32 v[102:103], v[78:79], v[56:57] op_sel:[1,1] op_sel_hi:[1,0] neg_lo:[0,1]\n\tv_pk_mul_f32 v[104:105], v[80:81], v[58:59] op_sel:[1,1] op_sel_hi:[1,0] neg_lo:[0,1]\n\tds_write2_b64 %[b8], v[76:77], v[96:97] offset0:0 offset1:66\n\tv_pk_fma_f32 v[98:99], v[70:71], v[70:71], v[98:99] op_sel:[0,0,0] op_sel_hi:[0,1,1]\n\tv_pk_fma_f32 v[100:101], v[72:73], v[72:73], v[100:101] op_sel:[0,0,0] op_sel_hi:[0,1,1]\n\tv_pk_fma_f32 v[102:103], v[78:79], v[56:57], v[102:103] op_sel:[0,0,0] op_sel_hi:[0,1,1]\n\tv_pk_fma_f32 v[104:105], v[80:81], v[58:59], v[104:105] op_sel:[0,0,0] op_sel_hi:[0,1,1]\n\tv_cvt_pk_f16_f32 v106, v88, -v89\n\tv_cvt_pk_f16_f32 v107, v90, -v91\n\tv_cvt_pk_f16_f32 v108, v92, -v93\n\tv_cvt_pk_f16_f32 v109, v94, -v95\n\tv_pk_mul_f32 v[110:111], v[78:79], v[70:71] op_sel:[1,1] op_sel_hi:[1,0] neg_lo:[0,1]\n\tv_pk_mul_f32 v[112:113], v[80:81], v[72:73] op_sel:[1,1] op_sel_hi:[1,0] neg_lo:[0,1]\n\tv_pk_mul_f32 v[114:115], v[48:49], v[70:71] op_sel:[1,1] op_sel_hi:[1,0] neg_lo:[0,1]\n\tv_pk_mul_f32 v[116:117], v[50:51], v[72:73] op_sel:[1,1] op_sel_hi:[1,0] neg_lo:[0,1]\n\tds_write2_b64 %[b0], v[86:87], v[108:109] offset0:132 offset1:198\n\tv_pk_fma_f32 v[110:111], v[78:79], v[70:71], v[110:111] op_sel:[0,0,0] op_sel_hi:[0,1,1]\n\tv_pk_fma_f32 v[112:113], v[80:81], v[72:73], v[112:113] op_sel:[0,0,0] op_sel_hi:[0,1,1]\n\tv_pk_fma_f32 v[114:115], v[48:49], v[70:71], v[114:115] op_sel:[0,0,0] op_sel_hi:[0,1,1]\n\tv_pk_fma_f32 v[116:117], v[50:51], v[72:73], v[116:117] op_sel:[0,0,0] op_sel_hi:[0,1,1]\n\tv_cvt_pk_f16_f32 v76, v102, v103\n\tv_cvt_pk_f16_f32 v77, v104, v105\n\tv_pk_mul_f32 v[96:97], v[82:83], v[70:71] op_sel:[1,1] op_sel_hi:[1,0] neg_lo:[0,1]\n\tv_pk_mul_f32 v[118:119], v[84:85], v[72:73] op_sel:[1,1] op_sel_hi:[1,0] neg_lo:[0,1]\n\tv_pk_mul_f32 v[120:121], v[88:89], v[88:89] op_sel:[1,1] op_sel_hi:[1,0] neg_lo:[0,1]\n\tv_pk_mul_f32 v[122:123], v[90:91], v[90:91] op_sel:[1,1] op_sel_hi:[1,0] neg_lo:[0,1]\n\tv_pk_fma_f32 v[96:97], v[82:83], v[70:71], v[96:97] op_sel:[0,0,0] op_sel_hi:[0,1,1]\n\tv_pk_fma_f32 v[118:119], v[84:85], v[72:73], v[118:119] op_sel:[0,0,0] op_sel_hi:[0,1,1]\n\tv_pk_fma_f32 v[120:121], v[88:89], v[88:89], v[120:121] op_sel:[0,0,0] op_sel_hi:[0,1,1]\n\tv_pk_fma_f32 v[122:123], v[90:91], v[90:91], v[122:123] op_sel:[0,0,0] op_sel_hi:[0,1,1]\n\tv_cvt_pk_f16_f32 v124, v110, v111\n\tv_cvt_pk_f16_f32 v125, v112, v113\n\tv_cvt_pk_f16_f32 v126, v114, v115\n\tv_cvt_pk_f16_f32 v127, v116, v117\n\tv_pk_mul_f32 v[70:71], v[88:89], v[56:57] op_sel:[1,1] op_sel_hi:[1,0] neg_lo:[0,1]\n\tv_pk_mul_f32 v[72:73], v[90:91], v[58:59] op_sel:[1,1] op_sel_hi:[1,0] neg_lo:[0,1]\n\tv_pk_mul_f32 v[128:129], v[102:103], v[60:61] op_sel:[1,1] op_sel_hi:[1,0] neg_lo:[0,1]\n\tv_pk_mul_f32 v[130:131], v[104:105], v[62:63] op_sel:[1,1] op_sel_hi:[1,0] neg_lo:[0,1]\n\tds_write2_b64 %[b11], v[64:65], v[124:125] offset0:0 offset1:66\n\tv_pk_fma_f32 v[70:71], v[88:89], v[56:57], v[70:71] op_sel:[0,0,0] op_sel_hi:[0,1,1]\n\tv_pk_fma_f32 v[72:73], v[90:91], v[58:59], v[72:73] op_sel:[0,0,0] op_sel_hi:[0,1,1]\n\tv_pk_fma_f32 v[128:129], v[102:103], v[60:61], v[128:129] op_sel:[0,0,0] op_sel_hi:[0,1,1]\n\tv_pk_fma_f32 v[130:131], v[104:105], v[62:63], v[130:131] op_sel:[0,0,0] op_sel_hi:[0,1,1]\n\tv_cvt_pk_f16_f32 v132, v96, v97\n\tv_cvt_pk_f16_f32 v133, v118, v119\n\tv_cvt_pk_f16_f32 v134, v120, -v121\n\tv_cvt_pk_f16_f32 v135, v122, -v123\n\tv_pk_mul_f32 v[102:103], v[78:79], v[98:99] op_sel:[1,1] op_sel_hi:[1,0] neg_lo:[0,1]\n\tv_pk_mul_f32 v[104:105], v[80:81], v[100:101] op_sel:[1,1] op_sel_hi:[1,0] neg_lo:[0,1]\n\tv_pk_mul_f32 v[136:137], v[88:89], v[66:67] op_sel:[1,1] op_sel_hi:[1,0] neg_lo:[0,1]\n\tv_pk_mul_f32 v[138:139], v[90:91], v[68:69] op_sel:[1,1] op_sel_hi:[1,0] neg_lo:[0,1]\n\tds_write2_b64 %[b8], v[126:127], v[132:133] offset0:132 offset1:198\n\tv_pk_fma_f32 v[102:103], v[78:79], v[98:99], v[102:103] op_sel:[0,0,0] op_sel_hi:[0,1,1]\n\tv_pk_fma_f32 v[104:105], v[80:81], v[100:101], v[104:105] op_sel:[0,0,0] op_sel_hi:[0,1,1]\n\tv_pk_fma_f32 v[136:137], v[88:89], v[66:67], v[136:137] op_sel:[0,0,0] op_sel_hi:[0,1,1]\n\tv_pk_fma_f32 v[138:139], v[90:91], v[68:69], v[138:139] op_sel:[0,0,0] op_sel_hi:[0,1,1]\n\tv_cvt_pk_f16_f32 v64, v70, -v71\n\tv_cvt_pk_f16_f32 v65, v72, -v73\n\tv_cvt_pk_f16_f32 v124, v128, v129\n\tv_cvt_pk_f16_f32 v125, v130, v131\n\tv_pk_mul_f32 v[78:79], v[88:89], v[92:93] op_sel:[1,1] op_sel_hi:[1,0] neg_lo:[0,1]\n\tv_pk_mul_f32 v[80:81], v[90:91], v[94:95] op_sel:[1,1] op_sel_hi:[1,0] neg_lo:[0,1]\n\tv_pk_mul_f32 v[140:141], v[120:121], v[120:121] op_sel:[1,1] op_sel_hi:[1,0] neg_lo:[0,1]\n\tv_pk_mul_f32 v[142:143], v[122:123], v[122:123] op_sel:[1,1] op_sel_hi:[1,0] neg_lo:[0,1]\n\tds_write2_b64 %[b1], v[106:107], v[64:65] offset0:0 offset1:66\n\tv_pk_fma_f32 v[78:79], v[88:89], v[92:93], v[78:79] op_sel:[0,0,0] op_sel_hi:[0,1,1]\n\tv_pk_fma_f32 v[80:81], v[90:91], v[94:95], v[80:81] op_sel:[0,0,0] op_sel_hi:[0,1,1]\n\tv_pk_fma_f32 v[140:141], v[120:121], v[120:121], v[140:141] op_sel:[0,0,0] op_sel_hi:[0,1,1]\n\tv_pk_fma_f32 v[142:143], v[122:123], v[122:123], v[142:143] op_sel:[0,0,0] op_sel_hi:[0,1,1]\n\tv_cvt_pk_f16_f32 v126, v102, v103\n\tv_cvt_pk_f16_f32 v127, v104, v105\n\tv_cvt_pk_f16_f32 v132, v136, -v137\n\tv_cvt_pk_f16_f32 v133, v138, -v139\n\tv_pk_mul_f32 v[144:145], v[120:121], v[56:57] op_sel:[1,1] op_sel_hi:[1,0] neg_lo:[0,1]\n\tv_pk_mul_f32 v[146:147], v[122:123], v[58:59] op_sel:[1,1] op_sel_hi:[1,0] neg_lo:[0,1]\n\tv_pk_mul_f32 v[148:149], v[128:129], v[60:61] op_sel:[1,1] op_sel_hi:[1,0] neg_lo:[0,1]\n\tv_pk_mul_f32 v[150:151], v[130:131], v[62:63] op_sel:[1,1] op_sel_hi:[1,0] neg_lo:[0,1]\n\tds_write2_b64 %[b9], v[76:77], v[124:125] offset0:0 offset1:66\n\tv_pk_fma_f32 v[144:145], v[120:121], v[56:57], v[144:145] op_sel:[0,0,0] op_sel_hi:[0,1,1]\n\tv_pk_fma_f32 v[146:147], v[122:123], v[58:59], v[146:147] op_sel:[0,0,0] op_sel_hi:[0,1,1]\n\tv_pk_fma_f32 v[148:149], v[128:129], v[60:61], v[148:149] op_sel:[0,0,0] op_sel_hi:[0,1,1]\n\tv_pk_fma_f32 v[150:151], v[130:131], v[62:63], v[150:151] op_sel:[0,0,0] op_sel_hi:[0,1,1]\n\tv_cvt_pk_f16_f32 v152, v78, -v79\n\tv_cvt_pk_f16_f32 v153, v80, -v81\n\tv_cvt_pk_f16_f32 v154, v140, -v141\n\tv_cvt_pk_f16_f32 v155, v142, -v143\n\tv_pk_mul_f32 v[128:129], v[102:103], v[60:61] op_sel:[1,1] op_sel_hi:[1,0] neg_lo:[0,1]\n\tv_pk_mul_f32 v[130:131], v[104:105], v[62:63] op_sel:[1,1] op_sel_hi:[1,0] neg_lo:[0,1]\n\tv_pk_mul_f32 v[140:141], v[110:111], v[98:99] op_sel:[1,1] op_sel_hi:[1,0] neg_lo:[0,1]\n\tv_pk_mul_f32 v[142:143], v[112:113], v[100:101] op_sel:[1,1] op_sel_hi:[1,0] neg_lo:[0,1]\n\tds_write2_b64 %[b1], v[132:133], v[152:153] offset0:132 offset1:198\n\tv_pk_fma_f32 v[128:129], v[102:103], v[60:61], v[128:129] op_sel:[0,0,0] op_sel_hi:[0,1,1]\n\tv_pk_fma_f32 v[130:131], v[104:105], v[62:63], v[130:131] op_sel:[0,0,0] op_sel_hi:[0,1,1]\n\tv_pk_fma_f32 v[140:141], v[110:111], v[98:99], v[140:141] op_sel:[0,0,0] op_sel_hi:[0,1,1]\n\tv_pk_fma_f32 v[142:143], v[112:113], v[100:101], v[142:143] op_sel:[0,0,0] op_sel_hi:[0,1,1]\n\tv_cvt_pk_f16_f32 v76, v144, -v145\n\tv_cvt_pk_f16_f32 v77, v146, -v147\n\tv_cvt_pk_f16_f32 v124, v148, v149\n\tv_cvt_pk_f16_f32 v125, v150, v151\n\tv_pk_mul_f32 v[102:103], v[120:121], v[66:67] op_sel:[1,1] op_sel_hi:[1,0] neg_lo:[0,1]\n\tv_pk_mul_f32 v[104:105], v[122:123], v[68:69] op_sel:[1,1] op_sel_hi:[1,0] neg_lo:[0,1]\n\tv_pk_mul_f32 v[110:111], v[120:121], v[92:93] op_sel:[1,1] op_sel_hi:[1,0] neg_lo:[0,1]\n\tv_pk_mul_f32 v[112:113], v[122:123], v[94:95] op_sel:[1,1] op_sel_hi:[1,0] neg_lo:[0,1]\n\tds_write2_b64 %[b2], v[134:135], v[76:77] offset0:0 offset1:66\n\tv_pk_fma_f32 v[102:103], v[120:121], v[66:67], v[102:103] op_sel:[0,0,0] op_sel_hi:[0,1,1]\n\tv_pk_fma_f32 v[104:105], v[122:123], v[68:69], v[104:105] op_sel:[0,0,0] op_sel_hi:[0,1,1]\n\tv_pk_fma_f32 v[110:111], v[120:121], v[92:93], v[110:111] op_sel:[0,0,0] op_sel_hi:[0,1,1]\n\tv_pk_fma_f32 v[112:113], v[122:123], v[94:95], v[112:113] op_sel:[0,0,0] op_sel_hi:[0,1,1]\n\tv_cvt_pk_f16_f32 v144, v128, v129\n\tv_cvt_pk_f16_f32 v145, v130, v131\n\tv_cvt_pk_f16_f32 v146, v140, v141\n\tv_cvt_pk_f16_f32 v147, v142, v143\n\tv_pk_mul_f32 v[66:67], v[148:149], v[60:61] op_sel:[1,1] op_sel_hi:[1,0] neg_lo:[0,1]\n\tv_pk_mul_f32 v[68:69], v[150:151], v[62:63] op_sel:[1,1] op_sel_hi:[1,0] neg_lo:[0,1]\n\tv_pk_mul_f32 v[92:93], v[128:129], v[60:61] op_sel:[1,1] op_sel_hi:[1,0] neg_lo:[0,1]\n\tv_pk_mul_f32 v[94:95], v[130:131], v[62:63] op_sel:[1,1] op_sel_hi:[1,0] neg_lo:[0,1]\n\tds_write2_b64 %[b9], v[124:125], v[146:147] offset0:132 offset1:198\n\tv_pk_fma_f32 v[66:67], v[148:149], v[60:61], v[66:67] op_sel:[0,0,0] op_sel_hi:[0,1,1]\n\tv_pk_fma_f32 v[68:69], v[150:151], v[62:63], v[68:69] op_sel:[0,0,0] op_sel_hi:[0,1,1]\n\tv_pk_fma_f32 v[92:93], v[128:129], v[60:61], v[92:93] op_sel:[0,0,0] op_sel_hi:[0,1,1]\n\tv_pk_fma_f32 v[94:95], v[130:131], v[62:63], v[94:95] op_sel:[0,0,0] op_sel_hi:[0,1,1]\n\tv_cvt_pk_f16_f32 v140, v102, -v103\n\tv_cvt_pk_f16_f32 v141, v104, -v105\n\tv_cvt_pk_f16_f32 v142, v110, -v111\n\tv_cvt_pk_f16_f32 v143, v112, -v113\n\tv_pk_mul_f32 v[102:103], v[120:121], v[88:89] op_sel:[1,1] op_sel_hi:[1,0] neg_lo:[0,1]\n\tv_pk_mul_f32 v[104:105], v[122:123], v[90:91] op_sel:[1,1] op_sel_hi:[1,0] neg_lo:[0,1]\n\tv_pk_mul_f32 v[110:111], v[120:121], v[70:71] op_sel:[1,1] op_sel_hi:[1,0] neg_lo:[0,1]\n\tv_pk_mul_f32 v[112:113], v[122:123], v[72:73] op_sel:[1,1] op_sel_hi:[1,0] neg_lo:[0,1]\n\tds_write2_b64 %[b2], v[140:141], v[142:143] offset0:132 offset1:198\n\tv_pk_fma_f32 v[102:103], v[120:121], v[88:89], v[102:103] op_sel:[0,0,0] op_sel_hi:[0,1,1]\n\tv_pk_fma_f32 v[104:105], v[122:123], v[90:91], v[104:105] op_sel:[0,0,0] op_sel_hi:[0,1,1]\n\tv_pk_fma_f32 v[110:111], v[120:121], v[70:71], v[110:111] op_sel:[0,0,0] op_sel_hi:[0,1,1]\n\tv_pk_fma_f32 v[112:113], v[122:123], v[72:73], v[112:113] op_sel:[0,0,0] op_sel_hi:[0,1,1]\n\tv_cvt_pk_f16_f32 v124, v66, v67\n\tv_cvt_pk_f16_f32 v125, v68, v69\n\tv_cvt_pk_f16_f32 v128, v92, v93\n\tv_cvt_pk_f16_f32 v129, v94, v95\n\tv_pk_mul_f32 v[70:71], v[48:49], v[98:99] op_sel:[1,1] op_sel_hi:[1,0] neg_lo:[0,1]\n\tv_pk_mul_f32 v[72:73], v[50:51], v[100:101] op_sel:[1,1] op_sel_hi:[1,0] neg_lo:[0,1]\n\tv_pk_mul_f32 v[88:89], v[82:83], v[98:99] op_sel:[1,1] op_sel_hi:[1,0] neg_lo:[0,1]\n\tv_pk_mul_f32 v[90:91], v[84:85], v[100:101] op_sel:[1,1] op_sel_hi:[1,0] neg_lo:[0,1]\n\tds_write2_b64 %[b10], v[126:127], v[144:145] offset0:132 offset1:198\n\tv_pk_fma_f32 v[70:71], v[48:49], v[98:99], v[70:71] op_sel:[0,0,0] op_sel_hi:[0,1,1]\n\tv_pk_fma_f32 v[72:73], v[50:51], v[100:101], v[72:73] op_sel:[0,0,0] op_sel_hi:[0,1,1]\n\tv_pk_fma_f32 v[88:89], v[82:83], v[98:99], v[88:89] op_sel:[0,0,0] op_sel_hi:[0,1,1]\n\tv_pk_fma_f32 v[90:91], v[84:85], v[100:101], v[90:91] op_sel:[0,0,0] op_sel_hi:[0,1,1]\n\tv_cvt_pk_f16_f32 v92, v102, -v103\n\tv_cvt_pk_f16_f32 v93, v104, -v105\n\tv_cvt_pk_f16_f32 v94, v110, -v111\n\tv_cvt_pk_f16_f32 v95, v112, -v113\n\tv_pk_mul_f32 v[82:83], v[66:67], v[60:61] op_sel:[1,1] op_sel_hi:[1,0] neg_lo:[0,1]\n\tv_pk_mul_f32 v[84:85], v[68:69], v[62:63] op_sel:[1,1] op_sel_hi:[1,0] neg_lo:[0,1]\n\tv_pk_mul_f32 v[102:103], v[120:121], v[136:137] op_sel:[1,1] op_sel_hi:[1,0] neg_lo:[0,1]\n\tv_pk_mul_f32 v[104:105], v[122:123], v[138:139] op_sel:[1,1] op_sel_hi:[1,0] neg_lo:[0,1]\n\tds_write2_b64 %[b3], v[92:93], v[94:95] offset0:0 offset1:66\n\tv_pk_fma_f32 v[82:83], v[66:67], v[60:61], v[82:83] op_sel:[0,0,0] op_sel_hi:[0,1,1]\n\tv_pk_fma_f32 v[84:85], v[68:69], v[62:63], v[84:85] op_sel:[0,0,0] op_sel_hi:[0,1,1]\n\tv_pk_fma_f32 v[102:103], v[120:121], v[136:137], v[102:103] op_sel:[0,0,0] op_sel_hi:[0,1,1]\n\tv_pk_fma_f32 v[104:105], v[122:123], v[138:139], v[104:105] op_sel:[0,0,0] op_sel_hi:[0,1,1]\n\tv_cvt_pk_f16_f32 v110, v70, v71\n\tv_cvt_pk_f16_f32 v111, v72, v73\n\tv_cvt_pk_f16_f32 v112, v88, v89\n\tv_cvt_pk_f16_f32 v113, v90, v91\n\tv_pk_mul_f32 v[66:67], v[120:121], v[78:79] op_sel:[1,1] op_sel_hi:[1,0] neg_lo:[0,1]\n\tv_pk_mul_f32 v[68:69], v[122:123], v[80:81] op_sel:[1,1] op_sel_hi:[1,0] neg_lo:[0,1]\n\tv_pk_mul_f32 v[70:71], v[114:115], v[98:99] op_sel:[1,1] op_sel_hi:[1,0] neg_lo:[0,1]\n\tv_pk_mul_f32 v[72:73], v[116:117], v[100:101] op_sel:[1,1] op_sel_hi:[1,0] neg_lo:[0,1]\n\tds_write2_b64 %[b11], v[110:111], v[112:113] offset0:132 offset1:198\n\tv_pk_fma_f32 v[66:67], v[120:121], v[78:79], v[66:67] op_sel:[0,0,0] op_sel_hi:[0,1,1]\n\tv_pk_fma_f32 v[68:69], v[122:123], v[80:81], v[68:69] op_sel:[0,0,0] op_sel_hi:[0,1,1]\n\tv_pk_fma_f32 v[70:71], v[114:115], v[98:99], v[70:71] op_sel:[0,0,0] op_sel_hi:[0,1,1]\n\tv_pk_fma_f32 v[72:73], v[116:117], v[100:101], v[72:73] op_sel:[0,0,0] op_sel_hi:[0,1,1]\n\tv_cvt_pk_f16_f32 v88, v82, v83\n\tv_cvt_pk_f16_f32 v89, v84, v85\n\tv_cvt_pk_f16_f32 v90, v102, -v103\n\tv_cvt_pk_f16_f32 v91, v104, -v105\n\tv_pk_mul_f32 v[78:79], v[96:97], v[98:99] op_sel:[1,1] op_sel_hi:[1,0] neg_lo:[0,1]\n\tv_pk_mul_f32 v[80:81], v[118:119], v[100:101] op_sel:[1,1] op_sel_hi:[1,0] neg_lo:[0,1]\n\tv_pk_mul_f32 v[102:103], v[82:83], v[60:61] op_sel:[1,1] op_sel_hi:[1,0] neg_lo:[0,1]\n\tv_pk_mul_f32 v[104:105], v[84:85], v[62:63] op_sel:[1,1] op_sel_hi:[1,0] neg_lo:[0,1]\n\tds_write2_b64 %[b12], v[128:129], v[124:125] offset0:132 offset1:198\n\tv_pk_fma_f32 v[78:79], v[96:97], v[98:99], v[78:79] op_sel:[0,0,0] op_sel_hi:[0,1,1]\n\tv_pk_fma_f32 v[80:81], v[118:119], v[100:101], v[80:81] op_sel:[0,0,0] op_sel_hi:[0,1,1]\n\tv_pk_fma_f32 v[102:103], v[82:83], v[60:61], v[102:103] op_sel:[0,0,0] op_sel_hi:[0,1,1]\n\tv_pk_fma_f32 v[104:105], v[84:85], v[62:63], v[104:105] op_sel:[0,0,0] op_sel_hi:[0,1,1]\n\tv_cvt_pk_f16_f32 v110, v66, -v67\n\tv_cvt_pk_f16_f32 v111, v68, -v69\n\tv_cvt_pk_f16_f32 v112, v70, v71\n\tv_cvt_pk_f16_f32 v113, v72, v73\n\tv_pk_mul_f16 v66, v154, v74 op_sel:[1,1] op_sel_hi:[1,0] neg_lo:[0,1]\n\tv_pk_mul_f16 v67, v155, v75 op_sel:[1,1] op_sel_hi:[1,0] neg_lo:[0,1]\n\tv_pk_mul_f16 v68, v154, v86 op_sel:[1,1] op_sel_hi:[1,0] neg_lo:[0,1]\n\tv_pk_mul_f16 v69, v155, v87 op_sel:[1,1] op_sel_hi:[1,0] neg_lo:[0,1]\n\tds_write2_b64 %[b3], v[90:91], v[110:111] offset0:132 offset1:198\n\tv_pk_fma_f16 v66, v154, v74, v66 op_sel:[0,0,0] op_sel_hi:[0,1,1]\n\tv_pk_fma_f16 v67, v155, v75, v67 op_sel:[0,0,0] op_sel_hi:[0,1,1]\n\tv_pk_fma_f16 v68, v154, v86, v68 op_sel:[0,0,0] op_sel_hi:[0,1,1]\n\tv_pk_fma_f16 v69, v155, v87, v69 op_sel:[0,0,0] op_sel_hi:[0,1,1]\n\tv_cvt_pk_f16_f32 v70, v78, v79\n\tv_cvt_pk_f16_f32 v71, v80, v81\n\tv_cvt_pk_f16_f32 v72, v102, v103\n\tv_cvt_pk_f16_f32 v73, v104, v105\n\tv_pk_mul_f16 v78, v154, v108 op_sel:[1,1] op_sel_hi:[1,0] neg_lo:[0,1]\n\tv_pk_mul_f16 v79, v155, v109 op_sel:[1,1] op_sel_hi:[1,0] neg_lo:[0,1]\n\tv_pk_mul_f32 v[80:81], v[102:103], v[60:61] op_sel:[1,1] op_sel_hi:[1,0] neg_lo:[0,1]\n\tv_pk_mul_f32 v[82:83], v[104:105], v[62:63] op_sel:[1,1] op_sel_hi:[1,0] neg_lo:[0,1]\n\tds_write2_b64 %[b4], v[154:155], v[66:67] offset0:0 offset1:66\n\tv_pk_fma_f16 v78, v154, v108, v78 op_sel:[0,0,0] op_sel_hi:[0,1,1]\n\tv_pk_fma_f16 v79, v155, v109, v79 op_sel:[0,0,0] op_sel_hi:[0,1,1]\n\tv_pk_fma_f32 v[80:81], v[102:103], v[60:61], v[80:81] op_sel:[0,0,0] op_sel_hi:[0,1,1]\n\tv_pk_fma_f32 v[82:83], v[104:105], v[62:63], v[82:83] op_sel:[0,0,0] op_sel_hi:[0,1,1]\n\tv_pk_mul_f16 v74, v154, v106 op_sel:[1,1] op_sel_hi:[1,0] neg_lo:[0,1]\n\tv_pk_mul_f16 v75, v155, v107 op_sel:[1,1] op_sel_hi:[1,0] neg_lo:[0,1]\n\tv_pk_mul_f16 v84, v154, v64 op_sel:[1,1] op_sel_hi:[1,0] neg_lo:[0,1]\n\tv_pk_mul_f16 v85, v155, v65 op_sel:[1,1] op_sel_hi:[1,0] neg_lo:[0,1]\n\tds_write2_b64 %[b4], v[68:69], v[78:79] offset0:132 offset1:198\n\tv_pk_fma_f16 v74, v154, v106, v74 op_sel:[0,0,0] op_sel_hi:[0,1,1]\n\tv_pk_fma_f16 v75, v155, v107, v75 op_sel:[0,0,0] op_sel_hi:[0,1,1]\n\tv_pk_fma_f16 v84, v154, v64, v84 op_sel:[0,0,0] op_sel_hi:[0,1,1]\n\tv_pk_fma_f16 v85, v155, v65, v85 op_sel:[0,0,0] op_sel_hi:[0,1,1]\n\tv_cvt_pk_f16_f32 v66, v80, v81\n\tv_cvt_pk_f16_f32 v67, v82, v83\n\tv_pk_mul_f16 v80, v154, v132 op_sel:[1,1] op_sel_hi:[1,0] neg_lo:[0,1]\n\tv_pk_mul_f16 v81, v155, v133 op_sel:[1,1] op_sel_hi:[1,0] neg_lo:[0,1]\n\tv_pk_mul_f16 v82, v154, v152 op_sel:[1,1] op_sel_hi:[1,0] neg_lo:[0,1]\n\tv_pk_mul_f16 v83, v155, v153 op_sel:[1,1] op_sel_hi:[1,0] neg_lo:[0,1]\n\tds_write2_b64 %[b5], v[74:75], v[84:85] offset0:0 offset1:66\n\tv_pk_fma_f16 v80, v154, v132, v80 op_sel:[0,0,0] op_sel_hi:[0,1,1]\n\tv_pk_fma_f16 v81, v155, v133, v81 op_sel:[0,0,0] op_sel_hi:[0,1,1]\n\tv_pk_fma_f16 v82, v154, v152, v82 op_sel:[0,0,0] op_sel_hi:[0,1,1]\n\tv_pk_fma_f16 v83, v155, v153, v83 op_sel:[0,0,0] op_sel_hi:[0,1,1]\n\tv_pk_mul_f16 v64, v154, v134 op_sel:[1,1] op_sel_hi:[1,0] neg_lo:[0,1]\n\tv_pk_mul_f16 v65, v155, v135 op_sel:[1,1] op_sel_hi:[1,0] neg_lo:[0,1]\n\tv_pk_mul_f16 v68, v154, v76 op_sel:[1,1] op_sel_hi:[1,0] neg_lo:[0,1]\n\tv_pk_mul_f16 v69, v155, v77 op_sel:[1,1] op_sel_hi:[1,0] neg_lo:[0,1]\n\tds_write2_b64 %[b5], v[80:81], v[82:83] offset0:132 offset1:198\n\tv_pk_fma_f16 v64, v154, v134, v64 op_sel:[0,0,0] op_sel_hi:[0,1,1]\n\tv_pk_fma_f16 v65, v155, v135, v65 op_sel:[0,0,0] op_sel_hi:[0,1,1]\n\tv_pk_fma_f16 v68, v154, v76, v68 op_sel:[0,0,0] op_sel_hi:[0,1,1]\n\tv_pk_fma_f16 v69, v155, v77, v69 op_sel:[0,0,0] op_sel_hi:[0,1,1]\n\tv_pk_mul_f16 v74, v154, v140 op_sel:[1,1] op_sel_hi:[1,0] neg_lo:[0,1]\n\tv_pk_mul_f16 v75, v155, v141 op_sel:[1,1] op_sel_hi:[1,0] neg_lo:[0,1]\n\tv_pk_mul_f16 v78, v154, v142 op_sel:[1,1] op_sel_hi:[1,0] neg_lo:[0,1]\n\tv_pk_mul_f16 v79, v155, v143 op_sel:[1,1] op_sel_hi:[1,0] neg_lo:[0,1]\n\tds_write2_b64 %[b6], v[64:65], v[68:69] offset0:0 offset1:66\n\tv_pk_fma_f16 v74, v154, v140, v74 op_sel:[0,0,0] op_sel_hi:[0,1,1]\n\tv_pk_fma_f16 v75, v155, v141, v75 op_sel:[0,0,0] op_sel_hi:[0,1,1]\n\tv_pk_fma_f16 v78, v154, v142, v78 op_sel:[0,0,0] op_sel_hi:[0,1,1]\n\tv_pk_fma_f16 v79, v155, v143, v79 op_sel:[0,0,0] op_sel_hi:[0,1,1]\n\tv_pk_mul_f16 v76, v154, v92 op_sel:[1,1] op_sel_hi:[1,0] neg_lo:[0,1]\n\tv_pk_mul_f16 v77, v155, v93 op_sel:[1,1] op_sel_hi:[1,0] neg_lo:[0,1]\n\tv_pk_mul_f16 v80, v154, v94 op_sel:[1,1] op_sel_hi:[1,0] neg_lo:[0,1]\n\tv_pk_mul_f16 v81, v155, v95 op_sel:[1,1] op_sel_hi:[1,0] neg_lo:[0,1]\n\tds_write2_b64 %[b6], v[74:75], v[78:79] offset0:132 offset1:198\n\tv_pk_fma_f16 v76, v154, v92, v76 op_sel:[0,0,0] op_sel_hi:[0,1,1]\n\tv_pk_fma_f16 v77, v155, v93, v77 op_sel:[0,0,0] op_sel_hi:[0,1,1]\n\tv_pk_fma_f16 v80, v154, v94, v80 op_sel:[0,0,0] op_sel_hi:[0,1,1]\n\tv_pk_fma_f16 v81, v155, v95, v81 op_sel:[0,0,0] op_sel_hi:[0,1,1]\n\tv_pk_mul_f16 v64, v154, v90 op_sel:[1,1] op_sel_hi:[1,0] neg_lo:[0,1]\n\tv_pk_mul_f16 v65, v155, v91 op_sel:[1,1] op_sel_hi:[1,0] neg_lo:[0,1]\n\tv_pk_mul_f16 v68, v154, v110 op_sel:[1,1] op_sel_hi:[1,0] neg_lo:[0,1]\n\tv_pk_mul_f16 v69, v155, v111 op_sel:[1,1] op_sel_hi:[1,0] neg_lo:[0,1]\n\tds_write2_b64 %[b7], v[76:77], v[80:81] offset0:0 offset1:66\n\tv_pk_fma_f16 v64, v154, v90, v64 op_sel:[0,0,0] op_sel_hi:[0,1,1]\n\tv_pk_fma_f16 v65, v155, v91, v65 op_sel:[0,0,0] op_sel_hi:[0,1,1]\n\tv_pk_fma_f16 v68, v154, v110, v68 op_sel:[0,0,0] op_sel_hi:[0,1,1]\n\tv_pk_fma_f16 v69, v155, v111, v69 op_sel:[0,0,0] op_sel_hi:[0,1,1]\n\ts_nop 0\n\tds_write2_b64 %[b7], v[64:65], v[68:69] offset0:132 offset1:198\n\tds_write2_b64 %[b10], v[88:89], v[66:67] offset0:0 offset1:66\n\tds_write2_b64 %[b12], v[112:113], v[70:71] offset0:0 offset1:66\n\tds_write_b64 %[b13], v[72:73] offset:0"
#define KGEN_CLOBBER "v64", "v65", "v66", "v67", "v68", "v69", "v70", "v71", "v72", "v73", "v74", "v75", "v76", "v77", "v78", "v79", "v80", "v81", "v82", "v83", "v84", "v85", "v86", "v87", "v88", "v89", "v90", "v91", "v92", "v93", "v94", "v95", "v96", "v97", "v98", "v99", "v100", "v101", "v102", "v103", "v104", "v105", "v106", "v107", "v108", "v109", "v110", "v111", "v112", "v113", "v114", "v115", "v116", "v117", "v118", "v119", "v120", "v121", "v122", "v123", "v124", "v125", "v126", "v127", "v128", "v129", "v130", "v131", "v132", "v133", "v134", "v135", "v136", "v137", "v138", "v139", "v140", "v141", "v142", "v143", "v144", "v145", "v146", "v147", "v148", "v149", "v150", "v151", "v152", "v153", "v154", "v155"
#define KROW_0LO 0x2b2a2d2c23222120ull
#define KROW_0HI 0x202020202b343332ull
#define KROW_1LO 0x2524272a31302f2eull
#define KROW_1HI 0x2b2b2b2b20292826ull
#define KMFMA_ASM "ds_read_b128 v[64:67], %[ap] offset:0\n\tds_read_b128 v[68:71], %[bp] offset:0\n\tds_read_b128 v[72:75], %[ap] offset:32\n\tds_read_b128 v[76:79], %[bp] offset:32\n\tds_read_b128 v[80:83], %[ap] offset:64\n\tds_read_b128 v[84:87], %[bp] offset:64\n\tds_read_b128 v[88:91], %[ap] offset:96\n\tds_read_b128 v[92:95], %[bp] offset:96\n\ts_waitcnt lgkmcnt(6)\n\tv_mfma_f32_32x32x16_f16 v[48:63], v[64:67], v[68:71], 0\n\tds_read_b128 v[96:99], %[ap] offset:128\n\tds_read_b128 v[100:103], %[bp] offset:128\n\ts_waitcnt lgkmcnt(6)\n\tv_mfma_f32_32x32x16_f16 v[48:63], v[72:75], v[76:79], v[48:63]\n\tds_read_b128 v[104:107], %[ap] offset:160\n\tds_read_b128 v[108:111], %[bp] offset:160\n\ts_waitcnt lgkmcnt(6)\n\tv_mfma_f32_32x32x16_f16 v[48:63], v[80:83], v[84:87], v[48:63]\n\tds_read_b128 v[64:67], %[ap] offset:192\n\tds_read_b128 v[68:71], %[bp] offset:192\n\ts_waitcnt lgkmcnt(6)\n\tv_mfma_f32_32x32x16_f16 v[48:63], v[88:91], v[92:95], v[48:63]\n\tds_read_b128 v[72:75], %[ap] offset:224\n\tds_read_b128 v[76:79], %[bp] offset:224\n\ts_waitcnt lgkmcnt(6)\n\tv_mfma_f32_32x32x16_f16 v[48:63], v[96:99], v[100:103], v[48:63]\n\tds_read_b128 v[80:83], %[ap] offset:256\n\tds_read_b128 v[84:87], %[bp] offset:256\n\ts_waitcnt lgkmcnt(6)\n\tv_mfma_f32_32x32x16_f16 v[48:63], v[104:107], v[108:111], v[48:63]\n\tds_read_b128 v[88:91], %[ap] offset:288\n\tds_read_b128 v[92:95], %[bp] offset:288\n\ts_waitcnt lgkmcnt(6)\n\tv_mfma_f32_32x32x16_f16 v[48:63], v[64:67], v[68:71], v[48:63]\n\tds_read_b128 v[96:99], %[ap] offset:320\n\tds_read_b128 v[100:103], %[bp] offset:320\n\ts_waitcnt lgkmcnt(6)\n\tv_mfma_f32_32x32x16_f16 v[48:63], v[72:75], v[76:79], v[48:63]\n\tds_read_b128 v[104:107], %[ap] offset:352\n\tds_read_b128 v[108:111], %[bp] offset:352\n\ts_waitcnt lgkmcnt(6)\n\tv_mfma_f32_32x32x16_f16 v[48:63], v[80:83], v[84:87], v[48:63]\n\tds_read_b128 v[64:67], %[ap] offset:384\n\tds_read_b128 v[68:71], %[bp] offset:384\n\ts_waitcnt lgkmcnt(6)\n\tv_mfma_f32_32x32x16_f16 v[48:63], v[88:91], v[92:95], v[48:63]\n\tds_read_b128 v[72:75], %[ap] offset:416\n\tds_read_b128 v[76:79], %[bp] offset:416\n\ts_waitcnt lgkmcnt(6)\n\tv_mfma_f32_32x32x16_f16 v[48:63], v[96:99], v[100:103], v[48:63]\n\tds_read_b128 v[80:83], %[ap] offset:448\n\tds_read_b128 v[84:87], %[bp] offset:448\n\ts_waitcnt lgkmcnt(6)\n\tv_mfma_f32_32x32x16_f16 v[48:63], v[104:107], v[108:111], v[48:63]\n\tds_read_b128 v[88:91], %[ap] offset:480\n\tds_read_b128 v[92:95], %[bp] offset:480\n\ts_waitcnt lgkmcnt(6)\n\tv_mfma_f32_32x32x16_f16 v[48:63], v[64:67], v[68:71], v[48:63]\n\tds_read_b32 %[stg], %[ctl] offset:4\n\ts_waitcnt lgkmcnt(4)\n\tv_mfma_f32_32x32x16_f16 v[48:63], v[72:75], v[76:79], v[48:63]\n\ts_waitcnt lgkmcnt(2)\n\tv_mfma_f32_32x32x16_f16 v[48:63], v[80:83], v[84:87], v[48:63]\n\ts_waitcnt lgkmcnt(0)\n\tv_mfma_f32_32x32x16_f16 v[48:63], v[88:91], v[92:95], v[48:63]\n\ts_nop 7\n\ts_nop 3\n\ts_waitcnt lgkmcnt(0)"
#define KMFMA_CLOBBER "v64", "v65", "v66", "v67", "v68", "v69", "v70", "v71", "v72", "v73", "v74", "v75", "v76", "v77", "v78", "v79", "v80", "v81", "v82", "v83", "v84", "v85", "v86", "v87", "v88", "v89", "v90", "v91", "v92", "v93", "v94", "v95", "v96", "v97", "v98", "v99", "v100", "v101", "v102", "v103", "v104", "v105", "v106", "v107", "v108", "v109", "v110", "v111"
#define CONV_ASM ".p2align 3\n\tds_read_b64_tr_b16 v[52:53], %[a0] offset:456\n\tds_read_b64_tr_b16 v[54:55], %[a0] offset:448\n\tds_read_b64_tr_b16 v[84:85], %[a0] offset:968\n\tds_read_b64_tr_b16 v[86:87], %[a0] offset:960\n\tds_read_b64_tr_b16 v[48:49], %[a0] offset:392\n\tds_read_b64_tr_b16 v[50:51], %[a0] offset:384\n\tds_read_b64_tr_b16 v[80:81], %[a0] offset:904\n\tds_read_b64_tr_b16 v[82:83], %[a0] offset:896\n\tds_read_b64_tr_b16 v[44:45], %[a0] offset:328\n\tds_read_b64_tr_b16 v[46:47], %[a0] offset:320\n\tds_read_b64_tr_b16 v[76:77], %[a0] offset:840\n\tds_read_b64_tr_b16 v[78:79], %[a0] offset:832\n\tv_readfirstlane_b32 %[s], %[stg]\n\ts_mov_b32 %[c], 0x400000\n\ts_cmp_ge_u32 %[s], 4\n\ts_cbranch_scc1 2f\n\t1:\n\tds_read_b32 v216, %[ctl] offset:4\n\ts_waitcnt lgkmcnt(0)\n\tv_readfirstlane_b32 %[s], v216\n\ts_cmp_ge_u32 %[s], 4\n\ts_cbranch_scc1 2f\n\ts_sleep 1\n\ts_sub_u32 %[c], %[c], 1\n\ts_cbranch_scc0 1b\n\t.p2align 3\n\t2:\n\tds_read_b128 v[164:167], %[bb] offset:64\n\tds_read_b128 v[168:171], %[bb] offset:128\n\tds_read_b128 v[172:175], %[bb] offset:192\n\tds_read_b64_tr_b16 v[40:41], %[a0] offset:264\n\tds_read_b64_tr_b16 v[42:43], %[a0] offset:256\n\tds_read_b128 v[176:179], %[bb] offset:256\n\ts_waitcnt lgkmcnt(5)\n\ts_nop 0\n\tv_mfma_f32_16x16x32_bf16 v[132:135], v[52:55], v[164:167], 0\n\tds_read_b64_tr_b16 v[72:73], %[a0] offset:776\n\tds_read_b64_tr_b16 v[74:75], %[a0] offset:768\n\tds_read_b64_tr_b16 v[36:37], %[a0] offset:200\n\tv_mfma_f32_16x16x32_bf16 v[136:139], v[84:87], v[164:167], 0\n\tds_read_b64_tr_b16 v[38:39], %[a0] offset:192\n\ts_waitcnt lgkmcnt(8)\n\ts_nop 0\n\tv_mfma_f32_16x16x32_bf16 v[132:135], v[48:51], v[168:171], v[132:135]\n\tds_read_b128 v[180:183], %[bb] offset:320\n\tds_read_b64_tr_b16 v[68:69], %[a0] offset:712\n\tds_read_b64_tr_b16 v[70:71], %[a0] offset:704\n\tv_mfma_f32_16x16x32_bf16 v[136:139], v[80:83], v[168:171], v[136:139]\n\tds_read_b64_tr_b16 v[32:33], %[a0] offset:136\n\ts_waitcnt lgkmcnt(8)\n\ts_nop 0\n\tv_mfma_f32_16x16x32_bf16 v[132:135], v[44:47], v[172:175], v[132:135]\n\tds_read_b64_tr_b16 v[34:35], %[a0] offset:128\n\tds_read_b128 v[184:187], %[bb] offset:384\n\tds_read_b64_tr_b16 v[64:65], %[a0] offset:648\n\tv_mfma_f32_16x16x32_bf16 v[136:139], v[76:79], v[172:175], v[136:139]\n\tds_read_b64_tr_b16 v[66:67], %[a0] offset:640\n\tds_read_b64_tr_b16 v[28:29], %[a0] offset:72\n\tds_read_b64_tr_b16 v[30:31], %[a0] offset:64\n\tv_mfma_f32_16x16x32_bf16 v[132:135], v[40:43], v[176:179], v[132:135]\n\tds_read_b128 v[164:167], %[bb] offset:448\n\ts_waitcnt lgkmcnt(8)\n\ts_nop 0\n\tv_mfma_f32_16x16x32_bf16 v[136:139], v[72:75], v[176:179], v[136:139]\n\tds_read_b64_tr_b16 v[60:61], %[a0] offset:584\n\tds_read_b64_tr_b16 v[62:63], %[a0] offset:576\n\tds_read_b64_tr_b16 v[24:25], %[a0] offset:8\n\tv_mfma_f32_16x16x32_bf16 v[132:135], v[36:39], v[180:183], v[132:135]\n\tds_read_b64_tr_b16 v[26:27], %[a0] offset:0\n\tds_read_b128 v[168:171], %[bb] offset:512\n\tds_read_b64_tr_b16 v[56:57], %[a0] offset:520\n\tv_mfma_f32_16x16x32_bf16 v[136:139], v[68:71], v[180:183], v[136:139]\n\tds_read_b64_tr_b16 v[58:59], %[a0] offset:512\n\ts_waitcnt lgkmcnt(8)\n\ts_nop 0\n\tv_mfma_f32_16x16x32_bf16 v[132:135], v[32:35], v[184:187], v[132:135]\n\tds_read_b64_tr_b16 v[88:89], %[a0] offset:1032\n\tds_read_b64_tr_b16 v[90:91], %[a0] offset:1024\n\tds_read_b128 v[188:191], %[bb] offset:496\n\tv_mfma_f32_16x16x32_bf16 v[136:139], v[64:67], v[184:187], v[136:139]\n\tds_read_b128 v[172:175], %[bb] offset:576\n\ts_waitcnt lgkmcnt(8)\n\ts_nop 0\n\tv_mfma_f32_16x16x32_bf16 v[132:135], v[28:31], v[164:167], v[132:135]\n\tds_read_b128 v[176:179], %[bb] offset:640\n\tv_mfma_f32_16x16x32_bf16 v[136:139], v[60:63], v[164:167], v[136:139]\n\ts_waitcnt lgkmcnt(5)\n\ts_nop 0\n\tv_mfma_f32_16x16x32_bf16 v[132:135], v[24:27], v[168:171], v[132:135]\n\tds_write_b16 %[ta], %[t78]\n\tds_read_b128 v[180:183], %[bb] offset:704\n\tv_mfma_f32_16x16x32_bf16 v[136:139], v[56:59], v[168:171], v[136:139]\n\ts_waitcnt lgkmcnt(3)\n\ts_nop 0\n\tv_mfma_f32_16x16x32_bf16 v[140:143], v[88:91], v[168:171], 0\n\tds_write_b16_d16_hi %[ta], %[t78] offset:64\n\tds_read_b128 v[184:187], %[bb] offset:768\n\tv_mfma_f32_16x16x32_bf16 v[204:207], v[188:191], v[4:7], 0\n\tds_write_b128 %[yb], v[132:135] offset:0\n\tv_mfma_f32_16x16x32_bf16 v[218:221], v[168:171], v[4:7], 0\n\tds_write_b16 %[ta], %[t78] offset:388\n\tds_read_b128 v[164:167], %[bb] offset:832\n\tv_mfma_f32_16x16x32_bf16 v[208:211], v[188:191], v[8:11], 0\n\tds_write_b8 %[ctc], %[one] offset:0\n\tds_write_b16_d16_hi %[ta], %[t78] offset:452\n\tv_mfma_f32_16x16x32_bf16 v[222:225], v[168:171], v[8:11], 0\n\tds_write_b16 %[tb], %[t9a]\n\tv_mfma_f32_16x16x32_bf16 v[212:215], v[188:191], v[12:15], 0\n\tds_write_b16_d16_hi %[tb], %[t9a] offset:128\n\tv_cvt_pk_bf16_f32 v192, v221, v207\n\tv_mfma_f32_16x16x32_bf16 v[226:229], v[168:171], v[16:19], 0\n\tds_write_b16 %[tb], %[t9a] offset:388\n\tv_cvt_pk_bf16_f32 v193, v220, v206\n\tv_mfma_f32_16x16x32_bf16 v[148:151], v[20:23], v[168:171], 0\n\tds_write_b16_d16_hi %[tb], %[t9a] offset:516\n\tds_read_b128 v[168:171], %[bb] offset:896\n\tv_mfma_f32_16x16x32_bf16 v[136:139], v[52:55], v[172:175], v[136:139]\n\tds_read_b64 v[92:93], %[tp] offset:0\n\tds_read_b64 v[94:95], %[tp] offset:8\n\tds_read_b64 v[96:97], %[tp] offset:64\n\tv_mfma_f32_16x16x32_bf16 v[140:143], v[84:87], v[172:175], v[140:143]\n\tds_read_b128 v[172:175], %[bb] offset:960\n\ts_waitcnt lgkmcnt(7)\n\ts_nop 0\n\tv_mfma_f32_16x16x32_bf16 v[136:139], v[48:51], v[176:179], v[136:139]\n\tds_read_b64 v[98:99], %[tp] offset:72\n\tds_read_b64 v[100:101], %[tp] offset:128\n\tds_read_b64 v[102:103], %[tp] offset:136\n\tv_mfma_f32_16x16x32_bf16 v[140:143], v[80:83], v[176:179], v[140:143]\n\tds_read_b128 v[176:179], %[bb] offset:1024\n\tds_read_b64 v[104:105], %[tp] offset:768\n\tds_read_b64 v[106:107], %[tp] offset:776\n\tv_mfma_f32_16x16x32_bf16 v[136:139], v[44:47], v[180:183], v[136:139]\n\tds_read_b64 v[108:109], %[tp] offset:832\n\tds_read_b64 v[110:111], %[tp] offset:840\n\tds_read_b64 v[112:113], %[tp] offset:896\n\tv_mfma_f32_16x16x32_bf16 v[140:143], v[76:79], v[180:183], v[140:143]\n\tds_read_b64 v[114:115], %[tp] offset:904\n\tds_read_b64 v[116:117], %[tp] offset:1536\n\tds_read_b64 v[118:119], %[tp] offset:1544\n\tv_mfma_f32_16x16x32_bf16 v[136:139], v[40:43], v[184:187], v[136:139]\n\tds_read_b64 v[120:121], %[tp] offset:1600\n\tds_read_b64 v[122:123], %[tp] offset:1608\n\tv_cvt_pk_bf16_f32 v194, v219, v205\n\tv_mfma_f32_16x16x32_bf16 v[140:143], v[72:75], v[184:187], v[140:143]\n\tv_cvt_pk_bf16_f32 v195, v218, v204\n\tds_read_b64 v[124:125], %[tp] offset:1664\n\tds_read_b64 v[126:127], %[tp] offset:1672\n\tv_mfma_f32_16x16x32_bf16 v[136:139], v[36:39], v[164:167], v[136:139]\n\tds_read_b64 v[128:129], %[tp] offset:1728\n\tds_read_b64 v[130:131], %[tp] offset:1736\n\tv_cvt_pk_bf16_f32 v196, v225, v211\n\tv_mfma_f32_16x16x32_bf16 v[140:143], v[68:71], v[164:167], v[140:143]\n\tv_cvt_pk_bf16_f32 v197, v224, v210\n\ts_waitcnt lgkmcnt(6)\n\ts_nop 0\n\tv_mfma_f32_16x16x32_bf16 v[136:139], v[32:35], v[168:171], v[136:139]\n\tv_cvt_pk_bf16_f32 v198, v223, v209\n\tv_mfma_f32_16x16x32_bf16 v[140:143], v[64:67], v[168:171], v[140:143]\n\tv_cvt_pk_bf16_f32 v199, v222, v208\n\tv_mfma_f32_16x16x32_bf16 v[136:139], v[28:31], v[172:175], v[136:139]\n\tv_cvt_pk_bf16_f32 v200, v229, v215\n\tv_mfma_f32_16x16x32_bf16 v[140:143], v[60:63], v[172:175], v[140:143]\n\tds_read_b128 v[188:191], %[bb] offset:1008\n\tv_cvt_pk_bf16_f32 v201, v228, v214\n\tv_mfma_f32_16x16x32_bf16 v[136:139], v[24:27], v[176:179], v[136:139]\n\tv_cvt_pk_bf16_f32 v202, v227, v213\n\tv_mfma_f32_16x16x32_bf16 v[140:143], v[56:59], v[176:179], v[140:143]\n\tv_cvt_pk_bf16_f32 v203, v226, v212\n\tv_mfma_f32_16x16x32_bf16 v[144:147], v[88:91], v[176:179], 0\n\tv_mfma_f32_16x16x32_bf16 v[140:143], v[192:195], v[92:95], v[140:143]\n\tv_mfma_f32_16x16x32_bf16 v[144:147], v[192:195], v[96:99], v[144:147]\n\tv_mfma_f32_16x16x32_bf16 v[148:151], v[192:195], v[100:103], v[148:151]\n\tv_mfma_f32_16x16x32_bf16 v[140:143], v[196:199], v[104:107], v[140:143]\n\tds_write_b128 %[yb], v[136:139] offset:1152\n\tds_read_b128 v[180:183], %[bb] offset:1088\n\tv_mfma_f32_16x16x32_bf16 v[144:147], v[196:199], v[108:111], v[144:147]\n\tv_mfma_f32_16x16x32_bf16 v[148:151], v[196:199], v[112:115], v[148:151]\n\tds_write_b8 %[ctc], %[one] offset:64\n\tds_read_b128 v[184:187], %[bb] offset:1152\n\tv_mfma_f32_16x16x32_bf16 v[148:151], v[200:203], v[116:119], v[148:151]\n\ts_waitcnt lgkmcnt(4)\n\ts_nop 0\n\tv_mfma_f32_16x16x32_bf16 v[152:155], v[200:203], v[120:123], 0\n\tds_read_b128 v[164:167], %[bb] offset:1216\n\tv_mfma_f32_16x16x32_bf16 v[156:159], v[200:203], v[124:127], 0\n\tv_mfma_f32_16x16x32_bf16 v[160:163], v[200:203], v[128:131], 0\n\tds_read_b128 v[168:171], %[bb] offset:1280\n\tv_mfma_f32_16x16x32_bf16 v[204:207], v[188:191], v[4:7], 0\n\tv_mfma_f32_16x16x32_bf16 v[218:221], v[176:179], v[4:7], 0\n\tds_read_b128 v[172:175], %[bb] offset:1344\n\tv_mfma_f32_16x16x32_bf16 v[208:211], v[188:191], v[8:11], 0\n\tv_mfma_f32_16x16x32_bf16 v[222:225], v[176:179], v[8:11], 0\n\tv_mfma_f32_16x16x32_bf16 v[212:215], v[188:191], v[12:15], 0\n\tv_mfma_f32_16x16x32_bf16 v[226:229], v[176:179], v[16:19], 0\n\tv_mfma_f32_16x16x32_bf16 v[152:155], v[20:23], v[176:179], v[152:155]\n\ts_waitcnt lgkmcnt(0)\n\ts_nop 0\n\tv_mfma_f32_16x16x32_bf16 v[140:143], v[52:55], v[180:183], v[140:143]\n\tds_read_b128 v[176:179], %[bb] offset:1408\n\tv_cvt_pk_bf16_f32 v192, v221, v207\n\tv_mfma_f32_16x16x32_bf16 v[144:147], v[84:87], v[180:183], v[144:147]\n\tds_read_b128 v[180:183], %[bb] offset:1472\n\tv_cvt_pk_bf16_f32 v193, v220, v206\n\tv_mfma_f32_16x16x32_bf16 v[140:143], v[48:51], v[184:187], v[140:143]\n\tv_cvt_pk_bf16_f32 v194, v219, v205\n\tv_mfma_f32_16x16x32_bf16 v[144:147], v[80:83], v[184:187], v[144:147]\n\tds_read_b128 v[184:187], %[bb] offset:1536\n\tv_cvt_pk_bf16_f32 v195, v218, v204\n\tv_mfma_f32_16x16x32_bf16 v[140:143], v[44:47], v[164:167], v[140:143]\n\tv_cvt_pk_bf16_f32 v196, v225, v211\n\tv_mfma_f32_16x16x32_bf16 v[144:147], v[76:79], v[164:167], v[144:147]\n\tv_cvt_pk_bf16_f32 v197, v224, v210\n\tv_mfma_f32_16x16x32_bf16 v[140:143], v[40:43], v[168:171], v[140:143]\n\tv_cvt_pk_bf16_f32 v198, v223, v209\n\tv_mfma_f32_16x16x32_bf16 v[144:147], v[72:75], v[168:171], v[144:147]\n\tv_cvt_pk_bf16_f32 v199, v222, v208\n\tv_mfma_f32_16x16x32_bf16 v[140:143], v[36:39], v[172:175], v[140:143]\n\tv_cvt_pk_bf16_f32 v200, v229, v215\n\tv_mfma_f32_16x16x32_bf16 v[144:147], v[68:71], v[172:175], v[144:147]\n\ts_waitcnt lgkmcnt(0)\n\ts_nop 0\n\tv_mfma_f32_16x16x32_bf16 v[140:143], v[32:35], v[176:179], v[140:143]\n\tv_cvt_pk_bf16_f32 v201, v228, v214\n\tv_mfma_f32_16x16x32_bf16 v[144:147], v[64:67], v[176:179], v[144:147]\n\tv_cvt_pk_bf16_f32 v202, v227, v213\n\tv_mfma_f32_16x16x32_bf16 v[140:143], v[28:31], v[180:183], v[140:143]\n\tds_read_b128 v[188:191], %[bb] offset:1520\n\tv_cvt_pk_bf16_f32 v203, v226, v212\n\tv_mfma_f32_16x16x32_bf16 v[144:147], v[60:63], v[180:183], v[144:147]\n\tv_mfma_f32_16x16x32_bf16 v[140:143], v[24:27], v[184:187], v[140:143]\n\tv_mfma_f32_16x16x32_bf16 v[144:147], v[56:59], v[184:187], v[144:147]\n\tv_mfma_f32_16x16x32_bf16 v[148:151], v[88:91], v[184:187], v[148:151]\n\tv_mfma_f32_16x16x32_bf16 v[144:147], v[192:195], v[92:95], v[144:147]\n\tv_mfma_f32_16x16x32_bf16 v[148:151], v[192:195], v[96:99], v[148:151]\n\tv_mfma_f32_16x16x32_bf16 v[152:155], v[192:195], v[100:103], v[152:155]\n\tds_read_b128 v[164:167], %[bb] offset:1600\n\tv_mfma_f32_16x16x32_bf16 v[144:147], v[196:199], v[104:107], v[144:147]\n\tv_mfma_f32_16x16x32_bf16 v[148:151], v[196:199], v[108:111], v[148:151]\n\tds_write_b128 %[yb], v[140:143] offset:2304\n\tv_mfma_f32_16x16x32_bf16 v[152:155], v[196:199], v[112:115], v[152:155]\n\tds_read_b128 v[168:171], %[bb] offset:1664\n\tv_mfma_f32_16x16x32_bf16 v[152:155], v[200:203], v[116:119], v[152:155]\n\tds_write_b8 %[ctc], %[one] offset:128\n\tds_read_b128 v[172:175], %[bb] offset:1728\n\tv_mfma_f32_16x16x32_bf16 v[156:159], v[200:203], v[120:123], v[156:159]\n\tv_mfma_f32_16x16x32_bf16 v[160:163], v[200:203], v[124:127], v[160:163]\n\ts_waitcnt lgkmcnt(3)\n\ts_nop 0\n\tv_mfma_f32_16x16x32_bf16 v[204:207], v[188:191], v[4:7], 0\n\tds_read_b128 v[176:179], %[bb] offset:1792\n\tv_mfma_f32_16x16x32_bf16 v[218:221], v[184:187], v[4:7], 0\n\tds_read_b128 v[180:183], %[bb] offset:1856\n\tv_mfma_f32_16x16x32_bf16 v[208:211], v[188:191], v[8:11], 0\n\tv_mfma_f32_16x16x32_bf16 v[222:225], v[184:187], v[8:11], 0\n\tv_mfma_f32_16x16x32_bf16 v[212:215], v[188:191], v[12:15], 0\n\tv_mfma_f32_16x16x32_bf16 v[226:229], v[184:187], v[16:19], 0\n\tv_mfma_f32_16x16x32_bf16 v[156:159], v[20:23], v[184:187], v[156:159]\n\tds_read_b128 v[184:187], %[bb] offset:1920\n\tv_mfma_f32_16x16x32_bf16 v[144:147], v[52:55], v[164:167], v[144:147]\n\tv_cvt_pk_bf16_f32 v192, v221, v207\n\tv_mfma_f32_16x16x32_bf16 v[148:151], v[84:87], v[164:167], v[148:151]\n\ts_waitcnt lgkmcnt(1)\n\ts_nop 0\n\tv_mfma_f32_16x16x32_bf16 v[144:147], v[48:51], v[168:171], v[144:147]\n\tds_read_b128 v[164:167], %[bb] offset:1984\n\tv_cvt_pk_bf16_f32 v193, v220, v206\n\tv_mfma_f32_16x16x32_bf16 v[148:151], v[80:83], v[168:171], v[148:151]\n\tds_read_b128 v[168:171], %[bb] offset:2048\n\tv_cvt_pk_bf16_f32 v194, v219, v205\n\tv_mfma_f32_16x16x32_bf16 v[144:147], v[44:47], v[172:175], v[144:147]\n\tds_read_b128 v[188:191], %[bb] offset:2032\n\tv_cvt_pk_bf16_f32 v195, v218, v204\n\tv_mfma_f32_16x16x32_bf16 v[148:151], v[76:79], v[172:175], v[148:151]\n\tv_cvt_pk_bf16_f32 v196, v225, v211\n\tv_mfma_f32_16x16x32_bf16 v[144:147], v[40:43], v[176:179], v[144:147]\n\tv_cvt_pk_bf16_f32 v197, v224, v210\n\tv_mfma_f32_16x16x32_bf16 v[148:151], v[72:75], v[176:179], v[148:151]\n\tv_cvt_pk_bf16_f32 v198, v223, v209\n\tv_mfma_f32_16x16x32_bf16 v[144:147], v[36:39], v[180:183], v[144:147]\n\tv_cvt_pk_bf16_f32 v199, v222, v208\n\tv_mfma_f32_16x16x32_bf16 v[148:151], v[68:71], v[180:183], v[148:151]\n\ts_waitcnt lgkmcnt(0)\n\ts_nop 0\n\tv_mfma_f32_16x16x32_bf16 v[144:147], v[32:35], v[184:187], v[144:147]\n\tv_cvt_pk_bf16_f32 v200, v229, v215\n\tv_mfma_f32_16x16x32_bf16 v[148:151], v[64:67], v[184:187], v[148:151]\n\tv_cvt_pk_bf16_f32 v201, v228, v214\n\tv_mfma_f32_16x16x32_bf16 v[144:147], v[28:31], v[164:167], v[144:147]\n\tv_cvt_pk_bf16_f32 v202, v227, v213\n\tv_mfma_f32_16x16x32_bf16 v[148:151], v[60:63], v[164:167], v[148:151]\n\tv_cvt_pk_bf16_f32 v203, v226, v212\n\tv_mfma_f32_16x16x32_bf16 v[144:147], v[24:27], v[168:171], v[144:147]\n\tv_mfma_f32_16x16x32_bf16 v[148:151], v[56:59], v[168:171], v[148:151]\n\tv_mfma_f32_16x16x32_bf16 v[152:155], v[88:91], v[168:171], v[152:155]\n\tv_mfma_f32_16x16x32_bf16 v[204:207], v[188:191], v[4:7], 0\n\tv_mfma_f32_16x16x32_bf16 v[218:221], v[168:171], v[4:7], 0\n\tv_mfma_f32_16x16x32_bf16 v[208:211], v[188:191], v[8:11], 0\n\tv_mfma_f32_16x16x32_bf16 v[222:225], v[168:171], v[8:11], 0\n\tv_mfma_f32_16x16x32_bf16 v[212:215], v[188:191], v[12:15], 0\n\tv_mfma_f32_16x16x32_bf16 v[226:229], v[168:171], v[16:19], 0\n\tds_write_b128 %[yb], v[144:147] offset:3456\n\tv_mfma_f32_16x16x32_bf16 v[160:163], v[20:23], v[168:171], v[160:163]\n\tds_read_b32 v216, %[ctl] offset:4\n\tv_mfma_f32_16x16x32_bf16 v[148:151], v[192:195], v[92:95], v[148:151]\n\tds_write_b8 %[ctc], %[one] offset:192\n\tv_mfma_f32_16x16x32_bf16 v[152:155], v[192:195], v[96:99], v[152:155]\n\tv_mfma_f32_16x16x32_bf16 v[156:159], v[192:195], v[100:103], v[156:159]\n\tv_mfma_f32_16x16x32_bf16 v[148:151], v[196:199], v[104:107], v[148:151]\n\tv_cvt_pk_bf16_f32 v192, v221, v207\n\tv_mfma_f32_16x16x32_bf16 v[152:155], v[196:199], v[108:111], v[152:155]\n\tv_cvt_pk_bf16_f32 v193, v220, v206\n\tv_mfma_f32_16x16x32_bf16 v[156:159], v[196:199], v[112:115], v[156:159]\n\tv_cvt_pk_bf16_f32 v194, v219, v205\n\tv_mfma_f32_16x16x32_bf16 v[156:159], v[200:203], v[116:119], v[156:159]\n\tv_cvt_pk_bf16_f32 v195, v218, v204\n\tv_mfma_f32_16x16x32_bf16 v[160:163], v[200:203], v[120:123], v[160:163]\n\ts_waitcnt lgkmcnt(0)\n\ts_nop 0\n\tv_readfirstlane_b32 %[s], v216\n\ts_mov_b32 %[c], 0x400000\n\ts_cmp_ge_u32 %[s], 8\n\ts_cbranch_scc1 2f\n\t1:\n\tds_read_b32 v216, %[ctl] offset:4\n\ts_waitcnt lgkmcnt(0)\n\tv_readfirstlane_b32 %[s], v216\n\ts_cmp_ge_u32 %[s], 8\n\ts_cbranch_scc1 2f\n\ts_sleep 1\n\ts_sub_u32 %[c], %[c], 1\n\ts_cbranch_scc0 1b\n\t.p2align 3\n\t2:\n\tv_mfma_f32_16x16x32_bf16 v[152:155], v[192:195], v[92:95], v[152:155]\n\tds_read_b128 v[172:175], %[bb] offset:2112\n\tv_cvt_pk_bf16_f32 v196, v225, v211\n\tv_mfma_f32_16x16x32_bf16 v[156:159], v[192:195], v[96:99], v[156:159]\n\tds_read_b128 v[176:179], %[bb] offset:2176\n\tv_cvt_pk_bf16_f32 v197, v224, v210\n\tv_mfma_f32_16x16x32_bf16 v[160:163], v[192:195], v[100:103], v[160:163]\n\tv_cvt_pk_bf16_f32 v198, v223, v209\n\tds_read_b128 v[180:183], %[bb] offset:2240\n\tv_cvt_pk_bf16_f32 v199, v222, v208\n\ts_nop 1\n\ts_nop 0\n\tv_mfma_f32_16x16x32_bf16 v[152:155], v[196:199], v[104:107], v[152:155]\n\tds_read_b128 v[184:187], %[bb] offset:2304\n\tv_cvt_pk_bf16_f32 v200, v229, v215\n\tv_mfma_f32_16x16x32_bf16 v[156:159], v[196:199], v[108:111], v[156:159]\n\tv_cvt_pk_bf16_f32 v201, v228, v214\n\tds_read_b128 v[164:167], %[bb] offset:2368\n\tv_mfma_f32_16x16x32_bf16 v[160:163], v[196:199], v[112:115], v[160:163]\n\tv_cvt_pk_bf16_f32 v202, v227, v213\n\tds_read_b128 v[168:171], %[bb] offset:2432\n\tv_cvt_pk_bf16_f32 v203, v226, v212\n\ts_nop 1\n\ts_nop 0\n\tv_mfma_f32_16x16x32_bf16 v[160:163], v[200:203], v[116:119], v[160:163]\n\ts_waitcnt lgkmcnt(3)\n\ts_nop 0\n\tv_mfma_f32_16x16x32_bf16 v[148:151], v[52:55], v[172:175], v[148:151]\n\tv_mfma_f32_16x16x32_bf16 v[152:155], v[84:87], v[172:175], v[152:155]\n\tds_read_b128 v[172:175], %[bb] offset:2496\n\tv_mfma_f32_16x16x32_bf16 v[148:151], v[48:51], v[176:179], v[148:151]\n\tv_mfma_f32_16x16x32_bf16 v[152:155], v[80:83], v[176:179], v[152:155]\n\tds_read_b128 v[176:179], %[bb] offset:2560\n\tv_mfma_f32_16x16x32_bf16 v[148:151], v[44:47], v[180:183], v[148:151]\n\tds_read_b128 v[188:191], %[bb] offset:2544\n\tv_mfma_f32_16x16x32_bf16 v[152:155], v[76:79], v[180:183], v[152:155]\n\ts_waitcnt lgkmcnt(2)\n\ts_nop 0\n\tv_mfma_f32_16x16x32_bf16 v[148:151], v[40:43], v[184:187], v[148:151]\n\tv_mfma_f32_16x16x32_bf16 v[152:155], v[72:75], v[184:187], v[152:155]\n\tds_read_b128 v[180:183], %[bb] offset:2624\n\tv_mfma_f32_16x16x32_bf16 v[148:151], v[36:39], v[164:167], v[148:151]\n\tv_mfma_f32_16x16x32_bf16 v[152:155], v[68:71], v[164:167], v[152:155]\n\tds_read_b128 v[184:187], %[bb] offset:2688\n\tv_mfma_f32_16x16x32_bf16 v[148:151], v[32:35], v[168:171], v[148:151]\n\tv_mfma_f32_16x16x32_bf16 v[152:155], v[64:67], v[168:171], v[152:155]\n\tds_read_b128 v[164:167], %[bb] offset:2752\n\tv_mfma_f32_16x16x32_bf16 v[148:151], v[28:31], v[172:175], v[148:151]\n\tv_mfma_f32_16x16x32_bf16 v[152:155], v[60:63], v[172:175], v[152:155]\n\ts_waitcnt lgkmcnt(1)\n\ts_nop 0\n\tv_mfma_f32_16x16x32_bf16 v[148:151], v[24:27], v[176:179], v[148:151]\n\tds_read_b128 v[168:171], %[bb] offset:2816\n\tv_mfma_f32_16x16x32_bf16 v[152:155], v[56:59], v[176:179], v[152:155]\n\tds_read_b128 v[172:175], %[bb] offset:2880\n\tv_mfma_f32_16x16x32_bf16 v[156:159], v[88:91], v[176:179], v[156:159]\n\tv_mfma_f32_16x16x32_bf16 v[204:207], v[188:191], v[4:7], 0\n\tv_mfma_f32_16x16x32_bf16 v[218:221], v[176:179], v[4:7], 0\n\tv_mfma_f32_16x16x32_bf16 v[208:211], v[188:191], v[8:11], 0\n\tv_mfma_f32_16x16x32_bf16 v[222:225], v[176:179], v[8:11], 0\n\tds_write_b128 %[yb], v[148:151] offset:4608\n\tv_mfma_f32_16x16x32_bf16 v[152:155], v[52:55], v[180:183], v[152:155]\n\tds_read_b128 v[176:179], %[bb] offset:2944\n\tv_mfma_f32_16x16x32_bf16 v[156:159], v[84:87], v[180:183], v[156:159]\n\tds_write_b8 %[ctc], %[one] offset:256\n\tds_read_b128 v[180:183], %[bb] offset:3008\n\tv_mfma_f32_16x16x32_bf16 v[152:155], v[48:51], v[184:187], v[152:155]\n\tv_cvt_pk_bf16_f32 v192, v221, v207\n\tv_mfma_f32_16x16x32_bf16 v[156:159], v[80:83], v[184:187], v[156:159]\n\ts_waitcnt lgkmcnt(3)\n\ts_nop 0\n\tv_mfma_f32_16x16x32_bf16 v[152:155], v[44:47], v[164:167], v[152:155]\n\tds_read_b128 v[184:187], %[bb] offset:3072\n\tv_cvt_pk_bf16_f32 v193, v220, v206\n\tv_mfma_f32_16x16x32_bf16 v[156:159], v[76:79], v[164:167], v[156:159]\n\tv_cvt_pk_bf16_f32 v194, v219, v205\n\tv_mfma_f32_16x16x32_bf16 v[152:155], v[40:43], v[168:171], v[152:155]\n\tv_cvt_pk_bf16_f32 v195, v218, v204\n\tv_mfma_f32_16x16x32_bf16 v[156:159], v[72:75], v[168:171], v[156:159]\n\tds_read_b128 v[188:191], %[bb] offset:3056\n\tv_cvt_pk_bf16_f32 v196, v225, v211\n\tv_mfma_f32_16x16x32_bf16 v[152:155], v[36:39], v[172:175], v[152:155]\n\tv_cvt_pk_bf16_f32 v197, v224, v210\n\tv_mfma_f32_16x16x32_bf16 v[156:159], v[68:71], v[172:175], v[156:159]\n\ts_waitcnt lgkmcnt(1)\n\ts_nop 0\n\tv_mfma_f32_16x16x32_bf16 v[152:155], v[32:35], v[176:179], v[152:155]\n\tv_cvt_pk_bf16_f32 v198, v223, v209\n\tv_mfma_f32_16x16x32_bf16 v[156:159], v[64:67], v[176:179], v[156:159]\n\tds_read_b128 v[164:167], %[bb] offset:3136\n\tv_cvt_pk_bf16_f32 v199, v222, v208\n\tv_mfma_f32_16x16x32_bf16 v[152:155], v[28:31], v[180:183], v[152:155]\n\tv_mfma_f32_16x16x32_bf16 v[156:159], v[60:63], v[180:183], v[156:159]\n\tds_read_b128 v[168:171], %[bb] offset:3200\n\tv_mfma_f32_16x16x32_bf16 v[152:155], v[24:27], v[184:187], v[152:155]\n\tv_mfma_f32_16x16x32_bf16 v[156:159], v[56:59], v[184:187], v[156:159]\n\tds_read_b128 v[172:175], %[bb] offset:3264\n\tv_mfma_f32_16x16x32_bf16 v[160:163], v[88:91], v[184:187], v[160:163]\n\tv_mfma_f32_16x16x32_bf16 v[156:159], v[192:195], v[92:95], v[156:159]\n\tds_read_b128 v[176:179], %[bb] offset:3328\n\tv_mfma_f32_16x16x32_bf16 v[160:163], v[192:195], v[96:99], v[160:163]\n\tv_mfma_f32_16x16x32_bf16 v[156:159], v[196:199], v[104:107], v[156:159]\n\tds_read_b128 v[180:183], %[bb] offset:3392\n\tv_mfma_f32_16x16x32_bf16 v[160:163], v[196:199], v[108:111], v[160:163]\n\ts_waitcnt lgkmcnt(2)\n\ts_nop 0\n\tv_mfma_f32_16x16x32_bf16 v[204:207], v[188:191], v[4:7], 0\n\tds_write_b128 %[yb], v[152:155] offset:5760\n\tv_mfma_f32_16x16x32_bf16 v[218:221], v[184:187], v[4:7], 0\n\tv_mfma_f32_16x16x32_bf16 v[208:211], v[188:191], v[8:11], 0\n\tds_write_b8 %[ctc], %[one] offset:320\n\tv_mfma_f32_16x16x32_bf16 v[222:225], v[184:187], v[8:11], 0\n\tds_read_b128 v[184:187], %[bb] offset:3456\n\tv_mfma_f32_16x16x32_bf16 v[156:159], v[52:55], v[164:167], v[156:159]\n\tv_mfma_f32_16x16x32_bf16 v[160:163], v[84:87], v[164:167], v[160:163]\n\tds_read_b128 v[164:167], %[bb] offset:3520\n\tv_mfma_f32_16x16x32_bf16 v[156:159], v[48:51], v[168:171], v[156:159]\n\tv_cvt_pk_bf16_f32 v192, v221, v207\n\tv_mfma_f32_16x16x32_bf16 v[160:163], v[80:83], v[168:171], v[160:163]\n\tds_read_b128 v[168:171], %[bb] offset:3584\n\tv_cvt_pk_bf16_f32 v193, v220, v206\n\tv_mfma_f32_16x16x32_bf16 v[156:159], v[44:47], v[172:175], v[156:159]\n\tv_cvt_pk_bf16_f32 v194, v219, v205\n\tv_mfma_f32_16x16x32_bf16 v[160:163], v[76:79], v[172:175], v[160:163]\n\ts_waitcnt lgkmcnt(1)\n\ts_nop 0\n\tv_mfma_f32_16x16x32_bf16 v[156:159], v[40:43], v[176:179], v[156:159]\n\tds_read_b128 v[172:175], %[bb] offset:3648\n\tglobal_load_dwordx2 v[240:241], %[so], %[ws] sc1\n\tv_mfma_f32_16x16x32_bf16 v[160:163], v[72:75], v[176:179], v[160:163]\n\tds_read_b128 v[176:179], %[bb] offset:3712\n\tv_cvt_pk_bf16_f32 v195, v218, v204\n\tv_mfma_f32_16x16x32_bf16 v[156:159], v[36:39], v[180:183], v[156:159]\n\tv_cvt_pk_bf16_f32 v196, v225, v211\n\tv_mfma_f32_16x16x32_bf16 v[160:163], v[68:71], v[180:183], v[160:163]\n\tds_read_b128 v[180:183], %[bb] offset:3776\n\tv_cvt_pk_bf16_f32 v197, v224, v210\n\tv_mfma_f32_16x16x32_bf16 v[156:159], v[32:35], v[184:187], v[156:159]\n\tv_cvt_pk_bf16_f32 v198, v223, v209\n\tv_mfma_f32_16x16x32_bf16 v[160:163], v[64:67], v[184:187], v[160:163]\n\tds_read_b128 v[184:187], %[bb] offset:3840\n\tv_cvt_pk_bf16_f32 v199, v222, v208\n\tv_mfma_f32_16x16x32_bf16 v[156:159], v[28:31], v[164:167], v[156:159]\n\tv_mfma_f32_16x16x32_bf16 v[160:163], v[60:63], v[164:167], v[160:163]\n\ts_waitcnt lgkmcnt(1)\n\ts_nop 0\n\tv_mfma_f32_16x16x32_bf16 v[156:159], v[24:27], v[168:171], v[156:159]\n\tds_read_b128 v[164:167], %[bb] offset:3904\n\tv_mfma_f32_16x16x32_bf16 v[160:163], v[56:59], v[168:171], v[160:163]\n\tds_read_b128 v[168:171], %[bb] offset:3968\n\tv_mfma_f32_16x16x32_bf16 v[160:163], v[192:195], v[92:95], v[160:163]\n\tv_mfma_f32_16x16x32_bf16 v[160:163], v[196:199], v[104:107], v[160:163]\n\tv_mfma_f32_16x16x32_bf16 v[160:163], v[52:55], v[172:175], v[160:163]\n\tds_read_b128 v[172:175], %[bb] offset:4032\n\tv_mfma_f32_16x16x32_bf16 v[160:163], v[48:51], v[176:179], v[160:163]\n\tds_write_b128 %[yb], v[156:159] offset:6912\n\tv_mfma_f32_16x16x32_bf16 v[160:163], v[44:47], v[180:183], v[160:163]\n\ts_waitcnt lgkmcnt(2)\n\ts_nop 0\n\tv_mfma_f32_16x16x32_bf16 v[160:163], v[40:43], v[184:187], v[160:163]\n\tds_write_b8 %[ctc], %[one] offset:384\n\tds_read_b128 v[176:179], %[bb] offset:4096\n\tv_mfma_f32_16x16x32_bf16 v[160:163], v[36:39], v[164:167], v[160:163]\n\tv_mfma_f32_16x16x32_bf16 v[160:163], v[32:35], v[168:171], v[160:163]\n\ts_waitcnt lgkmcnt(2)\n\ts_nop 0\n\tv_mfma_f32_16x16x32_bf16 v[160:163], v[28:31], v[172:175], v[160:163]\n\ts_waitcnt lgkmcnt(0)\n\ts_nop 0\n\tv_mfma_f32_16x16x32_bf16 v[160:163], v[24:27], v[176:179], v[160:163]\n\ts_nop 7\n\tds_write_b128 %[yb], v[160:163] offset:8064\n\tds_write_b8 %[ctc], %[one] offset:448"
#define CONV_CLOBBER "v24", "v25", "v26", "v27", "v28", "v29", "v30", "v31", "v32", "v33", "v34", "v35", "v36", "v37", "v38", "v39", "v40", "v41", "v42", "v43", "v44", "v45", "v46", "v47", "v48", "v49", "v50", "v51", "v52", "v53", "v54", "v55", "v56", "v57", "v58", "v59", "v60", "v61", "v62", "v63", "v64", "v65", "v66", "v67", "v68", "v69", "v70", "v71", "v72", "v73", "v74", "v75", "v76", "v77", "v78", "v79", "v80", "v81", "v82", "v83", "v84", "v85", "v86", "v87", "v88", "v89", "v90", "v91", "v92", "v93", "v94", "v95", "v96", "v97", "v98", "v99", "v100", "v101", "v102", "v103", "v104", "v105", "v106", "v107", "v108", "v109", "v110", "v111", "v112", "v113", "v114", "v115", "v116", "v117", "v118", "v119", "v120", "v121", "v122", "v123", "v124", "v125", "v126", "v127", "v128", "v129", "v130", "v131", "v132", "v133", "v134", "v135", "v136", "v137", "v138", "v139", "v140", "v141", "v142", "v143", "v144", "v145", "v146", "v147", "v148", "v149", "v150", "v151", "v152", "v153", "v154", "v155", "v156", "v157", "v158", "v159", "v160", "v161", "v162", "v163", "v164", "v165", "v166", "v167", "v168", "v169", "v170", "v171", "v172", "v173", "v174", "v175", "v176", "v177", "v178", "v179", "v180", "v181", "v182", "v183", "v184", "v185", "v186", "v187", "v188", "v189", "v190", "v191", "v192", "v193", "v194", "v195", "v196", "v197", "v198", "v199", "v200", "v201", "v202", "v203", "v204", "v205", "v206", "v207", "v208", "v209", "v210", "v211", "v212", "v213", "v214", "v215", "v216", "v217", "v218", "v219", "v220", "v221", "v222", "v223", "v224", "v225", "v226", "v227", "v228", "v229"

typedef __attribute__((address_space(4))) const Params* KArg;
#define GET_PARAMS() KArg kp = (KArg)__builtin_amdgcn_kernarg_segment_ptr(); asm volatile("" : "+s"(kp)); const Params p = {kp->x, kp->Are, kp->Aim, kp->Bre, kp->Bim, kp->Cre, kp->Cim, kp->logdt, kp->dskip, kp->wr, kp->wg, kp->wu, kp->wd, kp->gw, kp->out, kp->ws}; \
                     asm volatile("s_waitcnt lgkmcnt(0)" :: "s"(kw0), "s"(kw1)); STAMP0_FIX()
#define ROLE_END() do { STAMP_FLUSH(); __builtin_amdgcn_s_waitcnt(0x0070); asm volatile("s_endpgm" ::: "memory"); } while (0)
#define MF(A_, B_, C_) __builtin_amdgcn_mfma_f32_16x16x32_bf16(A_, B_, C_, 0, 0, 0)
#define GRP(mask, n) __builtin_amdgcn_sched_group_barrier(mask, n, 0)

__global__ __launch_bounds__(512, 2) void fused_kernel(Params p_unused) {
    u32 kw0, kw1;
    u64 pc0;
    asm volatile("s_getpc_b64 %0" : "=s"(pc0));
    pc0 &= ~(u64)255;
    asm volatile("s_load_dword %0, %2, 0x0\n\ts_load_dword %1, %2, 0x40" : "=&s"(kw0), "=&s"(kw1) : "s"(__builtin_amdgcn_kernarg_segment_ptr()));
    extern __shared__ __attribute__((aligned(16))) char smem_[];
    LAS char* smem = (LAS char*)smem_;
    const int tid = threadIdx.x, lane = tid & 63;
    const int wave = __builtin_amdgcn_readfirstlane(tid >> 6);
    const int b = blockIdx.x, xcd = b & 7, slot = b >> 3;

    if (slot < 4) glds16((const char*)(GAS const char*)pc0 + min(slot * 8 + wave, CODE_KB - 1) * 1024 + lane * 16, (u32)(uintptr_t)(smem + LDS_DUMMY), false);
    const int cw = slot < 16 ? xcd * 16 + slot : 128 + xcd * 2 + (slot - 16);
    const int chbase = cw * 4;
    const int w = xcd * 9 + (slot - 18);
    if (slot >= 18) {
    if (slot >= 27 || wave >= 6) {
    if (slot < 28) {
    if (slot == 27)
    {
        GET_PARAMS();
        const u32 tag = __builtin_amdgcn_readfirstlane(__hip_atomic_load((GAS u32*)p.ws, RLX_AGENT)) + 1u;
        if (tid < 128) ((LAS u32*)smem)[tid] = 0;
        u32 ga = (u32)(uintptr_t)(smem + (lane & 15) * 4), gv = (u32)(uintptr_t)(smem + (lane >> 2) * 4), gq_ = (u32)(uintptr_t)(smem + (lane & 3) * 16), zero = (u32)(uintptr_t)smem;
        u32 ro = OFF_RG + 4096 + tid * 16 , so = OFF_SEL + ((lane & 1) * 8 + xcd) * 256, xo = (lane & 3) * 128;
        asm volatile("" : "+v"(ga), "+v"(gv), "+v"(gq_), "+v"(zero), "+v"(ro), "+v"(so), "+v"(xo));
        if (TOUCH) {
            const u32 to = wave < 2 ? OFF_RG + (xcd * 16 + wave * 8 + (lane & 7)) * 128 : (wave < 4 ? OFF_G + (xcd * 16 + (wave - 2) * 8 + (lane & 7)) * 128 : OFF_SEL + ((lane & 1) * 8 + xcd) * 256);
            asm volatile("s_mov_b32 m0, 0x8000\n\ts_nop 0\n\tglobal_load_lds_dword %0, %1" :: "v"(to), "s"(p.ws) : "memory");
        }
        BAR();
        __builtin_amdgcn_s_sleep(SELWAIT);
        v4u qa, qb;
        u32 c_;
#define POLL_A "global_load_dwordx4 v[20:23], %[ro], %[ws] offset:-4096 sc1\n\tglobal_load_dwordx4 v[24:27], %[r2], %[ws] sc1\n\t"
#define POLL_B "global_load_dwordx4 v[28:31], %[ro], %[ws] offset:-4096 sc1\n\tglobal_load_dwordx4 v[32:35], %[r2], %[ws] sc1\n\t"
        asm volatile("s_mov_b32 %[c], 0x40000\n\t" POLL_A "s_sleep 8\n\t" POLL_B
                     "1:\n\ts_waitcnt vmcnt(2)\n\tv_cmpx_eq_u32 %[tag], v21\n\tv_cmpx_eq_u32 %[tag], v23\n\tv_cmpx_eq_u32 %[tag], v25\n\tv_cmpx_eq_u32 %[tag], v27\n\t"
                     "s_cmp_eq_u64 exec, -1\n\ts_mov_b64 exec, -1\n\ts_cbranch_scc1 3f\n\t" POLL_A
                     "s_waitcnt vmcnt(2)\n\tv_cmpx_eq_u32 %[tag], v29\n\tv_cmpx_eq_u32 %[tag], v31\n\tv_cmpx_eq_u32 %[tag], v33\n\tv_cmpx_eq_u32 %[tag], v35\n\t"
                     "s_cmp_eq_u64 exec, -1\n\ts_mov_b64 exec, -1\n\ts_cbranch_scc1 2f\n\t" POLL_B
                     "s_sub_u32 %[c], %[c], 1\n\ts_cbranch_scc0 1b\n\ts_waitcnt vmcnt(0)\n"
                     "2:\n\tv_mov_b32 v36, v28\n\tv_mov_b32 v37, v30\n\tv_mov_b32 v38, v32\n\tv_mov_b32 v39, v34\n\ts_branch 4f\n"
                     "3:\n\tv_mov_b32 v36, v20\n\tv_mov_b32 v37, v22\n\tv_mov_b32 v38, v24\n\tv_mov_b32 v39, v26\n"
                     "4:"
                     : "=&{v[36:39]}"(qa), [c] "=&s"(c_) : [ro] "v"(ro), [r2] "v"(ro + 4096), [ws] "s"(p.ws), [tag] "s"(tag)
                     : "memory", "scc", "vcc", "v20", "v21", "v22", "v23", "v24", "v25", "v26", "v27", "v28", "v29", "v30", "v31", "v32", "v33", "v34", "v35");
        asm volatile("" : "=&{v[20:23]}"(qb));
        v4u qc, qd, qe;
        asm volatile("" : "=&{v[24:27]}"(qc), "=&{v[28:31]}"(qd), "=&{v[32:35]}"(qe));
        STAMP(2);
        u32 rank, tau, cnt;
        u64 m0_, m1_, m2_, m3_;
        {
            u32 v, nv; v4u q4;
            asm volatile("ds_max_u32 %0, %1\n\ts_waitcnt lgkmcnt(0)\n\ts_barrier" :: "v"(ga), "v"(max(max(qa.x, qa.y), max(qa.z, qa.w))) : "memory");
#define RANK16(off) asm volatile("ds_read_b32 %[v], %[gv] offset:" #off "\n\tds_read_b128 v[40:43], %[gq] offset:" #off "\n\tds_read_b32 %[n], %[z] offset:64\n\tv_mov_b32 %[r], 0\n\ts_waitcnt lgkmcnt(0)\n\t" \
                         "v_cmp_gt_u32 %[a], v40, %[v]\n\tv_cmp_gt_u32 %[b], v41, %[v]\n\tv_cmp_gt_u32 %[c], v42, %[v]\n\tv_cmp_gt_u32 %[d], v43, %[v]\n\t" \
                         "v_addc_co_u32 %[r], vcc, 0, %[r], %[a]\n\tv_addc_co_u32 %[r], vcc, 0, %[r], %[b]\n\tv_addc_co_u32 %[r], vcc, 0, %[r], %[c]\n\tv_addc_co_u32 %[r], vcc, 0, %[r], %[d]\n\ts_nop 1\n\t" \
                         "v_add_u32_dpp %[r], %[r], %[r] quad_perm:[1,0,3,2] row_mask:0xf bank_mask:0xf\n\ts_nop 1\n\tv_add_u32_dpp %[r], %[r], %[r] quad_perm:[2,3,0,1] row_mask:0xf bank_mask:0xf\n\ts_nop 0" \
                         : [v] "=&v"(v), [r] "=&v"(rank), [n] "=&v"(nv), "=&{v[40:43]}"(q4), [a] "=&s"(m0_), [b] "=&s"(m1_), [c] "=&s"(m2_), [d] "=&s"(m3_) : [gv] "v"(gv), [gq] "v"(gq_), [z] "v"(zero) : "memory", "vcc")
            RANK16(0);
            tau = (u32)__builtin_amdgcn_readlane((int)v, (int)__builtin_ctzll(__ballot(rank == 7)));
            u32 pos;
            asm volatile("s_nop 1\n\tv_mov_b32 %[n], 0\n\tv_cmp_le_u32 %[a], %[t], v36\n\tv_cmp_le_u32 %[b], %[t], v37\n\tv_cmp_le_u32 %[c], %[t], v38\n\tv_cmp_le_u32 %[d], %[t], v39\n\t"
                         "v_addc_co_u32 %[n], vcc, 0, %[n], %[a]\n\tv_addc_co_u32 %[n], vcc, 0, %[n], %[b]\n\tv_addc_co_u32 %[n], vcc, 0, %[n], %[c]\n\tv_addc_co_u32 %[n], vcc, 0, %[n], %[d]\n\t"
                         "v_lshlrev_b32 %[n], 2, %[n]\n\tv_cmpx_ne_u32 0, %[n]\n\tds_add_rtn_u32 %[p], %[z], %[n] offset:64\n\ts_waitcnt lgkmcnt(0)\n\t"
                         "s_mov_b64 exec, %[a]\n\tds_write_b32 %[p], v36 offset:256\n\tv_add_u32 %[p], 4, %[p]\n\t"
                         "s_mov_b64 exec, %[b]\n\tds_write_b32 %[p], v37 offset:256\n\tv_add_u32 %[p], 4, %[p]\n\t"
                         "s_mov_b64 exec, %[c]\n\tds_write_b32 %[p], v38 offset:256\n\tv_add_u32 %[p], 4, %[p]\n\t"
                         "s_mov_b64 exec, %[d]\n\tds_write_b32 %[p], v39 offset:256\n\t"
                         "s_mov_b64 exec, -1\n\ts_waitcnt lgkmcnt(0)\n\ts_barrier"
                         : [n] "=&v"(cnt), [p] "=&v"(pos), [a] "=&s"(m0_), [b] "=&s"(m1_), [c] "=&s"(m2_), [d] "=&s"(m3_) : [t] "s"(tau), [z] "v"(zero), "{v[36:39]}"(qa) : "memory", "vcc");
            if (wave == 0) {
                RANK16(256);
                const int nc = __builtin_amdgcn_readfirstlane((int)nv) >> 2;
                if (__builtin_expect(nc > 16, 0)) {
                    v = (u32)__shfl((int)top8_d(smem, lane, nc), (lane >> 2) & 7);
                    rank = lane >> 2;
                }
                const u64 m8 = __ballot(rank < 8);
                asm volatile("s_and_b64 exec, %[m], %[k2]\n\tglobal_store_dwordx2 %[so], %[d], %[ws] sc1\n\ts_mov_b64 exec, %[m]\n\ts_mov_b32 m0, 0x8000\n\ts_nop 0\n\t"
                             "global_load_lds_dword %[xo], %[x]\n\tglobal_load_lds_dword %[xo], %[x] offset:512\n\ts_and_b64 exec, %[m], %[k1]\n\tglobal_load_lds_dword %[xo], %[x] offset:1024\n\ts_mov_b64 exec, -1"
                             :: [m] "s"(m8), [k2] "s"(0x3333333333333333ull), [k1] "s"(0x1111111111111111ull), [so] "v"(so + rank * 8), [d] "v"((v2u){v, tag}), [ws] "s"(p.ws),
                                [xo] "v"(xo + (~v & 2047u) * 1152u), [x] "s"(p.x) : "memory", "scc");
                asm volatile("s_mov_b64 exec, 1\n\tglobal_store_dwordx2 %0, %1, %2 sc1\n\ts_mov_b64 exec, -1" :: "v"(OFF_CI + (272 + xcd) * 8), "v"((v2u){tag, tag}), "s"(p.ws) : "memory");
            }
        }
        STAMP(3);
        asm volatile("s_waitcnt vmcnt(0)" :: "v"(qb), "v"(qc), "v"(qd), "v"(qe) : "memory");
        if (xcd == 0) {
            const u32 cio = OFF_CI + (tid < 140 ? tid : 0) * 16;
            for (u32 spins = 0;; ++spins) {
                REREAD();
                const v4u q2 = __builtin_amdgcn_raw_buffer_load_b128(WSR(), cio, 0, 16);
                if (__all((q2.y == tag) & (q2.w == tag)) || spins > SPIN_CAP) break;
                __builtin_amdgcn_s_sleep(16);
            }
            BAR();
            if (tid == 0) __hip_atomic_store((GAS u32*)p.ws, tag, RLX_AGENT);
        }
        ROLE_END();
    }
    else
        {
            GET_PARAMS();
            const u32 l16 = lane * 16;
            const char* dsrc = (const char*)(p.wd + (size_t)(64 * w + 32 * (wave - 6)) * D);
            const u32 ddst = (u32)(uintptr_t)(smem + FL_WD) + (wave - 6) * 32 * WDP;
            u32 tagv;
            asm volatile("global_load_dword %0, %1, %2 sc1" : "=v"(tagv) : "v"(0u), "s"(p.ws) : "memory");
            u32 dof[37];
#pragma unroll
            for (int j = 0; j < 37; ++j) {
                const u32 r0 = (64 * j) / 74, c = 64 * j - 74 * r0 + lane;
                dof[j] = c >= 74u ? (r0 + 1) * 1152u + (c - 74u) * 16u : r0 * 1152u + min(c, 71u) * 16u;
                asm volatile("" : "+v"(dof[j]));
            }
#define LD_WD(j) asm volatile("s_mov_b32 m0, %2\n\ts_nop 0\n\tglobal_load_lds_dwordx4 %0, %1" WNT :: "v"(dof[j]), "s"(dsrc), "s"(ddst + (j) * 1024) : "memory")
            asm volatile("s_waitcnt vmcnt(0)" : "+v"(tagv));
            const u32 tg = __builtin_amdgcn_readfirstlane(tagv) + 1u;
            *(LAS v4u*)(smem + FL_Z + min(l16, 256u)) = (v4u){lane >= 16 ? tg : 0u, 0u, 0u, 0u};
            asm volatile("s_mov_b64 exec, 1\n\tglobal_store_dwordx2 %0, %1, %2 sc1\n\ts_mov_b64 exec, -1" :: "v"(OFF_CI + (128 + 2 * w + (wave - 6)) * 8), "v"((v2u){tg, tg}), "s"(p.ws) : "memory");
            if (FDELAY) __builtin_amdgcn_s_sleep(FDELAY);
            v2u sel;
            u32 c_;
            asm volatile("s_mov_b32 %[c], 0x80000\n1:\n\tglobal_load_dwordx2 v[10:11], %[so], %[ws] sc1\n\ts_waitcnt vmcnt(0)\n\tv_cmp_ne_u32 vcc, %[tag], v11\n\ts_cbranch_vccz 2f\n\ts_sub_u32 %[c], %[c], 1\n\ts_cbranch_scc0 1b\n2:"
                         : "=&{v[10:11]}"(sel), [c] "=&s"(c_) : [so] "v"(OFF_SEL + xcd * 256 + (4 * (wave - 6) + (lane & 3)) * 8), [ws] "s"(p.ws), [tag] "s"(tg) : "memory", "scc", "vcc");
            STAMP(3);
#pragma unroll
            for (int k = 0; k < 4; ++k) {
                const u32 t = ~(u32)__builtin_amdgcn_readlane((int)sel.x, k) & 2047u;
                const char* xr = (const char*)p.x + t * (D * 2);
                asm volatile("s_mov_b32 m0, %2\n\ts_nop 0\n\tglobal_load_lds_dwordx4 %0, %1\n\ts_mov_b64 exec, 0xff\n\tglobal_load_lds_dwordx4 %0, %1 offset:1024\n\ts_mov_b64 exec, -1"
                             :: "v"(l16), "s"(xr), "s"((u32)(uintptr_t)(smem + FL_XS) + ((wave - 6) * 4 + k) * XSP) : "memory");
            }
#pragma unroll
            for (int j = 0; j < K2; ++j) LD_WD(j);
            asm volatile("s_waitcnt vmcnt(%0) lgkmcnt(0)\n\ts_barrier" :: "n"(K2) : "memory");
            STAMP(10);
#pragma unroll
            for (int j = K2; j < 37; ++j) LD_WD(j);
            STAMP(11);
            asm volatile("s_waitcnt vmcnt(0)" ::: "memory");
            STAMP(13);
            __builtin_amdgcn_s_barrier();
            ROLE_END();
        }
    } else
    {
        GET_PARAMS();
        if (FDELAY) __builtin_amdgcn_s_sleep(FDELAY);
        _Pragma("unroll 1") for (int e = (slot - 28) + 4 * wave; touch<0>(p, xcd, e, lane, (u32)(uintptr_t)(smem + wave * 256)); e += 32) {}
        STAMP(11);
        asm volatile("s_waitcnt vmcnt(0)" ::: "memory");
        STAMP(6);
        STAMP_FLUSH();
    }
    } else
        {
            const int n = lane & 15, q = lane >> 4;
            GET_PARAMS();
            v4u gu[3][8];
            if (FDELAY) __builtin_amdgcn_s_sleep(FDELAY);
            const u16* wsrc = (n < 8 ? p.wg : p.wu) + 64 * w + 8 * (n & 7) + (size_t)(96 * wave + 8 * q) * F;
#define LD_GU(s) _Pragma("unroll") for (int j = 0; j < 8; ++j) asm volatile("global_load_dwordx4 %0, %1, off" WNT : "=v"(gu[s][j]) : "v"(wsrc + (size_t)(32 * s + j) * F))
            LD_GU(0);
            LD_GU(1);
            LD_GU(2);
            STAMP(11);
            bf16x8 guf[8];
            auto tr88 = [&](const v4u (&in)[8], bf16x8 (&o)[8]) __attribute__((always_inline)) {
#pragma unroll
                for (int pp = 0; pp < 4; ++pp) {
                    v4u lo, hi;
#pragma unroll
                    for (int jj = 0; jj < 4; ++jj) {
                        lo[jj] = __builtin_amdgcn_perm(in[2 * jj + 1][pp], in[2 * jj][pp], 0x05040100u);
                        hi[jj] = __builtin_amdgcn_perm(in[2 * jj + 1][pp], in[2 * jj][pp], 0x07060302u);
                    }
                    o[2 * pp] = __builtin_bit_cast(bf16x8, lo); o[2 * pp + 1] = __builtin_bit_cast(bf16x8, hi);
                }
#pragma unroll
                for (int i = 0; i < 8; ++i) asm volatile("" : "+v"(o[i]));
            };
#define WAIT_GU(s, N) asm volatile("s_waitcnt vmcnt(%8)" : "+v"(gu[s][0]), "+v"(gu[s][1]), "+v"(gu[s][2]), "+v"(gu[s][3]), "+v"(gu[s][4]), "+v"(gu[s][5]), "+v"(gu[s][6]), "+v"(gu[s][7]) : "n"(N))
            LAS char* xrow = smem + FL_XS + (n & 7) * XSP + (96 * wave + 8 * q) * 2;
            LAS char* xa = n < 8 ? xrow : smem + FL_Z + q * 16;
            LAS char* xb = n < 8 ? smem + FL_Z + q * 16 : xrow;
            LAS char* ta = smem + FL_WD + (lane >> 2) * WDP + (lane & 3) * 8 + wave * 64;
            LAS char* rp = smem + FL_RED + wave * 4096 + (q & 1) * 2048 + (q >> 1) * 512 + n * 8;
            LAS char* rp2 = smem + FL_RED + wave * 4096 + (q & 1) * 2048 + (q >> 1) * 512 + (n ^ 8) * 8;
            const int sl_ = (lane >> 3) * 16 + ((lane & 7) ^ ((lane >> 1) & 8)), hc = 8 * (lane & 7) + (lane >> 3);
            LAS char* ra = smem + FL_RED + (wave & 3) * 1024 + sl_ * 8;
            LAS char* ru = smem + FL_RED + (wave & 3) * 1024 + (sl_ ^ 8) * 8;
            LAS char* hw_ = smem + FL_H + (wave & 3) * 288 + ((hc & 35) | ((hc & 12) << 1) | ((hc & 16) >> 2)) * 2;
            const u32 obase = OFF_PART + ((4 * (2 * wave + (n >> 3)) + q) * NFFN + w) * 128 + (n & 7) * 16;
            u32 obase_ = obase;
            LAS char* hp = smem + FL_H + (n & 7) * 144 + q * 16;
            asm volatile("" : "+v"(xa), "+v"(xb), "+v"(ta), "+v"(rp), "+v"(obase_), "+v"(hp), "+v"(ra), "+v"(hw_), "+v"(rp2), "+v"(ru));
            bf16x8 g0[8], g1[8];
            WAIT_GU(0, 16);
            STAMP(5);
            tr88(gu[0], g0);
            WAIT_GU(1, 8);
            tr88(gu[1], g1);
            STAMP(12);
            WAIT_GU(2, 0);
            STAMP(6);
            tr88(gu[2], guf);
            asm volatile("s_barrier" ::: "memory");
            bf16x8 xf[6];
#pragma unroll
            for (int s = 0; s < 3; ++s) { xf[2 * s] = *(LAS bf16x8*)(xa + s * 64); xf[2 * s + 1] = *(LAS bf16x8*)(xb + s * 64); }
            const u32 tag = *(LAS u32*)(smem + FL_TAG);
            SBAR();
            STAMP(1);
            STAMP(2);
            v4f ha[4];
#pragma unroll
            for (int i = 0; i < 24; ++i) {
                const int f = (i & 3) + 4 * ((i >> 2) & 1);
                ha[i & 3] = MF(xf[i >> 2], i < 8 ? g0[f] : (i < 16 ? g1[f] : guf[f]), i < 4 ? ((v4f){0.f, 0.f, 0.f, 0.f}) : ha[i & 3]);
            }
            STAMP(4);
#pragma unroll
            for (int r = 0; r < 2; ++r)
#pragma unroll
                for (int i = 0; i < 4; ++i) *(LAS v2f*)((i & 2 ? rp2 : rp) + r * 1024 + i * 128) = (v2f){ha[i][2 * r], ha[i][2 * r + 1]};
            STAMP(7);
            BAR();
            s16x4 a[6][4];
            if (wave < 4) {
                v2f pa[6], pu[6];
#pragma unroll
                for (int ww = 0; ww < 6; ++ww) pa[ww] = *(volatile LAS v2f*)(ra + ww * 4096);
#pragma unroll
                for (int ww = 0; ww < 6; ++ww) pu[ww] = *(volatile LAS v2f*)(ru + ww * 4096);
                SBAR();
                const v2f av = ((pa[0] + pa[1]) + (pa[2] + pa[3])) + (pa[4] + pa[5]), uv = ((pu[0] + pu[1]) + (pu[2] + pu[3])) + (pu[4] + pu[5]);
                const u32 ab = cvtpk(av.x, av.y), ub = cvtpk(uv.x, uv.y);
                const u32 sl = cvtpk(bflo(ab) * sigmoidf_(bflo(ab)), bfhi(ab) * sigmoidf_(bfhi(ab)));
                const u32 hh = cvtpk(bflo(sl) * bflo(ub), bfhi(sl) * bfhi(ub));
                *(LAS u16*)(hw_) = (u16)hh; *(LAS u16*)(hw_ + 144) = (u16)(hh >> 16);
                asm volatile("s_waitcnt lgkmcnt(0)" ::: "memory");
            }
            STAMP(8);
#pragma unroll
            for (int k = 0; k < 6; ++k) {
                const int ko = (k >> 1) * 384 + (k & 1) * 32;
                a[k][0] = __builtin_amdgcn_ds_read_tr16_b64_v4i16((LAS s16x4*)(ta + ko));
                a[k][1] = __builtin_amdgcn_ds_read_tr16_b64_v4i16((LAS s16x4*)(ta + ko + 16 * WDP));
                a[k][2] = __builtin_amdgcn_ds_read_tr16_b64_v4i16((LAS s16x4*)(ta + ko + 32 * WDP));
                a[k][3] = __builtin_amdgcn_ds_read_tr16_b64_v4i16((LAS s16x4*)(ta + ko + 48 * WDP));
            }
            asm volatile("s_barrier" ::: "memory");
            STAMP(13);
            const bf16x8 h0 = *(LAS bf16x8*)(hp);
            const bf16x8 h1 = *(LAS bf16x8*)(hp + 64);
            v4f ya[6];
#pragma unroll
            for (int k = 0; k < 6; ++k) ya[k] = __builtin_amdgcn_mfma_f32_16x16x32_bf16(__builtin_shufflevector(a[k][0], a[k][1], 0, 1, 2, 3, 4, 5, 6, 7), h0, (v4f){0.f, 0.f, 0.f, 0.f}, 0, 0, 0);
#pragma unroll
            for (int k = 0; k < 6; ++k) ya[k] = __builtin_amdgcn_mfma_f32_16x16x32_bf16(__builtin_shufflevector(a[k][2], a[k][3], 0, 1, 2, 3, 4, 5, 6, 7), h1, ya[k], 0, 0, 0);
#pragma unroll
            for (int k = 0; k < 3; ++k) {
                const v4f v = n < 8 ? ya[2 * k] : ya[2 * k + 1];
                __builtin_amdgcn_raw_buffer_store_b128((v4u){cvtpk(v[0], v[1]), tag, cvtpk(v[2], v[3]), tag}, WSR(), obase_ + k * (48 * NFFN * 128), 0, 16);
            }
            STAMP(9);
            ROLE_END();
        }
    } else {
    if (wave >= 4)
    {
        const int hw = wave - 4, ht = tid - 256;
        const v4u z4 = {0, 0, 0, 0};
        GET_PARAMS();
        u32 tagv;
        asm volatile("global_load_dword %0, %1, %2 sc1" : "=v"(tagv) : "v"(0u), "s"(p.ws) : "memory");
        const int sub = lane & 15;
        v4u xr[5];
        const int rrow = 16 * (cw & 127) + 4 * hw;
        {
            const u32 dst = (u32)(uintptr_t)(smem + LDS_RW) + hw * 3072;
            glds16(p.wr + lane * 8, dst, false);
            glds16(lane < 8 ? p.wr + 512 + lane * 8 : p.gw + (lane - 8) * 8, dst + 1024, false);
            glds16(p.gw + 448 + (lane & 15) * 8, dst + 2048, false);
            const u32 ro = (u32)((rrow + (lane >> 4)) * D + sub * 8) * 2, ro4 = ro - (sub & 8) * 16;
#pragma unroll
            for (int j = 0; j < 4; ++j) asm volatile("global_load_dwordx4 %0, %1, %2 offset:%3" : "=v"(xr[j]) : "v"(ro), "s"(p.x), "n"(j * 256));
            asm volatile("global_load_dwordx4 %0, %1, %2 offset:1024" : "=v"(xr[4]) : "v"(ro4), "s"(p.x));
        }
        __builtin_amdgcn_s_barrier();
        STAMP(1);
        if (XPF) {
            const int id = min((slot * 4 + hw) * 64 + lane, 4095);
            u32 keep;
            asm volatile("s_mov_b32 %0, m0\n\ts_mov_b32 m0, %3\n\ts_nop 0\n\tglobal_load_lds_dword %1, %2\n\ts_mov_b32 m0, %0" : "=&s"(keep) : "v"((u32)((id >> 1) * 1152 + ((id & 1) ? 1024 : xcd * 128))), "s"(p.x), "s"((u32)(uintptr_t)(smem + LDS_DUMMY)) : "memory");
        }
        v2u uu[8];
        const u32 uo0 = (u32)((256 * hw + 4 * lane) * D + chbase) * 2, uo1 = uo0 + 1024 * D * 2;
#define LD_U(i) asm volatile("global_load_dwordx2 %0, %1, %2 offset:%3" : "=v"(uu[i]) : "v"((i) < 4 ? uo0 : uo1), "s"(p.x), "n"(((i) & 3) * D * 2))
#pragma unroll
        for (int i = 0; i < NB1; ++i) LD_U(i);
        *(LAS v4u*)(smem + LDS_TAP + ht * 16) = z4; *(LAS v4u*)(smem + LDS_TAP + 4096 + ht * 16) = z4;
        *(LAS v4u*)(smem + (ht < 128 ? LDS_TAP + 8192 + ht * 16 : LDS_U + ((ht >> 5) & 3) * 4608 + (ht & 31) * 16)) = z4;
        u32 ctl = (u32)(uintptr_t)(smem + LDS_CTL), one = 1u, cty = ctl + 64 * hw + (lane & 3) * 16;
        u32 po = cw >= 128 ? OFF_CI + ((cw - 128) * 4 + hw) * 16 : (sub == 0 ? OFF_RG + rrow * 8 + (lane >> 1) : OFF_G + (lane - 1) * 8192 + rrow * 4);
        const u64 pmask = cw >= 128 ? 1ull : ((1ull << (1 + REPG)) - 1) | (1ull << 32);
        u32 nrow = 0xffffu - (rrow + (lane >> 4));
        u32 go = OFF_G + (cw % REPG) * 8192 + 2048 + (64 * hw + lane) * 16;
        LAS char* up = smem + LDS_U + (256 + 256 * hw + 4 * lane) * 2;
        LAS char* yp = smem + CH_Y + hw * 1152 + (lane + (lane >> 3)) * 16;
        LAS char* rw = smem + LDS_RW + hw * 3072 + sub * 16;
        asm volatile("" : "+v"(ctl), "+v"(one), "+v"(cty), "+v"(po), "+v"(go), "+v"(up), "+v"(yp), "+v"(rw), "+v"(nrow));
        BAR();
        __builtin_amdgcn_s_setprio(3);
        u32 tag;
        {
            asm volatile("s_waitcnt vmcnt(%6)" : "+v"(xr[0]), "+v"(xr[1]), "+v"(xr[2]), "+v"(xr[3]), "+v"(xr[4]), "+v"(tagv) : "n"(NB1 + XPF) : "memory");
            STAMP(2);
            tag = __builtin_amdgcn_readfirstlane(tagv) + 1u;
            if (sub >= 8) xr[4] = z4;
            float r = 0.f, s = 0.f;
#pragma unroll
            for (int j = 0; j < 5; ++j) {
                const v4u a = *(LAS v4u*)(rw + j * 256), g = *(LAS v4u*)(rw + 1152 + j * 256);
#pragma unroll
                for (int e = 0; e < 4; ++e) {
                    const u32 xe = xr[j][e], ae = a[e], ge = g[e];
                    r = __builtin_amdgcn_fdot2_f32_bf16(__builtin_bit_cast(bf16x2_t, xe), __builtin_bit_cast(bf16x2_t, ae), r, false);
                    s = __builtin_amdgcn_fdot2_f32_bf16(__builtin_bit_cast(bf16x2_t, xe), __builtin_bit_cast(bf16x2_t, ge), s, false);
                }
            }
            r = dpp_add<0x128, 0xf>(r); s = dpp_add<0x128, 0xf>(s); r = dpp_add<0x124, 0xf>(r); s = dpp_add<0x124, 0xf>(s);
            r = dpp_add<0x122, 0xf>(r); s = dpp_add<0x122, 0xf>(s); r = dpp_add<0x121, 0xf>(r); s = dpp_add<0x121, 0xf>(s);
            const u32 pay = make_payload(r, sigmoidf_(rbf(s))), kk = (pay & 0xffff0000u) | nrow;
            const u32 p0 = (u32)__builtin_amdgcn_readlane((int)pay, 0), p1 = (u32)__builtin_amdgcn_readlane((int)pay, 16), p2 = (u32)__builtin_amdgcn_readlane((int)pay, 32), p3 = (u32)__builtin_amdgcn_readlane((int)pay, 48);
            const auto sw = __builtin_amdgcn_permlane16_swap(kk, kk, false, false);
            const u32 g01 = (p0 & 0xffffu) | (p1 << 16), g23 = (p2 & 0xffffu) | (p3 << 16);
            const v4u pv = {sub ? g01 : sw[0], tag, sub ? g23 : sw[1], tag};
            asm volatile("s_mov_b64 exec, %3\n\tglobal_store_dwordx4 %0, %1, %2 sc1\n\ts_mov_b64 exec, -1\n\ts_nop 0" :: "v"(po), "v"(pv), "s"(p.ws), "s"(pmask) : "memory");
        }
        STAMP(3);
        __builtin_amdgcn_s_setprio(HPRIO);
        *(LAS u32*)(smem + LDS_CTL + C_TAG) = tag;
        LDS_ADD1(ctl, one, C_PUB);
        LWAIT(ctl, C_PUB, 4);
#pragma unroll
        for (int i = NB1; i < 4; ++i) LD_U(i);
        LDS_ADD1(ctl, one, C_PUB);
        LWAIT(ctl, C_PUB, 8);
#pragma unroll
        for (int i = 4; i < 8; ++i) LD_U(i);
        v4u gq[2];
#define POLL_G "global_load_dwordx4 v[100:103], %[go], %[ws] offset:-2048 sc1\n\tglobal_load_dwordx4 v[104:107], %[go], %[ws] offset:2048 sc1\n\t"
        asm volatile(POLL_G : "={v[100:103]}"(gq[0]), "={v[104:107]}"(gq[1]) : [go] "v"(go), [ws] "s"(p.ws) : "memory");
        STAMP(4);
        auto stage = [&](int g) __attribute__((always_inline)) {
            const v2u* u4 = uu + 4 * g;
            LAS char* d = up + g * 2048;
            *(LAS v2u*)(d) = (v2u){__builtin_amdgcn_perm(u4[1].x, u4[0].x, 0x05040100u), __builtin_amdgcn_perm(u4[3].x, u4[2].x, 0x05040100u)};
            *(LAS v2u*)(d + 4608) = (v2u){__builtin_amdgcn_perm(u4[1].x, u4[0].x, 0x07060302u), __builtin_amdgcn_perm(u4[3].x, u4[2].x, 0x07060302u)};
            *(LAS v2u*)(d + 2 * 4608) = (v2u){__builtin_amdgcn_perm(u4[1].y, u4[0].y, 0x05040100u), __builtin_amdgcn_perm(u4[3].y, u4[2].y, 0x05040100u)};
            *(LAS v2u*)(d + 3 * 4608) = (v2u){__builtin_amdgcn_perm(u4[1].y, u4[0].y, 0x07060302u), __builtin_amdgcn_perm(u4[3].y, u4[2].y, 0x07060302u)};
            LDS_ADD1(ctl, one, C_STG);
        };
        asm volatile("s_waitcnt vmcnt(6)" : "+v"(uu[0]), "+v"(uu[1]), "+v"(uu[2]), "+v"(uu[3]));
        stage(0);
        STAMP(5);
        touch<1>(p, xcd, slot * 4 + hw, lane, (u32)(uintptr_t)(smem + LDS_DUMMY));
        touch<1>(p, xcd, 72 + ((slot * 4 + hw) % 20), lane, (u32)(uintptr_t)(smem + LDS_DUMMY));
        asm volatile("s_waitcnt vmcnt(4)" : "+v"(uu[4]), "+v"(uu[5]), "+v"(uu[6]), "+v"(uu[7]));
        LWAIT(ctl, C_STG, 4);
        stage(1);
        STAMP(6);
        {
            u32 c_;
            asm volatile("s_mov_b32 %[c], 0x80000\n\ts_waitcnt vmcnt(2)\n1:\n\tv_cmpx_eq_u32 %[tag], v101\n\tv_cmpx_eq_u32 %[tag], v103\n\tv_cmpx_eq_u32 %[tag], v105\n\tv_cmpx_eq_u32 %[tag], v107\n\t"
                         "s_cmp_eq_u64 exec, -1\n\ts_mov_b64 exec, -1\n\ts_cbranch_scc1 2f\n\ts_sleep 4\n\t" POLL_G "s_waitcnt vmcnt(0)\n\ts_sub_u32 %[c], %[c], 1\n\ts_cbranch_scc0 1b\n2:\n\ts_nop 4"
                         : "+{v[100:103]}"(gq[0]), "+{v[104:107]}"(gq[1]), [c] "=&s"(c_) : [go] "v"(go), [ws] "s"(p.ws), [tag] "s"(tag) : "memory", "scc", "vcc");
        }
        STAMP(7);
        *(LAS v2u*)(smem + LDS_G + hw * 3072 + lane * 8) = (v2u){gq[0].x, gq[0].z}; *(LAS v2u*)(smem + LDS_G + hw * 3072 + 512 + lane * 8) = (v2u){gq[1].x, gq[1].z};
#pragma unroll
        for (int g = 0; g < 2; ++g) {
            const v2u* u4 = uu + 4 * g;
            const float gr[4] = {bflo(gq[g].x), bfhi(gq[g].x), bflo(gq[g].z), bfhi(gq[g].z)};
            float o[4][4];
#pragma unroll
            for (int r = 0; r < 4; ++r) {
                const float xv[4] = {bflo(u4[r].x), bfhi(u4[r].x), bflo(u4[r].y), bfhi(u4[r].y)};
#pragma unroll
                for (int c = 0; c < 4; ++c) asm("v_fma_f32 %0, -%1, %2, %2" : "=v"(o[r][c]) : "v"(gr[r]), "v"(xv[c]));
                asm volatile("" : "+v"(o[r][0]), "+v"(o[r][1]), "+v"(o[r][2]), "+v"(o[r][3]));
            }
            {
                u32 t_, c_;
                asm volatile("s_mov_b32 %1, 0x400000\n1:\n\tds_read_b32 %0, %2 offset:%3\n\ts_waitcnt lgkmcnt(0)\n\tv_cmp_eq_u32 vcc, 0, %0\n\ts_cmp_eq_u64 vcc, 0\n\ts_cbranch_scc1 2f\n\ts_sleep %4\n\ts_sub_u32 %1, %1, 1\n\ts_cbranch_scc0 1b\n2:"
                             : "=&v"(t_), "=&s"(c_) : "v"(cty), "n"(C_Y + 256 * g), "n"(YSLEEP) : "memory", "scc", "vcc");
            }
            if (g == 0) STAMP(11);
            v4f y[4];
#pragma unroll
            for (int c = 0; c < 4; ++c) y[c] = *(LAS v4f*)(yp + c * KCH + g * 4608);
            SBAR();
#define ST_O(i, v) asm volatile("global_store_dwordx2 %0, %1, %2 offset:%3" :: "v"(g ? uo1 : uo0), "v"(v), "s"(p.out), "n"((i) * D * 2) : "memory")
#pragma unroll
            for (int r = 0; r < 4; ++r) {
#pragma unroll
                for (int c = 0; c < 4; ++c) { const float yv = y[c][r]; asm("v_fmac_f32 %0, %1, %2" : "+v"(o[r][c]) : "v"(gr[r]), "v"(yv)); }
                ST_O(r, ((v2u){cvtpk(o[r][0], o[r][1]), cvtpk(o[r][2], o[r][3])}));
            }
            STAMP(8 + g);
        }
        asm volatile("s_waitcnt vmcnt(0)" ::: "memory");
        LDS_ADD1(ctl, one, C_ACK);
        STAMP(10);
        ROLE_END();
    }
    else
    {
        GET_PARAMS();
        const int ch = wave;
        const int dch = chbase + ch, pi = dch * NST + lane * 2;
        const u32 are = __builtin_nontemporal_load((const u32*)(p.Are + pi)), aim = __builtin_nontemporal_load((const u32*)(p.Aim + pi)), bre = __builtin_nontemporal_load((const u32*)(p.Bre + pi));
        const u32 bim = __builtin_nontemporal_load((const u32*)(p.Bim + pi)), cre = __builtin_nontemporal_load((const u32*)(p.Cre + pi)), cim = __builtin_nontemporal_load((const u32*)(p.Cim + pi));
        const u16 ldt = p.logdt[dch], dsk = p.dskip[dch];
        SBAR();
        __builtin_amdgcn_s_barrier();
        SBAR();
        LAS char* kb = smem + ch * KCH;
        const int r32 = lane & 31, h = lane >> 5, q = lane >> 4, n = lane & 15;
        const int ai = 4 * (r32 >> 3) + (r32 & 3), hh = (r32 >> 2) & 1;
        const u64 tsel = ai < 8 ? (hh ? KROW_1LO : KROW_0LO) : (hh ? KROW_1HI : KROW_0HI);
        const int o = (int)((tsel >> (8 * (ai & 7))) & 0xff);
        LAS char* ap = kb + o * STG_ROW + h * 16;
        LAS char* bp = kb + r32 * STG_ROW + h * 16;
        u32 sg[14];
#pragma unroll
        for (int k = 0; k < 14; ++k) { sg[k] = (u32)(uintptr_t)(kb + lane * 8 + k * 4 * STG_ROW); asm volatile("" : "+v"(sg[k])); }
        bf16x8 b5 = band(((0x1fu << 6) << n) >> (8 * q), 0x3f803f80u), b3 = band(((0x7u << 11) << n) >> (8 * q), 0x3f803f80u);
        bf16x8 b9 = band(((0x1ffu << 4) << n) >> (8 * q), 0x3f803f80u), b7 = band(((0x7fu << 5) << n) >> (8 * q), 0x3f803f80u);
        bf16x8 pat = band(((0x3u << 12) << n) >> (8 * q), 0xffffffffu);
        LAS char* k0 = kb + 32 + lane * 16;
        LAS char* kt = kb + (lane & 3) * KCOPY + (lane < 16 ? 32 + 1024 + (lane >> 2) * 16 : ((lane >> 2) & 1) * 16);
        LAS char* a0 = kb + ((lane >> 2) & 3) * KCOPY + (28 - 8 * q + 4 * (lane & 3)) * 2;
        LAS char* bb = smem + LDS_U + ch * 4608 + (16 * n + 8 * q - 16) * 2;
        LAS char* yb = kb + CH_Y + (4 * n + q + (n >> 1)) * 16;
        LAS char* tp = smem + LDS_TAP + ch * 2304 + ((n & 1) ? 13 + n - 4 * q : 96 + 14 + n - 4 * q) * 4;
        LAS char* ta = smem + LDS_TAP + ch * 2304 + (h ? 0 : 768) + 64 + 2 * r32;
        LAS char* tb = smem + LDS_TAP + ch * 2304 + 1600 + 64 * h + 2 * r32;
        u32 ctl = (u32)(uintptr_t)(smem + LDS_CTL), ctc = ctl + C_Y + ch * 16 + (lane & 15), one = 1u, so = (lane & 8) ? OFF_PART + (cw * NFFN + (cw >> 1)) * 128 : OFF_SEL + (8 + xcd) * 256 + (lane & 7) * 8;
        u32 m01 = lane < 2 ? ~0u : 0u, m2 = lane == 2 ? ~0u : 0u, m3 = lane == 3 ? ~0u : 0u, m0_ = lane == 0 ? ~0u : 0u;
        asm volatile("" : "+v"(b5), "+v"(b3), "+v"(b9), "+v"(b7), "+v"(pat), "+v"(ap), "+v"(bp), "+v"(m01), "+v"(m2), "+v"(m3), "+v"(m0_));
        asm volatile("" : "+v"(k0), "+v"(kt), "+v"(a0), "+v"(bb), "+v"(yb), "+v"(tp), "+v"(ta), "+v"(tb), "+v"(ctl), "+v"(ctc), "+v"(one), "+v"(so));
        __builtin_amdgcn_s_barrier();
        SBAR();
        const float dt = rbf(expf(bf2f(ldt)));
        STAMP(1);
        v16f acc;
        u32 stg;
        float kscale;
        {
            const v2f a2 = (v2f){bflo(are), bfhi(are)} * dt * 1.44269504088896341f;
            const v2f brev = (v2f){bflo(aim), bfhi(aim)} * dt * 0.15915494309189535f;
            const cf2 Bc = {{dt * bflo(bre), dt * bflo(bim)}, {dt * bfhi(bre), dt * bfhi(bim)}};
            const cf2 Cc = {{bflo(cre), bflo(cim)}, {bfhi(cre), bfhi(cim)}};
            cf2 W = cmul2(Cc, Bc);
            u32 mx = max(max(__float_as_uint(W.a.x) & 0x7f800000u, __float_as_uint(W.a.y) & 0x7f800000u), max(__float_as_uint(W.b.x) & 0x7f800000u, __float_as_uint(W.b.y) & 0x7f800000u));
            mx = dpp_umax<0x111, 0xf>(mx); mx = dpp_umax<0x112, 0xf>(mx); mx = dpp_umax<0x114, 0xf>(mx); mx = dpp_umax<0x118, 0xf>(mx);
            mx = dpp_umax<0x142, 0xa>(mx); mx = dpp_umax<0x143, 0xc>(mx);
            mx = min(max((u32)__builtin_amdgcn_readlane((int)mx, 63), 0x00800000u), 0x7e800000u);
            kscale = __uint_as_float(mx);
            const float inv = __uint_as_float(0x7f000000u - mx);
            W.a *= inv; W.b *= inv;
            const cf2 E1 = cexp2(a2, brev, 1.f), E8 = cexp2(a2, brev, 8.f), E256 = cexp2(a2, brev, 256.f);
            asm volatile("s_nop 0\n\t" KGEN_ASM :: "{v[48:49]}"(W.a), "{v[50:51]}"(W.b), "{v[52:53]}"(E1.a), "{v[54:55]}"(E1.b), "{v[56:57]}"(E8.a), "{v[58:59]}"(E8.b), "{v[60:61]}"(E256.a), "{v[62:63]}"(E256.b),
                         [b0] "v"(sg[0]), [b1] "v"(sg[1]), [b2] "v"(sg[2]), [b3] "v"(sg[3]), [b4] "v"(sg[4]), [b5] "v"(sg[5]), [b6] "v"(sg[6]), [b7] "v"(sg[7]), [b8] "v"(sg[8]), [b9] "v"(sg[9]),
                         [b10] "v"(sg[10]), [b11] "v"(sg[11]), [b12] "v"(sg[12]), [b13] "v"(sg[13]) : "memory", KGEN_CLOBBER);
            STAMP(2);
            asm volatile(KMFMA_ASM : "=&{v[48:63]}"(acc), [stg] "=&v"(stg) : [ap] "v"(ap), [bp] "v"(bp), [ctl] "v"(ctl) : "memory", KMFMA_CLOBBER);
            acc *= kscale;
        }
        STAMP(3);
        acc[0] += __uint_as_float(((u32)dsk << 16) & m0_);
        u32 t78 = cvtpk(acc[7], acc[8]), t9a = cvtpk(acc[9], acc[10]);
        {
            const auto s0 = __builtin_amdgcn_permlane32_swap(cvtpk(acc[0], acc[1]), cvtpk(acc[4], acc[4]), false, false);
            const auto s1 = __builtin_amdgcn_permlane32_swap(cvtpk(acc[2], acc[3]), cvtpk(acc[5], acc[6]), false, false);
            const v4u ww = {s0[0], s1[0], s0[1], s1[1]};
            const u32 pz = (u32)__builtin_amdgcn_update_dpp(0, (int)ww.z, 0x138, 0xf, 0xf, true), pw = (u32)__builtin_amdgcn_update_dpp(0, (int)ww.w, 0x138, 0xf, 0xf, true);
            const v4u c1 = {__builtin_amdgcn_alignbit(ww.x, pw, 16), __builtin_amdgcn_alignbit(ww.y, ww.x, 16), __builtin_amdgcn_alignbit(ww.z, ww.y, 16), __builtin_amdgcn_alignbit(ww.w, ww.z, 16)};
            *(LAS v4u*)(k0) = ww;
            *(LAS v4u*)(k0 + KCOPY) = c1;
            *(LAS v4u*)(k0 + 2 * KCOPY) = (v4u){pw, ww.x, ww.y, ww.z};
            *(LAS v4u*)(k0 + 3 * KCOPY) = (v4u){__builtin_amdgcn_alignbit(pw, pz, 16), c1.x, c1.y, c1.z};
            const u32 w0 = (u32)__builtin_amdgcn_readlane((int)ww.z, 63), w1 = (u32)__builtin_amdgcn_readlane((int)ww.w, 63);
            const u32 w3 = w1 >> 16, w2 = w3 * 0x10001u, w10 = (w0 >> 16) | (w1 << 16);
            *(LAS v4u*)kt = (v4u){(w2 & m01) | (w1 & m2) | (w10 & m3), (w3 & m0_) | (w2 & ((m01 ^ m0_) | m2 | m3)), (w3 & m2) | (w2 & m3), 0u};
        }
        asm volatile("" ::: "memory");
        pat = __builtin_bit_cast(bf16x8, __builtin_bit_cast(v4u, pat) & (((u32)__builtin_amdgcn_readlane((int)t78, 63) >> 16) * 0x10001u));
#define POLL_SEL "global_load_dwordx2 v[240:241], %[so], %[ws] sc1\n\t"
        v2u sq;
        const v4f zero4 = {0.f, 0.f, 0.f, 0.f};
        if (MPRIO) __builtin_amdgcn_s_setprio(MPRIO);
        STAMP(4);
        {
            u32 s_, c_;
            asm volatile(CONV_ASM : "=&{v[240:241]}"(sq), [s] "=&s"(s_), [c] "=&s"(c_)
                         : "{v[4:7]}"(b5), "{v[8:11]}"(b3), "{v[12:15]}"(b9), "{v[16:19]}"(b7), "{v[20:23]}"(pat), [a0] "v"(a0), [bb] "v"(bb), [yb] "v"(yb), [tp] "v"(tp), [ta] "v"(ta), [tb] "v"(tb),
                           [ctl] "v"(ctl), [ctc] "v"(ctc), [one] "v"(one), [so] "v"(so), [t78] "v"(t78), [t9a] "v"(t9a), [stg] "v"(stg), [ws] "s"(p.ws) : "memory", "scc", CONV_CLOBBER);
        }
        STAMP(9);
        asm volatile("s_cmp_eq_u32 %0, 0\n\ts_cbranch_scc1 1f\n\ts_endpgm\n1:" :: "s"(wave) : "memory", "scc");
        {
            const u32 tag = __builtin_amdgcn_readfirstlane(*(LAS u32*)(smem + LDS_CTL + C_TAG));
            u32 c_;
            asm volatile("s_mov_b32 %[c], 0x80000\n1:\n\ts_waitcnt vmcnt(0)\n\tv_cmpx_eq_u32 %[tag], v241\n\t"
                         "s_cmp_eq_u64 exec, -1\n\ts_mov_b64 exec, -1\n\ts_cbranch_scc1 2f\n\t" POLL_SEL "s_sub_u32 %[c], %[c], 1\n\ts_cbranch_scc0 1b\n\ts_waitcnt vmcnt(0)\n2:\n\ts_nop 4"
                         : "+{v[240:241]}"(sq), [c] "=&s"(c_) : [so] "v"(so), [ws] "s"(p.ws), [tag] "s"(tag) : "memory", "scc", "vcc");
            STAMP(10);
            const u32 pb = OFF_PART + cw * (NFFN * 128) + 4096 + lane * 16, pb2 = pb + 4096;
            v4u pq[9];
#define POLL_P "global_load_dwordx4 v[64:67], %[pb], %[ws] offset:-4096 sc1\n\tglobal_load_dwordx4 v[68:71], %[pb], %[ws] offset:-3072 sc1\n\tglobal_load_dwordx4 v[72:75], %[pb], %[ws] offset:-2048 sc1\n\t" \
               "global_load_dwordx4 v[76:79], %[pb], %[ws] offset:-1024 sc1\n\tglobal_load_dwordx4 v[80:83], %[pb], %[ws] sc1\n\tglobal_load_dwordx4 v[84:87], %[pb], %[ws] offset:1024 sc1\n\t" \
               "global_load_dwordx4 v[88:91], %[pb], %[ws] offset:2048 sc1\n\tglobal_load_dwordx4 v[92:95], %[pb], %[ws] offset:3072 sc1\n\tglobal_load_dwordx4 v[96:99], %[pb2], %[ws] sc1\n\t"
            asm volatile(POLL_P
                         : "=&{v[64:67]}"(pq[0]), "=&{v[68:71]}"(pq[1]), "=&{v[72:75]}"(pq[2]), "=&{v[76:79]}"(pq[3]), "=&{v[80:83]}"(pq[4]), "=&{v[84:87]}"(pq[5]), "=&{v[88:91]}"(pq[6]), "=&{v[92:95]}"(pq[7]), "=&{v[96:99]}"(pq[8])
                         : [pb] "v"(pb), [pb2] "v"(pb2), [ws] "s"(p.ws) : "memory");
            sq.x = (u32)__builtin_amdgcn_update_dpp((int)sq.x, (int)sq.x, 0x128, 0xf, 0xc, false);
            const int t = (int)(0xffffu - (sq.x & 0xffffu)) & 2047;
            const float rs = rbf(sigmoidf_(key_to_r(sq.x)));
            LWAIT(ctl, C_ACK, 4);
            const float g = bf2f(*(LAS u16*)(smem + LDS_G + ((t >> 8) & 3) * 3072 + (t >> 10) * 512 + (t & 255) * 2));
            const int uq = (t & 255) >> 2;
            float xv[4], av[4];
#pragma unroll
            for (int c = 0; c < 4; ++c) {
                xv[c] = bf2f(*(LAS u16*)(smem + LDS_U + c * 4608 + (256 + t) * 2));
                av[c] = g * *(LAS float*)(smem + c * KCH + CH_Y + (t >> 8) * 1152 + (uq + (uq >> 3)) * 16 + (t & 3) * 4);
            }
            const u32 e1 = n < 4 ? ((n & 1) ? 0x3f800000u : 0x00003f80u) : 0u;
            bf16x8 sel4 = __builtin_bit_cast(bf16x8, (n & 2) ? (v4u){0u, e1, 0u, e1} : (v4u){e1, 0u, e1, 0u});
            u32 oo = (u32)(t * D + chbase) * 2;
            float omg = 1.f - g;
            asm volatile("" : "+v"(sel4), "+v"(oo), "+v"(omg), "+v"(xv[0]), "+v"(xv[1]), "+v"(xv[2]), "+v"(xv[3]), "+v"(av[0]), "+v"(av[1]), "+v"(av[2]), "+v"(av[3]));
            asm volatile("s_mov_b32 %[c], 0x80000\n1:\n\ts_waitcnt vmcnt(0)\n\t"
                         "v_cmpx_eq_u32 %[tag], v65\n\tv_cmpx_eq_u32 %[tag], v67\n\tv_cmpx_eq_u32 %[tag], v69\n\tv_cmpx_eq_u32 %[tag], v71\n\tv_cmpx_eq_u32 %[tag], v73\n\tv_cmpx_eq_u32 %[tag], v75\n\t"
                         "v_cmpx_eq_u32 %[tag], v77\n\tv_cmpx_eq_u32 %[tag], v79\n\tv_cmpx_eq_u32 %[tag], v81\n\tv_cmpx_eq_u32 %[tag], v83\n\tv_cmpx_eq_u32 %[tag], v85\n\tv_cmpx_eq_u32 %[tag], v87\n\t"
                         "v_cmpx_eq_u32 %[tag], v89\n\tv_cmpx_eq_u32 %[tag], v91\n\tv_cmpx_eq_u32 %[tag], v93\n\tv_cmpx_eq_u32 %[tag], v95\n\tv_cmpx_eq_u32 %[tag], v97\n\tv_cmpx_eq_u32 %[tag], v99\n\t"
                         "s_cmp_eq_u64 exec, -1\n\ts_mov_b64 exec, -1\n\ts_cbranch_scc1 2f\n\t" POLL_P "s_sub_u32 %[c], %[c], 1\n\ts_cbranch_scc0 1b\n\ts_waitcnt vmcnt(0)\n2:\n\ts_nop 4"
                         : "+{v[64:67]}"(pq[0]), "+{v[68:71]}"(pq[1]), "+{v[72:75]}"(pq[2]), "+{v[76:79]}"(pq[3]), "+{v[80:83]}"(pq[4]), "+{v[84:87]}"(pq[5]), "+{v[88:91]}"(pq[6]), "+{v[92:95]}"(pq[7]), "+{v[96:99]}"(pq[8]), [c] "=&s"(c_)
                         : [pb] "v"(pb), [pb2] "v"(pb2), [ws] "s"(p.ws), [tag] "s"(tag) : "memory", "scc", "vcc");
            STAMP(11);
            v4f s0 = MF(sel4, __builtin_bit_cast(bf16x8, ((v4u){pq[0].x, pq[0].z, pq[1].x, pq[1].z})), zero4);
            v4f s1 = MF(sel4, __builtin_bit_cast(bf16x8, ((v4u){pq[2].x, pq[2].z, pq[3].x, pq[3].z})), zero4);
            s0 = MF(sel4, __builtin_bit_cast(bf16x8, ((v4u){pq[4].x, pq[4].z, pq[5].x, pq[5].z})), s0);
            s1 = MF(sel4, __builtin_bit_cast(bf16x8, ((v4u){pq[6].x, pq[6].z, pq[7].x, pq[7].z})), s1);
            s0 = MF(sel4, __builtin_bit_cast(bf16x8, ((v4u){pq[8].x, pq[8].z, 0u, 0u})), s0);
            float o[4];
#pragma unroll
            for (int c = 0; c < 4; ++c) o[c] = fmaf(omg, xv[c] + rbf(rbf(dpp_add<0x128, 0xf>(s0[c] + s1[c])) * rs), av[c]);
            asm volatile("s_mov_b64 exec, 0xff\n\tglobal_store_dwordx2 %0, %1, %2\n\ts_mov_b64 exec, -1" :: "v"(oo), "v"((v2u){cvtpk(o[0], o[1]), cvtpk(o[2], o[3])}), "s"(p.out) : "memory");
        }
        STAMP(12);
        ROLE_END();
    }
    }
}

extern "C" void kernel_launch(void* const* d_in, const int* in_sizes, int n_in,
                              void* d_out, int out_size, void* d_ws, size_t ws_size,
                              hipStream_t stream) {
    Params p;
    p.x = (const u16*)d_in[0]; p.Are = (const u16*)d_in[1]; p.Aim = (const u16*)d_in[2]; p.Bre = (const u16*)d_in[3];
    p.Bim = (const u16*)d_in[4]; p.Cre = (const u16*)d_in[5]; p.Cim = (const u16*)d_in[6]; p.logdt = (const u16*)d_in[7];
    p.dskip = (const u16*)d_in[8]; p.wr = (const u16*)d_in[10]; p.wg = (const u16*)d_in[11]; p.wu = (const u16*)d_in[12];
    p.wd = (const u16*)d_in[13]; p.gw = (const u16*)d_in[14]; p.out = (u16*)d_out; p.ws = (char*)d_ws;
    (void)hipFuncSetAttribute((const void*)fused_kernel, hipFuncAttributeMaxDynamicSharedMemorySize, LDS_TOTAL);
    fused_kernel<<<256, 512, LDS_TOTAL, stream>>>(p);
}
```

```cpp
#include <hip/hip_runtime.h>
#include <stdint.h>

typedef unsigned short u16;
typedef unsigned int u32;
typedef unsigned long long u64;
typedef __attribute__((ext_vector_type(4))) u32 v4u;
typedef __attribute__((ext_vector_type(2))) u32 v2u;
typedef __attribute__((ext_vector_type(2))) float v2f;
typedef __attribute__((ext_vector_type(4))) float v4f;
typedef __attribute__((ext_vector_type(16))) float v16f;
typedef __attribute__((ext_vector_type(8))) short bf16x8;
typedef __attribute__((ext_vector_type(4))) short s16x4;
typedef __attribute__((ext_vector_type(2))) __bf16 bf16x2_t;
typedef __attribute__((ext_vector_type(2))) _Float16 f16x2_t;
typedef __attribute__((ext_vector_type(8))) _Float16 f16x8;
#define LAS __attribute__((address_space(3)))
#define GAS __attribute__((address_space(1)))
#define RLX_AGENT __ATOMIC_RELAXED, __HIP_MEMORY_SCOPE_AGENT
#define SBAR() __builtin_amdgcn_sched_barrier(0)
#define BAR() asm volatile("s_waitcnt lgkmcnt(0)\n\ts_barrier" ::: "memory")
#define WSR() __builtin_amdgcn_make_buffer_rsrc(p.ws, 0, 0x7fffffff, 0x00020000)
#define REREAD() asm volatile("" ::: "memory")

#define WNT " nt"
#define CODE_KB 23
#define NB1 2
#define REPG 2
#define PFG0 400
#define FDELAY 0
#define PFR0 16
#define SELPF 0
#define SELWAIT 50
#define MPRIO 0
#define YSLEEP 1
#define HPRIO 0
#define K2 10
#define TOUCH 0
#define WT 0
#define XPF 0

constexpr int L = 2048, D = 576, NST = 128, F = 4608;
constexpr int NFFN = 72;
constexpr u32 OFF_RG = 4096;
constexpr u32 OFF_G = OFF_RG + L * 8;
constexpr u32 OFF_SEL = OFF_G + REPG * 8192;
constexpr u32 OFF_CI = OFF_SEL + 16 * 256;
constexpr u32 OFF_PART = OFF_CI + 4096;
constexpr u32 SPIN_CAP = 1u << 19;

#define STAMP(i)
#define STAMP0_FIX()
#define STAMP_FLUSH()

struct Params {
    const u16 *x, *Are, *Aim, *Bre, *Bim, *Cre, *Cim, *logdt, *dskip, *wr, *wg, *wu, *wd, *gw;
    u16* out;
    char* ws;
};

__device__ __forceinline__ float bflo(u32 w) { return __uint_as_float(w << 16); }
__device__ __forceinline__ float bfhi(u32 w) { return __uint_as_float(w & 0xffff0000u); }
__device__ __forceinline__ float bf2f(u16 h) { return __uint_as_float((u32)h << 16); }
__device__ __forceinline__ u32 cvtpk(float lo, float hi) { v2f v = {lo, hi}; bf16x2_t r = __builtin_convertvector(v, bf16x2_t); return __builtin_bit_cast(u32, r); }
__device__ __forceinline__ u32 cvth(v2f v) { return __builtin_bit_cast(u32, __builtin_convertvector(v, f16x2_t)); }
__device__ __forceinline__ float rbf(float f) { return bflo(cvtpk(f, f)); }
__device__ __forceinline__ float sigmoidf_(float s) { return __builtin_amdgcn_rcpf(1.f + __expf(-s)); }

template <int CTRL, int RM> __device__ __forceinline__ float dpp_add(float v) {
    return v + __builtin_bit_cast(float, __builtin_amdgcn_update_dpp(0, __builtin_bit_cast(int, v), CTRL, RM, 0xf, true));
}
__device__ __forceinline__ float wave_sum63(float v) {
    v = dpp_add<0x111, 0xf>(v); v = dpp_add<0x112, 0xf>(v); v = dpp_add<0x114, 0xf>(v); v = dpp_add<0x118, 0xf>(v);
    v = dpp_add<0x142, 0xa>(v); v = dpp_add<0x143, 0xc>(v);
    return v;
}
template <int CTRL, int RM> __device__ __forceinline__ u32 dpp_umax(u32 v) {
    u32 o = (u32)__builtin_amdgcn_update_dpp(0, (int)v, CTRL, RM, 0xf, true);
    return v > o ? v : o;
}

typedef v2f cf;
__device__ __forceinline__ cf cmul(cf a, cf b) { cf t = a.yy * (cf){-b.y, b.x}; return __builtin_elementwise_fma(a.xx, b, t); }
__device__ __forceinline__ cf cexp_(float a2, float brev, float rho) {
    float m = __builtin_amdgcn_exp2f(a2 * rho);
    float ph = __builtin_amdgcn_fractf(brev * rho);
    cf c; c.x = m * __builtin_amdgcn_cosf(ph); c.y = m * __builtin_amdgcn_sinf(ph); return c;
}

struct cf2 { cf a, b; };
__device__ __forceinline__ cf2 cmul2(cf2 x, cf2 y) { return {cmul(x.a, y.a), cmul(x.b, y.b)}; }
__device__ __forceinline__ cf2 cexp2(v2f a2, v2f brev, float rho) { return {cexp_(a2.x, brev.x, rho), cexp_(a2.y, brev.y, rho)}; }

__device__ __forceinline__ u32 make_payload(float r, float g) {
    const u32 w = cvtpk(g, r);
    return w ^ (((u32)((int)w >> 31) & 0x7fff0000u) | 0x80000000u);
}
__device__ __forceinline__ u32 make_key(u32 pay, u32 t) { return (pay & 0xffff0000u) | (0xffffu - t); }
__device__ __forceinline__ float key_to_r(u32 k) {
    u32 hi = k & 0xffff0000u;
    return __uint_as_float((hi & 0x80000000u) ? (hi ^ 0x80000000u) : (~hi & 0xffff0000u));
}

constexpr int STG_ROW = 528;
constexpr int NA = 21;
constexpr int KCH = (NA + 32) * STG_ROW;
constexpr int KCOPY = 1344;
constexpr int CH_Y = 4 * KCOPY;
constexpr int LDS_U = 4 * KCH;
constexpr int LDS_TAP = LDS_U + 4 * 4608;
constexpr int LDS_CTL = LDS_TAP + 4 * 2304;
constexpr int C_TAG = 0, C_STG = 4, C_PUB = 8, C_ACK = 12, C_Y = 64;
constexpr int LDS_RW = LDS_CTL + 1024;
constexpr int LDS_G = LDS_RW;
constexpr int LDS_DUMMY = LDS_RW + 4 * 3072;
constexpr int LDS_TOTAL = LDS_DUMMY + 1024;
static_assert(CH_Y + 8 * 1152 <= KCH && LDS_TOTAL <= 160 * 1024, "lds");
constexpr int WDP = 1184;
constexpr int FL_WD = 0;
constexpr int FL_RED = 74 * 1024;
constexpr int FL_H = FL_RED + 24576;
constexpr int XSP = 1168;
constexpr int FL_XS = FL_H + 8 * 144;
constexpr int FL_Z = FL_XS + 8 * XSP;
constexpr int FL_TAG = FL_Z + 256;

__device__ __forceinline__ int sum_rows(int v) {
    auto a = __builtin_amdgcn_permlane16_swap((u32)v, (u32)v, false, false);
    v = (int)(a[0] + a[1]);
    auto b = __builtin_amdgcn_permlane32_swap((u32)v, (u32)v, false, false);
    return (int)(b[0] + b[1]);
}
__device__ __forceinline__ float sum_rows_f(float v) {
    auto a = __builtin_amdgcn_permlane16_swap(__float_as_uint(v), __float_as_uint(v), false, false);
    v = __uint_as_float(a[0]) + __uint_as_float(a[1]);
    auto b = __builtin_amdgcn_permlane32_swap(__float_as_uint(v), __float_as_uint(v), false, false);
    return __uint_as_float(b[0]) + __uint_as_float(b[1]);
}
__device__ __forceinline__ u32 top8_d(LAS char* t8, int lane, int n) {
    LAS u32* cand = (LAS u32*)(t8 + 256);
    u32 res = 0;
    _Pragma("unroll 1") for (int i0 = 0; i0 < n; i0 += 64) {
        const int i = i0 + lane;
        const u32 k = i < n ? cand[i] : 0u;
        int rk = 0;
        _Pragma("unroll 1") for (int j0 = 0; j0 < n; j0 += 16) {
            v4u c[4];
#pragma unroll
            for (int e = 0; e < 4; ++e) c[e] = *(LAS v4u*)(cand + j0 + 4 * e);
#pragma unroll
            for (int e = 0; e < 16; ++e) rk += (c[e >> 2][e & 3] > k) & (j0 + e < n);
        }
        const bool hit = i < n && rk < 8;
        const u32 got = (u32)__builtin_amdgcn_ds_permute((hit ? rk : 63) * 4, (int)(hit ? k : 0u));
        if (lane < 8) res |= got;
    }
    return res;
}

__device__ __forceinline__ u32 lds_peek(LAS char* word) { return __hip_atomic_load((LAS u32*)word, __ATOMIC_RELAXED, __HIP_MEMORY_SCOPE_WORKGROUP); }
#define LWAIT_BODY "1:\n\tds_read_b32 %0, %3 offset:%4\n\ts_waitcnt lgkmcnt(0)\n\tv_readfirstlane_b32 %1, %0\n\ts_cmp_ge_u32 %1, %5\n\ts_cbranch_scc1 2f\n\ts_sleep 1\n\ts_sub_u32 %2, %2, 1\n\ts_cbranch_scc0 1b\n2:"
#define LSPIN(addr, off, want) do { u32 t_, s_, c_; asm volatile("s_mov_b32 %2, 0x800000\n1:\n\tds_read_b32 %0, %3 offset:%4\n\ts_waitcnt lgkmcnt(0)\n\tv_readfirstlane_b32 %1, %0\n\ts_cmp_ge_u32 %1, %5\n\ts_cbranch_scc1 2f\n\ts_sub_u32 %2, %2, 1\n\ts_cbranch_scc0 1b\n2:" : "=&v"(t_), "=&s"(s_), "=&s"(c_) : "v"(addr), "n"(off), "n"(want) : "memory", "scc"); } while (0)
#define LWAIT(addr, off, want) do { u32 t_, s_, c_; asm volatile("s_mov_b32 %2, 0x400000\n" LWAIT_BODY : "=&v"(t_), "=&s"(s_), "=&s"(c_) : "v"(addr), "n"(off), "n"(want) : "memory", "scc"); } while (0)
#define LWAIT_PEEK(peek, addr, off, want) do { u32 t_, s_, c_; asm volatile("s_nop 0\n\tv_readfirstlane_b32 %1, %6\n\ts_mov_b32 %2, 0x400000\n\ts_cmp_ge_u32 %1, %5\n\ts_cbranch_scc1 2f\n" LWAIT_BODY : "=&v"(t_), "=&s"(s_), "=&s"(c_) : "v"(addr), "n"(off), "n"(want), "v"(peek) : "memory", "scc"); } while (0)
#define LDS_ADD1(addr, one, off) asm volatile("s_mov_b64 exec, 1\n\tds_add_u32 %0, %1 offset:%2\n\ts_mov_b64 exec, -1" :: "v"(addr), "v"(one), "n"(off) : "memory")
#define LDS_SETB(addr, one, off) asm volatile("s_mov_b64 exec, 1\n\tds_write_b8 %0, %1 offset:%2\n\ts_mov_b64 exec, -1" :: "v"(addr), "v"(one), "n"(off) : "memory")
__device__ __forceinline__ void glds16(const void* g, u32 lds_dst, bool nt) {
    u32 keep;
    if (nt) asm volatile("s_mov_b32 %0, m0\n\ts_mov_b32 m0, %2\n\ts_nop 0\n\tglobal_load_lds_dwordx4 %1, off" WNT "\n\ts_mov_b32 m0, %0" : "=&s"(keep) : "v"(g), "s"(lds_dst) : "memory");
    else asm volatile("s_mov_b32 %0, m0\n\ts_mov_b32 m0, %2\n\ts_nop 0\n\tglobal_load_lds_dwordx4 %1, off\n\ts_mov_b32 m0, %0" : "=&s"(keep) : "v"(g), "s"(lds_dst) : "memory");
}
__device__ __forceinline__ bf16x8 band(u32 bits, u32 val) {
    v4u r;
#pragma unroll
    for (int j = 0; j < 4; ++j) r[j] = __builtin_amdgcn_perm((u32)__builtin_amdgcn_sbfe((int)bits, 2 * j + 1, 1), (u32)__builtin_amdgcn_sbfe((int)bits, 2 * j, 1), 0x05040100u) & val;
    return __builtin_bit_cast(bf16x8, r);
}

template <int LIST> __device__ __forceinline__ bool touch(const Params& p, int xcd, int e, int lane, u32 dst) {
    const int wa = xcd * 9 + ((3 - (int)(((uintptr_t)p.wg >> 7) & 7) - xcd * 9) & 7), two = wa == xcd * 9;
    const int na = LIST ? 0 : (PFG0 * 2) << two, nb = LIST ? 0 : (576 - PFG0) * 18, nc = LIST ? 64 * 81 : 0;
    if (!LIST && e * 64 >= na + nb) return false;
    const u32 i = min(e * 64 + lane, na + nb + nc - 1);
    const char* a;
    if (!LIST) {
        const u32 j = i - na, k = j / 9u;
        const bool first = i < (u32)na;
        const u32 seg = first ? i >> two : k + 2 * PFG0, col = first ? (wa + 8 * (i & two)) * 128u : xcd * 1152u + (j - 9u * k) * 128u;
        a = (const char*)((seg & 1) ? p.wu : p.wg) + ((seg >> 1) * (F * 2) + col);
    } else {
        const u32 k = i / 9u, rw = k / 9u;
        a = (const char*)p.wd + (((xcd * 9u + k - 9u * rw) * 64u + (rw >> 1) + 32u * (rw & 1)) * 1152u + (i - 9u * k) * 128u);
    }
    asm volatile("s_mov_b32 m0, %1\n\ts_nop 0\n\tglobal_load_lds_dword %0, off" :: "v"(a), "s"(dst) : "memory");
    return true;
}

#define KGEN_ASM ".p2align 3\n\tv_mov_b32 v64, 0x3c00\n\tv_mov_b32 v65, 0x3c00\n\tv_pk_mul_f32 v[66:67], v[56:57], v[56:57] op_sel:[1,1] op_sel_hi:[1,0] neg_lo:[0,1]\n\tv_pk_mul_f32 v[68:69], v[58:59], v[58:59] op_sel:[1,1] op_sel_hi:[1,0] neg_lo:[0,1]\n\tv_pk_mul_f32 v[70:71], v[52:53], v[52:53] op_sel:[1,1] op_sel_hi:[1,0] neg_lo:[0,1]\n\tv_pk_mul_f32 v[72:73], v[54:55], v[54:55] op_sel:[1,1] op_sel_hi:[1,0] neg_lo:[0,1]\n\tv_pk_fma_f32 v[66:67], v[56:57], v[56:57], v[66:67] op_sel:[0,0,0] op_sel_hi:[0,1,1]\n\tv_pk_fma_f32 v[68:69], v[58:59], v[58:59], v[68:69] op_sel:[0,0,0] op_sel_hi:[0,1,1]\n\tv_pk_fma_f32 v[70:71], v[52:53], v[52:53], v[70:71] op_sel:[0,0,0] op_sel_hi:[0,1,1]\n\tv_pk_fma_f32 v[72:73], v[54:55], v[54:55], v[72:73] op_sel:[0,0,0] op_sel_hi:[0,1,1]\n\tv_cvt_pk_f16_f32 v74, v56, -v57\n\tv_cvt_pk_f16_f32 v75, v58, -v59\n\tv_cvt_pk_f16_f32 v76, v48, v49\n\tv_cvt_pk_f16_f32 v77, v50, v51\n\tv_pk_mul_f32 v[78:79], v[48:49], v[60:61] op_sel:[1,1] op_sel_hi:[1,0] neg_lo:[0,1]\n\tv_pk_mul_f32 v[80:81], v[50:51], v[62:63] op_sel:[1,1] op_sel_hi:[1,0] neg_lo:[0,1]\n\tv_pk_mul_f32 v[82:83], v[48:49], v[52:53] op_sel:[1,1] op_sel_hi:[1,0] neg_lo:[0,1]\n\tv_pk_mul_f32 v[84:85], v[50:51], v[54:55] op_sel:[1,1] op_sel_hi:[1,0] neg_lo:[0,1]\n\tds_write2_b64 %[b0], v[64:65], v[74:75] offset0:0 offset1:66\n\tv_pk_fma_f32 v[78:79], v[48:49], v[60:61], v[78:79] op_sel:[0,0,0] op_sel_hi:[0,1,1]\n\tv_pk_fma_f32 v[80:81], v[50:51], v[62:63], v[80:81] op_sel:[0,0,0] op_sel_hi:[0,1,1]\n\tv_pk_fma_f32 v[82:83], v[48:49], v[52:53], v[82:83] op_sel:[0,0,0] op_sel_hi:[0,1,1]\n\tv_pk_fma_f32 v[84:85], v[50:51], v[54:55], v[84:85] op_sel:[0,0,0] op_sel_hi:[0,1,1]\n\tv_cvt_pk_f16_f32 v86, v66, -v67\n\tv_cvt_pk_f16_f32 v87, v68, -v69\n\tv_pk_mul_f32 v[88:89], v[66:67], v[66:67] op_sel:[1,1] op_sel_hi:[1,0] neg_lo:[0,1]\n\tv_pk_mul_f32 v[90:91], v[68:69], v[68:69] op_sel:[1,1] op_sel_hi:[1,0] neg_lo:[0,1]\n\tv_pk_mul_f32 v[92:93], v[66:67], v[56:57] op_sel:[1,1] op_sel_hi:[1,0] neg_lo:[0,1]\n\tv_pk_mul_f32 v[94:95], v[68:69], v[58:59] op_sel:[1,1] op_sel_hi:[1,0] neg_lo:[0,1]\n\tv_pk_fma_f32 v[88:89], v[66:67], v[66:67], v[88:89] op_sel:[0,0,0] op_sel_hi:[0,1,1]\n\tv_pk_fma_f32 v[90:91], v[68:69], v[68:69], v[90:91] op_sel:[0,0,0] op_sel_hi:[0,1,1]\n\tv_pk_fma_f32 v[92:93], v[66:67], v[56:57], v[92:93] op_sel:[0,0,0] op_sel_hi:[0,1,1]\n\tv_pk_fma_f32 v[94:95], v[68:69], v[58:59], v[94:95] op_sel:[0,0,0] op_sel_hi:[0,1,1]\n\tv_cvt_pk_f16_f32 v64, v78, v79\n\tv_cvt_pk_f16_f32 v65, v80, v81\n\tv_cvt_pk_f16_f32 v96, v82, v83\n\tv_cvt_pk_f16_f32 v97, v84, v85\n\tv_pk_mul_f32 v[98:99], v[70:71], v[70:71] op_sel:[1,1] op_sel_hi:[1,0] neg_lo:[0,1]\n\tv_pk_mul_f32 v[100:101], v[72:73], v[72:73] op_sel:[1,1] op_sel_hi:[1,0] neg_lo:[0,1]\n\tv_pk_mul_f32 v[102:103], v[78:79], v[56:57] op_sel:[1,1] op_sel_hi:[1,0] neg_lo:[0,1]\n\tv_pk_mul_f32 v[104:105], v[80:81], v[58:59] op_sel:[1,1] op_sel_hi:[1,0] neg_lo:[0,1]\n\tds_write2_b64 %[b8], v[76:77], v[96:97] offset0:0 offset1:66\n\tv_pk_fma_f32 v[98:99], v[70:71], v[70:71], v[98:99] op_sel:[0,0,0] op_sel_hi:[0,1,1]\n\tv_pk_fma_f32 v[100:101], v[72:73], v[72:73], v[100:101] op_sel:[0,0,0] op_sel_hi:[0,1,1]\n\tv_pk_fma_f32 v[102:103], v[78:79], v[56:57], v[102:103] op_sel:[0,0,0] op_sel_hi:[0,1,1]\n\tv_pk_fma_f32 v[104:105], v[80:81], v[58:59], v[104:105] op_sel:[0,0,0] op_sel_hi:[0,1,1]\n\tv_cvt_pk_f16_f32 v106, v88, -v89\n\tv_cvt_pk_f16_f32 v107, v90, -v91\n\tv_cvt_pk_f16_f32 v108, v92, -v93\n\tv_cvt_pk_f16_f32 v109, v94, -v95\n\tv_pk_mul_f32 v[110:111], v[78:79], v[70:71] op_sel:[1,1] op_sel_hi:[1,0] neg_lo:[0,1]\n\tv_pk_mul_f32 v[112:113], v[80:81], v[72:73] op_sel:[1,1] op_sel_hi:[1,0] neg_lo:[0,1]\n\tv_pk_mul_f32 v[114:115], v[48:49], v[70:71] op_sel:[1,1] op_sel_hi:[1,0] neg_lo:[0,1]\n\tv_pk_mul_f32 v[116:117], v[50:51], v[72:73] op_sel:[1,1] op_sel_hi:[1,0] neg_lo:[0,1]\n\tds_write2_b64 %[b0], v[86:87], v[108:109] offset0:132 offset1:198\n\tv_pk_fma_f32 v[110:111], v[78:79], v[70:71], v[110:111] op_sel:[0,0,0] op_sel_hi:[0,1,1]\n\tv_pk_fma_f32 v[112:113], v[80:81], v[72:73], v[112:113] op_sel:[0,0,0] op_sel_hi:[0,1,1]\n\tv_pk_fma_f32 v[114:115], v[48:49], v[70:71], v[114:115] op_sel:[0,0,0] op_sel_hi:[0,1,1]\n\tv_pk_fma_f32 v[116:117], v[50:51], v[72:73], v[116:117] op_sel:[0,0,0] op_sel_hi:[0,1,1]\n\tv_cvt_pk_f16_f32 v76, v102, v103\n\tv_cvt_pk_f16_f32 v77, v104, v105\n\tv_pk_mul_f32 v[96:97], v[82:83], v[70:71] op_sel:[1,1] op_sel_hi:[1,0] neg_lo:[0,1]\n\tv_pk_mul_f32 v[118:119], v[84:85], v[72:73] op_sel:[1,1] op_sel_hi:[1,0] neg_lo:[0,1]\n\tv_pk_mul_f32 v[120:121], v[88:89], v[88:89] op_sel:[1,1] op_sel_hi:[1,0] neg_lo:[0,1]\n\tv_pk_mul_f32 v[122:123], v[90:91], v[90:91] op_sel:[1,1] op_sel_hi:[1,0] neg_lo:[0,1]\n\tv_pk_fma_f32 v[96:97], v[82:83], v[70:71], v[96:97] op_sel:[0,0,0] op_sel_hi:[0,1,1]\n\tv_pk_fma_f32 v[118:119], v[84:85], v[72:73], v[118:119] op_sel:[0,0,0] op_sel_hi:[0,1,1]\n\tv_pk_fma_f32 v[120:121], v[88:89], v[88:89], v[120:121] op_sel:[0,0,0] op_sel_hi:[0,1,1]\n\tv_pk_fma_f32 v[122:123], v[90:91], v[90:91], v[122:123] op_sel:[0,0,0] op_sel_hi:[0,1,1]\n\tv_cvt_pk_f16_f32 v124, v110, v111\n\tv_cvt_pk_f16_f32 v125, v112, v113\n\tv_cvt_pk_f16_f32 v126, v114, v115\n\tv_cvt_pk_f16_f32 v127, v116, v117\n\tv_pk_mul_f32 v[70:71], v[88:89], v[56:57] op_sel:[1,1] op_sel_hi:[1,0] neg_lo:[0,1]\n\tv_pk_mul_f32 v[72:73], v[90:91], v[58:59] op_sel:[1,1] op_sel_hi:[1,0] neg_lo:[0,1]\n\tv_pk_mul_f32 v[128:129], v[102:103], v[60:61] op_sel:[1,1] op_sel_hi:[1,0] neg_lo:[0,1]\n\tv_pk_mul_f32 v[130:131], v[104:105], v[62:63] op_sel:[1,1] op_sel_hi:[1,0] neg_lo:[0,1]\n\tds_write2_b64 %[b11], v[64:65], v[124:125] offset0:0 offset1:66\n\tv_pk_fma_f32 v[70:71], v[88:89], v[56:57], v[70:71] op_sel:[0,0,0] op_sel_hi:[0,1,1]\n\tv_pk_fma_f32 v[72:73], v[90:91], v[58:59], v[72:73] op_sel:[0,0,0] op_sel_hi:[0,1,1]\n\tv_pk_fma_f32 v[128:129], v[102:103], v[60:61], v[128:129] op_sel:[0,0,0] op_sel_hi:[0,1,1]\n\tv_pk_fma_f32 v[130:131], v[104:105], v[62:63], v[130:131] op_sel:[0,0,0] op_sel_hi:[0,1,1]\n\tv_cvt_pk_f16_f32 v132, v96, v97\n\tv_cvt_pk_f16_f32 v133, v118, v119\n\tv_cvt_pk_f16_f32 v134, v120, -v121\n\tv_cvt_pk_f16_f32 v135, v122, -v123\n\tv_pk_mul_f32 v[102:103], v[78:79], v[98:99] op_sel:[1,1] op_sel_hi:[1,0] neg_lo:[0,1]\n\tv_pk_mul_f32 v[104:105], v[80:81], v[100:101] op_sel:[1,1] op_sel_hi:[1,0] neg_lo:[0,1]\n\tv_pk_mul_f32 v[136:137], v[88:89], v[66:67] op_sel:[1,1] op_sel_hi:[1,0] neg_lo:[0,1]\n\tv_pk_mul_f32 v[138:139], v[90:91], v[68:69] op_sel:[1,1] op_sel_hi:[1,0] neg_lo:[0,1]\n\tds_write2_b64 %[b8], v[126:127], v[132:133] offset0:132 offset1:198\n\tv_pk_fma_f32 v[102:103], v[78:79], v[98:99], v[102:103] op_sel:[0,0,0] op_sel_hi:[0,1,1]\n\tv_pk_fma_f32 v[104:105], v[80:81], v[100:101], v[104:105] op_sel:[0,0,0] op_sel_hi:[0,1,1]\n\tv_pk_fma_f32 v[136:137], v[88:89], v[66:67], v[136:137] op_sel:[0,0,0] op_sel_hi:[0,1,1]\n\tv_pk_fma_f32 v[138:139], v[90:91], v[68:69], v[138:139] op_sel:[0,0,0] op_sel_hi:[0,1,1]\n\tv_cvt_pk_f16_f32 v64, v70, -v71\n\tv_cvt_pk_f16_f32 v65, v72, -v73\n\tv_cvt_pk_f16_f32 v124, v128, v129\n\tv_cvt_pk_f16_f32 v125, v130, v131\n\tv_pk_mul_f32 v[78:79], v[88:89], v[92:93] op_sel:[1,1] op_sel_hi:[1,0] neg_lo:[0,1]\n\tv_pk_mul_f32 v[80:81], v[90:91], v[94:95] op_sel:[1,1] op_sel_hi:[1,0] neg_lo:[0,1]\n\tv_pk_mul_f32 v[140:141], v[120:121], v[120:121] op_sel:[1,1] op_sel_hi:[1,0] neg_lo:[0,1]\n\tv_pk_mul_f32 v[142:143], v[122:123], v[122:123] op_sel:[1,1] op_sel_hi:[1,0] neg_lo:[0,1]\n\tds_write2_b64 %[b1], v[106:107], v[64:65] offset0:0 offset1:66\n\tv_pk_fma_f32 v[78:79], v[88:89], v[92:93], v[78:79] op_sel:[0,0,0] op_sel_hi:[0,1,1]\n\tv_pk_fma_f32 v[80:81], v[90:91], v[94:95], v[80:81] op_sel:[0,0,0] op_sel_hi:[0,1,1]\n\tv_pk_fma_f32 v[140:141], v[120:121], v[120:121], v[140:141] op_sel:[0,0,0] op_sel_hi:[0,1,1]\n\tv_pk_fma_f32 v[142:143], v[122:123], v[122:123], v[142:143] op_sel:[0,0,0] op_sel_hi:[0,1,1]\n\tv_cvt_pk_f16_f32 v126, v102, v103\n\tv_cvt_pk_f16_f32 v127, v104, v105\n\tv_cvt_pk_f16_f32 v132, v136, -v137\n\tv_cvt_pk_f16_f32 v133, v138, -v139\n\tv_pk_mul_f32 v[144:145], v[120:121], v[56:57] op_sel:[1,1] op_sel_hi:[1,0] neg_lo:[0,1]\n\tv_pk_mul_f32 v[146:147], v[122:123], v[58:59] op_sel:[1,1] op_sel_hi:[1,0] neg_lo:[0,1]\n\tv_pk_mul_f32 v[148:149], v[128:129], v[60:61] op_sel:[1,1] op_sel_hi:[1,0] neg_lo:[0,1]\n\tv_pk_mul_f32 v[150:151], v[130:131], v[62:63] op_sel:[1,1] op_sel_hi:[1,0] neg_lo:[0,1]\n\tds_write2_b64 %[b9], v[76:77], v[124:125] offset0:0 offset1:66\n\tv_pk_fma_f32 v[144:145], v[120:121], v[56:57], v[144:145] op_sel:[0,0,0] op_sel_hi:[0,1,1]\n\tv_pk_fma_f32 v[146:147], v[122:123], v[58:59], v[146:147] op_sel:[0,0,0] op_sel_hi:[0,1,1]\n\tv_pk_fma_f32 v[148:149], v[128:129], v[60:61], v[148:149] op_sel:[0,0,0] op_sel_hi:[0,1,1]\n\tv_pk_fma_f32 v[150:151], v[130:131], v[62:63], v[150:151] op_sel:[0,0,0] op_sel_hi:[0,1,1]\n\tv_cvt_pk_f16_f32 v152, v78, -v79\n\tv_cvt_pk_f16_f32 v153, v80, -v81\n\tv_cvt_pk_f16_f32 v154, v140, -v141\n\tv_cvt_pk_f16_f32 v155, v142, -v143\n\tv_pk_mul_f32 v[128:129], v[102:103], v[60:61] op_sel:[1,1] op_sel_hi:[1,0] neg_lo:[0,1]\n\tv_pk_mul_f32 v[130:131], v[104:105], v[62:63] op_sel:[1,1] op_sel_hi:[1,0] neg_lo:[0,1]\n\tv_pk_mul_f32 v[140:141], v[110:111], v[98:99] op_sel:[1,1] op_sel_hi:[1,0] neg_lo:[0,1]\n\tv_pk_mul_f32 v[142:143], v[112:113], v[100:101] op_sel:[1,1] op_sel_hi:[1,0] neg_lo:[0,1]\n\tds_write2_b64 %[b1], v[132:133], v[152:153] offset0:132 offset1:198\n\tv_pk_fma_f32 v[128:129], v[102:103], v[60:61], v[128:129] op_sel:[0,0,0] op_sel_hi:[0,1,1]\n\tv_pk_fma_f32 v[130:131], v[104:105], v[62:63], v[130:131] op_sel:[0,0,0] op_sel_hi:[0,1,1]\n\tv_pk_fma_f32 v[140:141], v[110:111], v[98:99], v[140:141] op_sel:[0,0,0] op_sel_hi:[0,1,1]\n\tv_pk_fma_f32 v[142:143], v[112:113], v[100:101], v[142:143] op_sel:[0,0,0] op_sel_hi:[0,1,1]\n\tv_cvt_pk_f16_f32 v76, v144, -v145\n\tv_cvt_pk_f16_f32 v77, v146, -v147\n\tv_cvt_pk_f16_f32 v124, v148, v149\n\tv_cvt_pk_f16_f32 v125, v150, v151\n\tv_pk_mul_f32 v[102:103], v[120:121], v[66:67] op_sel:[1,1] op_sel_hi:[1,0] neg_lo:[0,1]\n\tv_pk_mul_f32 v[104:105], v[122:123], v[68:69] op_sel:[1,1] op_sel_hi:[1,0] neg_lo:[0,1]\n\tv_pk_mul_f32 v[110:111], v[120:121], v[92:93] op_sel:[1,1] op_sel_hi:[1,0] neg_lo:[0,1]\n\tv_pk_mul_f32 v[112:113], v[122:123], v[94:95] op_sel:[1,1] op_sel_hi:[1,0] neg_lo:[0,1]\n\tds_write2_b64 %[b2], v[134:135], v[76:77] offset0:0 offset1:66\n\tv_pk_fma_f32 v[102:103], v[120:121], v[66:67], v[102:103] op_sel:[0,0,0] op_sel_hi:[0,1,1]\n\tv_pk_fma_f32 v[104:105], v[122:123], v[68:69], v[104:105] op_sel:[0,0,0] op_sel_hi:[0,1,1]\n\tv_pk_fma_f32 v[110:111], v[120:121], v[92:93], v[110:111] op_sel:[0,0,0] op_sel_hi:[0,1,1]\n\tv_pk_fma_f32 v[112:113], v[122:123], v[94:95], v[112:113] op_sel:[0,0,0] op_sel_hi:[0,1,1]\n\tv_cvt_pk_f16_f32 v144, v128, v129\n\tv_cvt_pk_f16_f32 v145, v130, v131\n\tv_cvt_pk_f16_f32 v146, v140, v141\n\tv_cvt_pk_f16_f32 v147, v142, v143\n\tv_pk_mul_f32 v[66:67], v[148:149], v[60:61] op_sel:[1,1] op_sel_hi:[1,0] neg_lo:[0,1]\n\tv_pk_mul_f32 v[68:69], v[150:151], v[62:63] op_sel:[1,1] op_sel_hi:[1,0] neg_lo:[0,1]\n\tv_pk_mul_f32 v[92:93], v[128:129], v[60:61] op_sel:[1,1] op_sel_hi:[1,0] neg_lo:[0,1]\n\tv_pk_mul_f32 v[94:95], v[130:131], v[62:63] op_sel:[1,1] op_sel_hi:[1,0] neg_lo:[0,1]\n\tds_write2_b64 %[b9], v[124:125], v[146:147] offset0:132 offset1:198\n\tv_pk_fma_f32 v[66:67], v[148:149], v[60:61], v[66:67] op_sel:[0,0,0] op_sel_hi:[0,1,1]\n\tv_pk_fma_f32 v[68:69], v[150:151], v[62:63], v[68:69] op_sel:[0,0,0] op_sel_hi:[0,1,1]\n\tv_pk_fma_f32 v[92:93], v[128:129], v[60:61], v[92:93] op_sel:[0,0,0] op_sel_hi:[0,1,1]\n\tv_pk_fma_f32 v[94:95], v[130:131], v[62:63], v[94:95] op_sel:[0,0,0] op_sel_hi:[0,1,1]\n\tv_cvt_pk_f16_f32 v140, v102, -v103\n\tv_cvt_pk_f16_f32 v141, v104, -v105\n\tv_cvt_pk_f16_f32 v142, v110, -v111\n\tv_cvt_pk_f16_f32 v143, v112, -v113\n\tv_pk_mul_f32 v[102:103], v[120:121], v[88:89] op_sel:[1,1] op_sel_hi:[1,0] neg_lo:[0,1]\n\tv_pk_mul_f32 v[104:105], v[122:123], v[90:91] op_sel:[1,1] op_sel_hi:[1,0] neg_lo:[0,1]\n\tv_pk_mul_f32 v[110:111], v[120:121], v[70:71] op_sel:[1,1] op_sel_hi:[1,0] neg_lo:[0,1]\n\tv_pk_mul_f32 v[112:113], v[122:123], v[72:73] op_sel:[1,1] op_sel_hi:[1,0] neg_lo:[0,1]\n\tds_write2_b64 %[b2], v[140:141], v[142:143] offset0:132 offset1:198\n\tv_pk_fma_f32 v[102:103], v[120:121], v[88:89], v[102:103] op_sel:[0,0,0] op_sel_hi:[0,1,1]\n\tv_pk_fma_f32 v[104:105], v[122:123], v[90:91], v[104:105] op_sel:[0,0,0] op_sel_hi:[0,1,1]\n\tv_pk_fma_f32 v[110:111], v[120:121], v[70:71], v[110:111] op_sel:[0,0,0] op_sel_hi:[0,1,1]\n\tv_pk_fma_f32 v[112:113], v[122:123], v[72:73], v[112:113] op_sel:[0,0,0] op_sel_hi:[0,1,1]\n\tv_cvt_pk_f16_f32 v124, v66, v67\n\tv_cvt_pk_f16_f32 v125, v68, v69\n\tv_cvt_pk_f16_f32 v128, v92, v93\n\tv_cvt_pk_f16_f32 v129, v94, v95\n\tv_pk_mul_f32 v[70:71], v[48:49], v[98:99] op_sel:[1,1] op_sel_hi:[1,0] neg_lo:[0,1]\n\tv_pk_mul_f32 v[72:73], v[50:51], v[100:101] op_sel:[1,1] op_sel_hi:[1,0] neg_lo:[0,1]\n\tv_pk_mul_f32 v[88:89], v[82:83], v[98:99] op_sel:[1,1] op_sel_hi:[1,0] neg_lo:[0,1]\n\tv_pk_mul_f32 v[90:91], v[84:85], v[100:101] op_sel:[1,1] op_sel_hi:[1,0] neg_lo:[0,1]\n\tds_write2_b64 %[b10], v[126:127], v[144:145] offset0:132 offset1:198\n\tv_pk_fma_f32 v[70:71], v[48:49], v[98:99], v[70:71] op_sel:[0,0,0] op_sel_hi:[0,1,1]\n\tv_pk_fma_f32 v[72:73], v[50:51], v[100:101], v[72:73] op_sel:[0,0,0] op_sel_hi:[0,1,1]\n\tv_pk_fma_f32 v[88:89], v[82:83], v[98:99], v[88:89] op_sel:[0,0,0] op_sel_hi:[0,1,1]\n\tv_pk_fma_f32 v[90:91], v[84:85], v[100:101], v[90:91] op_sel:[0,0,0] op_sel_hi:[0,1,1]\n\tv_cvt_pk_f16_f32 v92, v102, -v103\n\tv_cvt_pk_f16_f32 v93, v104, -v105\n\tv_cvt_pk_f16_f32 v94, v110, -v111\n\tv_cvt_pk_f16_f32 v95, v112, -v113\n\tv_pk_mul_f32 v[82:83], v[66:67], v[60:61] op_sel:[1,1] op_sel_hi:[1,0] neg_lo:[0,1]\n\tv_pk_mul_f32 v[84:85], v[68:69], v[62:63] op_sel:[1,1] op_sel_hi:[1,0] neg_lo:[0,1]\n\tv_pk_mul_f32 v[102:103], v[120:121], v[136:137] op_sel:[1,1] op_sel_hi:[1,0] neg_lo:[0,1]\n\tv_pk_mul_f32 v[104:105], v[122:123], v[138:139] op_sel:[1,1] op_sel_hi:[1,0] neg_lo:[0,1]\n\tds_write2_b64 %[b3], v[92:93], v[94:95] offset0:0 offset1:66\n\tv_pk_fma_f32 v[82:83], v[66:67], v[60:61], v[82:83] op_sel:[0,0,0] op_sel_hi:[0,1,1]\n\tv_pk_fma_f32 v[84:85], v[68:69], v[62:63], v[84:85] op_sel:[0,0,0] op_sel_hi:[0,1,1]\n\tv_pk_fma_f32 v[102:103], v[120:121], v[136:137], v[102:103] op_sel:[0,0,0] op_sel_hi:[0,1,1]\n\tv_pk_fma_f32 v[104:105], v[122:123], v[138:139], v[104:105] op_sel:[0,0,0] op_sel_hi:[0,1,1]\n\tv_cvt_pk_f16_f32 v110, v70, v71\n\tv_cvt_pk_f16_f32 v111, v72, v73\n\tv_cvt_pk_f16_f32 v112, v88, v89\n\tv_cvt_pk_f16_f32 v113, v90, v91\n\tv_pk_mul_f32 v[66:67], v[120:121], v[78:79] op_sel:[1,1] op_sel_hi:[1,0] neg_lo:[0,1]\n\tv_pk_mul_f32 v[68:69], v[122:123], v[80:81] op_sel:[1,1] op_sel_hi:[1,0] neg_lo:[0,1]\n\tv_pk_mul_f32 v[70:71], v[114:115], v[98:99] op_sel:[1,1] op_sel_hi:[1,0] neg_lo:[0,1]\n\tv_pk_mul_f32 v[72:73], v[116:117], v[100:101] op_sel:[1,1] op_sel_hi:[1,0] neg_lo:[0,1]\n\tds_write2_b64 %[b11], v[110:111], v[112:113] offset0:132 offset1:198\n\tv_pk_fma_f32 v[66:67], v[120:121], v[78:79], v[66:67] op_sel:[0,0,0] op_sel_hi:[0,1,1]\n\tv_pk_fma_f32 v[68:69], v[122:123], v[80:81], v[68:69] op_sel:[0,0,0] op_sel_hi:[0,1,1]\n\tv_pk_fma_f32 v[70:71], v[114:115], v[98:99], v[70:71] op_sel:[0,0,0] op_sel_hi:[0,1,1]\n\tv_pk_fma_f32 v[72:73], v[116:117], v[100:101], v[72:73] op_sel:[0,0,0] op_sel_hi:[0,1,1]\n\tv_cvt_pk_f16_f32 v88, v82, v83\n\tv_cvt_pk_f16_f32 v89, v84, v85\n\tv_cvt_pk_f16_f32 v90, v102, -v103\n\tv_cvt_pk_f16_f32 v91, v104, -v105\n\tv_pk_mul_f32 v[78:79], v[96:97], v[98:99] op_sel:[1,1] op_sel_hi:[1,0] neg_lo:[0,1]\n\tv_pk_mul_f32 v[80:81], v[118:119], v[100:101] op_sel:[1,1] op_sel_hi:[1,0] neg_lo:[0,1]\n\tv_pk_mul_f32 v[102:103], v[82:83], v[60:61] op_sel:[1,1] op_sel_hi:[1,0] neg_lo:[0,1]\n\tv_pk_mul_f32 v[104:105], v[84:85], v[62:63] op_sel:[1,1] op_sel_hi:[1,0] neg_lo:[0,1]\n\tds_write2_b64 %[b12], v[128:129], v[124:125] offset0:132 offset1:198\n\tv_pk_fma_f32 v[78:79], v[96:97], v[98:99], v[78:79] op_sel:[0,0,0] op_sel_hi:[0,1,1]\n\tv_pk_fma_f32 v[80:81], v[118:119], v[100:101], v[80:81] op_sel:[0,0,0] op_sel_hi:[0,1,1]\n\tv_pk_fma_f32 v[102:103], v[82:83], v[60:61], v[102:103] op_sel:[0,0,0] op_sel_hi:[0,1,1]\n\tv_pk_fma_f32 v[104:105], v[84:85], v[62:63], v[104:105] op_sel:[0,0,0] op_sel_hi:[0,1,1]\n\tv_cvt_pk_f16_f32 v110, v66, -v67\n\tv_cvt_pk_f16_f32 v111, v68, -v69\n\tv_cvt_pk_f16_f32 v112, v70, v71\n\tv_cvt_pk_f16_f32 v113, v72, v73\n\tv_pk_mul_f16 v66, v154, v74 op_sel:[1,1] op_sel_hi:[1,0] neg_lo:[0,1]\n\tv_pk_mul_f16 v67, v155, v75 op_sel:[1,1] op_sel_hi:[1,0] neg_lo:[0,1]\n\tv_pk_mul_f16 v68, v154, v86 op_sel:[1,1] op_sel_hi:[1,0] neg_lo:[0,1]\n\tv_pk_mul_f16 v69, v155, v87 op_sel:[1,1] op_sel_hi:[1,0] neg_lo:[0,1]\n\tds_write2_b64 %[b3], v[90:91], v[110:111] offset0:132 offset1:198\n\tv_pk_fma_f16 v66, v154, v74, v66 op_sel:[0,0,0] op_sel_hi:[0,1,1]\n\tv_pk_fma_f16 v67, v155, v75, v67 op_sel:[0,0,0] op_sel_hi:[0,1,1]\n\tv_pk_fma_f16 v68, v154, v86, v68 op_sel:[0,0,0] op_sel_hi:[0,1,1]\n\tv_pk_fma_f16 v69, v155, v87, v69 op_sel:[0,0,0] op_sel_hi:[0,1,1]\n\tv_cvt_pk_f16_f32 v70, v78, v79\n\tv_cvt_pk_f16_f32 v71, v80, v81\n\tv_cvt_pk_f16_f32 v72, v102, v103\n\tv_cvt_pk_f16_f32 v73, v104, v105\n\tv_pk_mul_f16 v78, v154, v108 op_sel:[1,1] op_sel_hi:[1,0] neg_lo:[0,1]\n\tv_pk_mul_f16 v79, v155, v109 op_sel:[1,1] op_sel_hi:[1,0] neg_lo:[0,1]\n\tv_pk_mul_f32 v[80:81], v[102:103], v[60:61] op_sel:[1,1] op_sel_hi:[1,0] neg_lo:[0,1]\n\tv_pk_mul_f32 v[82:83], v[104:105], v[62:63] op_sel:[1,1] op_sel_hi:[1,0] neg_lo:[0,1]\n\tds_write2_b64 %[b4], v[154:155], v[66:67] offset0:0 offset1:66\n\tv_pk_fma_f16 v78, v154, v108, v78 op_sel:[0,0,0] op_sel_hi:[0,1,1]\n\tv_pk_fma_f16 v79, v155, v109, v79 op_sel:[0,0,0] op_sel_hi:[0,1,1]\n\tv_pk_fma_f32 v[80:81], v[102:103], v[60:61], v[80:81] op_sel:[0,0,0] op_sel_hi:[0,1,1]\n\tv_pk_fma_f32 v[82:83], v[104:105], v[62:63], v[82:83] op_sel:[0,0,0] op_sel_hi:[0,1,1]\n\tv_pk_mul_f16 v74, v154, v106 op_sel:[1,1] op_sel_hi:[1,0] neg_lo:[0,1]\n\tv_pk_mul_f16 v75, v155, v107 op_sel:[1,1] op_sel_hi:[1,0] neg_lo:[0,1]\n\tv_pk_mul_f16 v84, v154, v64 op_sel:[1,1] op_sel_hi:[1,0] neg_lo:[0,1]\n\tv_pk_mul_f16 v85, v155, v65 op_sel:[1,1] op_sel_hi:[1,0] neg_lo:[0,1]\n\tds_write2_b64 %[b4], v[68:69], v[78:79] offset0:132 offset1:198\n\tv_pk_fma_f16 v74, v154, v106, v74 op_sel:[0,0,0] op_sel_hi:[0,1,1]\n\tv_pk_fma_f16 v75, v155, v107, v75 op_sel:[0,0,0] op_sel_hi:[0,1,1]\n\tv_pk_fma_f16 v84, v154, v64, v84 op_sel:[0,0,0] op_sel_hi:[0,1,1]\n\tv_pk_fma_f16 v85, v155, v65, v85 op_sel:[0,0,0] op_sel_hi:[0,1,1]\n\tv_cvt_pk_f16_f32 v66, v80, v81\n\tv_cvt_pk_f16_f32 v67, v82, v83\n\tv_pk_mul_f16 v80, v154, v132 op_sel:[1,1] op_sel_hi:[1,0] neg_lo:[0,1]\n\tv_pk_mul_f16 v81, v155, v133 op_sel:[1,1] op_sel_hi:[1,0] neg_lo:[0,1]\n\tv_pk_mul_f16 v82, v154, v152 op_sel:[1,1] op_sel_hi:[1,0] neg_lo:[0,1]\n\tv_pk_mul_f16 v83, v155, v153 op_sel:[1,1] op_sel_hi:[1,0] neg_lo:[0,1]\n\tds_write2_b64 %[b5], v[74:75], v[84:85] offset0:0 offset1:66\n\tv_pk_fma_f16 v80, v154, v132, v80 op_sel:[0,0,0] op_sel_hi:[0,1,1]\n\tv_pk_fma_f16 v81, v155, v133, v81 op_sel:[0,0,0] op_sel_hi:[0,1,1]\n\tv_pk_fma_f16 v82, v154, v152, v82 op_sel:[0,0,0] op_sel_hi:[0,1,1]\n\tv_pk_fma_f16 v83, v155, v153, v83 op_sel:[0,0,0] op_sel_hi:[0,1,1]\n\tv_pk_mul_f16 v64, v154, v134 op_sel:[1,1] op_sel_hi:[1,0] neg_lo:[0,1]\n\tv_pk_mul_f16 v65, v155, v135 op_sel:[1,1] op_sel_hi:[1,0] neg_lo:[0,1]\n\tv_pk_mul_f16 v68, v154, v76 op_sel:[1,1] op_sel_hi:[1,0] neg_lo:[0,1]\n\tv_pk_mul_f16 v69, v155, v77 op_sel:[1,1] op_sel_hi:[1,0] neg_lo:[0,1]\n\tds_write2_b64 %[b5], v[80:81], v[82:83] offset0:132 offset1:198\n\tv_pk_fma_f16 v64, v154, v134, v64 op_sel:[0,0,0] op_sel_hi:[0,1,1]\n\tv_pk_fma_f16 v65, v155, v135, v65 op_sel:[0,0,0] op_sel_hi:[0,1,1]\n\tv_pk_fma_f16 v68, v154, v76, v68 op_sel:[0,0,0] op_sel_hi:[0,1,1]\n\tv_pk_fma_f16 v69, v155, v77, v69 op_sel:[0,0,0] op_sel_hi:[0,1,1]\n\tv_pk_mul_f16 v74, v154, v140 op_sel:[1,1] op_sel_hi:[1,0] neg_lo:[0,1]\n\tv_pk_mul_f16 v75, v155, v141 op_sel:[1,1] op_sel_hi:[1,0] neg_lo:[0,1]\n\tv_pk_mul_f16 v78, v154, v142 op_sel:[1,1] op_sel_hi:[1,0] neg_lo:[0,1]\n\tv_pk_mul_f16 v79, v155, v143 op_sel:[1,1] op_sel_hi:[1,0] neg_lo:[0,1]\n\tds_write2_b64 %[b6], v[64:65], v[68:69] offset0:0 offset1:66\n\tv_pk_fma_f16 v74, v154, v140, v74 op_sel:[0,0,0] op_sel_hi:[0,1,1]\n\tv_pk_fma_f16 v75, v155, v141, v75 op_sel:[0,0,0] op_sel_hi:[0,1,1]\n\tv_pk_fma_f16 v78, v154, v142, v78 op_sel:[0,0,0] op_sel_hi:[0,1,1]\n\tv_pk_fma_f16 v79, v155, v143, v79 op_sel:[0,0,0] op_sel_hi:[0,1,1]\n\tv_pk_mul_f16 v76, v154, v92 op_sel:[1,1] op_sel_hi:[1,0] neg_lo:[0,1]\n\tv_pk_mul_f16 v77, v155, v93 op_sel:[1,1] op_sel_hi:[1,0] neg_lo:[0,1]\n\tv_pk_mul_f16 v80, v154, v94 op_sel:[1,1] op_sel_hi:[1,0] neg_lo:[0,1]\n\tv_pk_mul_f16 v81, v155, v95 op_sel:[1,1] op_sel_hi:[1,0] neg_lo:[0,1]\n\tds_write2_b64 %[b6], v[74:75], v[78:79] offset0:132 offset1:198\n\tv_pk_fma_f16 v76, v154, v92, v76 op_sel:[0,0,0] op_sel_hi:[0,1,1]\n\tv_pk_fma_f16 v77, v155, v93, v77 op_sel:[0,0,0] op_sel_hi:[0,1,1]\n\tv_pk_fma_f16 v80, v154, v94, v80 op_sel:[0,0,0] op_sel_hi:[0,1,1]\n\tv_pk_fma_f16 v81, v155, v95, v81 op_sel:[0,0,0] op_sel_hi:[0,1,1]\n\tv_pk_mul_f16 v64, v154, v90 op_sel:[1,1] op_sel_hi:[1,0] neg_lo:[0,1]\n\tv_pk_mul_f16 v65, v155, v91 op_sel:[1,1] op_sel_hi:[1,0] neg_lo:[0,1]\n\tv_pk_mul_f16 v68, v154, v110 op_sel:[1,1] op_sel_hi:[1,0] neg_lo:[0,1]\n\tv_pk_mul_f16 v69, v155, v111 op_sel:[1,1] op_sel_hi:[1,0] neg_lo:[0,1]\n\tds_write2_b64 %[b7], v[76:77], v[80:81] offset0:0 offset1:66\n\tv_pk_fma_f16 v64, v154, v90, v64 op_sel:[0,0,0] op_sel_hi:[0,1,1]\n\tv_pk_fma_f16 v65, v155, v91, v65 op_sel:[0,0,0] op_sel_hi:[0,1,1]\n\tv_pk_fma_f16 v68, v154, v110, v68 op_sel:[0,0,0] op_sel_hi:[0,1,1]\n\tv_pk_fma_f16 v69, v155, v111, v69 op_sel:[0,0,0] op_sel_hi:[0,1,1]\n\ts_nop 0\n\tds_write2_b64 %[b7], v[64:65], v[68:69] offset0:132 offset1:198\n\tds_write2_b64 %[b10], v[88:89], v[66:67] offset0:0 offset1:66\n\tds_write2_b64 %[b12], v[112:113], v[70:71] offset0:0 offset1:66\n\tds_write_b64 %[b13], v[72:73] offset:0"
#define KGEN_CLOBBER "v64", "v65", "v66", "v67", "v68", "v69", "v70", "v71", "v72", "v73", "v74", "v75", "v76", "v77", "v78", "v79", "v80", "v81", "v82", "v83", "v84", "v85", "v86", "v87", "v88", "v89", "v90", "v91", "v92", "v93", "v94", "v95", "v96", "v97", "v98", "v99", "v100", "v101", "v102", "v103", "v104", "v105", "v106", "v107", "v108", "v109", "v110", "v111", "v112", "v113", "v114", "v115", "v116", "v117", "v118", "v119", "v120", "v121", "v122", "v123", "v124", "v125", "v126", "v127", "v128", "v129", "v130", "v131", "v132", "v133", "v134", "v135", "v136", "v137", "v138", "v139", "v140", "v141", "v142", "v143", "v144", "v145", "v146", "v147", "v148", "v149", "v150", "v151", "v152", "v153", "v154", "v155"
#define KROW_0LO 0x2b2a2d2c23222120ull
#define KROW_0HI 0x202020202b343332ull
#define KROW_1LO 0x2524272a31302f2eull
#define KROW_1HI 0x2b2b2b2b20292826ull
#define KMFMA_ASM "ds_read_b128 v[64:67], %[ap] offset:0\n\tds_read_b128 v[68:71], %[bp] offset:0\n\tds_read_b128 v[72:75], %[ap] offset:32\n\tds_read_b128 v[76:79], %[bp] offset:32\n\tds_read_b128 v[80:83], %[ap] offset:64\n\tds_read_b128 v[84:87], %[bp] offset:64\n\tds_read_b128 v[88:91], %[ap] offset:96\n\tds_read_b128 v[92:95], %[bp] offset:96\n\ts_waitcnt lgkmcnt(6)\n\tv_mfma_f32_32x32x16_f16 v[48:63], v[64:67], v[68:71], 0\n\tds_read_b128 v[96:99], %[ap] offset:128\n\tds_read_b128 v[100:103], %[bp] offset:128\n\ts_waitcnt lgkmcnt(6)\n\tv_mfma_f32_32x32x16_f16 v[48:63], v[72:75], v[76:79], v[48:63]\n\tds_read_b128 v[104:107], %[ap] offset:160\n\tds_read_b128 v[108:111], %[bp] offset:160\n\ts_waitcnt lgkmcnt(6)\n\tv_mfma_f32_32x32x16_f16 v[48:63], v[80:83], v[84:87], v[48:63]\n\tds_read_b128 v[64:67], %[ap] offset:192\n\tds_read_b128 v[68:71], %[bp] offset:192\n\ts_waitcnt lgkmcnt(6)\n\tv_mfma_f32_32x32x16_f16 v[48:63], v[88:91], v[92:95], v[48:63]\n\tds_read_b128 v[72:75], %[ap] offset:224\n\tds_read_b128 v[76:79], %[bp] offset:224\n\ts_waitcnt lgkmcnt(6)\n\tv_mfma_f32_32x32x16_f16 v[48:63], v[96:99], v[100:103], v[48:63]\n\tds_read_b128 v[80:83], %[ap] offset:256\n\tds_read_b128 v[84:87], %[bp] offset:256\n\ts_waitcnt lgkmcnt(6)\n\tv_mfma_f32_32x32x16_f16 v[48:63], v[104:107], v[108:111], v[48:63]\n\tds_read_b128 v[88:91], %[ap] offset:288\n\tds_read_b128 v[92:95], %[bp] offset:288\n\ts_waitcnt lgkmcnt(6)\n\tv_mfma_f32_32x32x16_f16 v[48:63], v[64:67], v[68:71], v[48:63]\n\tds_read_b128 v[96:99], %[ap] offset:320\n\tds_read_b128 v[100:103], %[bp] offset:320\n\ts_waitcnt lgkmcnt(6)\n\tv_mfma_f32_32x32x16_f16 v[48:63], v[72:75], v[76:79], v[48:63]\n\tds_read_b128 v[104:107], %[ap] offset:352\n\tds_read_b128 v[108:111], %[bp] offset:352\n\ts_waitcnt lgkmcnt(6)\n\tv_mfma_f32_32x32x16_f16 v[48:63], v[80:83], v[84:87], v[48:63]\n\tds_read_b128 v[64:67], %[ap] offset:384\n\tds_read_b128 v[68:71], %[bp] offset:384\n\ts_waitcnt lgkmcnt(6)\n\tv_mfma_f32_32x32x16_f16 v[48:63], v[88:91], v[92:95], v[48:63]\n\tds_read_b128 v[72:75], %[ap] offset:416\n\tds_read_b128 v[76:79], %[bp] offset:416\n\ts_waitcnt lgkmcnt(6)\n\tv_mfma_f32_32x32x16_f16 v[48:63], v[96:99], v[100:103], v[48:63]\n\tds_read_b128 v[80:83], %[ap] offset:448\n\tds_read_b128 v[84:87], %[bp] offset:448\n\ts_waitcnt lgkmcnt(6)\n\tv_mfma_f32_32x32x16_f16 v[48:63], v[104:107], v[108:111], v[48:63]\n\tds_read_b128 v[88:91], %[ap] offset:480\n\tds_read_b128 v[92:95], %[bp] offset:480\n\ts_waitcnt lgkmcnt(6)\n\tv_mfma_f32_32x32x16_f16 v[48:63], v[64:67], v[68:71], v[48:63]\n\tds_read_b32 %[stg], %[ctl] offset:4\n\ts_waitcnt lgkmcnt(4)\n\tv_mfma_f32_32x32x16_f16 v[48:63], v[72:75], v[76:79], v[48:63]\n\ts_waitcnt lgkmcnt(2)\n\tv_mfma_f32_32x32x16_f16 v[48:63], v[80:83], v[84:87], v[48:63]\n\ts_waitcnt lgkmcnt(0)\n\tv_mfma_f32_32x32x16_f16 v[48:63], v[88:91], v[92:95], v[48:63]\n\ts_nop 7\n\ts_nop 3\n\ts_waitcnt lgkmcnt(0)"
#define KMFMA_CLOBBER "v64", "v65", "v66", "v67", "v68", "v69", "v70", "v71", "v72", "v73", "v74", "v75", "v76", "v77", "v78", "v79", "v80", "v81", "v82", "v83", "v84", "v85", "v86", "v87", "v88", "v89", "v90", "v91", "v92", "v93", "v94", "v95", "v96", "v97", "v98", "v99", "v100", "v101", "v102", "v103", "v104", "v105", "v106", "v107", "v108", "v109", "v110", "v111"
#define CONV_ASM ".p2align 3\n\tds_read_b64_tr_b16 v[52:53], %[a0] offset:456\n\tds_read_b64_tr_b16 v[54:55], %[a0] offset:448\n\tds_read_b64_tr_b16 v[84:85], %[a0] offset:968\n\tds_read_b64_tr_b16 v[86:87], %[a0] offset:960\n\tds_read_b64_tr_b16 v[48:49], %[a0] offset:392\n\tds_read_b64_tr_b16 v[50:51], %[a0] offset:384\n\tds_read_b64_tr_b16 v[80:81], %[a0] offset:904\n\tds_read_b64_tr_b16 v[82:83], %[a0] offset:896\n\tds_read_b64_tr_b16 v[44:45], %[a0] offset:328\n\tds_read_b64_tr_b16 v[46:47], %[a0] offset:320\n\tds_read_b64_tr_b16 v[76:77], %[a0] offset:840\n\tds_read_b64_tr_b16 v[78:79], %[a0] offset:832\n\tv_readfirstlane_b32 %[s], %[stg]\n\ts_mov_b32 %[c], 0x400000\n\ts_cmp_ge_u32 %[s], 4\n\ts_cbranch_scc1 2f\n\t1:\n\tds_read_b32 v216, %[ctl] offset:4\n\ts_waitcnt lgkmcnt(0)\n\tv_readfirstlane_b32 %[s], v216\n\ts_cmp_ge_u32 %[s], 4\n\ts_cbranch_scc1 2f\n\ts_sleep 1\n\ts_sub_u32 %[c], %[c], 1\n\ts_cbranch_scc0 1b\n\t.p2align 3\n\t2:\n\tds_read_b128 v[164:167], %[bb] offset:64\n\tds_read_b128 v[168:171], %[bb] offset:128\n\tds_read_b128 v[172:175], %[bb] offset:192\n\tds_read_b64_tr_b16 v[40:41], %[a0] offset:264\n\tds_read_b64_tr_b16 v[42:43], %[a0] offset:256\n\tds_read_b128 v[176:179], %[bb] offset:256\n\ts_waitcnt lgkmcnt(5)\n\ts_nop 0\n\tv_mfma_f32_16x16x32_bf16 v[132:135], v[52:55], v[164:167], 0\n\tds_read_b64_tr_b16 v[72:73], %[a0] offset:776\n\tds_read_b64_tr_b16 v[74:75], %[a0] offset:768\n\tds_read_b64_tr_b16 v[36:37], %[a0] offset:200\n\tv_mfma_f32_16x16x32_bf16 v[136:139], v[84:87], v[164:167], 0\n\tds_read_b64_tr_b16 v[38:39], %[a0] offset:192\n\ts_waitcnt lgkmcnt(8)\n\ts_nop 0\n\tv_mfma_f32_16x16x32_bf16 v[132:135], v[48:51], v[168:171], v[132:135]\n\tds_read_b128 v[180:183], %[bb] offset:320\n\tds_read_b64_tr_b16 v[68:69], %[a0] offset:712\n\tds_read_b64_tr_b16 v[70:71], %[a0] offset:704\n\tv_mfma_f32_16x16x32_bf16 v[136:139], v[80:83], v[168:171], v[136:139]\n\tds_read_b64_tr_b16 v[32:33], %[a0] offset:136\n\ts_waitcnt lgkmcnt(8)\n\ts_nop 0\n\tv_mfma_f32_16x16x32_bf16 v[132:135], v[44:47], v[172:175], v[132:135]\n\tds_read_b64_tr_b16 v[34:35], %[a0] offset:128\n\tds_read_b128 v[184:187], %[bb] offset:384\n\tds_read_b64_tr_b16 v[64:65], %[a0] offset:648\n\tv_mfma_f32_16x16x32_bf16 v[136:139], v[76:79], v[172:175], v[136:139]\n\tds_read_b64_tr_b16 v[66:67], %[a0] offset:640\n\tds_read_b64_tr_b16 v[28:29], %[a0] offset:72\n\tds_read_b64_tr_b16 v[30:31], %[a0] offset:64\n\tv_mfma_f32_16x16x32_bf16 v[132:135], v[40:43], v[176:179], v[132:135]\n\tds_read_b128 v[164:167], %[bb] offset:448\n\ts_waitcnt lgkmcnt(8)\n\ts_nop 0\n\tv_mfma_f32_16x16x32_bf16 v[136:139], v[72:75], v[176:179], v[136:139]\n\tds_read_b64_tr_b16 v[60:61], %[a0] offset:584\n\tds_read_b64_tr_b16 v[62:63], %[a0] offset:576\n\tds_read_b64_tr_b16 v[24:25], %[a0] offset:8\n\tv_mfma_f32_16x16x32_bf16 v[132:135], v[36:39], v[180:183], v[132:135]\n\tds_read_b64_tr_b16 v[26:27], %[a0] offset:0\n\tds_read_b128 v[168:171], %[bb] offset:512\n\tds_read_b64_tr_b16 v[56:57], %[a0] offset:520\n\tv_mfma_f32_16x16x32_bf16 v[136:139], v[68:71], v[180:183], v[136:139]\n\tds_read_b64_tr_b16 v[58:59], %[a0] offset:512\n\ts_waitcnt lgkmcnt(8)\n\ts_nop 0\n\tv_mfma_f32_16x16x32_bf16 v[132:135], v[32:35], v[184:187], v[132:135]\n\tds_read_b64_tr_b16 v[88:89], %[a0] offset:1032\n\tds_read_b64_tr_b16 v[90:91], %[a0] offset:1024\n\tds_read_b128 v[188:191], %[bb] offset:496\n\tv_mfma_f32_16x16x32_bf16 v[136:139], v[64:67], v[184:187], v[136:139]\n\tds_read_b128 v[172:175], %[bb] offset:576\n\ts_waitcnt lgkmcnt(8)\n\ts_nop 0\n\tv_mfma_f32_16x16x32_bf16 v[132:135], v[28:31], v[164:167], v[132:135]\n\tds_read_b128 v[176:179], %[bb] offset:640\n\tv_mfma_f32_16x16x32_bf16 v[136:139], v[60:63], v[164:167], v[136:139]\n\ts_waitcnt lgkmcnt(5)\n\ts_nop 0\n\tv_mfma_f32_16x16x32_bf16 v[132:135], v[24:27], v[168:171], v[132:135]\n\tds_write_b16 %[ta], %[t78]\n\tds_read_b128 v[180:183], %[bb] offset:704\n\tv_mfma_f32_16x16x32_bf16 v[136:139], v[56:59], v[168:171], v[136:139]\n\ts_waitcnt lgkmcnt(3)\n\ts_nop 0\n\tv_mfma_f32_16x16x32_bf16 v[140:143], v[88:91], v[168:171], 0\n\tds_write_b16_d16_hi %[ta], %[t78] offset:64\n\tds_read_b128 v[184:187], %[bb] offset:768\n\tv_mfma_f32_16x16x32_bf16 v[204:207], v[188:191], v[4:7], 0\n\tds_write_b128 %[yb], v[132:135] offset:0\n\tv_mfma_f32_16x16x32_bf16 v[218:221], v[168:171], v[4:7], 0\n\tds_write_b16 %[ta], %[t78] offset:388\n\tds_read_b128 v[164:167], %[bb] offset:832\n\tv_mfma_f32_16x16x32_bf16 v[208:211], v[188:191], v[8:11], 0\n\tds_write_b8 %[ctc], %[one] offset:0\n\tds_write_b16_d16_hi %[ta], %[t78] offset:452\n\tv_mfma_f32_16x16x32_bf16 v[222:225], v[168:171], v[8:11], 0\n\tds_write_b16 %[tb], %[t9a]\n\tv_mfma_f32_16x16x32_bf16 v[212:215], v[188:191], v[12:15], 0\n\tds_write_b16_d16_hi %[tb], %[t9a] offset:128\n\tv_cvt_pk_bf16_f32 v192, v221, v207\n\tv_mfma_f32_16x16x32_bf16 v[226:229], v[168:171], v[16:19], 0\n\tds_write_b16 %[tb], %[t9a] offset:388\n\tv_cvt_pk_bf16_f32 v193, v220, v206\n\tv_mfma_f32_16x16x32_bf16 v[148:151], v[20:23], v[168:171], 0\n\tds_write_b16_d16_hi %[tb], %[t9a] offset:516\n\tds_read_b128 v[168:171], %[bb] offset:896\n\tv_mfma_f32_16x16x32_bf16 v[136:139], v[52:55], v[172:175], v[136:139]\n\tds_read_b64 v[92:93], %[tp] offset:0\n\tds_read_b64 v[94:95], %[tp] offset:8\n\tds_read_b64 v[96:97], %[tp] offset:64\n\tv_mfma_f32_16x16x32_bf16 v[140:143], v[84:87], v[172:175], v[140:143]\n\tds_read_b128 v[172:175], %[bb] offset:960\n\ts_waitcnt lgkmcnt(7)\n\ts_nop 0\n\tv_mfma_f32_16x16x32_bf16 v[136:139], v[48:51], v[176:179], v[136:139]\n\tds_read_b64 v[98:99], %[tp] offset:72\n\tds_read_b64 v[100:101], %[tp] offset:128\n\tds_read_b64 v[102:103], %[tp] offset:136\n\tv_mfma_f32_16x16x32_bf16 v[140:143], v[80:83], v[176:179], v[140:143]\n\tds_read_b128 v[176:179], %[bb] offset:1024\n\tds_read_b64 v[104:105], %[tp] offset:768\n\tds_read_b64 v[106:107], %[tp] offset:776\n\tv_mfma_f32_16x16x32_bf16 v[136:139], v[44:47], v[180:183], v[136:139]\n\tds_read_b64 v[108:109], %[tp] offset:832\n\tds_read_b64 v[110:111], %[tp] offset:840\n\tds_read_b64 v[112:113], %[tp] offset:896\n\tv_mfma_f32_16x16x32_bf16 v[140:143], v[76:79], v[180:183], v[140:143]\n\tds_read_b64 v[114:115], %[tp] offset:904\n\tds_read_b64 v[116:117], %[tp] offset:1536\n\tds_read_b64 v[118:119], %[tp] offset:1544\n\tv_mfma_f32_16x16x32_bf16 v[136:139], v[40:43], v[184:187], v[136:139]\n\tds_read_b64 v[120:121], %[tp] offset:1600\n\tds_read_b64 v[122:123], %[tp] offset:1608\n\tv_cvt_pk_bf16_f32 v194, v219, v205\n\tv_mfma_f32_16x16x32_bf16 v[140:143], v[72:75], v[184:187], v[140:143]\n\tv_cvt_pk_bf16_f32 v195, v218, v204\n\tds_read_b64 v[124:125], %[tp] offset:1664\n\tds_read_b64 v[126:127], %[tp] offset:1672\n\tv_mfma_f32_16x16x32_bf16 v[136:139], v[36:39], v[164:167], v[136:139]\n\tds_read_b64 v[128:129], %[tp] offset:1728\n\tds_read_b64 v[130:131], %[tp] offset:1736\n\tv_cvt_pk_bf16_f32 v196, v225, v211\n\tv_mfma_f32_16x16x32_bf16 v[140:143], v[68:71], v[164:167], v[140:143]\n\tv_cvt_pk_bf16_f32 v197, v224, v210\n\ts_waitcnt lgkmcnt(6)\n\ts_nop 0\n\tv_mfma_f32_16x16x32_bf16 v[136:139], v[32:35], v[168:171], v[136:139]\n\tv_cvt_pk_bf16_f32 v198, v223, v209\n\tv_mfma_f32_16x16x32_bf16 v[140:143], v[64:67], v[168:171], v[140:143]\n\tv_cvt_pk_bf16_f32 v199, v222, v208\n\tv_mfma_f32_16x16x32_bf16 v[136:139], v[28:31], v[172:175], v[136:139]\n\tv_cvt_pk_bf16_f32 v200, v229, v215\n\tv_mfma_f32_16x16x32_bf16 v[140:143], v[60:63], v[172:175], v[140:143]\n\tds_read_b128 v[188:191], %[bb] offset:1008\n\tv_cvt_pk_bf16_f32 v201, v228, v214\n\tv_mfma_f32_16x16x32_bf16 v[136:139], v[24:27], v[176:179], v[136:139]\n\tv_cvt_pk_bf16_f32 v202, v227, v213\n\tv_mfma_f32_16x16x32_bf16 v[140:143], v[56:59], v[176:179], v[140:143]\n\tv_cvt_pk_bf16_f32 v203, v226, v212\n\tv_mfma_f32_16x16x32_bf16 v[144:147], v[88:91], v[176:179], 0\n\tv_mfma_f32_16x16x32_bf16 v[140:143], v[192:195], v[92:95], v[140:143]\n\tv_mfma_f32_16x16x32_bf16 v[144:147], v[192:195], v[96:99], v[144:147]\n\tv_mfma_f32_16x16x32_bf16 v[148:151], v[192:195], v[100:103], v[148:151]\n\tv_mfma_f32_16x16x32_bf16 v[140:143], v[196:199], v[104:107], v[140:143]\n\tds_write_b128 %[yb], v[136:139] offset:1152\n\tds_read_b128 v[180:183], %[bb] offset:1088\n\tv_mfma_f32_16x16x32_bf16 v[144:147], v[196:199], v[108:111], v[144:147]\n\tv_mfma_f32_16x16x32_bf16 v[148:151], v[196:199], v[112:115], v[148:151]\n\tds_write_b8 %[ctc], %[one] offset:64\n\tds_read_b128 v[184:187], %[bb] offset:1152\n\tv_mfma_f32_16x16x32_bf16 v[148:151], v[200:203], v[116:119], v[148:151]\n\ts_waitcnt lgkmcnt(4)\n\ts_nop 0\n\tv_mfma_f32_16x16x32_bf16 v[152:155], v[200:203], v[120:123], 0\n\tds_read_b128 v[164:167], %[bb] offset:1216\n\tv_mfma_f32_16x16x32_bf16 v[156:159], v[200:203], v[124:127], 0\n\tv_mfma_f32_16x16x32_bf16 v[160:163], v[200:203], v[128:131], 0\n\tds_read_b128 v[168:171], %[bb] offset:1280\n\tv_mfma_f32_16x16x32_bf16 v[204:207], v[188:191], v[4:7], 0\n\tv_mfma_f32_16x16x32_bf16 v[218:221], v[176:179], v[4:7], 0\n\tds_read_b128 v[172:175], %[bb] offset:1344\n\tv_mfma_f32_16x16x32_bf16 v[208:211], v[188:191], v[8:11], 0\n\tv_mfma_f32_16x16x32_bf16 v[222:225], v[176:179], v[8:11], 0\n\tv_mfma_f32_16x16x32_bf16 v[212:215], v[188:191], v[12:15], 0\n\tv_mfma_f32_16x16x32_bf16 v[226:229], v[176:179], v[16:19], 0\n\tv_mfma_f32_16x16x32_bf16 v[152:155], v[20:23], v[176:179], v[152:155]\n\ts_waitcnt lgkmcnt(0)\n\ts_nop 0\n\tv_mfma_f32_16x16x32_bf16 v[140:143], v[52:55], v[180:183], v[140:143]\n\tds_read_b128 v[176:179], %[bb] offset:1408\n\tv_cvt_pk_bf16_f32 v192, v221, v207\n\tv_mfma_f32_16x16x32_bf16 v[144:147], v[84:87], v[180:183], v[144:147]\n\tds_read_b128 v[180:183], %[bb] offset:1472\n\tv_cvt_pk_bf16_f32 v193, v220, v206\n\tv_mfma_f32_16x16x32_bf16 v[140:143], v[48:51], v[184:187], v[140:143]\n\tv_cvt_pk_bf16_f32 v194, v219, v205\n\tv_mfma_f32_16x16x32_bf16 v[144:147], v[80:83], v[184:187], v[144:147]\n\tds_read_b128 v[184:187], %[bb] offset:1536\n\tv_cvt_pk_bf16_f32 v195, v218, v204\n\tv_mfma_f32_16x16x32_bf16 v[140:143], v[44:47], v[164:167], v[140:143]\n\tv_cvt_pk_bf16_f32 v196, v225, v211\n\tv_mfma_f32_16x16x32_bf16 v[144:147], v[76:79], v[164:167], v[144:147]\n\tv_cvt_pk_bf16_f32 v197, v224, v210\n\tv_mfma_f32_16x16x32_bf16 v[140:143], v[40:43], v[168:171], v[140:143]\n\tv_cvt_pk_bf16_f32 v198, v223, v209\n\tv_mfma_f32_16x16x32_bf16 v[144:147], v[72:75], v[168:171], v[144:147]\n\tv_cvt_pk_bf16_f32 v199, v222, v208\n\tv_mfma_f32_16x16x32_bf16 v[140:143], v[36:39], v[172:175], v[140:143]\n\tv_cvt_pk_bf16_f32 v200, v229, v215\n\tv_mfma_f32_16x16x32_bf16 v[144:147], v[68:71], v[172:175], v[144:147]\n\ts_waitcnt lgkmcnt(0)\n\ts_nop 0\n\tv_mfma_f32_16x16x32_bf16 v[140:143], v[32:35], v[176:179], v[140:143]\n\tv_cvt_pk_bf16_f32 v201, v228, v214\n\tv_mfma_f32_16x16x32_bf16 v[144:147], v[64:67], v[176:179], v[144:147]\n\tv_cvt_pk_bf16_f32 v202, v227, v213\n\tv_mfma_f32_16x16x32_bf16 v[140:143], v[28:31], v[180:183], v[140:143]\n\tds_read_b128 v[188:191], %[bb] offset:1520\n\tv_cvt_pk_bf16_f32 v203, v226, v212\n\tv_mfma_f32_16x16x32_bf16 v[144:147], v[60:63], v[180:183], v[144:147]\n\tv_mfma_f32_16x16x32_bf16 v[140:143], v[24:27], v[184:187], v[140:143]\n\tv_mfma_f32_16x16x32_bf16 v[144:147], v[56:59], v[184:187], v[144:147]\n\tv_mfma_f32_16x16x32_bf16 v[148:151], v[88:91], v[184:187], v[148:151]\n\tv_mfma_f32_16x16x32_bf16 v[144:147], v[192:195], v[92:95], v[144:147]\n\tv_mfma_f32_16x16x32_bf16 v[148:151], v[192:195], v[96:99], v[148:151]\n\tv_mfma_f32_16x16x32_bf16 v[152:155], v[192:195], v[100:103], v[152:155]\n\tds_read_b128 v[164:167], %[bb] offset:1600\n\tv_mfma_f32_16x16x32_bf16 v[144:147], v[196:199], v[104:107], v[144:147]\n\tv_mfma_f32_16x16x32_bf16 v[148:151], v[196:199], v[108:111], v[148:151]\n\tds_write_b128 %[yb], v[140:143] offset:2304\n\tv_mfma_f32_16x16x32_bf16 v[152:155], v[196:199], v[112:115], v[152:155]\n\tds_read_b128 v[168:171], %[bb] offset:1664\n\tv_mfma_f32_16x16x32_bf16 v[152:155], v[200:203], v[116:119], v[152:155]\n\tds_write_b8 %[ctc], %[one] offset:128\n\tds_read_b128 v[172:175], %[bb] offset:1728\n\tv_mfma_f32_16x16x32_bf16 v[156:159], v[200:203], v[120:123], v[156:159]\n\tv_mfma_f32_16x16x32_bf16 v[160:163], v[200:203], v[124:127], v[160:163]\n\ts_waitcnt lgkmcnt(3)\n\ts_nop 0\n\tv_mfma_f32_16x16x32_bf16 v[204:207], v[188:191], v[4:7], 0\n\tds_read_b128 v[176:179], %[bb] offset:1792\n\tv_mfma_f32_16x16x32_bf16 v[218:221], v[184:187], v[4:7], 0\n\tds_read_b128 v[180:183], %[bb] offset:1856\n\tv_mfma_f32_16x16x32_bf16 v[208:211], v[188:191], v[8:11], 0\n\tv_mfma_f32_16x16x32_bf16 v[222:225], v[184:187], v[8:11], 0\n\tv_mfma_f32_16x16x32_bf16 v[212:215], v[188:191], v[12:15], 0\n\tv_mfma_f32_16x16x32_bf16 v[226:229], v[184:187], v[16:19], 0\n\tv_mfma_f32_16x16x32_bf16 v[156:159], v[20:23], v[184:187], v[156:159]\n\tds_read_b128 v[184:187], %[bb] offset:1920\n\tv_mfma_f32_16x16x32_bf16 v[144:147], v[52:55], v[164:167], v[144:147]\n\tv_cvt_pk_bf16_f32 v192, v221, v207\n\tv_mfma_f32_16x16x32_bf16 v[148:151], v[84:87], v[164:167], v[148:151]\n\ts_waitcnt lgkmcnt(1)\n\ts_nop 0\n\tv_mfma_f32_16x16x32_bf16 v[144:147], v[48:51], v[168:171], v[144:147]\n\tds_read_b128 v[164:167], %[bb] offset:1984\n\tv_cvt_pk_bf16_f32 v193, v220, v206\n\tv_mfma_f32_16x16x32_bf16 v[148:151], v[80:83], v[168:171], v[148:151]\n\tds_read_b128 v[168:171], %[bb] offset:2048\n\tv_cvt_pk_bf16_f32 v194, v219, v205\n\tv_mfma_f32_16x16x32_bf16 v[144:147], v[44:47], v[172:175], v[144:147]\n\tds_read_b128 v[188:191], %[bb] offset:2032\n\tv_cvt_pk_bf16_f32 v195, v218, v204\n\tv_mfma_f32_16x16x32_bf16 v[148:151], v[76:79], v[172:175], v[148:151]\n\tv_cvt_pk_bf16_f32 v196, v225, v211\n\tv_mfma_f32_16x16x32_bf16 v[144:147], v[40:43], v[176:179], v[144:147]\n\tv_cvt_pk_bf16_f32 v197, v224, v210\n\tv_mfma_f32_16x16x32_bf16 v[148:151], v[72:75], v[176:179], v[148:151]\n\tv_cvt_pk_bf16_f32 v198, v223, v209\n\tv_mfma_f32_16x16x32_bf16 v[144:147], v[36:39], v[180:183], v[144:147]\n\tv_cvt_pk_bf16_f32 v199, v222, v208\n\tv_mfma_f32_16x16x32_bf16 v[148:151], v[68:71], v[180:183], v[148:151]\n\ts_waitcnt lgkmcnt(0)\n\ts_nop 0\n\tv_mfma_f32_16x16x32_bf16 v[144:147], v[32:35], v[184:187], v[144:147]\n\tv_cvt_pk_bf16_f32 v200, v229, v215\n\tv_mfma_f32_16x16x32_bf16 v[148:151], v[64:67], v[184:187], v[148:151]\n\tv_cvt_pk_bf16_f32 v201, v228, v214\n\tv_mfma_f32_16x16x32_bf16 v[144:147], v[28:31], v[164:167], v[144:147]\n\tv_cvt_pk_bf16_f32 v202, v227, v213\n\tv_mfma_f32_16x16x32_bf16 v[148:151], v[60:63], v[164:167], v[148:151]\n\tv_cvt_pk_bf16_f32 v203, v226, v212\n\tv_mfma_f32_16x16x32_bf16 v[144:147], v[24:27], v[168:171], v[144:147]\n\tv_mfma_f32_16x16x32_bf16 v[148:151], v[56:59], v[168:171], v[148:151]\n\tv_mfma_f32_16x16x32_bf16 v[152:155], v[88:91], v[168:171], v[152:155]\n\tv_mfma_f32_16x16x32_bf16 v[204:207], v[188:191], v[4:7], 0\n\tv_mfma_f32_16x16x32_bf16 v[218:221], v[168:171], v[4:7], 0\n\tv_mfma_f32_16x16x32_bf16 v[208:211], v[188:191], v[8:11], 0\n\tv_mfma_f32_16x16x32_bf16 v[222:225], v[168:171], v[8:11], 0\n\tv_mfma_f32_16x16x32_bf16 v[212:215], v[188:191], v[12:15], 0\n\tv_mfma_f32_16x16x32_bf16 v[226:229], v[168:171], v[16:19], 0\n\tds_write_b128 %[yb], v[144:147] offset:3456\n\tv_mfma_f32_16x16x32_bf16 v[160:163], v[20:23], v[168:171], v[160:163]\n\tds_read_b32 v216, %[ctl] offset:4\n\tv_mfma_f32_16x16x32_bf16 v[148:151], v[192:195], v[92:95], v[148:151]\n\tds_write_b8 %[ctc], %[one] offset:192\n\tv_mfma_f32_16x16x32_bf16 v[152:155], v[192:195], v[96:99], v[152:155]\n\tv_mfma_f32_16x16x32_bf16 v[156:159], v[192:195], v[100:103], v[156:159]\n\tv_mfma_f32_16x16x32_bf16 v[148:151], v[196:199], v[104:107], v[148:151]\n\tv_cvt_pk_bf16_f32 v192, v221, v207\n\tv_mfma_f32_16x16x32_bf16 v[152:155], v[196:199], v[108:111], v[152:155]\n\tv_cvt_pk_bf16_f32 v193, v220, v206\n\tv_mfma_f32_16x16x32_bf16 v[156:159], v[196:199], v[112:115], v[156:159]\n\tv_cvt_pk_bf16_f32 v194, v219, v205\n\tv_mfma_f32_16x16x32_bf16 v[156:159], v[200:203], v[116:119], v[156:159]\n\tv_cvt_pk_bf16_f32 v195, v218, v204\n\tv_mfma_f32_16x16x32_bf16 v[160:163], v[200:203], v[120:123], v[160:163]\n\ts_waitcnt lgkmcnt(0)\n\ts_nop 0\n\tv_readfirstlane_b32 %[s], v216\n\ts_mov_b32 %[c], 0x400000\n\ts_cmp_ge_u32 %[s], 8\n\ts_cbranch_scc1 2f\n\t1:\n\tds_read_b32 v216, %[ctl] offset:4\n\ts_waitcnt lgkmcnt(0)\n\tv_readfirstlane_b32 %[s], v216\n\ts_cmp_ge_u32 %[s], 8\n\ts_cbranch_scc1 2f\n\ts_sleep 1\n\ts_sub_u32 %[c], %[c], 1\n\ts_cbranch_scc0 1b\n\t.p2align 3\n\t2:\n\tv_mfma_f32_16x16x32_bf16 v[152:155], v[192:195], v[92:95], v[152:155]\n\tds_read_b128 v[172:175], %[bb] offset:2112\n\tv_cvt_pk_bf16_f32 v196, v225, v211\n\tv_mfma_f32_16x16x32_bf16 v[156:159], v[192:195], v[96:99], v[156:159]\n\tds_read_b128 v[176:179], %[bb] offset:2176\n\tv_cvt_pk_bf16_f32 v197, v224, v210\n\tv_mfma_f32_16x16x32_bf16 v[160:163], v[192:195], v[100:103], v[160:163]\n\tv_cvt_pk_bf16_f32 v198, v223, v209\n\tds_read_b128 v[180:183], %[bb] offset:2240\n\tv_cvt_pk_bf16_f32 v199, v222, v208\n\ts_nop 1\n\ts_nop 0\n\tv_mfma_f32_16x16x32_bf16 v[152:155], v[196:199], v[104:107], v[152:155]\n\tds_read_b128 v[184:187], %[bb] offset:2304\n\tv_cvt_pk_bf16_f32 v200, v229, v215\n\tv_mfma_f32_16x16x32_bf16 v[156:159], v[196:199], v[108:111], v[156:159]\n\tv_cvt_pk_bf16_f32 v201, v228, v214\n\tds_read_b128 v[164:167], %[bb] offset:2368\n\tv_mfma_f32_16x16x32_bf16 v[160:163], v[196:199], v[112:115], v[160:163]\n\tv_cvt_pk_bf16_f32 v202, v227, v213\n\tds_read_b128 v[168:171], %[bb] offset:2432\n\tv_cvt_pk_bf16_f32 v203, v226, v212\n\ts_nop 1\n\ts_nop 0\n\tv_mfma_f32_16x16x32_bf16 v[160:163], v[200:203], v[116:119], v[160:163]\n\ts_waitcnt lgkmcnt(3)\n\ts_nop 0\n\tv_mfma_f32_16x16x32_bf16 v[148:151], v[52:55], v[172:175], v[148:151]\n\tv_mfma_f32_16x16x32_bf16 v[152:155], v[84:87], v[172:175], v[152:155]\n\tds_read_b128 v[172:175], %[bb] offset:2496\n\tv_mfma_f32_16x16x32_bf16 v[148:151], v[48:51], v[176:179], v[148:151]\n\tv_mfma_f32_16x16x32_bf16 v[152:155], v[80:83], v[176:179], v[152:155]\n\tds_read_b128 v[176:179], %[bb] offset:2560\n\tv_mfma_f32_16x16x32_bf16 v[148:151], v[44:47], v[180:183], v[148:151]\n\tds_read_b128 v[188:191], %[bb] offset:2544\n\tv_mfma_f32_16x16x32_bf16 v[152:155], v[76:79], v[180:183], v[152:155]\n\ts_waitcnt lgkmcnt(2)\n\ts_nop 0\n\tv_mfma_f32_16x16x32_bf16 v[148:151], v[40:43], v[184:187], v[148:151]\n\tv_mfma_f32_16x16x32_bf16 v[152:155], v[72:75], v[184:187], v[152:155]\n\tds_read_b128 v[180:183], %[bb] offset:2624\n\tv_mfma_f32_16x16x32_bf16 v[148:151], v[36:39], v[164:167], v[148:151]\n\tv_mfma_f32_16x16x32_bf16 v[152:155], v[68:71], v[164:167], v[152:155]\n\tds_read_b128 v[184:187], %[bb] offset:2688\n\tv_mfma_f32_16x16x32_bf16 v[148:151], v[32:35], v[168:171], v[148:151]\n\tv_mfma_f32_16x16x32_bf16 v[152:155], v[64:67], v[168:171], v[152:155]\n\tds_read_b128 v[164:167], %[bb] offset:2752\n\tv_mfma_f32_16x16x32_bf16 v[148:151], v[28:31], v[172:175], v[148:151]\n\tv_mfma_f32_16x16x32_bf16 v[152:155], v[60:63], v[172:175], v[152:155]\n\ts_waitcnt lgkmcnt(1)\n\ts_nop 0\n\tv_mfma_f32_16x16x32_bf16 v[148:151], v[24:27], v[176:179], v[148:151]\n\tds_read_b128 v[168:171], %[bb] offset:2816\n\tv_mfma_f32_16x16x32_bf16 v[152:155], v[56:59], v[176:179], v[152:155]\n\tds_read_b128 v[172:175], %[bb] offset:2880\n\tv_mfma_f32_16x16x32_bf16 v[156:159], v[88:91], v[176:179], v[156:159]\n\tv_mfma_f32_16x16x32_bf16 v[204:207], v[188:191], v[4:7], 0\n\tv_mfma_f32_16x16x32_bf16 v[218:221], v[176:179], v[4:7], 0\n\tv_mfma_f32_16x16x32_bf16 v[208:211], v[188:191], v[8:11], 0\n\tv_mfma_f32_16x16x32_bf16 v[222:225], v[176:179], v[8:11], 0\n\tds_write_b128 %[yb], v[148:151] offset:4608\n\tv_mfma_f32_16x16x32_bf16 v[152:155], v[52:55], v[180:183], v[152:155]\n\tds_read_b128 v[176:179], %[bb] offset:2944\n\tv_mfma_f32_16x16x32_bf16 v[156:159], v[84:87], v[180:183], v[156:159]\n\tds_write_b8 %[ctc], %[one] offset:256\n\tds_read_b128 v[180:183], %[bb] offset:3008\n\tv_mfma_f32_16x16x32_bf16 v[152:155], v[48:51], v[184:187], v[152:155]\n\tv_cvt_pk_bf16_f32 v192, v221, v207\n\tv_mfma_f32_16x16x32_bf16 v[156:159], v[80:83], v[184:187], v[156:159]\n\ts_waitcnt lgkmcnt(3)\n\ts_nop 0\n\tv_mfma_f32_16x16x32_bf16 v[152:155], v[44:47], v[164:167], v[152:155]\n\tds_read_b128 v[184:187], %[bb] offset:3072\n\tv_cvt_pk_bf16_f32 v193, v220, v206\n\tv_mfma_f32_16x16x32_bf16 v[156:159], v[76:79], v[164:167], v[156:159]\n\tv_cvt_pk_bf16_f32 v194, v219, v205\n\tv_mfma_f32_16x16x32_bf16 v[152:155], v[40:43], v[168:171], v[152:155]\n\tv_cvt_pk_bf16_f32 v195, v218, v204\n\tv_mfma_f32_16x16x32_bf16 v[156:159], v[72:75], v[168:171], v[156:159]\n\tds_read_b128 v[188:191], %[bb] offset:3056\n\tv_cvt_pk_bf16_f32 v196, v225, v211\n\tv_mfma_f32_16x16x32_bf16 v[152:155], v[36:39], v[172:175], v[152:155]\n\tv_cvt_pk_bf16_f32 v197, v224, v210\n\tv_mfma_f32_16x16x32_bf16 v[156:159], v[68:71], v[172:175], v[156:159]\n\ts_waitcnt lgkmcnt(1)\n\ts_nop 0\n\tv_mfma_f32_16x16x32_bf16 v[152:155], v[32:35], v[176:179], v[152:155]\n\tv_cvt_pk_bf16_f32 v198, v223, v209\n\tv_mfma_f32_16x16x32_bf16 v[156:159], v[64:67], v[176:179], v[156:159]\n\tds_read_b128 v[164:167], %[bb] offset:3136\n\tv_cvt_pk_bf16_f32 v199, v222, v208\n\tv_mfma_f32_16x16x32_bf16 v[152:155], v[28:31], v[180:183], v[152:155]\n\tv_mfma_f32_16x16x32_bf16 v[156:159], v[60:63], v[180:183], v[156:159]\n\tds_read_b128 v[168:171], %[bb] offset:3200\n\tv_mfma_f32_16x16x32_bf16 v[152:155], v[24:27], v[184:187], v[152:155]\n\tv_mfma_f32_16x16x32_bf16 v[156:159], v[56:59], v[184:187], v[156:159]\n\tds_read_b128 v[172:175], %[bb] offset:3264\n\tv_mfma_f32_16x16x32_bf16 v[160:163], v[88:91], v[184:187], v[160:163]\n\tv_mfma_f32_16x16x32_bf16 v[156:159], v[192:195], v[92:95], v[156:159]\n\tds_read_b128 v[176:179], %[bb] offset:3328\n\tv_mfma_f32_16x16x32_bf16 v[160:163], v[192:195], v[96:99], v[160:163]\n\tv_mfma_f32_16x16x32_bf16 v[156:159], v[196:199], v[104:107], v[156:159]\n\tds_read_b128 v[180:183], %[bb] offset:3392\n\tv_mfma_f32_16x16x32_bf16 v[160:163], v[196:199], v[108:111], v[160:163]\n\ts_waitcnt lgkmcnt(2)\n\ts_nop 0\n\tv_mfma_f32_16x16x32_bf16 v[204:207], v[188:191], v[4:7], 0\n\tds_write_b128 %[yb], v[152:155] offset:5760\n\tv_mfma_f32_16x16x32_bf16 v[218:221], v[184:187], v[4:7], 0\n\tv_mfma_f32_16x16x32_bf16 v[208:211], v[188:191], v[8:11], 0\n\tds_write_b8 %[ctc], %[one] offset:320\n\tv_mfma_f32_16x16x32_bf16 v[222:225], v[184:187], v[8:11], 0\n\tds_read_b128 v[184:187], %[bb] offset:3456\n\tv_mfma_f32_16x16x32_bf16 v[156:159], v[52:55], v[164:167], v[156:159]\n\tv_mfma_f32_16x16x32_bf16 v[160:163], v[84:87], v[164:167], v[160:163]\n\tds_read_b128 v[164:167], %[bb] offset:3520\n\tv_mfma_f32_16x16x32_bf16 v[156:159], v[48:51], v[168:171], v[156:159]\n\tv_cvt_pk_bf16_f32 v192, v221, v207\n\tv_mfma_f32_16x16x32_bf16 v[160:163], v[80:83], v[168:171], v[160:163]\n\tds_read_b128 v[168:171], %[bb] offset:3584\n\tv_cvt_pk_bf16_f32 v193, v220, v206\n\tv_mfma_f32_16x16x32_bf16 v[156:159], v[44:47], v[172:175], v[156:159]\n\tv_cvt_pk_bf16_f32 v194, v219, v205\n\tv_mfma_f32_16x16x32_bf16 v[160:163], v[76:79], v[172:175], v[160:163]\n\ts_waitcnt lgkmcnt(1)\n\ts_nop 0\n\tv_mfma_f32_16x16x32_bf16 v[156:159], v[40:43], v[176:179], v[156:159]\n\tds_read_b128 v[172:175], %[bb] offset:3648\n\tglobal_load_dwordx2 v[240:241], %[so], %[ws] sc1\n\tv_mfma_f32_16x16x32_bf16 v[160:163], v[72:75], v[176:179], v[160:163]\n\tds_read_b128 v[176:179], %[bb] offset:3712\n\tv_cvt_pk_bf16_f32 v195, v218, v204\n\tv_mfma_f32_16x16x32_bf16 v[156:159], v[36:39], v[180:183], v[156:159]\n\tv_cvt_pk_bf16_f32 v196, v225, v211\n\tv_mfma_f32_16x16x32_bf16 v[160:163], v[68:71], v[180:183], v[160:163]\n\tds_read_b128 v[180:183], %[bb] offset:3776\n\tv_cvt_pk_bf16_f32 v197, v224, v210\n\tv_mfma_f32_16x16x32_bf16 v[156:159], v[32:35], v[184:187], v[156:159]\n\tv_cvt_pk_bf16_f32 v198, v223, v209\n\tv_mfma_f32_16x16x32_bf16 v[160:163], v[64:67], v[184:187], v[160:163]\n\tds_read_b128 v[184:187], %[bb] offset:3840\n\tv_cvt_pk_bf16_f32 v199, v222, v208\n\tv_mfma_f32_16x16x32_bf16 v[156:159], v[28:31], v[164:167], v[156:159]\n\tv_mfma_f32_16x16x32_bf16 v[160:163], v[60:63], v[164:167], v[160:163]\n\ts_waitcnt lgkmcnt(1)\n\ts_nop 0\n\tv_mfma_f32_16x16x32_bf16 v[156:159], v[24:27], v[168:171], v[156:159]\n\tds_read_b128 v[164:167], %[bb] offset:3904\n\tv_mfma_f32_16x16x32_bf16 v[160:163], v[56:59], v[168:171], v[160:163]\n\tds_read_b128 v[168:171], %[bb] offset:3968\n\tv_mfma_f32_16x16x32_bf16 v[160:163], v[192:195], v[92:95], v[160:163]\n\tv_mfma_f32_16x16x32_bf16 v[160:163], v[196:199], v[104:107], v[160:163]\n\tv_mfma_f32_16x16x32_bf16 v[160:163], v[52:55], v[172:175], v[160:163]\n\tds_read_b128 v[172:175], %[bb] offset:4032\n\tv_mfma_f32_16x16x32_bf16 v[160:163], v[48:51], v[176:179], v[160:163]\n\tds_write_b128 %[yb], v[156:159] offset:6912\n\tv_mfma_f32_16x16x32_bf16 v[160:163], v[44:47], v[180:183], v[160:163]\n\ts_waitcnt lgkmcnt(2)\n\ts_nop 0\n\tv_mfma_f32_16x16x32_bf16 v[160:163], v[40:43], v[184:187], v[160:163]\n\tds_write_b8 %[ctc], %[one] offset:384\n\tds_read_b128 v[176:179], %[bb] offset:4096\n\tv_mfma_f32_16x16x32_bf16 v[160:163], v[36:39], v[164:167], v[160:163]\n\tv_mfma_f32_16x16x32_bf16 v[160:163], v[32:35], v[168:171], v[160:163]\n\ts_waitcnt lgkmcnt(2)\n\ts_nop 0\n\tv_mfma_f32_16x16x32_bf16 v[160:163], v[28:31], v[172:175], v[160:163]\n\ts_waitcnt lgkmcnt(0)\n\ts_nop 0\n\tv_mfma_f32_16x16x32_bf16 v[160:163], v[24:27], v[176:179], v[160:163]\n\ts_nop 7\n\tds_write_b128 %[yb], v[160:163] offset:8064\n\tds_write_b8 %[ctc], %[one] offset:448"
#define CONV_CLOBBER "v24", "v25", "v26", "v27", "v28", "v29", "v30", "v31", "v32", "v33", "v34", "v35", "v36", "v37", "v38", "v39", "v40", "v41", "v42", "v43", "v44", "v45", "v46", "v47", "v48", "v49", "v50", "v51", "v52", "v53", "v54", "v55", "v56", "v57", "v58", "v59", "v60", "v61", "v62", "v63", "v64", "v65", "v66", "v67", "v68", "v69", "v70", "v71", "v72", "v73", "v74", "v75", "v76", "v77", "v78", "v79", "v80", "v81", "v82", "v83", "v84", "v85", "v86", "v87", "v88", "v89", "v90", "v91", "v92", "v93", "v94", "v95", "v96", "v97", "v98", "v99", "v100", "v101", "v102", "v103", "v104", "v105", "v106", "v107", "v108", "v109", "v110", "v111", "v112", "v113", "v114", "v115", "v116", "v117", "v118", "v119", "v120", "v121", "v122", "v123", "v124", "v125", "v126", "v127", "v128", "v129", "v130", "v131", "v132", "v133", "v134", "v135", "v136", "v137", "v138", "v139", "v140", "v141", "v142", "v143", "v144", "v145", "v146", "v147", "v148", "v149", "v150", "v151", "v152", "v153", "v154", "v155", "v156", "v157", "v158", "v159", "v160", "v161", "v162", "v163", "v164", "v165", "v166", "v167", "v168", "v169", "v170", "v171", "v172", "v173", "v174", "v175", "v176", "v177", "v178", "v179", "v180", "v181", "v182", "v183", "v184", "v185", "v186", "v187", "v188", "v189", "v190", "v191", "v192", "v193", "v194", "v195", "v196", "v197", "v198", "v199", "v200", "v201", "v202", "v203", "v204", "v205", "v206", "v207", "v208", "v209", "v210", "v211", "v212", "v213", "v214", "v215", "v216", "v217", "v218", "v219", "v220", "v221", "v222", "v223", "v224", "v225", "v226", "v227", "v228", "v229"

typedef __attribute__((address_space(4))) const Params* KArg;
#define ROLE_BEGIN(N) KArg kp; int b, tid; \
    asm volatile("fk_role" #N ":\n\tv_mov_b32 %0, v0" : "=&v"(tid) : "{s[0:1]}"(kp0), "{s2}"(b0), "{v0}"(tid0) : "memory"); \
    asm volatile("s_mov_b64 %0, s[0:1]" : "=&s"(kp) : "{s[0:1]}"(kp0), "{s2}"(b0)); asm volatile("s_mov_b32 %0, s2" : "=&s"(b) : "{s2}"(b0)); \
    const int lane = tid & 63, wave = __builtin_amdgcn_readfirstlane(tid >> 6), xcd = b & 7, slot = b >> 3; \
    const int cw = slot < 16 ? xcd * 16 + slot : 112 + xcd * 2 + slot, chbase = cw * 4, w = xcd * 9 + (slot - 18); (void)chbase; (void)w; (void)lane; \
    const Params p = {kp->x, kp->Are, kp->Aim, kp->Bre, kp->Bim, kp->Cre, kp->Cim, kp->logdt, kp->dskip, kp->wr, kp->wg, kp->wu, kp->wd, kp->gw, kp->out, kp->ws}; \
    asm volatile("s_waitcnt lgkmcnt(0)"); STAMP0_FIX()
#define ROLE_END() do { STAMP_FLUSH(); __builtin_amdgcn_s_waitcnt(0x0070); asm volatile("s_endpgm" ::: "memory"); } while (0)
#define MF(A_, B_, C_) __builtin_amdgcn_mfma_f32_16x16x32_bf16(A_, B_, C_, 0, 0, 0)
#define GRP(mask, n) __builtin_amdgcn_sched_group_barrier(mask, n, 0)

__global__ __launch_bounds__(512, 2) void fused_kernel(Params p_unused) {
    extern __shared__ __attribute__((aligned(16))) char smem_[];
    LAS char* smem = (LAS char*)smem_;
    const KArg kp0 = (KArg)__builtin_amdgcn_kernarg_segment_ptr();
    const int tid0 = threadIdx.x, b0 = blockIdx.x;
    asm volatile("s_load_dword s94, s[0:1], 0x0\n\ts_load_dword s95, s[0:1], 0x40\n\tv_readfirstlane_b32 s92, v0\n\t"
                 "s_cmp_ge_u32 s2, 32\n\ts_cbranch_scc1 1f\n\t"
                 "s_getpc_b64 s[90:91]\n\ts_and_b32 s90, s90, 0xffffff00\n\ts_and_b32 s93, s2, -8\n\ts_lshr_b32 s89, s92, 6\n\ts_add_u32 s93, s93, s89\n\ts_min_u32 s93, s93, %3\n\ts_lshl_b32 s93, s93, 10\n\t"
                 "v_and_b32 v1, 63, v0\n\tv_lshlrev_b32 v1, 4, v1\n\tv_add_u32 v1, s93, v1\n\ts_mov_b32 m0, %4\n\ts_nop 0\n\tglobal_load_lds_dwordx4 v1, s[90:91]\n"
                 "1:\n\ts_cmp_lt_u32 s2, 144\n\ts_cbranch_scc0 2f\n\ts_cmp_lt_u32 s92, 256\n\ts_cbranch_scc1 fk_role0\n\ts_branch fk_role1\n"
                 "2:\n\ts_cmp_lt_u32 s2, 216\n\ts_cbranch_scc0 3f\n\ts_cmp_lt_u32 s92, 384\n\ts_cbranch_scc1 fk_role2\n\ts_branch fk_role3\n"
                 "3:\n\ts_cmp_lt_u32 s2, 224\n\ts_cbranch_scc1 fk_role4\n\ts_branch fk_role5"
                 :: "{s[0:1]}"(kp0), "{s2}"(b0), "{v0}"(tid0), "n"(CODE_KB - 1), "n"(LDS_DUMMY) : "memory", "scc", "s89", "s90", "s91", "s92", "s93", "s94", "s95", "v1");
    const int slot = b0 >> 3, wave = __builtin_amdgcn_readfirstlane(tid0 >> 6);
    if (slot >= 18) {
    if (slot >= 27 || wave >= 6) {
    if (slot < 28) {
    if (slot == 27)
    {
        ROLE_BEGIN(4);
        const u32 tag = __builtin_amdgcn_readfirstlane(__hip_atomic_load((GAS u32*)p.ws, RLX_AGENT)) + 1u;
        if (tid < 128) ((LAS u32*)smem)[tid] = 0;
        u32 ga = (u32)(uintptr_t)(smem + (lane & 15) * 4), gv = (u32)(uintptr_t)(smem + (lane >> 2) * 4), gq_ = (u32)(uintptr_t)(smem + (lane & 3) * 16), zero = (u32)(uintptr_t)smem;
        u32 ro = OFF_RG + 4096 + tid * 16 , so = OFF_SEL + ((lane & 1) * 8 + xcd) * 256, xo = (lane & 3) * 128;
        asm volatile("" : "+v"(ga), "+v"(gv), "+v"(gq_), "+v"(zero), "+v"(ro), "+v"(so), "+v"(xo));
        if (TOUCH) {
            const u32 to = wave < 2 ? OFF_RG + (xcd * 16 + wave * 8 + (lane & 7)) * 128 : (wave < 4 ? OFF_G + (xcd * 16 + (wave - 2) * 8 + (lane & 7)) * 128 : OFF_SEL + ((lane & 1) * 8 + xcd) * 256);
            asm volatile("s_mov_b32 m0, 0x8000\n\ts_nop 0\n\tglobal_load_lds_dword %0, %1" :: "v"(to), "s"(p.ws) : "memory");
        }
        BAR();
        __builtin_amdgcn_s_sleep(SELWAIT);
        v4u qa, qb;
        u32 c_;
#define POLL_A "global_load_dwordx4 v[20:23], %[ro], %[ws] offset:-4096 sc1\n\tglobal_load_dwordx4 v[24:27], %[r2], %[ws] sc1\n\t"
#define POLL_B "global_load_dwordx4 v[28:31], %[ro], %[ws] offset:-4096 sc1\n\tglobal_load_dwordx4 v[32:35], %[r2], %[ws] sc1\n\t"
        asm volatile("s_mov_b32 %[c], 0x40000\n\t" POLL_A "s_sleep 8\n\t" POLL_B
                     "1:\n\ts_waitcnt vmcnt(2)\n\tv_cmpx_eq_u32 %[tag], v21\n\tv_cmpx_eq_u32 %[tag], v23\n\tv_cmpx_eq_u32 %[tag], v25\n\tv_cmpx_eq_u32 %[tag], v27\n\t"
                     "s_cmp_eq_u64 exec, -1\n\ts_mov_b64 exec, -1\n\ts_cbranch_scc1 3f\n\t" POLL_A
                     "s_waitcnt vmcnt(2)\n\tv_cmpx_eq_u32 %[tag], v29\n\tv_cmpx_eq_u32 %[tag], v31\n\tv_cmpx_eq_u32 %[tag], v33\n\tv_cmpx_eq_u32 %[tag], v35\n\t"
                     "s_cmp_eq_u64 exec, -1\n\ts_mov_b64 exec, -1\n\ts_cbranch_scc1 2f\n\t" POLL_B
                     "s_sub_u32 %[c], %[c], 1\n\ts_cbranch_scc0 1b\n\ts_waitcnt vmcnt(0)\n"
                     "2:\n\tv_mov_b32 v36, v28\n\tv_mov_b32 v37, v30\n\tv_mov_b32 v38, v32\n\tv_mov_b32 v39, v34\n\ts_branch 4f\n"
                     "3:\n\tv_mov_b32 v36, v20\n\tv_mov_b32 v37, v22\n\tv_mov_b32 v38, v24\n\tv_mov_b32 v39, v26\n"
                     "4:"
                     : "=&{v[36:39]}"(qa), [c] "=&s"(c_) : [ro] "v"(ro), [r2] "v"(ro + 4096), [ws] "s"(p.ws), [tag] "s"(tag)
                     : "memory", "scc", "vcc", "v20", "v21", "v22", "v23", "v24", "v25", "v26", "v27", "v28", "v29", "v30", "v31", "v32", "v33", "v34", "v35");
        asm volatile("" : "=&{v[20:23]}"(qb));
        v4u qc, qd, qe;
        asm volatile("" : "=&{v[24:27]}"(qc), "=&{v[28:31]}"(qd), "=&{v[32:35]}"(qe));
        STAMP(2);
        u32 rank, tau, cnt;
        u64 m0_, m1_, m2_, m3_;
        {
            u32 v, nv; v4u q4;
            asm volatile("ds_max_u32 %0, %1\n\ts_waitcnt lgkmcnt(0)\n\ts_barrier" :: "v"(ga), "v"(max(max(qa.x, qa.y), max(qa.z, qa.w))) : "memory");
#define RANK16(off) asm volatile("ds_read_b32 %[v], %[gv] offset:" #off "\n\tds_read_b128 v[40:43], %[gq] offset:" #off "\n\tds_read_b32 %[n], %[z] offset:64\n\tv_mov_b32 %[r], 0\n\ts_waitcnt lgkmcnt(0)\n\t" \
                         "v_cmp_gt_u32 %[a], v40, %[v]\n\tv_cmp_gt_u32 %[b], v41, %[v]\n\tv_cmp_gt_u32 %[c], v42, %[v]\n\tv_cmp_gt_u32 %[d], v43, %[v]\n\t" \
                         "v_addc_co_u32 %[r], vcc, 0, %[r], %[a]\n\tv_addc_co_u32 %[r], vcc, 0, %[r], %[b]\n\tv_addc_co_u32 %[r], vcc, 0, %[r], %[c]\n\tv_addc_co_u32 %[r], vcc, 0, %[r], %[d]\n\ts_nop 1\n\t" \
                         "v_add_u32_dpp %[r], %[r], %[r] quad_perm:[1,0,3,2] row_mask:0xf bank_mask:0xf\n\ts_nop 1\n\tv_add_u32_dpp %[r], %[r], %[r] quad_perm:[2,3,0,1] row_mask:0xf bank_mask:0xf\n\ts_nop 0" \
                         : [v] "=&v"(v), [r] "=&v"(rank), [n] "=&v"(nv), "=&{v[40:43]}"(q4), [a] "=&s"(m0_), [b] "=&s"(m1_), [c] "=&s"(m2_), [d] "=&s"(m3_) : [gv] "v"(gv), [gq] "v"(gq_), [z] "v"(zero) : "memory", "vcc")
            RANK16(0);
            tau = (u32)__builtin_amdgcn_readlane((int)v, (int)__builtin_ctzll(__ballot(rank == 7)));
            u32 pos;
            asm volatile("s_nop 1\n\tv_mov_b32 %[n], 0\n\tv_cmp_le_u32 %[a], %[t], v36\n\tv_cmp_le_u32 %[b], %[t], v37\n\tv_cmp_le_u32 %[c], %[t], v38\n\tv_cmp_le_u32 %[d], %[t], v39\n\t"
                         "v_addc_co_u32 %[n], vcc, 0, %[n], %[a]\n\tv_addc_co_u32 %[n], vcc, 0, %[n], %[b]\n\tv_addc_co_u32 %[n], vcc, 0, %[n], %[c]\n\tv_addc_co_u32 %[n], vcc, 0, %[n], %[d]\n\t"
                         "v_lshlrev_b32 %[n], 2, %[n]\n\tv_cmpx_ne_u32 0, %[n]\n\tds_add_rtn_u32 %[p], %[z], %[n] offset:64\n\ts_waitcnt lgkmcnt(0)\n\t"
                         "s_mov_b64 exec, %[a]\n\tds_write_b32 %[p], v36 offset:256\n\tv_add_u32 %[p], 4, %[p]\n\t"
                         "s_mov_b64 exec, %[b]\n\tds_write_b32 %[p], v37 offset:256\n\tv_add_u32 %[p], 4, %[p]\n\t"
                         "s_mov_b64 exec, %[c]\n\tds_write_b32 %[p], v38 offset:256\n\tv_add_u32 %[p], 4, %[p]\n\t"
                         "s_mov_b64 exec, %[d]\n\tds_write_b32 %[p], v39 offset:256\n\t"
                         "s_mov_b64 exec, -1\n\ts_waitcnt lgkmcnt(0)\n\ts_barrier"
                         : [n] "=&v"(cnt), [p] "=&v"(pos), [a] "=&s"(m0_), [b] "=&s"(m1_), [c] "=&s"(m2_), [d] "=&s"(m3_) : [t] "s"(tau), [z] "v"(zero), "{v[36:39]}"(qa) : "memory", "vcc");
            if (wave == 0) {
                RANK16(256);
                const int nc = __builtin_amdgcn_readfirstlane((int)nv) >> 2;
                if (__builtin_expect(nc > 16, 0)) {
                    v = (u32)__shfl((int)top8_d(smem, lane, nc), (lane >> 2) & 7);
                    rank = lane >> 2;
                }
                const u64 m8 = __ballot(rank < 8);
                asm volatile("s_and_b64 exec, %[m], %[k2]\n\tglobal_store_dwordx2 %[so], %[d], %[ws] sc1\n\ts_mov_b64 exec, %[m]\n\ts_mov_b32 m0, 0x8000\n\ts_nop 0\n\t"
                             "global_load_lds_dword %[xo], %[x]\n\tglobal_load_lds_dword %[xo], %[x] offset:512\n\ts_and_b64 exec, %[m], %[k1]\n\tglobal_load_lds_dword %[xo], %[x] offset:1024\n\ts_mov_b64 exec, -1"
                             :: [m] "s"(m8), [k2] "s"(0x3333333333333333ull), [k1] "s"(0x1111111111111111ull), [so] "v"(so + rank * 8), [d] "v"((v2u){v, tag}), [ws] "s"(p.ws),
                                [xo] "v"(xo + (~v & 2047u) * 1152u), [x] "s"(p.x) : "memory", "scc");
                asm volatile("s_mov_b64 exec, 1\n\tglobal_store_dwordx2 %0, %1, %2 sc1\n\ts_mov_b64 exec, -1" :: "v"(OFF_CI + (272 + xcd) * 8), "v"((v2u){tag, tag}), "s"(p.ws) : "memory");
            }
        }
        STAMP(3);
        asm volatile("s_waitcnt vmcnt(0)" :: "v"(qb), "v"(qc), "v"(qd), "v"(qe) : "memory");
        if (xcd == 0) {
            const u32 cio = OFF_CI + (tid < 140 ? tid : 0) * 16;
            for (u32 spins = 0;; ++spins) {
                REREAD();
                const v4u q2 = __builtin_amdgcn_raw_buffer_load_b128(WSR(), cio, 0, 16);
                if (__all((q2.y == tag) & (q2.w == tag)) || spins > SPIN_CAP) break;
                __builtin_amdgcn_s_sleep(16);
            }
            BAR();
            if (tid == 0) __hip_atomic_store((GAS u32*)p.ws, tag, RLX_AGENT);
        }
        ROLE_END();
    }
    else
        {
            ROLE_BEGIN(3);
            const u32 l16 = lane * 16;
            const char* dsrc = (const char*)(p.wd + (size_t)(64 * w + 32 * (wave - 6)) * D);
            const u32 ddst = (u32)(uintptr_t)(smem + FL_WD) + (wave - 6) * 32 * WDP;
            u32 tagv;
            asm volatile("global_load_dword %0, %1, %2 sc1" : "=v"(tagv) : "v"(0u), "s"(p.ws) : "memory");
            u32 dof[37];
#pragma unroll
            for (int j = 0; j < 37; ++j) {
                const u32 r0 = (64 * j) / 74, c = 64 * j - 74 * r0 + lane;
                dof[j] = c >= 74u ? (r0 + 1) * 1152u + (c - 74u) * 16u : r0 * 1152u + min(c, 71u) * 16u;
                asm volatile("" : "+v"(dof[j]));
            }
#define LD_WD(j) asm volatile("s_mov_b32 m0, %2\n\ts_nop 0\n\tglobal_load_lds_dwordx4 %0, %1" WNT :: "v"(dof[j]), "s"(dsrc), "s"(ddst + (j) * 1024) : "memory")
            asm volatile("s_waitcnt vmcnt(0)" : "+v"(tagv));
            const u32 tg = __builtin_amdgcn_readfirstlane(tagv) + 1u;
            *(LAS v4u*)(smem + FL_Z + min(l16, 256u)) = (v4u){lane >= 16 ? tg : 0u, 0u, 0u, 0u};
            asm volatile("s_mov_b64 exec, 1\n\tglobal_store_dwordx2 %0, %1, %2 sc1\n\ts_mov_b64 exec, -1" :: "v"(OFF_CI + (128 + 2 * w + (wave - 6)) * 8), "v"((v2u){tg, tg}), "s"(p.ws) : "memory");
            if (FDELAY) __builtin_amdgcn_s_sleep(FDELAY);
            v2u sel;
            u32 c_;
            asm volatile("s_mov_b32 %[c], 0x80000\n1:\n\tglobal_load_dwordx2 v[10:11], %[so], %[ws] sc1\n\ts_waitcnt vmcnt(0)\n\tv_cmp_ne_u32 vcc, %[tag], v11\n\ts_cbranch_vccz 2f\n\ts_sub_u32 %[c], %[c], 1\n\ts_cbranch_scc0 1b\n2:"
                         : "=&{v[10:11]}"(sel), [c] "=&s"(c_) : [so] "v"(OFF_SEL + xcd * 256 + (4 * (wave - 6) + (lane & 3)) * 8), [ws] "s"(p.ws), [tag] "s"(tg) : "memory", "scc", "vcc");
            STAMP(3);
#pragma unroll
            for (int k = 0; k < 4; ++k) {
                const u32 t = ~(u32)__builtin_amdgcn_readlane((int)sel.x, k) & 2047u;
                const char* xr = (const char*)p.x + t * (D * 2);
                asm volatile("s_mov_b32 m0, %2\n\ts_nop 0\n\tglobal_load_lds_dwordx4 %0, %1\n\ts_mov_b64 exec, 0xff\n\tglobal_load_lds_dwordx4 %0, %1 offset:1024\n\ts_mov_b64 exec, -1"
                             :: "v"(l16), "s"(xr), "s"((u32)(uintptr_t)(smem + FL_XS) + ((wave - 6) * 4 + k) * XSP) : "memory");
            }
#pragma unroll
            for (int j = 0; j < K2; ++j) LD_WD(j);
            asm volatile("s_waitcnt vmcnt(%0) lgkmcnt(0)\n\ts_barrier" :: "n"(K2) : "memory");
            STAMP(10);
#pragma unroll
            for (int j = K2; j < 37; ++j) LD_WD(j);
            STAMP(11);
            asm volatile("s_waitcnt vmcnt(0)" ::: "memory");
            STAMP(13);
            __builtin_amdgcn_s_barrier();
            ROLE_END();
        }
    } else
    {
        ROLE_BEGIN(5);
        if (FDELAY) __builtin_amdgcn_s_sleep(FDELAY);
        _Pragma("unroll 1") for (int e = (slot - 28) + 4 * wave; touch<0>(p, xcd, e, lane, (u32)(uintptr_t)(smem + wave * 256)); e += 32) {}
        STAMP(11);
        asm volatile("s_waitcnt vmcnt(0)" ::: "memory");
        STAMP(6);
        ROLE_END();
    }
    } else
        {
            ROLE_BEGIN(2);
            const int n = lane & 15, q = lane >> 4;
            v4u gu[3][8];
            if (FDELAY) __builtin_amdgcn_s_sleep(FDELAY);
            const u16* wsrc = (n < 8 ? p.wg : p.wu) + 64 * w + 8 * (n & 7) + (size_t)(96 * wave + 8 * q) * F;
#define LD_GU(s) _Pragma("unroll") for (int j = 0; j < 8; ++j) asm volatile("global_load_dwordx4 %0, %1, off" WNT : "=v"(gu[s][j]) : "v"(wsrc + (size_t)(32 * s + j) * F))
            LD_GU(0);
            LD_GU(1);
            LD_GU(2);
            STAMP(11);
            bf16x8 guf[8];
            auto tr88 = [&](const v4u (&in)[8], bf16x8 (&o)[8]) __attribute__((always_inline)) {
#pragma unroll
                for (int pp = 0; pp < 4; ++pp) {
                    v4u lo, hi;
#pragma unroll
                    for (int jj = 0; jj < 4; ++jj) {
                        lo[jj] = __builtin_amdgcn_perm(in[2 * jj + 1][pp], in[2 * jj][pp], 0x05040100u);
                        hi[jj] = __builtin_amdgcn_perm(in[2 * jj + 1][pp], in[2 * jj][pp], 0x07060302u);
                    }
                    o[2 * pp] = __builtin_bit_cast(bf16x8, lo); o[2 * pp + 1] = __builtin_bit_cast(bf16x8, hi);
                }
#pragma unroll
                for (int i = 0; i < 8; ++i) asm volatile("" : "+v"(o[i]));
            };
#define WAIT_GU(s, N) asm volatile("s_waitcnt vmcnt(%8)" : "+v"(gu[s][0]), "+v"(gu[s][1]), "+v"(gu[s][2]), "+v"(gu[s][3]), "+v"(gu[s][4]), "+v"(gu[s][5]), "+v"(gu[s][6]), "+v"(gu[s][7]) : "n"(N))
            LAS char* xrow = smem + FL_XS + (n & 7) * XSP + (96 * wave + 8 * q) * 2;
            LAS char* xa = n < 8 ? xrow : smem + FL_Z + q * 16;
            LAS char* xb = n < 8 ? smem + FL_Z + q * 16 : xrow;
            LAS char* ta = smem + FL_WD + (lane >> 2) * WDP + (lane & 3) * 8 + wave * 64;
            LAS char* rp = smem + FL_RED + wave * 4096 + (q & 1) * 2048 + (q >> 1) * 512 + n * 8;
            LAS char* rp2 = smem + FL_RED + wave * 4096 + (q & 1) * 2048 + (q >> 1) * 512 + (n ^ 8) * 8;
            const int sl_ = (lane >> 3) * 16 + ((lane & 7) ^ ((lane >> 1) & 8)), hc = 8 * (lane & 7) + (lane >> 3);
            LAS char* ra = smem + FL_RED + (wave & 3) * 1024 + sl_ * 8;
            LAS char* ru = smem + FL_RED + (wave & 3) * 1024 + (sl_ ^ 8) * 8;
            LAS char* hw_ = smem + FL_H + (wave & 3) * 288 + ((hc & 35) | ((hc & 12) << 1) | ((hc & 16) >> 2)) * 2;
            const u32 obase = OFF_PART + ((4 * (2 * wave + (n >> 3)) + q) * NFFN + w) * 128 + (n & 7) * 16;
            u32 obase_ = obase;
            LAS char* hp = smem + FL_H + (n & 7) * 144 + q * 16;
            asm volatile("" : "+v"(xa), "+v"(xb), "+v"(ta), "+v"(rp), "+v"(obase_), "+v"(hp), "+v"(ra), "+v"(hw_), "+v"(rp2), "+v"(ru));
            bf16x8 g0[8], g1[8];
            WAIT_GU(0, 16);
            STAMP(5);
            tr88(gu[0], g0);
            WAIT_GU(1, 8);
            tr88(gu[1], g1);
            STAMP(12);
            WAIT_GU(2, 0);
            STAMP(6);
            tr88(gu[2], guf);
            asm volatile("s_barrier" ::: "memory");
            bf16x8 xf[6];
#pragma unroll
            for (int s = 0; s < 3; ++s) { xf[2 * s] = *(LAS bf16x8*)(xa + s * 64); xf[2 * s + 1] = *(LAS bf16x8*)(xb + s * 64); }
            const u32 tag = *(LAS u32*)(smem + FL_TAG);
            SBAR();
            STAMP(1);
            STAMP(2);
            v4f ha[4];
#pragma unroll
            for (int i = 0; i < 24; ++i) {
                const int f = (i & 3) + 4 * ((i >> 2) & 1);
                ha[i & 3] = MF(xf[i >> 2], i < 8 ? g0[f] : (i < 16 ? g1[f] : guf[f]), i < 4 ? ((v4f){0.f, 0.f, 0.f, 0.f}) : ha[i & 3]);
            }
            STAMP(4);
#pragma unroll
            for (int r = 0; r < 2; ++r)
#pragma unroll
                for (int i = 0; i < 4; ++i) *(LAS v2f*)((i & 2 ? rp2 : rp) + r * 1024 + i * 128) = (v2f){ha[i][2 * r], ha[i][2 * r + 1]};
            STAMP(7);
            BAR();
            s16x4 a[6][4];
            if (wave < 4) {
                v2f pa[6], pu[6];
#pragma unroll
                for (int ww = 0; ww < 6; ++ww) pa[ww] = *(volatile LAS v2f*)(ra + ww * 4096);
#pragma unroll
                for (int ww = 0; ww < 6; ++ww) pu[ww] = *(volatile LAS v2f*)(ru + ww * 4096);
                SBAR();
                const v2f av = ((pa[0] + pa[1]) + (pa[2] + pa[3])) + (pa[4] + pa[5]), uv = ((pu[0] + pu[1]) + (pu[2] + pu[3])) + (pu[4] + pu[5]);
                const u32 ab = cvtpk(av.x, av.y), ub = cvtpk(uv.x, uv.y);
                const u32 sl = cvtpk(bflo(ab) * sigmoidf_(bflo(ab)), bfhi(ab) * sigmoidf_(bfhi(ab)));
                const u32 hh = cvtpk(bflo(sl) * bflo(ub), bfhi(sl) * bfhi(ub));
                *(LAS u16*)(hw_) = (u16)hh; *(LAS u16*)(hw_ + 144) = (u16)(hh >> 16);
                asm volatile("s_waitcnt lgkmcnt(0)" ::: "memory");
            }
            STAMP(8);
#pragma unroll
            for (int k = 0; k < 6; ++k) {
                const int ko = (k >> 1) * 384 + (k & 1) * 32;
                a[k][0] = __builtin_amdgcn_ds_read_tr16_b64_v4i16((LAS s16x4*)(ta + ko));
                a[k][1] = __builtin_amdgcn_ds_read_tr16_b64_v4i16((LAS s16x4*)(ta + ko + 16 * WDP));
                a[k][2] = __builtin_amdgcn_ds_read_tr16_b64_v4i16((LAS s16x4*)(ta + ko + 32 * WDP));
                a[k][3] = __builtin_amdgcn_ds_read_tr16_b64_v4i16((LAS s16x4*)(ta + ko + 48 * WDP));
            }
            asm volatile("s_barrier" ::: "memory");
            STAMP(13);
            const bf16x8 h0 = *(LAS bf16x8*)(hp);
            const bf16x8 h1 = *(LAS bf16x8*)(hp + 64);
            v4f ya[6];
#pragma unroll
            for (int k = 0; k < 6; ++k) ya[k] = __builtin_amdgcn_mfma_f32_16x16x32_bf16(__builtin_shufflevector(a[k][0], a[k][1], 0, 1, 2, 3, 4, 5, 6, 7), h0, (v4f){0.f, 0.f, 0.f, 0.f}, 0, 0, 0);
#pragma unroll
            for (int k = 0; k < 6; ++k) ya[k] = __builtin_amdgcn_mfma_f32_16x16x32_bf16(__builtin_shufflevector(a[k][2], a[k][3], 0, 1, 2, 3, 4, 5, 6, 7), h1, ya[k], 0, 0, 0);
#pragma unroll
            for (int k = 0; k < 3; ++k) {
                const v4f v = n < 8 ? ya[2 * k] : ya[2 * k + 1];
                __builtin_amdgcn_raw_buffer_store_b128((v4u){cvtpk(v[0], v[1]), tag, cvtpk(v[2], v[3]), tag}, WSR(), obase_ + k * (48 * NFFN * 128), 0, 16);
            }
            STAMP(9);
            ROLE_END();
        }
    } else {
    if (wave >= 4)
    {
        ROLE_BEGIN(1);
        const int hw = wave - 4, ht = tid - 256;
        const v4u z4 = {0, 0, 0, 0};
        const int sub = lane & 15;
        v4u xr[5];
        u32 tagv;
        const int rrow = 16 * (cw & 127) + 4 * hw;
        {
            const u32 ro = (u32)((rrow + (lane >> 4)) * D + sub * 8) * 2, ro4 = ro - (sub & 8) * 16;
#pragma unroll
            for (int j = 0; j < 4; ++j) asm volatile("global_load_dwordx4 %0, %1, %2 offset:%3" : "=v"(xr[j]) : "v"(ro), "s"(p.x), "n"(j * 256));
            asm volatile("global_load_dwordx4 %0, %1, %2 offset:1024" : "=v"(xr[4]) : "v"(ro4), "s"(p.x));
            SBAR();
            const u32 dst = (u32)(uintptr_t)(smem + LDS_RW) + hw * 3072, l16 = lane * 16;
            asm volatile("s_mov_b32 m0, %5\n\tglobal_load_dword %0, %1, %2 sc1\n\tglobal_load_lds_dwordx4 %3, %4" : "=&v"(tagv) : "v"(0u), "s"(p.ws), "v"(l16), "s"(p.wr), "s"(dst) : "memory");
            glds16(lane < 8 ? p.wr + 512 + lane * 8 : p.gw + (lane - 8) * 8, dst + 1024, false);
            asm volatile("s_mov_b32 m0, %2\n\ts_nop 0\n\tglobal_load_lds_dwordx4 %0, %1" :: "v"(896 + sub * 16), "s"(p.gw), "s"(dst + 2048) : "memory");
        }
        __builtin_amdgcn_s_barrier();
        STAMP(1);
        if (XPF) {
            const int id = min((slot * 4 + hw) * 64 + lane, 4095);
            u32 keep;
            asm volatile("s_mov_b32 %0, m0\n\ts_mov_b32 m0, %3\n\ts_nop 0\n\tglobal_load_lds_dword %1, %2\n\ts_mov_b32 m0, %0" : "=&s"(keep) : "v"((u32)((id >> 1) * 1152 + ((id & 1) ? 1024 : xcd * 128))), "s"(p.x), "s"((u32)(uintptr_t)(smem + LDS_DUMMY)) : "memory");
        }
        v2u uu[8];
        const u32 uo0 = (u32)((256 * hw + 4 * lane) * D + chbase) * 2, uo1 = uo0 + 1024 * D * 2;
#define LD_U(i) asm volatile("global_load_dwordx2 %0, %1, %2 offset:%3" : "=v"(uu[i]) : "v"((i) < 4 ? uo0 : uo1), "s"(p.x), "n"(((i) & 3) * D * 2))
#pragma unroll
        for (int i = 0; i < NB1; ++i) LD_U(i);
        *(LAS v4u*)(smem + LDS_TAP + ht * 16) = z4; *(LAS v4u*)(smem + LDS_TAP + 4096 + ht * 16) = z4;
        *(LAS v4u*)(smem + LDS_TAP + 8192 + ht * 16 + (ht >> 7) * (LDS_U - LDS_TAP - 8192 + ((ht >> 5) & 3) * 4608 + (ht & 31) * 16 - ht * 16)) = z4;
        u32 ctl = (u32)(uintptr_t)(smem + LDS_CTL), one = 1u, cty = ctl + 64 * hw + (lane & 3) * 16;
        const u32 late = (u32)((127 - cw) >> 31), pa_ = OFF_RG + rrow * 8, pk_ = pa_ ^ ((pa_ ^ (OFF_CI + ((cw - 128) * 4 + hw) * 16)) & late);
        u32 po = pk_ + (lane >> 1) + (sub != 0) * (OFF_G - 8192 + rrow * 4 - pk_ + lane * 8192 - (lane >> 1));
        const u64 pmask = 1ull | ((u64)(~late & 1u) << 32) | (~late & ((1u << (1 + REPG)) - 1));
        u32 nrow = 0xffffu - (rrow + (lane >> 4));
        u32 go = OFF_G + (cw % REPG) * 8192 + 2048 + (64 * hw + lane) * 16;
        LAS char* up = smem + LDS_U + (256 + 256 * hw + 4 * lane) * 2;
        LAS char* yp = smem + CH_Y + hw * 1152 + (lane + (lane >> 3)) * 16;
        LAS char* rw = smem + LDS_RW + hw * 3072 + sub * 16;
        asm volatile("" : "+v"(ctl), "+v"(one), "+v"(cty), "+v"(po), "+v"(go), "+v"(up), "+v"(yp), "+v"(rw), "+v"(nrow));
        BAR();
        __builtin_amdgcn_s_setprio(3);
        u32 tag;
        {
            asm volatile("s_waitcnt vmcnt(%6)" : "+v"(xr[0]), "+v"(xr[1]), "+v"(xr[2]), "+v"(xr[3]), "+v"(xr[4]), "+v"(tagv) : "n"(NB1 + XPF) : "memory");
            STAMP(2);
            tag = __builtin_amdgcn_readfirstlane(tagv) + 1u;
            if (sub >= 8) xr[4] = z4;
            float r = 0.f, s = 0.f;
#pragma unroll
            for (int j = 0; j < 5; ++j) {
                const v4u a = *(LAS v4u*)(rw + j * 256), g = *(LAS v4u*)(rw + 1152 + j * 256);
#pragma unroll
                for (int e = 0; e < 4; ++e) {
                    const u32 xe = xr[j][e], ae = a[e], ge = g[e];
                    r = __builtin_amdgcn_fdot2_f32_bf16(__builtin_bit_cast(bf16x2_t, xe), __builtin_bit_cast(bf16x2_t, ae), r, false);
                    s = __builtin_amdgcn_fdot2_f32_bf16(__builtin_bit_cast(bf16x2_t, xe), __builtin_bit_cast(bf16x2_t, ge), s, false);
                }
            }
            r = dpp_add<0x128, 0xf>(r); s = dpp_add<0x128, 0xf>(s); r = dpp_add<0x124, 0xf>(r); s = dpp_add<0x124, 0xf>(s);
            r = dpp_add<0x122, 0xf>(r); s = dpp_add<0x122, 0xf>(s); r = dpp_add<0x121, 0xf>(r); s = dpp_add<0x121, 0xf>(s);
            const u32 pay = make_payload(r, sigmoidf_(rbf(s))), kk = (pay & 0xffff0000u) | nrow;
            const u32 p0 = (u32)__builtin_amdgcn_readlane((int)pay, 0), p1 = (u32)__builtin_amdgcn_readlane((int)pay, 16), p2 = (u32)__builtin_amdgcn_readlane((int)pay, 32), p3 = (u32)__builtin_amdgcn_readlane((int)pay, 48);
            const auto sw = __builtin_amdgcn_permlane16_swap(kk, kk, false, false);
            const u32 g01 = (p0 & 0xffffu) | (p1 << 16), g23 = (p2 & 0xffffu) | (p3 << 16);
            const v4u pv = {sub ? g01 : sw[0], tag, sub ? g23 : sw[1], tag};
            asm volatile("s_mov_b64 exec, %3\n\tglobal_store_dwordx4 %0, %1, %2 sc1\n\ts_mov_b64 exec, -1\n\ts_nop 0" :: "v"(po), "v"(pv), "s"(p.ws), "s"(pmask) : "memory");
        }
        STAMP(3);
        __builtin_amdgcn_s_setprio(HPRIO);
        *(LAS u32*)(smem + LDS_CTL + C_TAG) = tag;
        LDS_ADD1(ctl, one, C_PUB);
        LWAIT(ctl, C_PUB, 4);
#pragma unroll
        for (int i = NB1; i < 4; ++i) LD_U(i);
        LDS_ADD1(ctl, one, C_PUB);
        LWAIT(ctl, C_PUB, 8);
#pragma unroll
        for (int i = 4; i < 8; ++i) LD_U(i);
        v4u gq[2];
#define POLL_G "global_load_dwordx4 v[100:103], %[go], %[ws] offset:-2048 sc1\n\tglobal_load_dwordx4 v[104:107], %[go], %[ws] offset:2048 sc1\n\t"
        asm volatile(POLL_G : "={v[100:103]}"(gq[0]), "={v[104:107]}"(gq[1]) : [go] "v"(go), [ws] "s"(p.ws) : "memory");
        STAMP(4);
        auto stage = [&](int g) __attribute__((always_inline)) {
            const v2u* u4 = uu + 4 * g;
            LAS char* d = up + g * 2048;
            *(LAS v2u*)(d) = (v2u){__builtin_amdgcn_perm(u4[1].x, u4[0].x, 0x05040100u), __builtin_amdgcn_perm(u4[3].x, u4[2].x, 0x05040100u)};
            *(LAS v2u*)(d + 4608) = (v2u){__builtin_amdgcn_perm(u4[1].x, u4[0].x, 0x07060302u), __builtin_amdgcn_perm(u4[3].x, u4[2].x, 0x07060302u)};
            *(LAS v2u*)(d + 2 * 4608) = (v2u){__builtin_amdgcn_perm(u4[1].y, u4[0].y, 0x05040100u), __builtin_amdgcn_perm(u4[3].y, u4[2].y, 0x05040100u)};
            *(LAS v2u*)(d + 3 * 4608) = (v2u){__builtin_amdgcn_perm(u4[1].y, u4[0].y, 0x07060302u), __builtin_amdgcn_perm(u4[3].y, u4[2].y, 0x07060302u)};
            LDS_ADD1(ctl, one, C_STG);
        };
        asm volatile("s_waitcnt vmcnt(6)" : "+v"(uu[0]), "+v"(uu[1]), "+v"(uu[2]), "+v"(uu[3]));
        stage(0);
        STAMP(5);
        touch<1>(p, xcd, slot * 4 + hw, lane, (u32)(uintptr_t)(smem + LDS_DUMMY));
        touch<1>(p, xcd, 72 + ((slot * 4 + hw) % 20), lane, (u32)(uintptr_t)(smem + LDS_DUMMY));
        asm volatile("s_waitcnt vmcnt(4)" : "+v"(uu[4]), "+v"(uu[5]), "+v"(uu[6]), "+v"(uu[7]));
        LWAIT(ctl, C_STG, 4);
        stage(1);
        STAMP(6);
        {
            u32 c_;
            asm volatile("s_mov_b32 %[c], 0x80000\n\ts_waitcnt vmcnt(2)\n1:\n\tv_cmpx_eq_u32 %[tag], v101\n\tv_cmpx_eq_u32 %[tag], v103\n\tv_cmpx_eq_u32 %[tag], v105\n\tv_cmpx_eq_u32 %[tag], v107\n\t"
                         "s_cmp_eq_u64 exec, -1\n\ts_mov_b64 exec, -1\n\ts_cbranch_scc1 2f\n\ts_sleep 4\n\t" POLL_G "s_waitcnt vmcnt(0)\n\ts_sub_u32 %[c], %[c], 1\n\ts_cbranch_scc0 1b\n2:\n\ts_nop 4"
                         : "+{v[100:103]}"(gq[0]), "+{v[104:107]}"(gq[1]), [c] "=&s"(c_) : [go] "v"(go), [ws] "s"(p.ws), [tag] "s"(tag) : "memory", "scc", "vcc");
        }
        STAMP(7);
        *(LAS v2u*)(smem + LDS_G + hw * 3072 + lane * 8) = (v2u){gq[0].x, gq[0].z}; *(LAS v2u*)(smem + LDS_G + hw * 3072 + 512 + lane * 8) = (v2u){gq[1].x, gq[1].z};
#pragma unroll
        for (int g = 0; g < 2; ++g) {
            const v2u* u4 = uu + 4 * g;
            const float gr[4] = {bflo(gq[g].x), bfhi(gq[g].x), bflo(gq[g].z), bfhi(gq[g].z)};
            float o[4][4];
#pragma unroll
            for (int r = 0; r < 4; ++r) {
                const float xv[4] = {bflo(u4[r].x), bfhi(u4[r].x), bflo(u4[r].y), bfhi(u4[r].y)};
#pragma unroll
                for (int c = 0; c < 4; ++c) asm("v_fma_f32 %0, -%1, %2, %2" : "=v"(o[r][c]) : "v"(gr[r]), "v"(xv[c]));
                asm volatile("" : "+v"(o[r][0]), "+v"(o[r][1]), "+v"(o[r][2]), "+v"(o[r][3]));
            }
            {
                u32 t_, c_;
                asm volatile("s_mov_b32 %1, 0x400000\n1:\n\tds_read_b32 %0, %2 offset:%3\n\ts_waitcnt lgkmcnt(0)\n\tv_cmp_eq_u32 vcc, 0, %0\n\ts_cmp_eq_u64 vcc, 0\n\ts_cbranch_scc1 2f\n\ts_sleep %4\n\ts_sub_u32 %1, %1, 1\n\ts_cbranch_scc0 1b\n2:"
                             : "=&v"(t_), "=&s"(c_) : "v"(cty), "n"(C_Y + 256 * g), "n"(YSLEEP) : "memory", "scc", "vcc");
            }
            if (g == 0) STAMP(11);
            v4f y[4];
#pragma unroll
            for (int c = 0; c < 4; ++c) y[c] = *(LAS v4f*)(yp + c * KCH + g * 4608);
            SBAR();
#define ST_O(i, v) asm volatile("global_store_dwordx2 %0, %1, %2 offset:%3" :: "v"(g ? uo1 : uo0), "v"(v), "s"(p.out), "n"((i) * D * 2) : "memory")
#pragma unroll
            for (int r = 0; r < 4; ++r) {
#pragma unroll
                for (int c = 0; c < 4; ++c) { const float yv = y[c][r]; asm("v_fmac_f32 %0, %1, %2" : "+v"(o[r][c]) : "v"(gr[r]), "v"(yv)); }
                ST_O(r, ((v2u){cvtpk(o[r][0], o[r][1]), cvtpk(o[r][2], o[r][3])}));
            }
            STAMP(8 + g);
        }
        asm volatile("s_waitcnt vmcnt(0)" ::: "memory");
        LDS_ADD1(ctl, one, C_ACK);
        STAMP(10);
        ROLE_END();
    }
    else
    {
        ROLE_BEGIN(0);
        const int ch = wave;
        const int dch = chbase + ch, pi = dch * NST + lane * 2;
        const u32 are = __builtin_nontemporal_load((const u32*)(p.Are + pi)), aim = __builtin_nontemporal_load((const u32*)(p.Aim + pi)), bre = __builtin_nontemporal_load((const u32*)(p.Bre + pi));
        const u32 bim = __builtin_nontemporal_load((const u32*)(p.Bim + pi)), cre = __builtin_nontemporal_load((const u32*)(p.Cre + pi)), cim = __builtin_nontemporal_load((const u32*)(p.Cim + pi));
        const u16 ldt = p.logdt[dch], dsk = p.dskip[dch];
        SBAR();
        __builtin_amdgcn_s_barrier();
        SBAR();
        LAS char* kb = smem + ch * KCH;
        const int r32 = lane & 31, h = lane >> 5, q = lane >> 4, n = lane & 15;
        const int ai = 4 * (r32 >> 3) + (r32 & 3), hh = (r32 >> 2) & 1;
        const u64 tsel = ai < 8 ? (hh ? KROW_1LO : KROW_0LO) : (hh ? KROW_1HI : KROW_0HI);
        const int o = (int)((tsel >> (8 * (ai & 7))) & 0xff);
        LAS char* ap = kb + o * STG_ROW + h * 16;
        LAS char* bp = kb + r32 * STG_ROW + h * 16;
        u32 sg[14];
#pragma unroll
        for (int k = 0; k < 14; ++k) { sg[k] = (u32)(uintptr_t)(kb + lane * 8 + k * 4 * STG_ROW); asm volatile("" : "+v"(sg[k])); }
        bf16x8 b5 = band(((0x1fu << 6) << n) >> (8 * q), 0x3f803f80u), b3 = band(((0x7u << 11) << n) >> (8 * q), 0x3f803f80u);
        bf16x8 b9 = band(((0x1ffu << 4) << n) >> (8 * q), 0x3f803f80u), b7 = band(((0x7fu << 5) << n) >> (8 * q), 0x3f803f80u);
        bf16x8 pat = band(((0x3u << 12) << n) >> (8 * q), 0xffffffffu);
        LAS char* k0 = kb + 32 + lane * 16;
        LAS char* kt = kb + (lane & 3) * KCOPY + (lane < 16 ? 32 + 1024 + (lane >> 2) * 16 : ((lane >> 2) & 1) * 16);
        LAS char* a0 = kb + ((lane >> 2) & 3) * KCOPY + (28 - 8 * q + 4 * (lane & 3)) * 2;
        LAS char* bb = smem + LDS_U + ch * 4608 + (16 * n + 8 * q - 16) * 2;
        LAS char* yb = kb + CH_Y + (4 * n + q + (n >> 1)) * 16;
        LAS char* tp = smem + LDS_TAP + ch * 2304 + ((n & 1) ? 13 + n - 4 * q : 96 + 14 + n - 4 * q) * 4;
        LAS char* ta = smem + LDS_TAP + ch * 2304 + (h ? 0 : 768) + 64 + 2 * r32;
        LAS char* tb = smem + LDS_TAP + ch * 2304 + 1600 + 64 * h + 2 * r32;
        u32 ctl = (u32)(uintptr_t)(smem + LDS_CTL), ctc = ctl + C_Y + ch * 16 + (lane & 15), one = 1u, so = (lane & 8) ? OFF_PART + (cw * NFFN + (cw >> 1)) * 128 : OFF_SEL + (8 + xcd) * 256 + (lane & 7) * 8;
        u32 m01 = lane < 2 ? ~0u : 0u, m2 = lane == 2 ? ~0u : 0u, m3 = lane == 3 ? ~0u : 0u, m0_ = lane == 0 ? ~0u : 0u;
        asm volatile("" : "+v"(b5), "+v"(b3), "+v"(b9), "+v"(b7), "+v"(pat), "+v"(ap), "+v"(bp), "+v"(m01), "+v"(m2), "+v"(m3), "+v"(m0_));
        asm volatile("" : "+v"(k0), "+v"(kt), "+v"(a0), "+v"(bb), "+v"(yb), "+v"(tp), "+v"(ta), "+v"(tb), "+v"(ctl), "+v"(ctc), "+v"(one), "+v"(so));
        __builtin_amdgcn_s_barrier();
        SBAR();
        const float dt = rbf(expf(bf2f(ldt)));
        STAMP(1);
        v16f acc;
        u32 stg;
        float kscale;
        {
            const v2f a2 = (v2f){bflo(are), bfhi(are)} * dt * 1.44269504088896341f;
            const v2f brev = (v2f){bflo(aim), bfhi(aim)} * dt * 0.15915494309189535f;
            const cf2 Bc = {{dt * bflo(bre), dt * bflo(bim)}, {dt * bfhi(bre), dt * bfhi(bim)}};
            const cf2 Cc = {{bflo(cre), bflo(cim)}, {bfhi(cre), bfhi(cim)}};
            cf2 W = cmul2(Cc, Bc);
            u32 mx = max(max(__float_as_uint(W.a.x) & 0x7f800000u, __float_as_uint(W.a.y) & 0x7f800000u), max(__float_as_uint(W.b.x) & 0x7f800000u, __float_as_uint(W.b.y) & 0x7f800000u));
            mx = dpp_umax<0x111, 0xf>(mx); mx = dpp_umax<0x112, 0xf>(mx); mx = dpp_umax<0x114, 0xf>(mx); mx = dpp_umax<0x118, 0xf>(mx);
            mx = dpp_umax<0x142, 0xa>(mx); mx = dpp_umax<0x143, 0xc>(mx);
            mx = min(max((u32)__builtin_amdgcn_readlane((int)mx, 63), 0x00800000u), 0x7e800000u);
            kscale = __uint_as_float(mx);
            const float inv = __uint_as_float(0x7f000000u - mx);
            W.a *= inv; W.b *= inv;
            const cf2 E1 = cexp2(a2, brev, 1.f), E8 = cexp2(a2, brev, 8.f), E256 = cexp2(a2, brev, 256.f);
            asm volatile("s_nop 0\n\t" KGEN_ASM :: "{v[48:49]}"(W.a), "{v[50:51]}"(W.b), "{v[52:53]}"(E1.a), "{v[54:55]}"(E1.b), "{v[56:57]}"(E8.a), "{v[58:59]}"(E8.b), "{v[60:61]}"(E256.a), "{v[62:63]}"(E256.b),
                         [b0] "v"(sg[0]), [b1] "v"(sg[1]), [b2] "v"(sg[2]), [b3] "v"(sg[3]), [b4] "v"(sg[4]), [b5] "v"(sg[5]), [b6] "v"(sg[6]), [b7] "v"(sg[7]), [b8] "v"(sg[8]), [b9] "v"(sg[9]),
                         [b10] "v"(sg[10]), [b11] "v"(sg[11]), [b12] "v"(sg[12]), [b13] "v"(sg[13]) : "memory", KGEN_CLOBBER);
            STAMP(2);
            asm volatile(KMFMA_ASM : "=&{v[48:63]}"(acc), [stg] "=&v"(stg) : [ap] "v"(ap), [bp] "v"(bp), [ctl] "v"(ctl) : "memory", KMFMA_CLOBBER);
            acc *= kscale;
        }
        STAMP(3);
        acc[0] += __uint_as_float(((u32)dsk << 16) & m0_);
        u32 t78 = cvtpk(acc[7], acc[8]), t9a = cvtpk(acc[9], acc[10]);
        {
            const auto s0 = __builtin_amdgcn_permlane32_swap(cvtpk(acc[0], acc[1]), cvtpk(acc[4], acc[4]), false, false);
            const auto s1 = __builtin_amdgcn_permlane32_swap(cvtpk(acc[2], acc[3]), cvtpk(acc[5], acc[6]), false, false);
            const v4u ww = {s0[0], s1[0], s0[1], s1[1]};
            const u32 pz = (u32)__builtin_amdgcn_update_dpp(0, (int)ww.z, 0x138, 0xf, 0xf, true), pw = (u32)__builtin_amdgcn_update_dpp(0, (int)ww.w, 0x138, 0xf, 0xf, true);
            const v4u c1 = {__builtin_amdgcn_alignbit(ww.x, pw, 16), __builtin_amdgcn_alignbit(ww.y, ww.x, 16), __builtin_amdgcn_alignbit(ww.z, ww.y, 16), __builtin_amdgcn_alignbit(ww.w, ww.z, 16)};
            *(LAS v4u*)(k0) = ww;
            *(LAS v4u*)(k0 + KCOPY) = c1;
            *(LAS v4u*)(k0 + 2 * KCOPY) = (v4u){pw, ww.x, ww.y, ww.z};
            *(LAS v4u*)(k0 + 3 * KCOPY) = (v4u){__builtin_amdgcn_alignbit(pw, pz, 16), c1.x, c1.y, c1.z};
            const u32 w0 = (u32)__builtin_amdgcn_readlane((int)ww.z, 63), w1 = (u32)__builtin_amdgcn_readlane((int)ww.w, 63);
            const u32 w3 = w1 >> 16, w2 = w3 * 0x10001u, w10 = (w0 >> 16) | (w1 << 16);
            *(LAS v4u*)kt = (v4u){(w2 & m01) | (w1 & m2) | (w10 & m3), (w3 & m0_) | (w2 & ((m01 ^ m0_) | m2 | m3)), (w3 & m2) | (w2 & m3), 0u};
        }
        asm volatile("" ::: "memory");
        pat = __builtin_bit_cast(bf16x8, __builtin_bit_cast(v4u, pat) & (((u32)__builtin_amdgcn_readlane((int)t78, 63) >> 16) * 0x10001u));
#define POLL_SEL "global_load_dwordx2 v[240:241], %[so], %[ws] sc1\n\t"
        v2u sq;
        const v4f zero4 = {0.f, 0.f, 0.f, 0.f};
        if (MPRIO) __builtin_amdgcn_s_setprio(MPRIO);
        STAMP(4);
        {
            u32 s_, c_;
            asm volatile(CONV_ASM : "=&{v[240:241]}"(sq), [s] "=&s"(s_), [c] "=&s"(c_)
                         : "{v[4:7]}"(b5), "{v[8:11]}"(b3), "{v[12:15]}"(b9), "{v[16:19]}"(b7), "{v[20:23]}"(pat), [a0] "v"(a0), [bb] "v"(bb), [yb] "v"(yb), [tp] "v"(tp), [ta] "v"(ta), [tb] "v"(tb),
                           [ctl] "v"(ctl), [ctc] "v"(ctc), [one] "v"(one), [so] "v"(so), [t78] "v"(t78), [t9a] "v"(t9a), [stg] "v"(stg), [ws] "s"(p.ws) : "memory", "scc", CONV_CLOBBER);
        }
        STAMP(9);
        asm volatile("s_cmp_eq_u32 %0, 0\n\ts_cbranch_scc1 1f\n\ts_endpgm\n1:" :: "s"(wave) : "memory", "scc");
        {
            const u32 tag = __builtin_amdgcn_readfirstlane(*(LAS u32*)(smem + LDS_CTL + C_TAG));
            u32 c_;
            asm volatile("s_mov_b32 %[c], 0x80000\n1:\n\ts_waitcnt vmcnt(0)\n\tv_cmpx_eq_u32 %[tag], v241\n\t"
                         "s_cmp_eq_u64 exec, -1\n\ts_mov_b64 exec, -1\n\ts_cbranch_scc1 2f\n\t" POLL_SEL "s_sub_u32 %[c], %[c], 1\n\ts_cbranch_scc0 1b\n\ts_waitcnt vmcnt(0)\n2:\n\ts_nop 4"
                         : "+{v[240:241]}"(sq), [c] "=&s"(c_) : [so] "v"(so), [ws] "s"(p.ws), [tag] "s"(tag) : "memory", "scc", "vcc");
            STAMP(10);
            const u32 pb = OFF_PART + cw * (NFFN * 128) + 4096 + lane * 16, pb2 = pb + 4096;
            v4u pq[9];
#define POLL_P "global_load_dwordx4 v[64:67], %[pb], %[ws] offset:-4096 sc1\n\tglobal_load_dwordx4 v[68:71], %[pb], %[ws] offset:-3072 sc1\n\tglobal_load_dwordx4 v[72:75], %[pb], %[ws] offset:-2048 sc1\n\t" \
               "global_load_dwordx4 v[76:79], %[pb], %[ws] offset:-1024 sc1\n\tglobal_load_dwordx4 v[80:83], %[pb], %[ws] sc1\n\tglobal_load_dwordx4 v[84:87], %[pb], %[ws] offset:1024 sc1\n\t" \
               "global_load_dwordx4 v[88:91], %[pb], %[ws] offset:2048 sc1\n\tglobal_load_dwordx4 v[92:95], %[pb], %[ws] offset:3072 sc1\n\tglobal_load_dwordx4 v[96:99], %[pb2], %[ws] sc1\n\t"
            asm volatile(POLL_P
                         : "=&{v[64:67]}"(pq[0]), "=&{v[68:71]}"(pq[1]), "=&{v[72:75]}"(pq[2]), "=&{v[76:79]}"(pq[3]), "=&{v[80:83]}"(pq[4]), "=&{v[84:87]}"(pq[5]), "=&{v[88:91]}"(pq[6]), "=&{v[92:95]}"(pq[7]), "=&{v[96:99]}"(pq[8])
                         : [pb] "v"(pb), [pb2] "v"(pb2), [ws] "s"(p.ws) : "memory");
            sq.x = (u32)__builtin_amdgcn_update_dpp((int)sq.x, (int)sq.x, 0x128, 0xf, 0xc, false);
            const int t = (int)(0xffffu - (sq.x & 0xffffu)) & 2047;
            const float rs = rbf(sigmoidf_(key_to_r(sq.x)));
            LWAIT(ctl, C_ACK, 4);
            const float g = bf2f(*(LAS u16*)(smem + LDS_G + ((t >> 8) & 3) * 3072 + (t >> 10) * 512 + (t & 255) * 2));
            const int uq = (t & 255) >> 2;
            float xv[4], av[4];
#pragma unroll
            for (int c = 0; c < 4; ++c) {
                xv[c] = bf2f(*(LAS u16*)(smem + LDS_U + c * 4608 + (256 + t) * 2));
                av[c] = g * *(LAS float*)(smem + c * KCH + CH_Y + (t >> 8) * 1152 + (uq + (uq >> 3)) * 16 + (t & 3) * 4);
            }
            const u32 e1 = n < 4 ? ((n & 1) ? 0x3f800000u : 0x00003f80u) : 0u;
            bf16x8 sel4 = __builtin_bit_cast(bf16x8, (n & 2) ? (v4u){0u, e1, 0u, e1} : (v4u){e1, 0u, e1, 0u});
            u32 oo = (u32)(t * D + chbase) * 2;
            float omg = 1.f - g;
            asm volatile("" : "+v"(sel4), "+v"(oo), "+v"(omg), "+v"(xv[0]), "+v"(xv[1]), "+v"(xv[2]), "+v"(xv[3]), "+v"(av[0]), "+v"(av[1]), "+v"(av[2]), "+v"(av[3]));
            asm volatile("s_mov_b32 %[c], 0x80000\n1:\n\ts_waitcnt vmcnt(0)\n\t"
                         "v_cmpx_eq_u32 %[tag], v65\n\tv_cmpx_eq_u32 %[tag], v67\n\tv_cmpx_eq_u32 %[tag], v69\n\tv_cmpx_eq_u32 %[tag], v71\n\tv_cmpx_eq_u32 %[tag], v73\n\tv_cmpx_eq_u32 %[tag], v75\n\t"
                         "v_cmpx_eq_u32 %[tag], v77\n\tv_cmpx_eq_u32 %[tag], v79\n\tv_cmpx_eq_u32 %[tag], v81\n\tv_cmpx_eq_u32 %[tag], v83\n\tv_cmpx_eq_u32 %[tag], v85\n\tv_cmpx_eq_u32 %[tag], v87\n\t"
                         "v_cmpx_eq_u32 %[tag], v89\n\tv_cmpx_eq_u32 %[tag], v91\n\tv_cmpx_eq_u32 %[tag], v93\n\tv_cmpx_eq_u32 %[tag], v95\n\tv_cmpx_eq_u32 %[tag], v97\n\tv_cmpx_eq_u32 %[tag], v99\n\t"
                         "s_cmp_eq_u64 exec, -1\n\ts_mov_b64 exec, -1\n\ts_cbranch_scc1 2f\n\t" POLL_P "s_sub_u32 %[c], %[c], 1\n\ts_cbranch_scc0 1b\n\ts_waitcnt vmcnt(0)\n2:\n\ts_nop 4"
                         : "+{v[64:67]}"(pq[0]), "+{v[68:71]}"(pq[1]), "+{v[72:75]}"(pq[2]), "+{v[76:79]}"(pq[3]), "+{v[80:83]}"(pq[4]), "+{v[84:87]}"(pq[5]), "+{v[88:91]}"(pq[6]), "+{v[92:95]}"(pq[7]), "+{v[96:99]}"(pq[8]), [c] "=&s"(c_)
                         : [pb] "v"(pb), [pb2] "v"(pb2), [ws] "s"(p.ws), [tag] "s"(tag) : "memory", "scc", "vcc");
            STAMP(11);
            v4f s0 = MF(sel4, __builtin_bit_cast(bf16x8, ((v4u){pq[0].x, pq[0].z, pq[1].x, pq[1].z})), zero4);
            v4f s1 = MF(sel4, __builtin_bit_cast(bf16x8, ((v4u){pq[2].x, pq[2].z, pq[3].x, pq[3].z})), zero4);
            s0 = MF(sel4, __builtin_bit_cast(bf16x8, ((v4u){pq[4].x, pq[4].z, pq[5].x, pq[5].z})), s0);
            s1 = MF(sel4, __builtin_bit_cast(bf16x8, ((v4u){pq[6].x, pq[6].z, pq[7].x, pq[7].z})), s1);
            s0 = MF(sel4, __builtin_bit_cast(bf16x8, ((v4u){pq[8].x, pq[8].z, 0u, 0u})), s0);
            float o[4];
#pragma unroll
            for (int c = 0; c < 4; ++c) o[c] = fmaf(omg, xv[c] + rbf(rbf(dpp_add<0x128, 0xf>(s0[c] + s1[c])) * rs), av[c]);
            asm volatile("s_mov_b64 exec, 0xff\n\tglobal_store_dwordx2 %0, %1, %2\n\ts_mov_b64 exec, -1" :: "v"(oo), "v"((v2u){cvtpk(o[0], o[1]), cvtpk(o[2], o[3])}), "s"(p.out) : "memory");
        }
        STAMP(12);
        ROLE_END();
    }
    }
}

extern "C" void kernel_launch(void* const* d_in, const int* in_sizes, int n_in,
                              void* d_out, int out_size, void* d_ws, size_t ws_size,
                              hipStream_t stream) {
    Params p;
    p.x = (const u16*)d_in[0]; p.Are = (const u16*)d_in[1]; p.Aim = (const u16*)d_in[2]; p.Bre = (const u16*)d_in[3];
    p.Bim = (const u16*)d_in[4]; p.Cre = (const u16*)d_in[5]; p.Cim = (const u16*)d_in[6]; p.logdt = (const u16*)d_in[7];
    p.dskip = (const u16*)d_in[8]; p.wr = (const u16*)d_in[10]; p.wg = (const u16*)d_in[11]; p.wu = (const u16*)d_in[12];
    p.wd = (const u16*)d_in[13]; p.gw = (const u16*)d_in[14]; p.out = (u16*)d_out; p.ws = (char*)d_ws;
    (void)hipFuncSetAttribute((const void*)fused_kernel, hipFuncAttributeMaxDynamicSharedMemorySize, LDS_TOTAL);
    fused_kernel<<<256, 512, LDS_TOTAL, stream>>>(p);
}
```

```cpp
#include <hip/hip_runtime.h>
#include <stdint.h>

typedef unsigned short u16;
typedef unsigned int u32;
typedef unsigned long long u64;
typedef __attribute__((ext_vector_type(4))) u32 v4u;
typedef __attribute__((ext_vector_type(2))) u32 v2u;
typedef __attribute__((ext_vector_type(2))) float v2f;
typedef __attribute__((ext_vector_type(4))) float v4f;
typedef __attribute__((ext_vector_type(16))) float v16f;
typedef __attribute__((ext_vector_type(8))) short bf16x8;
typedef __attribute__((ext_vector_type(4))) short s16x4;
typedef __attribute__((ext_vector_type(2))) __bf16 bf16x2_t;
typedef __attribute__((ext_vector_type(2))) _Float16 f16x2_t;
typedef __attribute__((ext_vector_type(8))) _Float16 f16x8;
#define LAS __attribute__((address_space(3)))
#define GAS __attribute__((address_space(1)))
#define RLX_AGENT __ATOMIC_RELAXED, __HIP_MEMORY_SCOPE_AGENT
#define SBAR() __builtin_amdgcn_sched_barrier(0)
#define BAR() asm volatile("s_waitcnt lgkmcnt(0)\n\ts_barrier" ::: "memory")
#define WSR() __builtin_amdgcn_make_buffer_rsrc(p.ws, 0, 0x7fffffff, 0x00020000)
#define REREAD() asm volatile("" ::: "memory")

#define WNT " nt"
#define CODE_KB 24
#define NB1 2
#define REPG 2
#define PFG0 480
#define FDELAY 0
#define PFR0 16
#define SELPF 0
#define SELWAIT 50
#define MPRIO 0
#define YSLEEP 1
#define HPRIO 0
#define K2 10
#define TOUCH 0
#define XT 0
#define WT 0
#define XPF 0

constexpr int L = 2048, D = 576, NST = 128, F = 4608;
constexpr int NFFN = 72;
constexpr u32 OFF_RG = 4096;
constexpr u32 OFF_G = OFF_RG + 20480;
constexpr u32 OFF_SEL = OFF_G + REPG * 10240;
constexpr u32 OFF_CI = OFF_SEL + 16 * 256;
constexpr u32 OFF_PART = OFF_CI + 4096;
constexpr u32 PP = 11 * 1024;
constexpr u32 SPIN_CAP = 1u << 19;

#define STAMP(i)
#define STAMP0_FIX()
#define STAMP_FLUSH()

struct Params {
    const u16 *x, *Are, *Aim, *Bre, *Bim, *Cre, *Cim, *logdt, *dskip, *wr, *wg, *wu, *wd, *gw;
    u16* out;
    char* ws;
};

__device__ __forceinline__ float bflo(u32 w) { return __uint_as_float(w << 16); }
__device__ __forceinline__ float bfhi(u32 w) { return __uint_as_float(w & 0xffff0000u); }
__device__ __forceinline__ float bf2f(u16 h) { return __uint_as_float((u32)h << 16); }
__device__ __forceinline__ u32 cvtpk(float lo, float hi) { v2f v = {lo, hi}; bf16x2_t r = __builtin_convertvector(v, bf16x2_t); return __builtin_bit_cast(u32, r); }
__device__ __forceinline__ u32 cvth(v2f v) { return __builtin_bit_cast(u32, __builtin_convertvector(v, f16x2_t)); }
__device__ __forceinline__ float rbf(float f) { return bflo(cvtpk(f, f)); }
__device__ __forceinline__ float sigmoidf_(float s) { return __builtin_amdgcn_rcpf(1.f + __expf(-s)); }

template <int CTRL, int RM> __device__ __forceinline__ float dpp_add(float v) {
    return v + __builtin_bit_cast(float, __builtin_amdgcn_update_dpp(0, __builtin_bit_cast(int, v), CTRL, RM, 0xf, true));
}
__device__ __forceinline__ float wave_sum63(float v) {
    v = dpp_add<0x111, 0xf>(v); v = dpp_add<0x112, 0xf>(v); v = dpp_add<0x114, 0xf>(v); v = dpp_add<0x118, 0xf>(v);
    v = dpp_add<0x142, 0xa>(v); v = dpp_add<0x143, 0xc>(v);
    return v;
}
template <int CTRL, int RM> __device__ __forceinline__ u32 dpp_umax(u32 v) {
    u32 o = (u32)__builtin_amdgcn_update_dpp(0, (int)v, CTRL, RM, 0xf, true);
    return v > o ? v : o;
}

typedef v2f cf;
__device__ __forceinline__ cf cmul(cf a, cf b) { cf t = a.yy * (cf){-b.y, b.x}; return __builtin_elementwise_fma(a.xx, b, t); }
__device__ __forceinline__ cf cexp_(float a2, float brev, float rho) {
    float m = __builtin_amdgcn_exp2f(a2 * rho);
    float ph = __builtin_amdgcn_fractf(brev * rho);
    cf c; c.x = m * __builtin_amdgcn_cosf(ph); c.y = m * __builtin_amdgcn_sinf(ph); return c;
}

struct cf2 { cf a, b; };
__device__ __forceinline__ cf2 cmul2(cf2 x, cf2 y) { return {cmul(x.a, y.a), cmul(x.b, y.b)}; }
__device__ __forceinline__ cf2 cexp2(v2f a2, v2f brev, float rho) { return {cexp_(a2.x, brev.x, rho), cexp_(a2.y, brev.y, rho)}; }

__device__ __forceinline__ u32 make_payload(float r, float g) {
    const u32 w = cvtpk(g, r);
    return w ^ (((u32)((int)w >> 31) & 0x7fff0000u) | 0x80000000u);
}
__device__ __forceinline__ u32 make_key(u32 pay, u32 t) { return (pay & 0xffff0000u) | (0xffffu - t); }
__device__ __forceinline__ float key_to_r(u32 k) {
    u32 hi = k & 0xffff0000u;
    return __uint_as_float((hi & 0x80000000u) ? (hi ^ 0x80000000u) : (~hi & 0xffff0000u));
}

constexpr int STG_ROW = 528;
constexpr int NA = 21;
constexpr int KCH = (NA + 32) * STG_ROW;
constexpr int KCOPY = 1344;
constexpr int CH_Y = 4 * KCOPY;
constexpr int LDS_U = 4 * KCH;
constexpr int LDS_TAP = LDS_U + 4 * 4608;
constexpr int LDS_CTL = LDS_TAP + 4 * 2304;
constexpr int C_TAG = 0, C_STG = 4, C_PUB = 8, C_ACK = 12, C_ST2 = 20, C_Y = 64;
constexpr int LDS_RW = LDS_CTL + 1024;
constexpr int LDS_G = LDS_RW;
constexpr int LDS_DUMMY = LDS_RW + 4 * 3072;
constexpr int LDS_TOTAL = LDS_DUMMY + 1024;
static_assert(CH_Y + 8 * 1152 <= KCH && LDS_TOTAL <= 160 * 1024, "lds");
constexpr int WDP = 1184;
constexpr int FL_WD = 0;
constexpr int FL_RED = 74 * 1024;
constexpr int FL_H = FL_RED + 24576;
constexpr int XSP = 1168;
constexpr int FL_XS = FL_H + 8 * 144;
constexpr int FL_Z = FL_XS + 8 * XSP;
constexpr int FL_TAG = FL_Z + 256;

__device__ __forceinline__ int sum_rows(int v) {
    auto a = __builtin_amdgcn_permlane16_swap((u32)v, (u32)v, false, false);
    v = (int)(a[0] + a[1]);
    auto b = __builtin_amdgcn_permlane32_swap((u32)v, (u32)v, false, false);
    return (int)(b[0] + b[1]);
}
__device__ __forceinline__ float sum_rows_f(float v) {
    auto a = __builtin_amdgcn_permlane16_swap(__float_as_uint(v), __float_as_uint(v), false, false);
    v = __uint_as_float(a[0]) + __uint_as_float(a[1]);
    auto b = __builtin_amdgcn_permlane32_swap(__float_as_uint(v), __float_as_uint(v), false, false);
    return __uint_as_float(b[0]) + __uint_as_float(b[1]);
}
__device__ __forceinline__ u32 top8_d(LAS char* t8, int lane, int n) {
    LAS u32* cand = (LAS u32*)(t8 + 256);
    u32 res = 0;
    _Pragma("unroll 1") for (int i0 = 0; i0 < n; i0 += 64) {
        const int i = i0 + lane;
        const u32 k = i < n ? cand[i] : 0u;
        int rk = 0;
        _Pragma("unroll 1") for (int j0 = 0; j0 < n; j0 += 16) {
            v4u c[4];
#pragma unroll
            for (int e = 0; e < 4; ++e) c[e] = *(LAS v4u*)(cand + j0 + 4 * e);
#pragma unroll
            for (int e = 0; e < 16; ++e) rk += (c[e >> 2][e & 3] > k) & (j0 + e < n);
        }
        const bool hit = i < n && rk < 8;
        const u32 got = (u32)__builtin_amdgcn_ds_permute((hit ? rk : 63) * 4, (int)(hit ? k : 0u));
        if (lane < 8) res |= got;
    }
    return res;
}

__device__ __forceinline__ u32 lds_peek(LAS char* word) { return __hip_atomic_load((LAS u32*)word, __ATOMIC_RELAXED, __HIP_MEMORY_SCOPE_WORKGROUP); }
#define LWAIT_BODY "1:\n\tds_read_b32 %0, %3 offset:%4\n\ts_waitcnt lgkmcnt(0)\n\tv_readfirstlane_b32 %1, %0\n\ts_cmp_ge_u32 %1, %5\n\ts_cbranch_scc1 2f\n\ts_sleep 1\n\ts_sub_u32 %2, %2, 1\n\ts_cbranch_scc0 1b\n2:"
#define LSPIN(addr, off, want) do { u32 t_, s_, c_; asm volatile("s_mov_b32 %2, 0x800000\n1:\n\tds_read_b32 %0, %3 offset:%4\n\ts_waitcnt lgkmcnt(0)\n\tv_readfirstlane_b32 %1, %0\n\ts_cmp_ge_u32 %1, %5\n\ts_cbranch_scc1 2f\n\ts_sub_u32 %2, %2, 1\n\ts_cbranch_scc0 1b\n2:" : "=&v"(t_), "=&s"(s_), "=&s"(c_) : "v"(addr), "n"(off), "n"(want) : "memory", "scc"); } while (0)
#define LWAIT(addr, off, want) do { u32 t_, s_, c_; asm volatile("s_mov_b32 %2, 0x400000\n" LWAIT_BODY : "=&v"(t_), "=&s"(s_), "=&s"(c_) : "v"(addr), "n"(off), "n"(want) : "memory", "scc"); } while (0)
#define LWAIT_PEEK(peek, addr, off, want) do { u32 t_, s_, c_; asm volatile("s_nop 0\n\tv_readfirstlane_b32 %1, %6\n\ts_mov_b32 %2, 0x400000\n\ts_cmp_ge_u32 %1, %5\n\ts_cbranch_scc1 2f\n" LWAIT_BODY : "=&v"(t_), "=&s"(s_), "=&s"(c_) : "v"(addr), "n"(off), "n"(want), "v"(peek) : "memory", "scc"); } while (0)
#define LDS_ADD1(addr, one, off) asm volatile("s_mov_b64 exec, 1\n\tds_add_u32 %0, %1 offset:%2\n\ts_mov_b64 exec, -1" :: "v"(addr), "v"(one), "n"(off) : "memory")
#define LDS_SETB(addr, one, off) asm volatile("s_mov_b64 exec, 1\n\tds_write_b8 %0, %1 offset:%2\n\ts_mov_b64 exec, -1" :: "v"(addr), "v"(one), "n"(off) : "memory")
__device__ __forceinline__ void glds16(const void* g, u32 lds_dst, bool nt) {
    u32 keep;
    if (nt) asm volatile("s_mov_b32 %0, m0\n\ts_mov_b32 m0, %2\n\ts_nop 0\n\tglobal_load_lds_dwordx4 %1, off" WNT "\n\ts_mov_b32 m0, %0" : "=&s"(keep) : "v"(g), "s"(lds_dst) : "memory");
    else asm volatile("s_mov_b32 %0, m0\n\ts_mov_b32 m0, %2\n\ts_nop 0\n\tglobal_load_lds_dwordx4 %1, off\n\ts_mov_b32 m0, %0" : "=&s"(keep) : "v"(g), "s"(lds_dst) : "memory");
}
__device__ __forceinline__ bf16x8 band(u32 bits, u32 val) {
    v4u r;
#pragma unroll
    for (int j = 0; j < 4; ++j) r[j] = __builtin_amdgcn_perm((u32)__builtin_amdgcn_sbfe((int)bits, 2 * j + 1, 1), (u32)__builtin_amdgcn_sbfe((int)bits, 2 * j, 1), 0x05040100u) & val;
    return __builtin_bit_cast(bf16x8, r);
}

__device__ __forceinline__ u32 hline(const char* ws, u32 i) { const u32 q = i / 7u, r = i - 7u * q; return (8u * q + r + (r >= ((3u - (u32)((uintptr_t)ws >> 7)) & 7u))) * 128u; }
template <int LIST> __device__ __forceinline__ bool touch(const Params& p, int xcd, int e, int lane, u32 dst) {
    const int wa = xcd * 9 + ((3 - (int)(((uintptr_t)p.wg >> 7) & 7) - xcd * 9) & 7), two = wa == xcd * 9;
    const int na = LIST ? 0 : (PFG0 * 2) << two, nb = LIST ? 0 : (576 - PFG0) * 18, nc = LIST ? 64 * 81 : 0;
    if (!LIST && e * 64 >= na + nb) return false;
    const u32 i = min(e * 64 + lane, na + nb + nc - 1);
    const char* a;
    if (!LIST) {
        const u32 j = i - na, k = j / 9u;
        const bool first = i < (u32)na;
        const u32 seg = first ? i >> two : k + 2 * PFG0, col = first ? (wa + 8 * (i & two)) * 128u : xcd * 1152u + (j - 9u * k) * 128u;
        a = (const char*)((seg & 1) ? p.wu : p.wg) + ((seg >> 1) * (F * 2) + col);
    } else {
        const u32 k = i / 9u, rw = k / 9u;
        a = (const char*)p.wd + (((xcd * 9u + k - 9u * rw) * 64u + (rw >> 1) + 32u * (rw & 1)) * 1152u + (i - 9u * k) * 128u);
    }
    asm volatile("s_mov_b32 m0, %1\n\ts_nop 0\n\tglobal_load_lds_dword %0, off" :: "v"(a), "s"(dst) : "memory");
    return true;
}

#define KGEN_ASM ".p2align 3\n\tv_mov_b32 v64, 0x3c00\n\tv_mov_b32 v65, 0x3c00\n\tv_pk_mul_f32 v[66:67], v[56:57], v[56:57] op_sel:[1,1] op_sel_hi:[1,0] neg_lo:[0,1]\n\tv_pk_mul_f32 v[68:69], v[58:59], v[58:59] op_sel:[1,1] op_sel_hi:[1,0] neg_lo:[0,1]\n\tv_pk_mul_f32 v[70:71], v[52:53], v[52:53] op_sel:[1,1] op_sel_hi:[1,0] neg_lo:[0,1]\n\tv_pk_mul_f32 v[72:73], v[54:55], v[54:55] op_sel:[1,1] op_sel_hi:[1,0] neg_lo:[0,1]\n\tv_pk_fma_f32 v[66:67], v[56:57], v[56:57], v[66:67] op_sel:[0,0,0] op_sel_hi:[0,1,1]\n\tv_pk_fma_f32 v[68:69], v[58:59], v[58:59], v[68:69] op_sel:[0,0,0] op_sel_hi:[0,1,1]\n\tv_pk_fma_f32 v[70:71], v[52:53], v[52:53], v[70:71] op_sel:[0,0,0] op_sel_hi:[0,1,1]\n\tv_pk_fma_f32 v[72:73], v[54:55], v[54:55], v[72:73] op_sel:[0,0,0] op_sel_hi:[0,1,1]\n\tv_cvt_pk_f16_f32 v74, v56, -v57\n\tv_cvt_pk_f16_f32 v75, v58, -v59\n\tv_cvt_pk_f16_f32 v76, v48, v49\n\tv_cvt_pk_f16_f32 v77, v50, v51\n\tv_pk_mul_f32 v[78:79], v[48:49], v[60:61] op_sel:[1,1] op_sel_hi:[1,0] neg_lo:[0,1]\n\tv_pk_mul_f32 v[80:81], v[50:51], v[62:63] op_sel:[1,1] op_sel_hi:[1,0] neg_lo:[0,1]\n\tv_pk_mul_f32 v[82:83], v[48:49], v[52:53] op_sel:[1,1] op_sel_hi:[1,0] neg_lo:[0,1]\n\tv_pk_mul_f32 v[84:85], v[50:51], v[54:55] op_sel:[1,1] op_sel_hi:[1,0] neg_lo:[0,1]\n\tds_write2_b64 %[b0], v[64:65], v[74:75] offset0:0 offset1:66\n\tv_pk_fma_f32 v[78:79], v[48:49], v[60:61], v[78:79] op_sel:[0,0,0] op_sel_hi:[0,1,1]\n\tv_pk_fma_f32 v[80:81], v[50:51], v[62:63], v[80:81] op_sel:[0,0,0] op_sel_hi:[0,1,1]\n\tv_pk_fma_f32 v[82:83], v[48:49], v[52:53], v[82:83] op_sel:[0,0,0] op_sel_hi:[0,1,1]\n\tv_pk_fma_f32 v[84:85], v[50:51], v[54:55], v[84:85] op_sel:[0,0,0] op_sel_hi:[0,1,1]\n\tv_cvt_pk_f16_f32 v86, v66, -v67\n\tv_cvt_pk_f16_f32 v87, v68, -v69\n\tv_pk_mul_f32 v[88:89], v[66:67], v[66:67] op_sel:[1,1] op_sel_hi:[1,0] neg_lo:[0,1]\n\tv_pk_mul_f32 v[90:91], v[68:69], v[68:69] op_sel:[1,1] op_sel_hi:[1,0] neg_lo:[0,1]\n\tv_pk_mul_f32 v[92:93], v[66:67], v[56:57] op_sel:[1,1] op_sel_hi:[1,0] neg_lo:[0,1]\n\tv_pk_mul_f32 v[94:95], v[68:69], v[58:59] op_sel:[1,1] op_sel_hi:[1,0] neg_lo:[0,1]\n\tv_pk_fma_f32 v[88:89], v[66:67], v[66:67], v[88:89] op_sel:[0,0,0] op_sel_hi:[0,1,1]\n\tv_pk_fma_f32 v[90:91], v[68:69], v[68:69], v[90:91] op_sel:[0,0,0] op_sel_hi:[0,1,1]\n\tv_pk_fma_f32 v[92:93], v[66:67], v[56:57], v[92:93] op_sel:[0,0,0] op_sel_hi:[0,1,1]\n\tv_pk_fma_f32 v[94:95], v[68:69], v[58:59], v[94:95] op_sel:[0,0,0] op_sel_hi:[0,1,1]\n\tv_cvt_pk_f16_f32 v64, v78, v79\n\tv_cvt_pk_f16_f32 v65, v80, v81\n\tv_cvt_pk_f16_f32 v96, v82, v83\n\tv_cvt_pk_f16_f32 v97, v84, v85\n\tv_pk_mul_f32 v[98:99], v[70:71], v[70:71] op_sel:[1,1] op_sel_hi:[1,0] neg_lo:[0,1]\n\tv_pk_mul_f32 v[100:101], v[72:73], v[72:73] op_sel:[1,1] op_sel_hi:[1,0] neg_lo:[0,1]\n\tv_pk_mul_f32 v[102:103], v[78:79], v[56:57] op_sel:[1,1] op_sel_hi:[1,0] neg_lo:[0,1]\n\tv_pk_mul_f32 v[104:105], v[80:81], v[58:59] op_sel:[1,1] op_sel_hi:[1,0] neg_lo:[0,1]\n\tds_write2_b64 %[b8], v[76:77], v[96:97] offset0:0 offset1:66\n\tv_pk_fma_f32 v[98:99], v[70:71], v[70:71], v[98:99] op_sel:[0,0,0] op_sel_hi:[0,1,1]\n\tv_pk_fma_f32 v[100:101], v[72:73], v[72:73], v[100:101] op_sel:[0,0,0] op_sel_hi:[0,1,1]\n\tv_pk_fma_f32 v[102:103], v[78:79], v[56:57], v[102:103] op_sel:[0,0,0] op_sel_hi:[0,1,1]\n\tv_pk_fma_f32 v[104:105], v[80:81], v[58:59], v[104:105] op_sel:[0,0,0] op_sel_hi:[0,1,1]\n\tv_cvt_pk_f16_f32 v106, v88, -v89\n\tv_cvt_pk_f16_f32 v107, v90, -v91\n\tv_cvt_pk_f16_f32 v108, v92, -v93\n\tv_cvt_pk_f16_f32 v109, v94, -v95\n\tv_pk_mul_f32 v[110:111], v[78:79], v[70:71] op_sel:[1,1] op_sel_hi:[1,0] neg_lo:[0,1]\n\tv_pk_mul_f32 v[112:113], v[80:81], v[72:73] op_sel:[1,1] op_sel_hi:[1,0] neg_lo:[0,1]\n\tv_pk_mul_f32 v[114:115], v[48:49], v[70:71] op_sel:[1,1] op_sel_hi:[1,0] neg_lo:[0,1]\n\tv_pk_mul_f32 v[116:117], v[50:51], v[72:73] op_sel:[1,1] op_sel_hi:[1,0] neg_lo:[0,1]\n\tds_write2_b64 %[b0], v[86:87], v[108:109] offset0:132 offset1:198\n\tv_pk_fma_f32 v[110:111], v[78:79], v[70:71], v[110:111] op_sel:[0,0,0] op_sel_hi:[0,1,1]\n\tv_pk_fma_f32 v[112:113], v[80:81], v[72:73], v[112:113] op_sel:[0,0,0] op_sel_hi:[0,1,1]\n\tv_pk_fma_f32 v[114:115], v[48:49], v[70:71], v[114:115] op_sel:[0,0,0] op_sel_hi:[0,1,1]\n\tv_pk_fma_f32 v[116:117], v[50:51], v[72:73], v[116:117] op_sel:[0,0,0] op_sel_hi:[0,1,1]\n\tv_cvt_pk_f16_f32 v76, v102, v103\n\tv_cvt_pk_f16_f32 v77, v104, v105\n\tv_pk_mul_f32 v[96:97], v[82:83], v[70:71] op_sel:[1,1] op_sel_hi:[1,0] neg_lo:[0,1]\n\tv_pk_mul_f32 v[118:119], v[84:85], v[72:73] op_sel:[1,1] op_sel_hi:[1,0] neg_lo:[0,1]\n\tv_pk_mul_f32 v[120:121], v[88:89], v[88:89] op_sel:[1,1] op_sel_hi:[1,0] neg_lo:[0,1]\n\tv_pk_mul_f32 v[122:123], v[90:91], v[90:91] op_sel:[1,1] op_sel_hi:[1,0] neg_lo:[0,1]\n\tv_pk_fma_f32 v[96:97], v[82:83], v[70:71], v[96:97] op_sel:[0,0,0] op_sel_hi:[0,1,1]\n\tv_pk_fma_f32 v[118:119], v[84:85], v[72:73], v[118:119] op_sel:[0,0,0] op_sel_hi:[0,1,1]\n\tv_pk_fma_f32 v[120:121], v[88:89], v[88:89], v[120:121] op_sel:[0,0,0] op_sel_hi:[0,1,1]\n\tv_pk_fma_f32 v[122:123], v[90:91], v[90:91], v[122:123] op_sel:[0,0,0] op_sel_hi:[0,1,1]\n\tv_cvt_pk_f16_f32 v124, v110, v111\n\tv_cvt_pk_f16_f32 v125, v112, v113\n\tv_cvt_pk_f16_f32 v126, v114, v115\n\tv_cvt_pk_f16_f32 v127, v116, v117\n\tv_pk_mul_f32 v[70:71], v[88:89], v[56:57] op_sel:[1,1] op_sel_hi:[1,0] neg_lo:[0,1]\n\tv_pk_mul_f32 v[72:73], v[90:91], v[58:59] op_sel:[1,1] op_sel_hi:[1,0] neg_lo:[0,1]\n\tv_pk_mul_f32 v[128:129], v[102:103], v[60:61] op_sel:[1,1] op_sel_hi:[1,0] neg_lo:[0,1]\n\tv_pk_mul_f32 v[130:131], v[104:105], v[62:63] op_sel:[1,1] op_sel_hi:[1,0] neg_lo:[0,1]\n\tds_write2_b64 %[b11], v[64:65], v[124:125] offset0:0 offset1:66\n\tv_pk_fma_f32 v[70:71], v[88:89], v[56:57], v[70:71] op_sel:[0,0,0] op_sel_hi:[0,1,1]\n\tv_pk_fma_f32 v[72:73], v[90:91], v[58:59], v[72:73] op_sel:[0,0,0] op_sel_hi:[0,1,1]\n\tv_pk_fma_f32 v[128:129], v[102:103], v[60:61], v[128:129] op_sel:[0,0,0] op_sel_hi:[0,1,1]\n\tv_pk_fma_f32 v[130:131], v[104:105], v[62:63], v[130:131] op_sel:[0,0,0] op_sel_hi:[0,1,1]\n\tv_cvt_pk_f16_f32 v132, v96, v97\n\tv_cvt_pk_f16_f32 v133, v118, v119\n\tv_cvt_pk_f16_f32 v134, v120, -v121\n\tv_cvt_pk_f16_f32 v135, v122, -v123\n\tv_pk_mul_f32 v[102:103], v[78:79], v[98:99] op_sel:[1,1] op_sel_hi:[1,0] neg_lo:[0,1]\n\tv_pk_mul_f32 v[104:105], v[80:81], v[100:101] op_sel:[1,1] op_sel_hi:[1,0] neg_lo:[0,1]\n\tv_pk_mul_f32 v[136:137], v[88:89], v[66:67] op_sel:[1,1] op_sel_hi:[1,0] neg_lo:[0,1]\n\tv_pk_mul_f32 v[138:139], v[90:91], v[68:69] op_sel:[1,1] op_sel_hi:[1,0] neg_lo:[0,1]\n\tds_write2_b64 %[b8], v[126:127], v[132:133] offset0:132 offset1:198\n\tv_pk_fma_f32 v[102:103], v[78:79], v[98:99], v[102:103] op_sel:[0,0,0] op_sel_hi:[0,1,1]\n\tv_pk_fma_f32 v[104:105], v[80:81], v[100:101], v[104:105] op_sel:[0,0,0] op_sel_hi:[0,1,1]\n\tv_pk_fma_f32 v[136:137], v[88:89], v[66:67], v[136:137] op_sel:[0,0,0] op_sel_hi:[0,1,1]\n\tv_pk_fma_f32 v[138:139], v[90:91], v[68:69], v[138:139] op_sel:[0,0,0] op_sel_hi:[0,1,1]\n\tv_cvt_pk_f16_f32 v64, v70, -v71\n\tv_cvt_pk_f16_f32 v65, v72, -v73\n\tv_cvt_pk_f16_f32 v124, v128, v129\n\tv_cvt_pk_f16_f32 v125, v130, v131\n\tv_pk_mul_f32 v[78:79], v[88:89], v[92:93] op_sel:[1,1] op_sel_hi:[1,0] neg_lo:[0,1]\n\tv_pk_mul_f32 v[80:81], v[90:91], v[94:95] op_sel:[1,1] op_sel_hi:[1,0] neg_lo:[0,1]\n\tv_pk_mul_f32 v[140:141], v[120:121], v[120:121] op_sel:[1,1] op_sel_hi:[1,0] neg_lo:[0,1]\n\tv_pk_mul_f32 v[142:143], v[122:123], v[122:123] op_sel:[1,1] op_sel_hi:[1,0] neg_lo:[0,1]\n\tds_write2_b64 %[b1], v[106:107], v[64:65] offset0:0 offset1:66\n\tv_pk_fma_f32 v[78:79], v[88:89], v[92:93], v[78:79] op_sel:[0,0,0] op_sel_hi:[0,1,1]\n\tv_pk_fma_f32 v[80:81], v[90:91], v[94:95], v[80:81] op_sel:[0,0,0] op_sel_hi:[0,1,1]\n\tv_pk_fma_f32 v[140:141], v[120:121], v[120:121], v[140:141] op_sel:[0,0,0] op_sel_hi:[0,1,1]\n\tv_pk_fma_f32 v[142:143], v[122:123], v[122:123], v[142:143] op_sel:[0,0,0] op_sel_hi:[0,1,1]\n\tv_cvt_pk_f16_f32 v126, v102, v103\n\tv_cvt_pk_f16_f32 v127, v104, v105\n\tv_cvt_pk_f16_f32 v132, v136, -v137\n\tv_cvt_pk_f16_f32 v133, v138, -v139\n\tv_pk_mul_f32 v[144:145], v[120:121], v[56:57] op_sel:[1,1] op_sel_hi:[1,0] neg_lo:[0,1]\n\tv_pk_mul_f32 v[146:147], v[122:123], v[58:59] op_sel:[1,1] op_sel_hi:[1,0] neg_lo:[0,1]\n\tv_pk_mul_f32 v[148:149], v[128:129], v[60:61] op_sel:[1,1] op_sel_hi:[1,0] neg_lo:[0,1]\n\tv_pk_mul_f32 v[150:151], v[130:131], v[62:63] op_sel:[1,1] op_sel_hi:[1,0] neg_lo:[0,1]\n\tds_write2_b64 %[b9], v[76:77], v[124:125] offset0:0 offset1:66\n\tv_pk_fma_f32 v[144:145], v[120:121], v[56:57], v[144:145] op_sel:[0,0,0] op_sel_hi:[0,1,1]\n\tv_pk_fma_f32 v[146:147], v[122:123], v[58:59], v[146:147] op_sel:[0,0,0] op_sel_hi:[0,1,1]\n\tv_pk_fma_f32 v[148:149], v[128:129], v[60:61], v[148:149] op_sel:[0,0,0] op_sel_hi:[0,1,1]\n\tv_pk_fma_f32 v[150:151], v[130:131], v[62:63], v[150:151] op_sel:[0,0,0] op_sel_hi:[0,1,1]\n\tv_cvt_pk_f16_f32 v152, v78, -v79\n\tv_cvt_pk_f16_f32 v153, v80, -v81\n\tv_cvt_pk_f16_f32 v154, v140, -v141\n\tv_cvt_pk_f16_f32 v155, v142, -v143\n\tv_pk_mul_f32 v[128:129], v[102:103], v[60:61] op_sel:[1,1] op_sel_hi:[1,0] neg_lo:[0,1]\n\tv_pk_mul_f32 v[130:131], v[104:105], v[62:63] op_sel:[1,1] op_sel_hi:[1,0] neg_lo:[0,1]\n\tv_pk_mul_f32 v[140:141], v[110:111], v[98:99] op_sel:[1,1] op_sel_hi:[1,0] neg_lo:[0,1]\n\tv_pk_mul_f32 v[142:143], v[112:113], v[100:101] op_sel:[1,1] op_sel_hi:[1,0] neg_lo:[0,1]\n\tds_write2_b64 %[b1], v[132:133], v[152:153] offset0:132 offset1:198\n\tv_pk_fma_f32 v[128:129], v[102:103], v[60:61], v[128:129] op_sel:[0,0,0] op_sel_hi:[0,1,1]\n\tv_pk_fma_f32 v[130:131], v[104:105], v[62:63], v[130:131] op_sel:[0,0,0] op_sel_hi:[0,1,1]\n\tv_pk_fma_f32 v[140:141], v[110:111], v[98:99], v[140:141] op_sel:[0,0,0] op_sel_hi:[0,1,1]\n\tv_pk_fma_f32 v[142:143], v[112:113], v[100:101], v[142:143] op_sel:[0,0,0] op_sel_hi:[0,1,1]\n\tv_cvt_pk_f16_f32 v76, v144, -v145\n\tv_cvt_pk_f16_f32 v77, v146, -v147\n\tv_cvt_pk_f16_f32 v124, v148, v149\n\tv_cvt_pk_f16_f32 v125, v150, v151\n\tv_pk_mul_f32 v[102:103], v[120:121], v[66:67] op_sel:[1,1] op_sel_hi:[1,0] neg_lo:[0,1]\n\tv_pk_mul_f32 v[104:105], v[122:123], v[68:69] op_sel:[1,1] op_sel_hi:[1,0] neg_lo:[0,1]\n\tv_pk_mul_f32 v[110:111], v[120:121], v[92:93] op_sel:[1,1] op_sel_hi:[1,0] neg_lo:[0,1]\n\tv_pk_mul_f32 v[112:113], v[122:123], v[94:95] op_sel:[1,1] op_sel_hi:[1,0] neg_lo:[0,1]\n\tds_write2_b64 %[b2], v[134:135], v[76:77] offset0:0 offset1:66\n\tv_pk_fma_f32 v[102:103], v[120:121], v[66:67], v[102:103] op_sel:[0,0,0] op_sel_hi:[0,1,1]\n\tv_pk_fma_f32 v[104:105], v[122:123], v[68:69], v[104:105] op_sel:[0,0,0] op_sel_hi:[0,1,1]\n\tv_pk_fma_f32 v[110:111], v[120:121], v[92:93], v[110:111] op_sel:[0,0,0] op_sel_hi:[0,1,1]\n\tv_pk_fma_f32 v[112:113], v[122:123], v[94:95], v[112:113] op_sel:[0,0,0] op_sel_hi:[0,1,1]\n\tv_cvt_pk_f16_f32 v144, v128, v129\n\tv_cvt_pk_f16_f32 v145, v130, v131\n\tv_cvt_pk_f16_f32 v146, v140, v141\n\tv_cvt_pk_f16_f32 v147, v142, v143\n\tv_pk_mul_f32 v[66:67], v[148:149], v[60:61] op_sel:[1,1] op_sel_hi:[1,0] neg_lo:[0,1]\n\tv_pk_mul_f32 v[68:69], v[150:151], v[62:63] op_sel:[1,1] op_sel_hi:[1,0] neg_lo:[0,1]\n\tv_pk_mul_f32 v[92:93], v[128:129], v[60:61] op_sel:[1,1] op_sel_hi:[1,0] neg_lo:[0,1]\n\tv_pk_mul_f32 v[94:95], v[130:131], v[62:63] op_sel:[1,1] op_sel_hi:[1,0] neg_lo:[0,1]\n\tds_write2_b64 %[b9], v[124:125], v[146:147] offset0:132 offset1:198\n\tv_pk_fma_f32 v[66:67], v[148:149], v[60:61], v[66:67] op_sel:[0,0,0] op_sel_hi:[0,1,1]\n\tv_pk_fma_f32 v[68:69], v[150:151], v[62:63], v[68:69] op_sel:[0,0,0] op_sel_hi:[0,1,1]\n\tv_pk_fma_f32 v[92:93], v[128:129], v[60:61], v[92:93] op_sel:[0,0,0] op_sel_hi:[0,1,1]\n\tv_pk_fma_f32 v[94:95], v[130:131], v[62:63], v[94:95] op_sel:[0,0,0] op_sel_hi:[0,1,1]\n\tv_cvt_pk_f16_f32 v140, v102, -v103\n\tv_cvt_pk_f16_f32 v141, v104, -v105\n\tv_cvt_pk_f16_f32 v142, v110, -v111\n\tv_cvt_pk_f16_f32 v143, v112, -v113\n\tv_pk_mul_f32 v[102:103], v[120:121], v[88:89] op_sel:[1,1] op_sel_hi:[1,0] neg_lo:[0,1]\n\tv_pk_mul_f32 v[104:105], v[122:123], v[90:91] op_sel:[1,1] op_sel_hi:[1,0] neg_lo:[0,1]\n\tv_pk_mul_f32 v[110:111], v[120:121], v[70:71] op_sel:[1,1] op_sel_hi:[1,0] neg_lo:[0,1]\n\tv_pk_mul_f32 v[112:113], v[122:123], v[72:73] op_sel:[1,1] op_sel_hi:[1,0] neg_lo:[0,1]\n\tds_write2_b64 %[b2], v[140:141], v[142:143] offset0:132 offset1:198\n\tv_pk_fma_f32 v[102:103], v[120:121], v[88:89], v[102:103] op_sel:[0,0,0] op_sel_hi:[0,1,1]\n\tv_pk_fma_f32 v[104:105], v[122:123], v[90:91], v[104:105] op_sel:[0,0,0] op_sel_hi:[0,1,1]\n\tv_pk_fma_f32 v[110:111], v[120:121], v[70:71], v[110:111] op_sel:[0,0,0] op_sel_hi:[0,1,1]\n\tv_pk_fma_f32 v[112:113], v[122:123], v[72:73], v[112:113] op_sel:[0,0,0] op_sel_hi:[0,1,1]\n\tv_cvt_pk_f16_f32 v124, v66, v67\n\tv_cvt_pk_f16_f32 v125, v68, v69\n\tv_cvt_pk_f16_f32 v128, v92, v93\n\tv_cvt_pk_f16_f32 v129, v94, v95\n\tv_pk_mul_f32 v[70:71], v[48:49], v[98:99] op_sel:[1,1] op_sel_hi:[1,0] neg_lo:[0,1]\n\tv_pk_mul_f32 v[72:73], v[50:51], v[100:101] op_sel:[1,1] op_sel_hi:[1,0] neg_lo:[0,1]\n\tv_pk_mul_f32 v[88:89], v[82:83], v[98:99] op_sel:[1,1] op_sel_hi:[1,0] neg_lo:[0,1]\n\tv_pk_mul_f32 v[90:91], v[84:85], v[100:101] op_sel:[1,1] op_sel_hi:[1,0] neg_lo:[0,1]\n\tds_write2_b64 %[b10], v[126:127], v[144:145] offset0:132 offset1:198\n\tv_pk_fma_f32 v[70:71], v[48:49], v[98:99], v[70:71] op_sel:[0,0,0] op_sel_hi:[0,1,1]\n\tv_pk_fma_f32 v[72:73], v[50:51], v[100:101], v[72:73] op_sel:[0,0,0] op_sel_hi:[0,1,1]\n\tv_pk_fma_f32 v[88:89], v[82:83], v[98:99], v[88:89] op_sel:[0,0,0] op_sel_hi:[0,1,1]\n\tv_pk_fma_f32 v[90:91], v[84:85], v[100:101], v[90:91] op_sel:[0,0,0] op_sel_hi:[0,1,1]\n\tv_cvt_pk_f16_f32 v92, v102, -v103\n\tv_cvt_pk_f16_f32 v93, v104, -v105\n\tv_cvt_pk_f16_f32 v94, v110, -v111\n\tv_cvt_pk_f16_f32 v95, v112, -v113\n\tv_pk_mul_f32 v[82:83], v[66:67], v[60:61] op_sel:[1,1] op_sel_hi:[1,0] neg_lo:[0,1]\n\tv_pk_mul_f32 v[84:85], v[68:69], v[62:63] op_sel:[1,1] op_sel_hi:[1,0] neg_lo:[0,1]\n\tv_pk_mul_f32 v[102:103], v[120:121], v[136:137] op_sel:[1,1] op_sel_hi:[1,0] neg_lo:[0,1]\n\tv_pk_mul_f32 v[104:105], v[122:123], v[138:139] op_sel:[1,1] op_sel_hi:[1,0] neg_lo:[0,1]\n\tds_write2_b64 %[b3], v[92:93], v[94:95] offset0:0 offset1:66\n\tv_pk_fma_f32 v[82:83], v[66:67], v[60:61], v[82:83] op_sel:[0,0,0] op_sel_hi:[0,1,1]\n\tv_pk_fma_f32 v[84:85], v[68:69], v[62:63], v[84:85] op_sel:[0,0,0] op_sel_hi:[0,1,1]\n\tv_pk_fma_f32 v[102:103], v[120:121], v[136:137], v[102:103] op_sel:[0,0,0] op_sel_hi:[0,1,1]\n\tv_pk_fma_f32 v[104:105], v[122:123], v[138:139], v[104:105] op_sel:[0,0,0] op_sel_hi:[0,1,1]\n\tv_cvt_pk_f16_f32 v110, v70, v71\n\tv_cvt_pk_f16_f32 v111, v72, v73\n\tv_cvt_pk_f16_f32 v112, v88, v89\n\tv_cvt_pk_f16_f32 v113, v90, v91\n\tv_pk_mul_f32 v[66:67], v[120:121], v[78:79] op_sel:[1,1] op_sel_hi:[1,0] neg_lo:[0,1]\n\tv_pk_mul_f32 v[68:69], v[122:123], v[80:81] op_sel:[1,1] op_sel_hi:[1,0] neg_lo:[0,1]\n\tv_pk_mul_f32 v[70:71], v[114:115], v[98:99] op_sel:[1,1] op_sel_hi:[1,0] neg_lo:[0,1]\n\tv_pk_mul_f32 v[72:73], v[116:117], v[100:101] op_sel:[1,1] op_sel_hi:[1,0] neg_lo:[0,1]\n\tds_write2_b64 %[b11], v[110:111], v[112:113] offset0:132 offset1:198\n\tv_pk_fma_f32 v[66:67], v[120:121], v[78:79], v[66:67] op_sel:[0,0,0] op_sel_hi:[0,1,1]\n\tv_pk_fma_f32 v[68:69], v[122:123], v[80:81], v[68:69] op_sel:[0,0,0] op_sel_hi:[0,1,1]\n\tv_pk_fma_f32 v[70:71], v[114:115], v[98:99], v[70:71] op_sel:[0,0,0] op_sel_hi:[0,1,1]\n\tv_pk_fma_f32 v[72:73], v[116:117], v[100:101], v[72:73] op_sel:[0,0,0] op_sel_hi:[0,1,1]\n\tv_cvt_pk_f16_f32 v88, v82, v83\n\tv_cvt_pk_f16_f32 v89, v84, v85\n\tv_cvt_pk_f16_f32 v90, v102, -v103\n\tv_cvt_pk_f16_f32 v91, v104, -v105\n\tv_pk_mul_f32 v[78:79], v[96:97], v[98:99] op_sel:[1,1] op_sel_hi:[1,0] neg_lo:[0,1]\n\tv_pk_mul_f32 v[80:81], v[118:119], v[100:101] op_sel:[1,1] op_sel_hi:[1,0] neg_lo:[0,1]\n\tv_pk_mul_f32 v[102:103], v[82:83], v[60:61] op_sel:[1,1] op_sel_hi:[1,0] neg_lo:[0,1]\n\tv_pk_mul_f32 v[104:105], v[84:85], v[62:63] op_sel:[1,1] op_sel_hi:[1,0] neg_lo:[0,1]\n\tds_write2_b64 %[b12], v[128:129], v[124:125] offset0:132 offset1:198\n\tv_pk_fma_f32 v[78:79], v[96:97], v[98:99], v[78:79] op_sel:[0,0,0] op_sel_hi:[0,1,1]\n\tv_pk_fma_f32 v[80:81], v[118:119], v[100:101], v[80:81] op_sel:[0,0,0] op_sel_hi:[0,1,1]\n\tv_pk_fma_f32 v[102:103], v[82:83], v[60:61], v[102:103] op_sel:[0,0,0] op_sel_hi:[0,1,1]\n\tv_pk_fma_f32 v[104:105], v[84:85], v[62:63], v[104:105] op_sel:[0,0,0] op_sel_hi:[0,1,1]\n\tv_cvt_pk_f16_f32 v110, v66, -v67\n\tv_cvt_pk_f16_f32 v111, v68, -v69\n\tv_cvt_pk_f16_f32 v112, v70, v71\n\tv_cvt_pk_f16_f32 v113, v72, v73\n\tv_pk_mul_f16 v66, v154, v74 op_sel:[1,1] op_sel_hi:[1,0] neg_lo:[0,1]\n\tv_pk_mul_f16 v67, v155, v75 op_sel:[1,1] op_sel_hi:[1,0] neg_lo:[0,1]\n\tv_pk_mul_f16 v68, v154, v86 op_sel:[1,1] op_sel_hi:[1,0] neg_lo:[0,1]\n\tv_pk_mul_f16 v69, v155, v87 op_sel:[1,1] op_sel_hi:[1,0] neg_lo:[0,1]\n\tds_write2_b64 %[b3], v[90:91], v[110:111] offset0:132 offset1:198\n\tv_pk_fma_f16 v66, v154, v74, v66 op_sel:[0,0,0] op_sel_hi:[0,1,1]\n\tv_pk_fma_f16 v67, v155, v75, v67 op_sel:[0,0,0] op_sel_hi:[0,1,1]\n\tv_pk_fma_f16 v68, v154, v86, v68 op_sel:[0,0,0] op_sel_hi:[0,1,1]\n\tv_pk_fma_f16 v69, v155, v87, v69 op_sel:[0,0,0] op_sel_hi:[0,1,1]\n\tv_cvt_pk_f16_f32 v70, v78, v79\n\tv_cvt_pk_f16_f32 v71, v80, v81\n\tv_cvt_pk_f16_f32 v72, v102, v103\n\tv_cvt_pk_f16_f32 v73, v104, v105\n\tv_pk_mul_f16 v78, v154, v108 op_sel:[1,1] op_sel_hi:[1,0] neg_lo:[0,1]\n\tv_pk_mul_f16 v79, v155, v109 op_sel:[1,1] op_sel_hi:[1,0] neg_lo:[0,1]\n\tv_pk_mul_f32 v[80:81], v[102:103], v[60:61] op_sel:[1,1] op_sel_hi:[1,0] neg_lo:[0,1]\n\tv_pk_mul_f32 v[82:83], v[104:105], v[62:63] op_sel:[1,1] op_sel_hi:[1,0] neg_lo:[0,1]\n\tds_write2_b64 %[b4], v[154:155], v[66:67] offset0:0 offset1:66\n\tv_pk_fma_f16 v78, v154, v108, v78 op_sel:[0,0,0] op_sel_hi:[0,1,1]\n\tv_pk_fma_f16 v79, v155, v109, v79 op_sel:[0,0,0] op_sel_hi:[0,1,1]\n\tv_pk_fma_f32 v[80:81], v[102:103], v[60:61], v[80:81] op_sel:[0,0,0] op_sel_hi:[0,1,1]\n\tv_pk_fma_f32 v[82:83], v[104:105], v[62:63], v[82:83] op_sel:[0,0,0] op_sel_hi:[0,1,1]\n\tv_pk_mul_f16 v74, v154, v106 op_sel:[1,1] op_sel_hi:[1,0] neg_lo:[0,1]\n\tv_pk_mul_f16 v75, v155, v107 op_sel:[1,1] op_sel_hi:[1,0] neg_lo:[0,1]\n\tv_pk_mul_f16 v84, v154, v64 op_sel:[1,1] op_sel_hi:[1,0] neg_lo:[0,1]\n\tv_pk_mul_f16 v85, v155, v65 op_sel:[1,1] op_sel_hi:[1,0] neg_lo:[0,1]\n\tds_write2_b64 %[b4], v[68:69], v[78:79] offset0:132 offset1:198\n\tv_pk_fma_f16 v74, v154, v106, v74 op_sel:[0,0,0] op_sel_hi:[0,1,1]\n\tv_pk_fma_f16 v75, v155, v107, v75 op_sel:[0,0,0] op_sel_hi:[0,1,1]\n\tv_pk_fma_f16 v84, v154, v64, v84 op_sel:[0,0,0] op_sel_hi:[0,1,1]\n\tv_pk_fma_f16 v85, v155, v65, v85 op_sel:[0,0,0] op_sel_hi:[0,1,1]\n\tv_cvt_pk_f16_f32 v66, v80, v81\n\tv_cvt_pk_f16_f32 v67, v82, v83\n\tv_pk_mul_f16 v80, v154, v132 op_sel:[1,1] op_sel_hi:[1,0] neg_lo:[0,1]\n\tv_pk_mul_f16 v81, v155, v133 op_sel:[1,1] op_sel_hi:[1,0] neg_lo:[0,1]\n\tv_pk_mul_f16 v82, v154, v152 op_sel:[1,1] op_sel_hi:[1,0] neg_lo:[0,1]\n\tv_pk_mul_f16 v83, v155, v153 op_sel:[1,1] op_sel_hi:[1,0] neg_lo:[0,1]\n\tds_write2_b64 %[b5], v[74:75], v[84:85] offset0:0 offset1:66\n\tv_pk_fma_f16 v80, v154, v132, v80 op_sel:[0,0,0] op_sel_hi:[0,1,1]\n\tv_pk_fma_f16 v81, v155, v133, v81 op_sel:[0,0,0] op_sel_hi:[0,1,1]\n\tv_pk_fma_f16 v82, v154, v152, v82 op_sel:[0,0,0] op_sel_hi:[0,1,1]\n\tv_pk_fma_f16 v83, v155, v153, v83 op_sel:[0,0,0] op_sel_hi:[0,1,1]\n\tv_pk_mul_f16 v64, v154, v134 op_sel:[1,1] op_sel_hi:[1,0] neg_lo:[0,1]\n\tv_pk_mul_f16 v65, v155, v135 op_sel:[1,1] op_sel_hi:[1,0] neg_lo:[0,1]\n\tv_pk_mul_f16 v68, v154, v76 op_sel:[1,1] op_sel_hi:[1,0] neg_lo:[0,1]\n\tv_pk_mul_f16 v69, v155, v77 op_sel:[1,1] op_sel_hi:[1,0] neg_lo:[0,1]\n\tds_write2_b64 %[b5], v[80:81], v[82:83] offset0:132 offset1:198\n\tv_pk_fma_f16 v64, v154, v134, v64 op_sel:[0,0,0] op_sel_hi:[0,1,1]\n\tv_pk_fma_f16 v65, v155, v135, v65 op_sel:[0,0,0] op_sel_hi:[0,1,1]\n\tv_pk_fma_f16 v68, v154, v76, v68 op_sel:[0,0,0] op_sel_hi:[0,1,1]\n\tv_pk_fma_f16 v69, v155, v77, v69 op_sel:[0,0,0] op_sel_hi:[0,1,1]\n\tv_pk_mul_f16 v74, v154, v140 op_sel:[1,1] op_sel_hi:[1,0] neg_lo:[0,1]\n\tv_pk_mul_f16 v75, v155, v141 op_sel:[1,1] op_sel_hi:[1,0] neg_lo:[0,1]\n\tv_pk_mul_f16 v78, v154, v142 op_sel:[1,1] op_sel_hi:[1,0] neg_lo:[0,1]\n\tv_pk_mul_f16 v79, v155, v143 op_sel:[1,1] op_sel_hi:[1,0] neg_lo:[0,1]\n\tds_write2_b64 %[b6], v[64:65], v[68:69] offset0:0 offset1:66\n\tv_pk_fma_f16 v74, v154, v140, v74 op_sel:[0,0,0] op_sel_hi:[0,1,1]\n\tv_pk_fma_f16 v75, v155, v141, v75 op_sel:[0,0,0] op_sel_hi:[0,1,1]\n\tv_pk_fma_f16 v78, v154, v142, v78 op_sel:[0,0,0] op_sel_hi:[0,1,1]\n\tv_pk_fma_f16 v79, v155, v143, v79 op_sel:[0,0,0] op_sel_hi:[0,1,1]\n\tv_pk_mul_f16 v76, v154, v92 op_sel:[1,1] op_sel_hi:[1,0] neg_lo:[0,1]\n\tv_pk_mul_f16 v77, v155, v93 op_sel:[1,1] op_sel_hi:[1,0] neg_lo:[0,1]\n\tv_pk_mul_f16 v80, v154, v94 op_sel:[1,1] op_sel_hi:[1,0] neg_lo:[0,1]\n\tv_pk_mul_f16 v81, v155, v95 op_sel:[1,1] op_sel_hi:[1,0] neg_lo:[0,1]\n\tds_write2_b64 %[b6], v[74:75], v[78:79] offset0:132 offset1:198\n\tv_pk_fma_f16 v76, v154, v92, v76 op_sel:[0,0,0] op_sel_hi:[0,1,1]\n\tv_pk_fma_f16 v77, v155, v93, v77 op_sel:[0,0,0] op_sel_hi:[0,1,1]\n\tv_pk_fma_f16 v80, v154, v94, v80 op_sel:[0,0,0] op_sel_hi:[0,1,1]\n\tv_pk_fma_f16 v81, v155, v95, v81 op_sel:[0,0,0] op_sel_hi:[0,1,1]\n\tv_pk_mul_f16 v64, v154, v90 op_sel:[1,1] op_sel_hi:[1,0] neg_lo:[0,1]\n\tv_pk_mul_f16 v65, v155, v91 op_sel:[1,1] op_sel_hi:[1,0] neg_lo:[0,1]\n\tv_pk_mul_f16 v68, v154, v110 op_sel:[1,1] op_sel_hi:[1,0] neg_lo:[0,1]\n\tv_pk_mul_f16 v69, v155, v111 op_sel:[1,1] op_sel_hi:[1,0] neg_lo:[0,1]\n\tds_write2_b64 %[b7], v[76:77], v[80:81] offset0:0 offset1:66\n\tv_pk_fma_f16 v64, v154, v90, v64 op_sel:[0,0,0] op_sel_hi:[0,1,1]\n\tv_pk_fma_f16 v65, v155, v91, v65 op_sel:[0,0,0] op_sel_hi:[0,1,1]\n\tv_pk_fma_f16 v68, v154, v110, v68 op_sel:[0,0,0] op_sel_hi:[0,1,1]\n\tv_pk_fma_f16 v69, v155, v111, v69 op_sel:[0,0,0] op_sel_hi:[0,1,1]\n\ts_nop 0\n\tds_write2_b64 %[b7], v[64:65], v[68:69] offset0:132 offset1:198\n\tds_write2_b64 %[b10], v[88:89], v[66:67] offset0:0 offset1:66\n\tds_write2_b64 %[b12], v[112:113], v[70:71] offset0:0 offset1:66\n\tds_write_b64 %[b13], v[72:73] offset:0"
#define KGEN_CLOBBER "v64", "v65", "v66", "v67", "v68", "v69", "v70", "v71", "v72", "v73", "v74", "v75", "v76", "v77", "v78", "v79", "v80", "v81", "v82", "v83", "v84", "v85", "v86", "v87", "v88", "v89", "v90", "v91", "v92", "v93", "v94", "v95", "v96", "v97", "v98", "v99", "v100", "v101", "v102", "v103", "v104", "v105", "v106", "v107", "v108", "v109", "v110", "v111", "v112", "v113", "v114", "v115", "v116", "v117", "v118", "v119", "v120", "v121", "v122", "v123", "v124", "v125", "v126", "v127", "v128", "v129", "v130", "v131", "v132", "v133", "v134", "v135", "v136", "v137", "v138", "v139", "v140", "v141", "v142", "v143", "v144", "v145", "v146", "v147", "v148", "v149", "v150", "v151", "v152", "v153", "v154", "v155"
#define KROW_0LO 0x2b2a2d2c23222120ull
#define KROW_0HI 0x202020202b343332ull
#define KROW_1LO 0x2524272a31302f2eull
#define KROW_1HI 0x2b2b2b2b20292826ull
#define KMFMA_ASM "ds_read_b128 v[64:67], %[ap] offset:0\n\tds_read_b128 v[68:71], %[bp] offset:0\n\tds_read_b128 v[72:75], %[ap] offset:32\n\tds_read_b128 v[76:79], %[bp] offset:32\n\tds_read_b128 v[80:83], %[ap] offset:64\n\tds_read_b128 v[84:87], %[bp] offset:64\n\tds_read_b128 v[88:91], %[ap] offset:96\n\tds_read_b128 v[92:95], %[bp] offset:96\n\ts_waitcnt lgkmcnt(6)\n\tv_mfma_f32_32x32x16_f16 v[48:63], v[64:67], v[68:71], 0\n\tds_read_b128 v[96:99], %[ap] offset:128\n\tds_read_b128 v[100:103], %[bp] offset:128\n\ts_waitcnt lgkmcnt(6)\n\tv_mfma_f32_32x32x16_f16 v[48:63], v[72:75], v[76:79], v[48:63]\n\tds_read_b128 v[104:107], %[ap] offset:160\n\tds_read_b128 v[108:111], %[bp] offset:160\n\ts_waitcnt lgkmcnt(6)\n\tv_mfma_f32_32x32x16_f16 v[48:63], v[80:83], v[84:87], v[48:63]\n\tds_read_b128 v[64:67], %[ap] offset:192\n\tds_read_b128 v[68:71], %[bp] offset:192\n\ts_waitcnt lgkmcnt(6)\n\tv_mfma_f32_32x32x16_f16 v[48:63], v[88:91], v[92:95], v[48:63]\n\tds_read_b128 v[72:75], %[ap] offset:224\n\tds_read_b128 v[76:79], %[bp] offset:224\n\ts_waitcnt lgkmcnt(6)\n\tv_mfma_f32_32x32x16_f16 v[48:63], v[96:99], v[100:103], v[48:63]\n\tds_read_b128 v[80:83], %[ap] offset:256\n\tds_read_b128 v[84:87], %[bp] offset:256\n\ts_waitcnt lgkmcnt(6)\n\tv_mfma_f32_32x32x16_f16 v[48:63], v[104:107], v[108:111], v[48:63]\n\tds_read_b128 v[88:91], %[ap] offset:288\n\tds_read_b128 v[92:95], %[bp] offset:288\n\ts_waitcnt lgkmcnt(6)\n\tv_mfma_f32_32x32x16_f16 v[48:63], v[64:67], v[68:71], v[48:63]\n\tds_read_b128 v[96:99], %[ap] offset:320\n\tds_read_b128 v[100:103], %[bp] offset:320\n\ts_waitcnt lgkmcnt(6)\n\tv_mfma_f32_32x32x16_f16 v[48:63], v[72:75], v[76:79], v[48:63]\n\tds_read_b128 v[104:107], %[ap] offset:352\n\tds_read_b128 v[108:111], %[bp] offset:352\n\ts_waitcnt lgkmcnt(6)\n\tv_mfma_f32_32x32x16_f16 v[48:63], v[80:83], v[84:87], v[48:63]\n\tds_read_b128 v[64:67], %[ap] offset:384\n\tds_read_b128 v[68:71], %[bp] offset:384\n\ts_waitcnt lgkmcnt(6)\n\tv_mfma_f32_32x32x16_f16 v[48:63], v[88:91], v[92:95], v[48:63]\n\tds_read_b128 v[72:75], %[ap] offset:416\n\tds_read_b128 v[76:79], %[bp] offset:416\n\ts_waitcnt lgkmcnt(6)\n\tv_mfma_f32_32x32x16_f16 v[48:63], v[96:99], v[100:103], v[48:63]\n\tds_read_b128 v[80:83], %[ap] offset:448\n\tds_read_b128 v[84:87], %[bp] offset:448\n\ts_waitcnt lgkmcnt(6)\n\tv_mfma_f32_32x32x16_f16 v[48:63], v[104:107], v[108:111], v[48:63]\n\tds_read_b128 v[88:91], %[ap] offset:480\n\tds_read_b128 v[92:95], %[bp] offset:480\n\ts_waitcnt lgkmcnt(6)\n\tv_mfma_f32_32x32x16_f16 v[48:63], v[64:67], v[68:71], v[48:63]\n\tds_read_b32 %[stg], %[ctl] offset:4\n\ts_waitcnt lgkmcnt(4)\n\tv_mfma_f32_32x32x16_f16 v[48:63], v[72:75], v[76:79], v[48:63]\n\ts_waitcnt lgkmcnt(2)\n\tv_mfma_f32_32x32x16_f16 v[48:63], v[80:83], v[84:87], v[48:63]\n\ts_waitcnt lgkmcnt(0)\n\tv_mfma_f32_32x32x16_f16 v[48:63], v[88:91], v[92:95], v[48:63]\n\ts_nop 7\n\ts_nop 3\n\ts_waitcnt lgkmcnt(0)"
#define KMFMA_CLOBBER "v64", "v65", "v66", "v67", "v68", "v69", "v70", "v71", "v72", "v73", "v74", "v75", "v76", "v77", "v78", "v79", "v80", "v81", "v82", "v83", "v84", "v85", "v86", "v87", "v88", "v89", "v90", "v91", "v92", "v93", "v94", "v95", "v96", "v97", "v98", "v99", "v100", "v101", "v102", "v103", "v104", "v105", "v106", "v107", "v108", "v109", "v110", "v111"
#define CONV_ASM ".p2align 3\n\tds_read_b64_tr_b16 v[52:53], %[a0] offset:456\n\tds_read_b64_tr_b16 v[54:55], %[a0] offset:448\n\tds_read_b64_tr_b16 v[84:85], %[a0] offset:968\n\tds_read_b64_tr_b16 v[86:87], %[a0] offset:960\n\tds_read_b64_tr_b16 v[48:49], %[a0] offset:392\n\tds_read_b64_tr_b16 v[50:51], %[a0] offset:384\n\tds_read_b64_tr_b16 v[80:81], %[a0] offset:904\n\tds_read_b64_tr_b16 v[82:83], %[a0] offset:896\n\tds_read_b64_tr_b16 v[44:45], %[a0] offset:328\n\tds_read_b64_tr_b16 v[46:47], %[a0] offset:320\n\tds_read_b64_tr_b16 v[76:77], %[a0] offset:840\n\tds_read_b64_tr_b16 v[78:79], %[a0] offset:832\n\tv_readfirstlane_b32 %[s], %[stg]\n\ts_mov_b32 %[c], 0x400000\n\ts_cmp_ge_u32 %[s], 4\n\ts_cbranch_scc1 2f\n\t1:\n\tds_read_b32 v216, %[ctl] offset:4\n\ts_waitcnt lgkmcnt(0)\n\tv_readfirstlane_b32 %[s], v216\n\ts_cmp_ge_u32 %[s], 4\n\ts_cbranch_scc1 2f\n\ts_sleep 1\n\ts_sub_u32 %[c], %[c], 1\n\ts_cbranch_scc0 1b\n\t.p2align 3\n\t2:\n\tds_read_b128 v[164:167], %[bb] offset:64\n\tds_read_b128 v[168:171], %[bb] offset:128\n\tds_read_b128 v[172:175], %[bb] offset:192\n\tds_read_b64_tr_b16 v[40:41], %[a0] offset:264\n\tds_read_b64_tr_b16 v[42:43], %[a0] offset:256\n\tds_read_b128 v[176:179], %[bb] offset:256\n\ts_waitcnt lgkmcnt(5)\n\ts_nop 0\n\tv_mfma_f32_16x16x32_bf16 v[132:135], v[52:55], v[164:167], 0\n\tds_read_b64_tr_b16 v[72:73], %[a0] offset:776\n\tds_read_b64_tr_b16 v[74:75], %[a0] offset:768\n\tds_read_b64_tr_b16 v[36:37], %[a0] offset:200\n\tv_mfma_f32_16x16x32_bf16 v[136:139], v[84:87], v[164:167], 0\n\tds_read_b64_tr_b16 v[38:39], %[a0] offset:192\n\ts_waitcnt lgkmcnt(8)\n\ts_nop 0\n\tv_mfma_f32_16x16x32_bf16 v[132:135], v[48:51], v[168:171], v[132:135]\n\tds_read_b128 v[180:183], %[bb] offset:320\n\tds_read_b64_tr_b16 v[68:69], %[a0] offset:712\n\tds_read_b64_tr_b16 v[70:71], %[a0] offset:704\n\tv_mfma_f32_16x16x32_bf16 v[136:139], v[80:83], v[168:171], v[136:139]\n\tds_read_b64_tr_b16 v[32:33], %[a0] offset:136\n\ts_waitcnt lgkmcnt(8)\n\ts_nop 0\n\tv_mfma_f32_16x16x32_bf16 v[132:135], v[44:47], v[172:175], v[132:135]\n\tds_read_b64_tr_b16 v[34:35], %[a0] offset:128\n\tds_read_b128 v[184:187], %[bb] offset:384\n\tds_read_b64_tr_b16 v[64:65], %[a0] offset:648\n\tv_mfma_f32_16x16x32_bf16 v[136:139], v[76:79], v[172:175], v[136:139]\n\tds_read_b64_tr_b16 v[66:67], %[a0] offset:640\n\tds_read_b64_tr_b16 v[28:29], %[a0] offset:72\n\tds_read_b64_tr_b16 v[30:31], %[a0] offset:64\n\tv_mfma_f32_16x16x32_bf16 v[132:135], v[40:43], v[176:179], v[132:135]\n\tds_read_b128 v[164:167], %[bb] offset:448\n\ts_waitcnt lgkmcnt(8)\n\ts_nop 0\n\tv_mfma_f32_16x16x32_bf16 v[136:139], v[72:75], v[176:179], v[136:139]\n\tds_read_b64_tr_b16 v[60:61], %[a0] offset:584\n\tds_read_b64_tr_b16 v[62:63], %[a0] offset:576\n\tds_read_b64_tr_b16 v[24:25], %[a0] offset:8\n\tv_mfma_f32_16x16x32_bf16 v[132:135], v[36:39], v[180:183], v[132:135]\n\tds_read_b64_tr_b16 v[26:27], %[a0] offset:0\n\tds_read_b128 v[168:171], %[bb] offset:512\n\tds_read_b64_tr_b16 v[56:57], %[a0] offset:520\n\tv_mfma_f32_16x16x32_bf16 v[136:139], v[68:71], v[180:183], v[136:139]\n\tds_read_b64_tr_b16 v[58:59], %[a0] offset:512\n\ts_waitcnt lgkmcnt(8)\n\ts_nop 0\n\tv_mfma_f32_16x16x32_bf16 v[132:135], v[32:35], v[184:187], v[132:135]\n\tds_read_b64_tr_b16 v[88:89], %[a0] offset:1032\n\tds_read_b64_tr_b16 v[90:91], %[a0] offset:1024\n\tds_read_b128 v[188:191], %[bb] offset:496\n\tv_mfma_f32_16x16x32_bf16 v[136:139], v[64:67], v[184:187], v[136:139]\n\tds_read_b128 v[172:175], %[bb] offset:576\n\ts_waitcnt lgkmcnt(8)\n\ts_nop 0\n\tv_mfma_f32_16x16x32_bf16 v[132:135], v[28:31], v[164:167], v[132:135]\n\tds_read_b128 v[176:179], %[bb] offset:640\n\tv_mfma_f32_16x16x32_bf16 v[136:139], v[60:63], v[164:167], v[136:139]\n\ts_waitcnt lgkmcnt(5)\n\ts_nop 0\n\tv_mfma_f32_16x16x32_bf16 v[132:135], v[24:27], v[168:171], v[132:135]\n\tds_write_b16 %[ta], %[t78]\n\tds_read_b128 v[180:183], %[bb] offset:704\n\tv_mfma_f32_16x16x32_bf16 v[136:139], v[56:59], v[168:171], v[136:139]\n\ts_waitcnt lgkmcnt(3)\n\ts_nop 0\n\tv_mfma_f32_16x16x32_bf16 v[140:143], v[88:91], v[168:171], 0\n\tds_write_b16_d16_hi %[ta], %[t78] offset:64\n\tds_read_b128 v[184:187], %[bb] offset:768\n\tv_mfma_f32_16x16x32_bf16 v[204:207], v[188:191], v[4:7], 0\n\tds_write_b128 %[yb], v[132:135] offset:0\n\tv_mfma_f32_16x16x32_bf16 v[218:221], v[168:171], v[4:7], 0\n\tds_write_b16 %[ta], %[t78] offset:388\n\tds_read_b128 v[164:167], %[bb] offset:832\n\tv_mfma_f32_16x16x32_bf16 v[208:211], v[188:191], v[8:11], 0\n\tds_write_b8 %[ctc], %[one] offset:0\n\tds_write_b16_d16_hi %[ta], %[t78] offset:452\n\tv_mfma_f32_16x16x32_bf16 v[222:225], v[168:171], v[8:11], 0\n\tds_write_b16 %[tb], %[t9a]\n\tv_mfma_f32_16x16x32_bf16 v[212:215], v[188:191], v[12:15], 0\n\tds_write_b16_d16_hi %[tb], %[t9a] offset:128\n\tv_cvt_pk_bf16_f32 v192, v221, v207\n\tv_mfma_f32_16x16x32_bf16 v[226:229], v[168:171], v[16:19], 0\n\tds_write_b16 %[tb], %[t9a] offset:388\n\tv_cvt_pk_bf16_f32 v193, v220, v206\n\tv_mfma_f32_16x16x32_bf16 v[148:151], v[20:23], v[168:171], 0\n\tds_write_b16_d16_hi %[tb], %[t9a] offset:516\n\tds_read_b128 v[168:171], %[bb] offset:896\n\tv_mfma_f32_16x16x32_bf16 v[136:139], v[52:55], v[172:175], v[136:139]\n\tds_read_b64 v[92:93], %[tp] offset:0\n\tds_read_b64 v[94:95], %[tp] offset:8\n\tds_read_b64 v[96:97], %[tp] offset:64\n\tv_mfma_f32_16x16x32_bf16 v[140:143], v[84:87], v[172:175], v[140:143]\n\tds_read_b128 v[172:175], %[bb] offset:960\n\ts_waitcnt lgkmcnt(7)\n\ts_nop 0\n\tv_mfma_f32_16x16x32_bf16 v[136:139], v[48:51], v[176:179], v[136:139]\n\tds_read_b64 v[98:99], %[tp] offset:72\n\tds_read_b64 v[100:101], %[tp] offset:128\n\tds_read_b64 v[102:103], %[tp] offset:136\n\tv_mfma_f32_16x16x32_bf16 v[140:143], v[80:83], v[176:179], v[140:143]\n\tds_read_b128 v[176:179], %[bb] offset:1024\n\tds_read_b64 v[104:105], %[tp] offset:768\n\tds_read_b64 v[106:107], %[tp] offset:776\n\tv_mfma_f32_16x16x32_bf16 v[136:139], v[44:47], v[180:183], v[136:139]\n\tds_read_b64 v[108:109], %[tp] offset:832\n\tds_read_b64 v[110:111], %[tp] offset:840\n\tds_read_b64 v[112:113], %[tp] offset:896\n\tv_mfma_f32_16x16x32_bf16 v[140:143], v[76:79], v[180:183], v[140:143]\n\tds_read_b64 v[114:115], %[tp] offset:904\n\tds_read_b64 v[116:117], %[tp] offset:1536\n\tds_read_b64 v[118:119], %[tp] offset:1544\n\tv_mfma_f32_16x16x32_bf16 v[136:139], v[40:43], v[184:187], v[136:139]\n\tds_read_b64 v[120:121], %[tp] offset:1600\n\tds_read_b64 v[122:123], %[tp] offset:1608\n\tv_cvt_pk_bf16_f32 v194, v219, v205\n\tv_mfma_f32_16x16x32_bf16 v[140:143], v[72:75], v[184:187], v[140:143]\n\tv_cvt_pk_bf16_f32 v195, v218, v204\n\tds_read_b64 v[124:125], %[tp] offset:1664\n\tds_read_b64 v[126:127], %[tp] offset:1672\n\tv_mfma_f32_16x16x32_bf16 v[136:139], v[36:39], v[164:167], v[136:139]\n\tds_read_b64 v[128:129], %[tp] offset:1728\n\tds_read_b64 v[130:131], %[tp] offset:1736\n\tv_cvt_pk_bf16_f32 v196, v225, v211\n\tv_mfma_f32_16x16x32_bf16 v[140:143], v[68:71], v[164:167], v[140:143]\n\tv_cvt_pk_bf16_f32 v197, v224, v210\n\ts_waitcnt lgkmcnt(6)\n\ts_nop 0\n\tv_mfma_f32_16x16x32_bf16 v[136:139], v[32:35], v[168:171], v[136:139]\n\tv_cvt_pk_bf16_f32 v198, v223, v209\n\tv_mfma_f32_16x16x32_bf16 v[140:143], v[64:67], v[168:171], v[140:143]\n\tv_cvt_pk_bf16_f32 v199, v222, v208\n\tv_mfma_f32_16x16x32_bf16 v[136:139], v[28:31], v[172:175], v[136:139]\n\tv_cvt_pk_bf16_f32 v200, v229, v215\n\tv_mfma_f32_16x16x32_bf16 v[140:143], v[60:63], v[172:175], v[140:143]\n\tds_read_b128 v[188:191], %[bb] offset:1008\n\tv_cvt_pk_bf16_f32 v201, v228, v214\n\tv_mfma_f32_16x16x32_bf16 v[136:139], v[24:27], v[176:179], v[136:139]\n\tv_cvt_pk_bf16_f32 v202, v227, v213\n\tv_mfma_f32_16x16x32_bf16 v[140:143], v[56:59], v[176:179], v[140:143]\n\tv_cvt_pk_bf16_f32 v203, v226, v212\n\tv_mfma_f32_16x16x32_bf16 v[144:147], v[88:91], v[176:179], 0\n\tv_mfma_f32_16x16x32_bf16 v[140:143], v[192:195], v[92:95], v[140:143]\n\tv_mfma_f32_16x16x32_bf16 v[144:147], v[192:195], v[96:99], v[144:147]\n\tv_mfma_f32_16x16x32_bf16 v[148:151], v[192:195], v[100:103], v[148:151]\n\tv_mfma_f32_16x16x32_bf16 v[140:143], v[196:199], v[104:107], v[140:143]\n\tds_write_b128 %[yb], v[136:139] offset:1152\n\tds_read_b128 v[180:183], %[bb] offset:1088\n\tv_mfma_f32_16x16x32_bf16 v[144:147], v[196:199], v[108:111], v[144:147]\n\tv_mfma_f32_16x16x32_bf16 v[148:151], v[196:199], v[112:115], v[148:151]\n\tds_write_b8 %[ctc], %[one] offset:64\n\tds_read_b128 v[184:187], %[bb] offset:1152\n\tv_mfma_f32_16x16x32_bf16 v[148:151], v[200:203], v[116:119], v[148:151]\n\ts_waitcnt lgkmcnt(4)\n\ts_nop 0\n\tv_mfma_f32_16x16x32_bf16 v[152:155], v[200:203], v[120:123], 0\n\tds_read_b128 v[164:167], %[bb] offset:1216\n\tv_mfma_f32_16x16x32_bf16 v[156:159], v[200:203], v[124:127], 0\n\tv_mfma_f32_16x16x32_bf16 v[160:163], v[200:203], v[128:131], 0\n\tds_read_b128 v[168:171], %[bb] offset:1280\n\tv_mfma_f32_16x16x32_bf16 v[204:207], v[188:191], v[4:7], 0\n\tv_mfma_f32_16x16x32_bf16 v[218:221], v[176:179], v[4:7], 0\n\tds_read_b128 v[172:175], %[bb] offset:1344\n\tv_mfma_f32_16x16x32_bf16 v[208:211], v[188:191], v[8:11], 0\n\tv_mfma_f32_16x16x32_bf16 v[222:225], v[176:179], v[8:11], 0\n\tv_mfma_f32_16x16x32_bf16 v[212:215], v[188:191], v[12:15], 0\n\tv_mfma_f32_16x16x32_bf16 v[226:229], v[176:179], v[16:19], 0\n\tv_mfma_f32_16x16x32_bf16 v[152:155], v[20:23], v[176:179], v[152:155]\n\ts_waitcnt lgkmcnt(0)\n\ts_nop 0\n\tv_mfma_f32_16x16x32_bf16 v[140:143], v[52:55], v[180:183], v[140:143]\n\tds_read_b128 v[176:179], %[bb] offset:1408\n\tv_cvt_pk_bf16_f32 v192, v221, v207\n\tv_mfma_f32_16x16x32_bf16 v[144:147], v[84:87], v[180:183], v[144:147]\n\tds_read_b128 v[180:183], %[bb] offset:1472\n\tv_cvt_pk_bf16_f32 v193, v220, v206\n\tv_mfma_f32_16x16x32_bf16 v[140:143], v[48:51], v[184:187], v[140:143]\n\tv_cvt_pk_bf16_f32 v194, v219, v205\n\tv_mfma_f32_16x16x32_bf16 v[144:147], v[80:83], v[184:187], v[144:147]\n\tds_read_b128 v[184:187], %[bb] offset:1536\n\tv_cvt_pk_bf16_f32 v195, v218, v204\n\tv_mfma_f32_16x16x32_bf16 v[140:143], v[44:47], v[164:167], v[140:143]\n\tv_cvt_pk_bf16_f32 v196, v225, v211\n\tv_mfma_f32_16x16x32_bf16 v[144:147], v[76:79], v[164:167], v[144:147]\n\tv_cvt_pk_bf16_f32 v197, v224, v210\n\tv_mfma_f32_16x16x32_bf16 v[140:143], v[40:43], v[168:171], v[140:143]\n\tv_cvt_pk_bf16_f32 v198, v223, v209\n\tv_mfma_f32_16x16x32_bf16 v[144:147], v[72:75], v[168:171], v[144:147]\n\tv_cvt_pk_bf16_f32 v199, v222, v208\n\tv_mfma_f32_16x16x32_bf16 v[140:143], v[36:39], v[172:175], v[140:143]\n\tv_cvt_pk_bf16_f32 v200, v229, v215\n\tv_mfma_f32_16x16x32_bf16 v[144:147], v[68:71], v[172:175], v[144:147]\n\ts_waitcnt lgkmcnt(0)\n\ts_nop 0\n\tv_mfma_f32_16x16x32_bf16 v[140:143], v[32:35], v[176:179], v[140:143]\n\tv_cvt_pk_bf16_f32 v201, v228, v214\n\tv_mfma_f32_16x16x32_bf16 v[144:147], v[64:67], v[176:179], v[144:147]\n\tv_cvt_pk_bf16_f32 v202, v227, v213\n\tv_mfma_f32_16x16x32_bf16 v[140:143], v[28:31], v[180:183], v[140:143]\n\tds_read_b128 v[188:191], %[bb] offset:1520\n\tv_cvt_pk_bf16_f32 v203, v226, v212\n\tv_mfma_f32_16x16x32_bf16 v[144:147], v[60:63], v[180:183], v[144:147]\n\tv_mfma_f32_16x16x32_bf16 v[140:143], v[24:27], v[184:187], v[140:143]\n\tv_mfma_f32_16x16x32_bf16 v[144:147], v[56:59], v[184:187], v[144:147]\n\tv_mfma_f32_16x16x32_bf16 v[148:151], v[88:91], v[184:187], v[148:151]\n\tv_mfma_f32_16x16x32_bf16 v[144:147], v[192:195], v[92:95], v[144:147]\n\tv_mfma_f32_16x16x32_bf16 v[148:151], v[192:195], v[96:99], v[148:151]\n\tv_mfma_f32_16x16x32_bf16 v[152:155], v[192:195], v[100:103], v[152:155]\n\tds_read_b128 v[164:167], %[bb] offset:1600\n\tv_mfma_f32_16x16x32_bf16 v[144:147], v[196:199], v[104:107], v[144:147]\n\tv_mfma_f32_16x16x32_bf16 v[148:151], v[196:199], v[108:111], v[148:151]\n\tds_write_b128 %[yb], v[140:143] offset:2304\n\tv_mfma_f32_16x16x32_bf16 v[152:155], v[196:199], v[112:115], v[152:155]\n\tds_read_b128 v[168:171], %[bb] offset:1664\n\tv_mfma_f32_16x16x32_bf16 v[152:155], v[200:203], v[116:119], v[152:155]\n\tds_write_b8 %[ctc], %[one] offset:128\n\tds_read_b128 v[172:175], %[bb] offset:1728\n\tv_mfma_f32_16x16x32_bf16 v[156:159], v[200:203], v[120:123], v[156:159]\n\tv_mfma_f32_16x16x32_bf16 v[160:163], v[200:203], v[124:127], v[160:163]\n\ts_waitcnt lgkmcnt(3)\n\ts_nop 0\n\tv_mfma_f32_16x16x32_bf16 v[204:207], v[188:191], v[4:7], 0\n\tds_read_b128 v[176:179], %[bb] offset:1792\n\tv_mfma_f32_16x16x32_bf16 v[218:221], v[184:187], v[4:7], 0\n\tds_read_b128 v[180:183], %[bb] offset:1856\n\tv_mfma_f32_16x16x32_bf16 v[208:211], v[188:191], v[8:11], 0\n\tv_mfma_f32_16x16x32_bf16 v[222:225], v[184:187], v[8:11], 0\n\tv_mfma_f32_16x16x32_bf16 v[212:215], v[188:191], v[12:15], 0\n\tv_mfma_f32_16x16x32_bf16 v[226:229], v[184:187], v[16:19], 0\n\tv_mfma_f32_16x16x32_bf16 v[156:159], v[20:23], v[184:187], v[156:159]\n\tds_read_b128 v[184:187], %[bb] offset:1920\n\tv_mfma_f32_16x16x32_bf16 v[144:147], v[52:55], v[164:167], v[144:147]\n\tv_cvt_pk_bf16_f32 v192, v221, v207\n\tv_mfma_f32_16x16x32_bf16 v[148:151], v[84:87], v[164:167], v[148:151]\n\ts_waitcnt lgkmcnt(1)\n\ts_nop 0\n\tv_mfma_f32_16x16x32_bf16 v[144:147], v[48:51], v[168:171], v[144:147]\n\tds_read_b128 v[164:167], %[bb] offset:1984\n\tv_cvt_pk_bf16_f32 v193, v220, v206\n\tv_mfma_f32_16x16x32_bf16 v[148:151], v[80:83], v[168:171], v[148:151]\n\tds_read_b128 v[168:171], %[bb] offset:2048\n\tv_cvt_pk_bf16_f32 v194, v219, v205\n\tv_mfma_f32_16x16x32_bf16 v[144:147], v[44:47], v[172:175], v[144:147]\n\tds_read_b128 v[188:191], %[bb] offset:2032\n\tv_cvt_pk_bf16_f32 v195, v218, v204\n\tv_mfma_f32_16x16x32_bf16 v[148:151], v[76:79], v[172:175], v[148:151]\n\tv_cvt_pk_bf16_f32 v196, v225, v211\n\tv_mfma_f32_16x16x32_bf16 v[144:147], v[40:43], v[176:179], v[144:147]\n\tv_cvt_pk_bf16_f32 v197, v224, v210\n\tv_mfma_f32_16x16x32_bf16 v[148:151], v[72:75], v[176:179], v[148:151]\n\tv_cvt_pk_bf16_f32 v198, v223, v209\n\tv_mfma_f32_16x16x32_bf16 v[144:147], v[36:39], v[180:183], v[144:147]\n\tv_cvt_pk_bf16_f32 v199, v222, v208\n\tv_mfma_f32_16x16x32_bf16 v[148:151], v[68:71], v[180:183], v[148:151]\n\ts_waitcnt lgkmcnt(0)\n\ts_nop 0\n\tv_mfma_f32_16x16x32_bf16 v[144:147], v[32:35], v[184:187], v[144:147]\n\tv_cvt_pk_bf16_f32 v200, v229, v215\n\tv_mfma_f32_16x16x32_bf16 v[148:151], v[64:67], v[184:187], v[148:151]\n\tv_cvt_pk_bf16_f32 v201, v228, v214\n\tv_mfma_f32_16x16x32_bf16 v[144:147], v[28:31], v[164:167], v[144:147]\n\tv_cvt_pk_bf16_f32 v202, v227, v213\n\tv_mfma_f32_16x16x32_bf16 v[148:151], v[60:63], v[164:167], v[148:151]\n\tv_cvt_pk_bf16_f32 v203, v226, v212\n\tv_mfma_f32_16x16x32_bf16 v[144:147], v[24:27], v[168:171], v[144:147]\n\tv_mfma_f32_16x16x32_bf16 v[148:151], v[56:59], v[168:171], v[148:151]\n\tv_mfma_f32_16x16x32_bf16 v[152:155], v[88:91], v[168:171], v[152:155]\n\tv_mfma_f32_16x16x32_bf16 v[204:207], v[188:191], v[4:7], 0\n\tv_mfma_f32_16x16x32_bf16 v[218:221], v[168:171], v[4:7], 0\n\tv_mfma_f32_16x16x32_bf16 v[208:211], v[188:191], v[8:11], 0\n\tv_mfma_f32_16x16x32_bf16 v[222:225], v[168:171], v[8:11], 0\n\tv_mfma_f32_16x16x32_bf16 v[212:215], v[188:191], v[12:15], 0\n\tv_mfma_f32_16x16x32_bf16 v[226:229], v[168:171], v[16:19], 0\n\tds_write_b128 %[yb], v[144:147] offset:3456\n\tv_mfma_f32_16x16x32_bf16 v[160:163], v[20:23], v[168:171], v[160:163]\n\tds_read_b32 v216, %[ctl] offset:20\n\tv_mfma_f32_16x16x32_bf16 v[148:151], v[192:195], v[92:95], v[148:151]\n\tds_write_b8 %[ctc], %[one] offset:192\n\tv_mfma_f32_16x16x32_bf16 v[152:155], v[192:195], v[96:99], v[152:155]\n\tv_mfma_f32_16x16x32_bf16 v[156:159], v[192:195], v[100:103], v[156:159]\n\tv_mfma_f32_16x16x32_bf16 v[148:151], v[196:199], v[104:107], v[148:151]\n\tv_cvt_pk_bf16_f32 v192, v221, v207\n\tv_mfma_f32_16x16x32_bf16 v[152:155], v[196:199], v[108:111], v[152:155]\n\tv_cvt_pk_bf16_f32 v193, v220, v206\n\tv_mfma_f32_16x16x32_bf16 v[156:159], v[196:199], v[112:115], v[156:159]\n\tv_cvt_pk_bf16_f32 v194, v219, v205\n\tv_mfma_f32_16x16x32_bf16 v[156:159], v[200:203], v[116:119], v[156:159]\n\tv_cvt_pk_bf16_f32 v195, v218, v204\n\tv_mfma_f32_16x16x32_bf16 v[160:163], v[200:203], v[120:123], v[160:163]\n\ts_waitcnt lgkmcnt(0)\n\ts_nop 0\n\tv_readfirstlane_b32 %[s], v216\n\ts_mov_b32 %[c], 0x400000\n\ts_cmp_ge_u32 %[s], 2\n\ts_cbranch_scc1 2f\n\t1:\n\tds_read_b32 v216, %[ctl] offset:20\n\ts_waitcnt lgkmcnt(0)\n\tv_readfirstlane_b32 %[s], v216\n\ts_cmp_ge_u32 %[s], 2\n\ts_cbranch_scc1 2f\n\ts_sleep 1\n\ts_sub_u32 %[c], %[c], 1\n\ts_cbranch_scc0 1b\n\t.p2align 3\n\t2:\n\tv_mfma_f32_16x16x32_bf16 v[152:155], v[192:195], v[92:95], v[152:155]\n\tds_read_b128 v[172:175], %[bb] offset:2112\n\tv_cvt_pk_bf16_f32 v196, v225, v211\n\tv_mfma_f32_16x16x32_bf16 v[156:159], v[192:195], v[96:99], v[156:159]\n\tds_read_b128 v[176:179], %[bb] offset:2176\n\tv_cvt_pk_bf16_f32 v197, v224, v210\n\tv_mfma_f32_16x16x32_bf16 v[160:163], v[192:195], v[100:103], v[160:163]\n\tv_cvt_pk_bf16_f32 v198, v223, v209\n\tds_read_b128 v[180:183], %[bb] offset:2240\n\tv_cvt_pk_bf16_f32 v199, v222, v208\n\ts_nop 1\n\ts_nop 0\n\tv_mfma_f32_16x16x32_bf16 v[152:155], v[196:199], v[104:107], v[152:155]\n\tds_read_b128 v[184:187], %[bb] offset:2304\n\tv_cvt_pk_bf16_f32 v200, v229, v215\n\tv_mfma_f32_16x16x32_bf16 v[156:159], v[196:199], v[108:111], v[156:159]\n\tv_cvt_pk_bf16_f32 v201, v228, v214\n\tds_read_b128 v[164:167], %[bb] offset:2368\n\tv_mfma_f32_16x16x32_bf16 v[160:163], v[196:199], v[112:115], v[160:163]\n\tv_cvt_pk_bf16_f32 v202, v227, v213\n\tds_read_b128 v[168:171], %[bb] offset:2432\n\tv_cvt_pk_bf16_f32 v203, v226, v212\n\ts_nop 1\n\ts_nop 0\n\tv_mfma_f32_16x16x32_bf16 v[160:163], v[200:203], v[116:119], v[160:163]\n\ts_waitcnt lgkmcnt(3)\n\ts_nop 0\n\tv_mfma_f32_16x16x32_bf16 v[148:151], v[52:55], v[172:175], v[148:151]\n\tv_mfma_f32_16x16x32_bf16 v[152:155], v[84:87], v[172:175], v[152:155]\n\tds_read_b128 v[172:175], %[bb] offset:2496\n\tv_mfma_f32_16x16x32_bf16 v[148:151], v[48:51], v[176:179], v[148:151]\n\tv_mfma_f32_16x16x32_bf16 v[152:155], v[80:83], v[176:179], v[152:155]\n\tds_read_b128 v[176:179], %[bb] offset:2560\n\tv_mfma_f32_16x16x32_bf16 v[148:151], v[44:47], v[180:183], v[148:151]\n\tds_read_b128 v[188:191], %[bb] offset:2544\n\tv_mfma_f32_16x16x32_bf16 v[152:155], v[76:79], v[180:183], v[152:155]\n\ts_waitcnt lgkmcnt(2)\n\ts_nop 0\n\tv_mfma_f32_16x16x32_bf16 v[148:151], v[40:43], v[184:187], v[148:151]\n\tv_mfma_f32_16x16x32_bf16 v[152:155], v[72:75], v[184:187], v[152:155]\n\tds_read_b128 v[180:183], %[bb] offset:2624\n\tv_mfma_f32_16x16x32_bf16 v[148:151], v[36:39], v[164:167], v[148:151]\n\tv_mfma_f32_16x16x32_bf16 v[152:155], v[68:71], v[164:167], v[152:155]\n\tds_read_b128 v[184:187], %[bb] offset:2688\n\tv_mfma_f32_16x16x32_bf16 v[148:151], v[32:35], v[168:171], v[148:151]\n\tv_mfma_f32_16x16x32_bf16 v[152:155], v[64:67], v[168:171], v[152:155]\n\tds_read_b128 v[164:167], %[bb] offset:2752\n\tv_mfma_f32_16x16x32_bf16 v[148:151], v[28:31], v[172:175], v[148:151]\n\tv_mfma_f32_16x16x32_bf16 v[152:155], v[60:63], v[172:175], v[152:155]\n\ts_waitcnt lgkmcnt(1)\n\ts_nop 0\n\tv_mfma_f32_16x16x32_bf16 v[148:151], v[24:27], v[176:179], v[148:151]\n\tds_read_b128 v[168:171], %[bb] offset:2816\n\tv_mfma_f32_16x16x32_bf16 v[152:155], v[56:59], v[176:179], v[152:155]\n\tds_read_b128 v[172:175], %[bb] offset:2880\n\tv_mfma_f32_16x16x32_bf16 v[156:159], v[88:91], v[176:179], v[156:159]\n\tv_mfma_f32_16x16x32_bf16 v[204:207], v[188:191], v[4:7], 0\n\tv_mfma_f32_16x16x32_bf16 v[218:221], v[176:179], v[4:7], 0\n\tv_mfma_f32_16x16x32_bf16 v[208:211], v[188:191], v[8:11], 0\n\tv_mfma_f32_16x16x32_bf16 v[222:225], v[176:179], v[8:11], 0\n\tds_write_b128 %[yb], v[148:151] offset:4608\n\tv_mfma_f32_16x16x32_bf16 v[152:155], v[52:55], v[180:183], v[152:155]\n\tds_read_b128 v[176:179], %[bb] offset:2944\n\tv_mfma_f32_16x16x32_bf16 v[156:159], v[84:87], v[180:183], v[156:159]\n\tds_write_b8 %[ctc], %[one] offset:256\n\tds_read_b128 v[180:183], %[bb] offset:3008\n\tv_mfma_f32_16x16x32_bf16 v[152:155], v[48:51], v[184:187], v[152:155]\n\tv_cvt_pk_bf16_f32 v192, v221, v207\n\tv_mfma_f32_16x16x32_bf16 v[156:159], v[80:83], v[184:187], v[156:159]\n\ts_waitcnt lgkmcnt(3)\n\ts_nop 0\n\tv_mfma_f32_16x16x32_bf16 v[152:155], v[44:47], v[164:167], v[152:155]\n\tds_read_b128 v[184:187], %[bb] offset:3072\n\tv_cvt_pk_bf16_f32 v193, v220, v206\n\tv_mfma_f32_16x16x32_bf16 v[156:159], v[76:79], v[164:167], v[156:159]\n\tv_cvt_pk_bf16_f32 v194, v219, v205\n\tv_mfma_f32_16x16x32_bf16 v[152:155], v[40:43], v[168:171], v[152:155]\n\tv_cvt_pk_bf16_f32 v195, v218, v204\n\tv_mfma_f32_16x16x32_bf16 v[156:159], v[72:75], v[168:171], v[156:159]\n\tds_read_b128 v[188:191], %[bb] offset:3056\n\tv_cvt_pk_bf16_f32 v196, v225, v211\n\tv_mfma_f32_16x16x32_bf16 v[152:155], v[36:39], v[172:175], v[152:155]\n\tv_cvt_pk_bf16_f32 v197, v224, v210\n\tv_mfma_f32_16x16x32_bf16 v[156:159], v[68:71], v[172:175], v[156:159]\n\ts_waitcnt lgkmcnt(1)\n\ts_nop 0\n\tv_mfma_f32_16x16x32_bf16 v[152:155], v[32:35], v[176:179], v[152:155]\n\tds_read_b32 v216, %[ctl] offset:24\n\tv_cvt_pk_bf16_f32 v198, v223, v209\n\tv_mfma_f32_16x16x32_bf16 v[156:159], v[64:67], v[176:179], v[156:159]\n\tv_cvt_pk_bf16_f32 v199, v222, v208\n\tv_mfma_f32_16x16x32_bf16 v[152:155], v[28:31], v[180:183], v[152:155]\n\tv_mfma_f32_16x16x32_bf16 v[156:159], v[60:63], v[180:183], v[156:159]\n\tv_mfma_f32_16x16x32_bf16 v[152:155], v[24:27], v[184:187], v[152:155]\n\tv_mfma_f32_16x16x32_bf16 v[156:159], v[56:59], v[184:187], v[156:159]\n\tv_mfma_f32_16x16x32_bf16 v[160:163], v[88:91], v[184:187], v[160:163]\n\tv_mfma_f32_16x16x32_bf16 v[156:159], v[192:195], v[92:95], v[156:159]\n\ts_waitcnt lgkmcnt(0)\n\ts_nop 0\n\tv_readfirstlane_b32 %[s], v216\n\ts_mov_b32 %[c], 0x400000\n\ts_cmp_ge_u32 %[s], 2\n\ts_cbranch_scc1 2f\n\t1:\n\tds_read_b32 v216, %[ctl] offset:24\n\ts_waitcnt lgkmcnt(0)\n\tv_readfirstlane_b32 %[s], v216\n\ts_cmp_ge_u32 %[s], 2\n\ts_cbranch_scc1 2f\n\ts_sleep 1\n\ts_sub_u32 %[c], %[c], 1\n\ts_cbranch_scc0 1b\n\t.p2align 3\n\t2:\n\tv_mfma_f32_16x16x32_bf16 v[160:163], v[192:195], v[96:99], v[160:163]\n\tds_write_b128 %[yb], v[152:155] offset:5760\n\tv_mfma_f32_16x16x32_bf16 v[156:159], v[196:199], v[104:107], v[156:159]\n\tds_read_b128 v[164:167], %[bb] offset:3136\n\tv_mfma_f32_16x16x32_bf16 v[160:163], v[196:199], v[108:111], v[160:163]\n\tds_write_b8 %[ctc], %[one] offset:320\n\tds_read_b128 v[168:171], %[bb] offset:3200\n\tv_mfma_f32_16x16x32_bf16 v[204:207], v[188:191], v[4:7], 0\n\tds_read_b128 v[172:175], %[bb] offset:3264\n\tv_mfma_f32_16x16x32_bf16 v[218:221], v[184:187], v[4:7], 0\n\tds_read_b128 v[176:179], %[bb] offset:3328\n\tv_mfma_f32_16x16x32_bf16 v[208:211], v[188:191], v[8:11], 0\n\tds_read_b128 v[180:183], %[bb] offset:3392\n\tv_mfma_f32_16x16x32_bf16 v[222:225], v[184:187], v[8:11], 0\n\ts_waitcnt lgkmcnt(3)\n\ts_nop 0\n\tv_mfma_f32_16x16x32_bf16 v[156:159], v[52:55], v[164:167], v[156:159]\n\tds_read_b128 v[184:187], %[bb] offset:3456\n\tv_cvt_pk_bf16_f32 v192, v221, v207\n\tv_mfma_f32_16x16x32_bf16 v[160:163], v[84:87], v[164:167], v[160:163]\n\tds_read_b128 v[164:167], %[bb] offset:3520\n\tv_cvt_pk_bf16_f32 v193, v220, v206\n\tv_mfma_f32_16x16x32_bf16 v[156:159], v[48:51], v[168:171], v[156:159]\n\tv_cvt_pk_bf16_f32 v194, v219, v205\n\tv_mfma_f32_16x16x32_bf16 v[160:163], v[80:83], v[168:171], v[160:163]\n\ts_waitcnt lgkmcnt(2)\n\ts_nop 0\n\tv_mfma_f32_16x16x32_bf16 v[156:159], v[44:47], v[172:175], v[156:159]\n\tds_read_b128 v[168:171], %[bb] offset:3584\n\tv_cvt_pk_bf16_f32 v195, v218, v204\n\tv_mfma_f32_16x16x32_bf16 v[160:163], v[76:79], v[172:175], v[160:163]\n\tds_read_b128 v[172:175], %[bb] offset:3648\n\tv_cvt_pk_bf16_f32 v196, v225, v211\n\tv_mfma_f32_16x16x32_bf16 v[156:159], v[40:43], v[176:179], v[156:159]\n\tglobal_load_dwordx2 v[240:241], %[so], %[ws] sc1\n\tv_cvt_pk_bf16_f32 v197, v224, v210\n\tv_mfma_f32_16x16x32_bf16 v[160:163], v[72:75], v[176:179], v[160:163]\n\tds_read_b128 v[176:179], %[bb] offset:3712\n\tv_cvt_pk_bf16_f32 v198, v223, v209\n\tv_mfma_f32_16x16x32_bf16 v[156:159], v[36:39], v[180:183], v[156:159]\n\tv_cvt_pk_bf16_f32 v199, v222, v208\n\tv_mfma_f32_16x16x32_bf16 v[160:163], v[68:71], v[180:183], v[160:163]\n\ts_waitcnt lgkmcnt(1)\n\ts_nop 0\n\tv_mfma_f32_16x16x32_bf16 v[156:159], v[32:35], v[184:187], v[156:159]\n\tds_read_b128 v[180:183], %[bb] offset:3776\n\tv_mfma_f32_16x16x32_bf16 v[160:163], v[64:67], v[184:187], v[160:163]\n\tds_read_b128 v[184:187], %[bb] offset:3840\n\tv_mfma_f32_16x16x32_bf16 v[156:159], v[28:31], v[164:167], v[156:159]\n\tv_mfma_f32_16x16x32_bf16 v[160:163], v[60:63], v[164:167], v[160:163]\n\tds_read_b128 v[164:167], %[bb] offset:3904\n\tv_mfma_f32_16x16x32_bf16 v[156:159], v[24:27], v[168:171], v[156:159]\n\tv_mfma_f32_16x16x32_bf16 v[160:163], v[56:59], v[168:171], v[160:163]\n\tds_read_b128 v[168:171], %[bb] offset:3968\n\tv_mfma_f32_16x16x32_bf16 v[160:163], v[192:195], v[92:95], v[160:163]\n\tv_mfma_f32_16x16x32_bf16 v[160:163], v[196:199], v[104:107], v[160:163]\n\tv_mfma_f32_16x16x32_bf16 v[160:163], v[52:55], v[172:175], v[160:163]\n\ts_waitcnt lgkmcnt(1)\n\ts_nop 0\n\tv_mfma_f32_16x16x32_bf16 v[160:163], v[48:51], v[176:179], v[160:163]\n\tds_write_b128 %[yb], v[156:159] offset:6912\n\tv_mfma_f32_16x16x32_bf16 v[160:163], v[44:47], v[180:183], v[160:163]\n\tds_read_b128 v[172:175], %[bb] offset:4032\n\tv_mfma_f32_16x16x32_bf16 v[160:163], v[40:43], v[184:187], v[160:163]\n\tds_write_b8 %[ctc], %[one] offset:384\n\tds_read_b128 v[176:179], %[bb] offset:4096\n\tv_mfma_f32_16x16x32_bf16 v[160:163], v[36:39], v[164:167], v[160:163]\n\ts_waitcnt lgkmcnt(4)\n\ts_nop 0\n\tv_mfma_f32_16x16x32_bf16 v[160:163], v[32:35], v[168:171], v[160:163]\n\ts_waitcnt lgkmcnt(2)\n\ts_nop 0\n\tv_mfma_f32_16x16x32_bf16 v[160:163], v[28:31], v[172:175], v[160:163]\n\ts_waitcnt lgkmcnt(0)\n\ts_nop 0\n\tv_mfma_f32_16x16x32_bf16 v[160:163], v[24:27], v[176:179], v[160:163]\n\ts_nop 7\n\tds_write_b128 %[yb], v[160:163] offset:8064\n\tds_write_b8 %[ctc], %[one] offset:448"
#define CONV_CLOBBER "v24", "v25", "v26", "v27", "v28", "v29", "v30", "v31", "v32", "v33", "v34", "v35", "v36", "v37", "v38", "v39", "v40", "v41", "v42", "v43", "v44", "v45", "v46", "v47", "v48", "v49", "v50", "v51", "v52", "v53", "v54", "v55", "v56", "v57", "v58", "v59", "v60", "v61", "v62", "v63", "v64", "v65", "v66", "v67", "v68", "v69", "v70", "v71", "v72", "v73", "v74", "v75", "v76", "v77", "v78", "v79", "v80", "v81", "v82", "v83", "v84", "v85", "v86", "v87", "v88", "v89", "v90", "v91", "v92", "v93", "v94", "v95", "v96", "v97", "v98", "v99", "v100", "v101", "v102", "v103", "v104", "v105", "v106", "v107", "v108", "v109", "v110", "v111", "v112", "v113", "v114", "v115", "v116", "v117", "v118", "v119", "v120", "v121", "v122", "v123", "v124", "v125", "v126", "v127", "v128", "v129", "v130", "v131", "v132", "v133", "v134", "v135", "v136", "v137", "v138", "v139", "v140", "v141", "v142", "v143", "v144", "v145", "v146", "v147", "v148", "v149", "v150", "v151", "v152", "v153", "v154", "v155", "v156", "v157", "v158", "v159", "v160", "v161", "v162", "v163", "v164", "v165", "v166", "v167", "v168", "v169", "v170", "v171", "v172", "v173", "v174", "v175", "v176", "v177", "v178", "v179", "v180", "v181", "v182", "v183", "v184", "v185", "v186", "v187", "v188", "v189", "v190", "v191", "v192", "v193", "v194", "v195", "v196", "v197", "v198", "v199", "v200", "v201", "v202", "v203", "v204", "v205", "v206", "v207", "v208", "v209", "v210", "v211", "v212", "v213", "v214", "v215", "v216", "v217", "v218", "v219", "v220", "v221", "v222", "v223", "v224", "v225", "v226", "v227", "v228", "v229"

typedef __attribute__((address_space(4))) const Params* KArg;
#define ROLE_BEGIN(N) KArg kp; int b, tid; \
    asm volatile("fk_role" #N ":\n\tv_mov_b32 %0, v0" : "=&v"(tid) : "{s[0:1]}"(kp0), "{s2}"(b0), "{v0}"(tid0) : "memory"); \
    asm volatile("s_mov_b64 %0, s[0:1]" : "=&s"(kp) : "{s[0:1]}"(kp0), "{s2}"(b0)); asm volatile("s_mov_b32 %0, s2" : "=&s"(b) : "{s2}"(b0)); \
    const int lane = tid & 63, wave = __builtin_amdgcn_readfirstlane(tid >> 6), xcd = b & 7, slot = b >> 3; \
    const int cw = slot < 16 ? xcd * 16 + slot : 112 + xcd * 2 + slot, chbase = cw * 4, w = xcd * 9 + (slot - 18); (void)chbase; (void)w; (void)lane; \
    const Params p = {kp->x, kp->Are, kp->Aim, kp->Bre, kp->Bim, kp->Cre, kp->Cim, kp->logdt, kp->dskip, kp->wr, kp->wg, kp->wu, kp->wd, kp->gw, kp->out, kp->ws}; \
    asm volatile("s_waitcnt lgkmcnt(0)"); STAMP0_FIX()
#define ROLE_END() do { STAMP_FLUSH(); __builtin_amdgcn_s_waitcnt(0x0070); asm volatile("s_endpgm" ::: "memory"); } while (0)
#define MF(A_, B_, C_) __builtin_amdgcn_mfma_f32_16x16x32_bf16(A_, B_, C_, 0, 0, 0)
#define GRP(mask, n) __builtin_amdgcn_sched_group_barrier(mask, n, 0)

__global__ __launch_bounds__(512, 2) void fused_kernel(Params p_unused) {
    extern __shared__ __attribute__((aligned(16))) char smem_[];
    LAS char* smem = (LAS char*)smem_;
    const KArg kp0 = (KArg)__builtin_amdgcn_kernarg_segment_ptr();
    const int tid0 = threadIdx.x, b0 = blockIdx.x;
    asm volatile("s_load_dword s94, s[0:1], 0x0\n\ts_load_dword s95, s[0:1], 0x40\n\tv_readfirstlane_b32 s92, v0\n\t"
                 "s_cmp_ge_u32 s2, 32\n\ts_cbranch_scc1 1f\n\t"
                 "s_getpc_b64 s[90:91]\n\ts_and_b32 s90, s90, 0xffffff00\n\ts_and_b32 s93, s2, -8\n\ts_lshr_b32 s89, s92, 6\n\ts_add_u32 s93, s93, s89\n\ts_min_u32 s93, s93, %3\n\ts_lshl_b32 s93, s93, 10\n\t"
                 "v_and_b32 v1, 63, v0\n\tv_lshlrev_b32 v1, 4, v1\n\tv_add_u32 v1, s93, v1\n\ts_mov_b32 m0, %4\n\ts_nop 0\n\tglobal_load_lds_dwordx4 v1, s[90:91]\n"
                 "1:\n\ts_cmp_lt_u32 s2, 144\n\ts_cbranch_scc0 2f\n\ts_cmp_lt_u32 s92, 256\n\ts_cbranch_scc1 fk_role0\n\ts_branch fk_role1\n"
                 "2:\n\ts_cmp_lt_u32 s2, 216\n\ts_cbranch_scc0 3f\n\ts_cmp_lt_u32 s92, 384\n\ts_cbranch_scc1 fk_role2\n\ts_branch fk_role3\n"
                 "3:\n\ts_cmp_lt_u32 s2, 224\n\ts_cbranch_scc1 fk_role4\n\ts_branch fk_role5"
                 :: "{s[0:1]}"(kp0), "{s2}"(b0), "{v0}"(tid0), "n"(CODE_KB - 1), "n"(LDS_DUMMY) : "memory", "scc", "s89", "s90", "s91", "s92", "s93", "s94", "s95", "v1");
    const int slot = b0 >> 3, wave = __builtin_amdgcn_readfirstlane(tid0 >> 6);
    if (slot >= 18) {
    if (slot >= 27 || wave >= 6) {
    if (slot < 28) {
    if (slot == 27)
    {
        ROLE_BEGIN(4);
        const u32 tag = __builtin_amdgcn_readfirstlane(__hip_atomic_load((GAS u32*)p.ws, RLX_AGENT)) + 1u;
        if (tid < 128) ((LAS u32*)smem)[tid] = 0;
        u32 ga = (u32)(uintptr_t)(smem + (lane & 15) * 4), gv = (u32)(uintptr_t)(smem + (lane >> 2) * 4), gq_ = (u32)(uintptr_t)(smem + (lane & 3) * 16), zero = (u32)(uintptr_t)smem;
        u32 ro = OFF_RG + hline(p.ws, tid >> 3) + (tid & 7) * 16, ro2 = OFF_RG + hline(p.ws, 64 + (tid >> 3)) + (tid & 7) * 16, so = OFF_SEL + ((lane & 1) * 8 + xcd) * 256, xo = (lane & 3) * 128;
        asm volatile("" : "+v"(ga), "+v"(gv), "+v"(gq_), "+v"(zero), "+v"(ro), "+v"(ro2), "+v"(so), "+v"(xo));
        BAR();
        __builtin_amdgcn_s_sleep(SELWAIT);
        v4u qa, qb;
        u32 c_;
#define POLL_A "global_load_dwordx4 v[20:23], %[ro], %[ws] sc1\n\tglobal_load_dwordx4 v[24:27], %[r2], %[ws] sc1\n\t"
#define POLL_B "global_load_dwordx4 v[28:31], %[ro], %[ws] sc1\n\tglobal_load_dwordx4 v[32:35], %[r2], %[ws] sc1\n\t"
        asm volatile("s_mov_b32 %[c], 0x40000\n\t" POLL_A "s_sleep 8\n\t" POLL_B
                     "1:\n\ts_waitcnt vmcnt(2)\n\tv_cmpx_eq_u32 %[tag], v21\n\tv_cmpx_eq_u32 %[tag], v23\n\tv_cmpx_eq_u32 %[tag], v25\n\tv_cmpx_eq_u32 %[tag], v27\n\t"
                     "s_cmp_eq_u64 exec, -1\n\ts_mov_b64 exec, -1\n\ts_cbranch_scc1 3f\n\t" POLL_A
                     "s_waitcnt vmcnt(2)\n\tv_cmpx_eq_u32 %[tag], v29\n\tv_cmpx_eq_u32 %[tag], v31\n\tv_cmpx_eq_u32 %[tag], v33\n\tv_cmpx_eq_u32 %[tag], v35\n\t"
                     "s_cmp_eq_u64 exec, -1\n\ts_mov_b64 exec, -1\n\ts_cbranch_scc1 2f\n\t" POLL_B
                     "s_sub_u32 %[c], %[c], 1\n\ts_cbranch_scc0 1b\n\ts_waitcnt vmcnt(0)\n"
                     "2:\n\tv_mov_b32 v36, v28\n\tv_mov_b32 v37, v30\n\tv_mov_b32 v38, v32\n\tv_mov_b32 v39, v34\n\ts_branch 4f\n"
                     "3:\n\tv_mov_b32 v36, v20\n\tv_mov_b32 v37, v22\n\tv_mov_b32 v38, v24\n\tv_mov_b32 v39, v26\n"
                     "4:"
                     : "=&{v[36:39]}"(qa), [c] "=&s"(c_) : [ro] "v"(ro), [r2] "v"(ro2), [ws] "s"(p.ws), [tag] "s"(tag)
                     : "memory", "scc", "vcc", "v20", "v21", "v22", "v23", "v24", "v25", "v26", "v27", "v28", "v29", "v30", "v31", "v32", "v33", "v34", "v35");
        asm volatile("" : "=&{v[20:23]}"(qb));
        v4u qc, qd, qe;
        asm volatile("" : "=&{v[24:27]}"(qc), "=&{v[28:31]}"(qd), "=&{v[32:35]}"(qe));
        STAMP(2);
        u32 rank, tau, cnt;
        u64 m0_, m1_, m2_, m3_;
        {
            u32 v, nv; v4u q4;
            asm volatile("ds_max_u32 %0, %1\n\ts_waitcnt lgkmcnt(0)\n\ts_barrier" :: "v"(ga), "v"(max(max(qa.x, qa.y), max(qa.z, qa.w))) : "memory");
#define RANK16(off) asm volatile("ds_read_b32 %[v], %[gv] offset:" #off "\n\tds_read_b128 v[40:43], %[gq] offset:" #off "\n\tds_read_b32 %[n], %[z] offset:64\n\tv_mov_b32 %[r], 0\n\ts_waitcnt lgkmcnt(0)\n\t" \
                         "v_cmp_gt_u32 %[a], v40, %[v]\n\tv_cmp_gt_u32 %[b], v41, %[v]\n\tv_cmp_gt_u32 %[c], v42, %[v]\n\tv_cmp_gt_u32 %[d], v43, %[v]\n\t" \
                         "v_addc_co_u32 %[r], vcc, 0, %[r], %[a]\n\tv_addc_co_u32 %[r], vcc, 0, %[r], %[b]\n\tv_addc_co_u32 %[r], vcc, 0, %[r], %[c]\n\tv_addc_co_u32 %[r], vcc, 0, %[r], %[d]\n\ts_nop 1\n\t" \
                         "v_add_u32_dpp %[r], %[r], %[r] quad_perm:[1,0,3,2] row_mask:0xf bank_mask:0xf\n\ts_nop 1\n\tv_add_u32_dpp %[r], %[r], %[r] quad_perm:[2,3,0,1] row_mask:0xf bank_mask:0xf\n\ts_nop 0" \
                         : [v] "=&v"(v), [r] "=&v"(rank), [n] "=&v"(nv), "=&{v[40:43]}"(q4), [a] "=&s"(m0_), [b] "=&s"(m1_), [c] "=&s"(m2_), [d] "=&s"(m3_) : [gv] "v"(gv), [gq] "v"(gq_), [z] "v"(zero) : "memory", "vcc")
            RANK16(0);
            tau = (u32)__builtin_amdgcn_readlane((int)v, (int)__builtin_ctzll(__ballot(rank == 7)));
            u32 pos;
            asm volatile("s_nop 1\n\tv_mov_b32 %[n], 0\n\tv_cmp_le_u32 %[a], %[t], v36\n\tv_cmp_le_u32 %[b], %[t], v37\n\tv_cmp_le_u32 %[c], %[t], v38\n\tv_cmp_le_u32 %[d], %[t], v39\n\t"
                         "v_addc_co_u32 %[n], vcc, 0, %[n], %[a]\n\tv_addc_co_u32 %[n], vcc, 0, %[n], %[b]\n\tv_addc_co_u32 %[n], vcc, 0, %[n], %[c]\n\tv_addc_co_u32 %[n], vcc, 0, %[n], %[d]\n\t"
                         "v_lshlrev_b32 %[n], 2, %[n]\n\tv_cmpx_ne_u32 0, %[n]\n\tds_add_rtn_u32 %[p], %[z], %[n] offset:64\n\ts_waitcnt lgkmcnt(0)\n\t"
                         "s_mov_b64 exec, %[a]\n\tds_write_b32 %[p], v36 offset:256\n\tv_add_u32 %[p], 4, %[p]\n\t"
                         "s_mov_b64 exec, %[b]\n\tds_write_b32 %[p], v37 offset:256\n\tv_add_u32 %[p], 4, %[p]\n\t"
                         "s_mov_b64 exec, %[c]\n\tds_write_b32 %[p], v38 offset:256\n\tv_add_u32 %[p], 4, %[p]\n\t"
                         "s_mov_b64 exec, %[d]\n\tds_write_b32 %[p], v39 offset:256\n\t"
                         "s_mov_b64 exec, -1\n\ts_waitcnt lgkmcnt(0)\n\ts_barrier"
                         : [n] "=&v"(cnt), [p] "=&v"(pos), [a] "=&s"(m0_), [b] "=&s"(m1_), [c] "=&s"(m2_), [d] "=&s"(m3_) : [t] "s"(tau), [z] "v"(zero), "{v[36:39]}"(qa) : "memory", "vcc");
            if (wave == 0) {
                RANK16(256);
                const int nc = __builtin_amdgcn_readfirstlane((int)nv) >> 2;
                if (__builtin_expect(nc > 16, 0)) {
                    v = (u32)__shfl((int)top8_d(smem, lane, nc), (lane >> 2) & 7);
                    rank = lane >> 2;
                }
                const u64 m8 = __ballot(rank < 8);
                asm volatile("s_and_b64 exec, %[m], %[k2]\n\tglobal_store_dwordx2 %[so], %[d], %[ws] sc1\n\ts_mov_b64 exec, %[m]\n\ts_mov_b32 m0, 0x8000\n\ts_nop 0\n\t"
                             "global_load_lds_dword %[xo], %[x]\n\tglobal_load_lds_dword %[xo], %[x] offset:512\n\ts_and_b64 exec, %[m], %[k1]\n\tglobal_load_lds_dword %[xo], %[x] offset:1024\n\ts_mov_b64 exec, -1"
                             :: [m] "s"(m8), [k2] "s"(0x3333333333333333ull), [k1] "s"(0x1111111111111111ull), [so] "v"(so + rank * 8), [d] "v"((v2u){v, tag}), [ws] "s"(p.ws),
                                [xo] "v"(xo + (~v & 2047u) * 1152u), [x] "s"(p.x) : "memory", "scc");
                asm volatile("s_mov_b64 exec, 1\n\tglobal_store_dwordx2 %0, %1, %2 sc1\n\ts_mov_b64 exec, -1" :: "v"(OFF_CI + (272 + xcd) * 8), "v"((v2u){tag, tag}), "s"(p.ws) : "memory");
            }
        }
        STAMP(3);
        asm volatile("s_waitcnt vmcnt(0)" :: "v"(qb), "v"(qc), "v"(qd), "v"(qe) : "memory");
        if (xcd == 0) {
            const u32 cio = OFF_CI + (tid < 140 ? tid : 0) * 16;
            for (u32 spins = 0;; ++spins) {
                REREAD();
                const v4u q2 = __builtin_amdgcn_raw_buffer_load_b128(WSR(), cio, 0, 16);
                if (__all((q2.y == tag) & (q2.w == tag)) || spins > SPIN_CAP) break;
                __builtin_amdgcn_s_sleep(16);
            }
            BAR();
            if (tid == 0) __hip_atomic_store((GAS u32*)p.ws, tag, RLX_AGENT);
        }
        ROLE_END();
    }
    else
        {
            ROLE_BEGIN(3);
            const u32 l16 = lane * 16;
            const char* dsrc = (const char*)(p.wd + (size_t)(64 * w + 32 * (wave - 6)) * D);
            const u32 ddst = (u32)(uintptr_t)(smem + FL_WD) + (wave - 6) * 32 * WDP;
            u32 tagv;
            asm volatile("global_load_dword %0, %1, %2 sc1" : "=v"(tagv) : "v"(0u), "s"(p.ws) : "memory");
            u32 dof[37];
#pragma unroll
            for (int j = 0; j < 37; ++j) {
                const u32 r0 = (64 * j) / 74, c = 64 * j - 74 * r0 + lane;
                dof[j] = c >= 74u ? (r0 + 1) * 1152u + (c - 74u) * 16u : r0 * 1152u + min(c, 71u) * 16u;
                asm volatile("" : "+v"(dof[j]));
            }
#define LD_WD(j) asm volatile("s_mov_b32 m0, %2\n\ts_nop 0\n\tglobal_load_lds_dwordx4 %0, %1" WNT :: "v"(dof[j]), "s"(dsrc), "s"(ddst + (j) * 1024) : "memory")
            asm volatile("s_waitcnt vmcnt(0)" : "+v"(tagv));
            const u32 tg = __builtin_amdgcn_readfirstlane(tagv) + 1u;
            *(LAS v4u*)(smem + FL_Z + min(l16, 256u)) = (v4u){lane >= 16 ? tg : 0u, 0u, 0u, 0u};
            asm volatile("s_mov_b64 exec, 1\n\tglobal_store_dwordx2 %0, %1, %2 sc1\n\ts_mov_b64 exec, -1" :: "v"(OFF_CI + (128 + 2 * w + (wave - 6)) * 8), "v"((v2u){tg, tg}), "s"(p.ws) : "memory");
            if (FDELAY) __builtin_amdgcn_s_sleep(FDELAY);
            v2u sel;
            u32 c_;
            asm volatile("s_mov_b32 %[c], 0x80000\n1:\n\tglobal_load_dwordx2 v[10:11], %[so], %[ws] sc1\n\ts_waitcnt vmcnt(0)\n\tv_cmp_ne_u32 vcc, %[tag], v11\n\ts_cbranch_vccz 2f\n\ts_sub_u32 %[c], %[c], 1\n\ts_cbranch_scc0 1b\n2:"
                         : "=&{v[10:11]}"(sel), [c] "=&s"(c_) : [so] "v"(OFF_SEL + xcd * 256 + (4 * (wave - 6) + (lane & 3)) * 8), [ws] "s"(p.ws), [tag] "s"(tg) : "memory", "scc", "vcc");
            STAMP(3);
#pragma unroll
            for (int k = 0; k < 4; ++k) {
                const u32 t = ~(u32)__builtin_amdgcn_readlane((int)sel.x, k) & 2047u;
                const char* xr = (const char*)p.x + t * (D * 2);
                asm volatile("s_mov_b32 m0, %2\n\ts_nop 0\n\tglobal_load_lds_dwordx4 %0, %1\n\ts_mov_b64 exec, 0xff\n\tglobal_load_lds_dwordx4 %0, %1 offset:1024\n\ts_mov_b64 exec, -1"
                             :: "v"(l16), "s"(xr), "s"((u32)(uintptr_t)(smem + FL_XS) + ((wave - 6) * 4 + k) * XSP) : "memory");
            }
#pragma unroll
            for (int j = 0; j < K2; ++j) LD_WD(j);
            asm volatile("s_waitcnt vmcnt(%0) lgkmcnt(0)\n\ts_barrier" :: "n"(K2) : "memory");
            STAMP(10);
#pragma unroll
            for (int j = K2; j < 37; ++j) LD_WD(j);
            STAMP(11);
            asm volatile("s_waitcnt vmcnt(0)" ::: "memory");
            STAMP(13);
            __builtin_amdgcn_s_barrier();
            ROLE_END();
        }
    } else
    {
        ROLE_BEGIN(5);
        if (XT) {
            const u32 id = ((slot - 28) + 4 * wave) * 64 + lane, dst = (u32)(uintptr_t)(smem + wave * 256), lo = (id & 1) ? 1024u : xcd * 128u;
            if (XT & 2) asm volatile("s_mov_b32 m0, %2\n\ts_nop 0\n\tglobal_load_lds_dword %0, %1" :: "v"((((id >> 3) & 255) * 4 + 2 + ((id >> 1) & 1)) * 1152u + lo), "s"(p.x), "s"(dst) : "memory");
            asm volatile("s_mov_b32 m0, %2\n\ts_nop 0\n\tglobal_load_lds_dword %0, %1" :: "v"((1024 + (id >> 1)) * 1152u + lo), "s"(p.x), "s"(dst) : "memory");
        }
        if (FDELAY) __builtin_amdgcn_s_sleep(FDELAY);
        _Pragma("unroll 1") for (int e = (slot - 28) + 4 * wave; touch<0>(p, xcd, e, lane, (u32)(uintptr_t)(smem + wave * 256)); e += 32) {}
        STAMP(11);
        asm volatile("s_waitcnt vmcnt(0)" ::: "memory");
        STAMP(6);
        ROLE_END();
    }
    } else
        {
            ROLE_BEGIN(2);
            const int n = lane & 15, q = lane >> 4;
            v4u gu[3][8];
            if (FDELAY) __builtin_amdgcn_s_sleep(FDELAY);
            const u16* wsrc = (n < 8 ? p.wg : p.wu) + 64 * w + 8 * (n & 7) + (size_t)(96 * wave + 8 * q) * F;
#define LD_GU(s) _Pragma("unroll") for (int j = 0; j < 8; ++j) asm volatile("global_load_dwordx4 %0, %1, off" WNT : "=v"(gu[s][j]) : "v"(wsrc + (size_t)(32 * s + j) * F))
            LD_GU(0);
            LD_GU(1);
            LD_GU(2);
            STAMP(11);
            bf16x8 guf[8];
            auto tr88 = [&](const v4u (&in)[8], bf16x8 (&o)[8]) __attribute__((always_inline)) {
#pragma unroll
                for (int pp = 0; pp < 4; ++pp) {
                    v4u lo, hi;
#pragma unroll
                    for (int jj = 0; jj < 4; ++jj) {
                        lo[jj] = __builtin_amdgcn_perm(in[2 * jj + 1][pp], in[2 * jj][pp], 0x05040100u);
                        hi[jj] = __builtin_amdgcn_perm(in[2 * jj + 1][pp], in[2 * jj][pp], 0x07060302u);
                    }
                    o[2 * pp] = __builtin_bit_cast(bf16x8, lo); o[2 * pp + 1] = __builtin_bit_cast(bf16x8, hi);
                }
#pragma unroll
                for (int i = 0; i < 8; ++i) asm volatile("" : "+v"(o[i]));
            };
#define WAIT_GU(s, N) asm volatile("s_waitcnt vmcnt(%8)" : "+v"(gu[s][0]), "+v"(gu[s][1]), "+v"(gu[s][2]), "+v"(gu[s][3]), "+v"(gu[s][4]), "+v"(gu[s][5]), "+v"(gu[s][6]), "+v"(gu[s][7]) : "n"(N))
            LAS char* xrow = smem + FL_XS + (n & 7) * XSP + (96 * wave + 8 * q) * 2;
            LAS char* xa = n < 8 ? xrow : smem + FL_Z + q * 16;
            LAS char* xb = n < 8 ? smem + FL_Z + q * 16 : xrow;
            LAS char* ta = smem + FL_WD + (lane >> 2) * WDP + (lane & 3) * 8 + wave * 64;
            LAS char* rp = smem + FL_RED + wave * 4096 + (q & 1) * 2048 + (q >> 1) * 512 + n * 8;
            LAS char* rp2 = smem + FL_RED + wave * 4096 + (q & 1) * 2048 + (q >> 1) * 512 + (n ^ 8) * 8;
            const int sl_ = (lane >> 3) * 16 + ((lane & 7) ^ ((lane >> 1) & 8)), hc = 8 * (lane & 7) + (lane >> 3);
            LAS char* ra = smem + FL_RED + (wave & 3) * 1024 + sl_ * 8;
            LAS char* ru = smem + FL_RED + (wave & 3) * 1024 + (sl_ ^ 8) * 8;
            LAS char* hw_ = smem + FL_H + (wave & 3) * 288 + ((hc & 35) | ((hc & 12) << 1) | ((hc & 16) >> 2)) * 2;
            const u32 obase = OFF_PART + (4 * (2 * wave + (n >> 3)) + q) * PP + hline(p.ws, w) + (n & 7) * 16;
            u32 obase_ = obase;
            LAS char* hp = smem + FL_H + (n & 7) * 144 + q * 16;
            asm volatile("" : "+v"(xa), "+v"(xb), "+v"(ta), "+v"(rp), "+v"(obase_), "+v"(hp), "+v"(ra), "+v"(hw_), "+v"(rp2), "+v"(ru));
            bf16x8 g0[8], g1[8];
            WAIT_GU(0, 16);
            STAMP(5);
            tr88(gu[0], g0);
            WAIT_GU(1, 8);
            tr88(gu[1], g1);
            STAMP(12);
            WAIT_GU(2, 0);
            STAMP(6);
            tr88(gu[2], guf);
            asm volatile("s_barrier" ::: "memory");
            bf16x8 xf[6];
#pragma unroll
            for (int s = 0; s < 3; ++s) { xf[2 * s] = *(LAS bf16x8*)(xa + s * 64); xf[2 * s + 1] = *(LAS bf16x8*)(xb + s * 64); }
            const u32 tag = *(LAS u32*)(smem + FL_TAG);
            SBAR();
            STAMP(1);
            STAMP(2);
            v4f ha[4];
#pragma unroll
            for (int i = 0; i < 24; ++i) {
                const int f = (i & 3) + 4 * ((i >> 2) & 1);
                ha[i & 3] = MF(xf[i >> 2], i < 8 ? g0[f] : (i < 16 ? g1[f] : guf[f]), i < 4 ? ((v4f){0.f, 0.f, 0.f, 0.f}) : ha[i & 3]);
            }
            STAMP(4);
#pragma unroll
            for (int r = 0; r < 2; ++r)
#pragma unroll
                for (int i = 0; i < 4; ++i) *(LAS v2f*)((i & 2 ? rp2 : rp) + r * 1024 + i * 128) = (v2f){ha[i][2 * r], ha[i][2 * r + 1]};
            STAMP(7);
            BAR();
            s16x4 a[6][4];
            if (wave < 4) {
                v2f pa[6], pu[6];
#pragma unroll
                for (int ww = 0; ww < 6; ++ww) pa[ww] = *(volatile LAS v2f*)(ra + ww * 4096);
#pragma unroll
                for (int ww = 0; ww < 6; ++ww) pu[ww] = *(volatile LAS v2f*)(ru + ww * 4096);
                SBAR();
                const v2f av = ((pa[0] + pa[1]) + (pa[2] + pa[3])) + (pa[4] + pa[5]), uv = ((pu[0] + pu[1]) + (pu[2] + pu[3])) + (pu[4] + pu[5]);
                const u32 ab = cvtpk(av.x, av.y), ub = cvtpk(uv.x, uv.y);
                const u32 sl = cvtpk(bflo(ab) * sigmoidf_(bflo(ab)), bfhi(ab) * sigmoidf_(bfhi(ab)));
                const u32 hh = cvtpk(bflo(sl) * bflo(ub), bfhi(sl) * bfhi(ub));
                *(LAS u16*)(hw_) = (u16)hh; *(LAS u16*)(hw_ + 144) = (u16)(hh >> 16);
                asm volatile("s_waitcnt lgkmcnt(0)" ::: "memory");
            }
            STAMP(8);
#pragma unroll
            for (int k = 0; k < 6; ++k) {
                const int ko = (k >> 1) * 384 + (k & 1) * 32;
                a[k][0] = __builtin_amdgcn_ds_read_tr16_b64_v4i16((LAS s16x4*)(ta + ko));
                a[k][1] = __builtin_amdgcn_ds_read_tr16_b64_v4i16((LAS s16x4*)(ta + ko + 16 * WDP));
                a[k][2] = __builtin_amdgcn_ds_read_tr16_b64_v4i16((LAS s16x4*)(ta + ko + 32 * WDP));
                a[k][3] = __builtin_amdgcn_ds_read_tr16_b64_v4i16((LAS s16x4*)(ta + ko + 48 * WDP));
            }
            asm volatile("s_barrier" ::: "memory");
            STAMP(13);
            const bf16x8 h0 = *(LAS bf16x8*)(hp);
            const bf16x8 h1 = *(LAS bf16x8*)(hp + 64);
            v4f ya[6];
#pragma unroll
            for (int k = 0; k < 6; ++k) ya[k] = __builtin_amdgcn_mfma_f32_16x16x32_bf16(__builtin_shufflevector(a[k][0], a[k][1], 0, 1, 2, 3, 4, 5, 6, 7), h0, (v4f){0.f, 0.f, 0.f, 0.f}, 0, 0, 0);
#pragma unroll
            for (int k = 0; k < 6; ++k) ya[k] = __builtin_amdgcn_mfma_f32_16x16x32_bf16(__builtin_shufflevector(a[k][2], a[k][3], 0, 1, 2, 3, 4, 5, 6, 7), h1, ya[k], 0, 0, 0);
#pragma unroll
            for (int k = 0; k < 3; ++k) {
                const v4f v = n < 8 ? ya[2 * k] : ya[2 * k + 1];
                __builtin_amdgcn_raw_buffer_store_b128((v4u){cvtpk(v[0], v[1]), tag, cvtpk(v[2], v[3]), tag}, WSR(), obase_ + k * (48 * PP), 0, 16);
            }
            STAMP(9);
            ROLE_END();
        }
    } else {
    if (wave >= 4)
    {
        ROLE_BEGIN(1);
        const int hw = wave - 4, ht = tid - 256;
        const v4u z4 = {0, 0, 0, 0};
        const int sub = lane & 15;
        v4u xr[5];
        u32 tagv;
        const int rrow = 16 * (cw & 127) + 4 * hw;
        {
            const u32 ro = (u32)((rrow + (lane >> 4)) * D + sub * 8) * 2, ro4 = ro - (sub & 8) * 16;
#pragma unroll
            for (int j = 0; j < 4; ++j) asm volatile("global_load_dwordx4 %0, %1, %2 offset:%3" : "=v"(xr[j]) : "v"(ro), "s"(p.x), "n"(j * 256));
            asm volatile("global_load_dwordx4 %0, %1, %2 offset:1024" : "=v"(xr[4]) : "v"(ro4), "s"(p.x));
            SBAR();
            const u32 dst = (u32)(uintptr_t)(smem + LDS_RW) + hw * 3072, l16 = lane * 16;
            asm volatile("s_mov_b32 m0, %5\n\tglobal_load_dword %0, %1, %2 sc1\n\tglobal_load_lds_dwordx4 %3, %4" : "=&v"(tagv) : "v"(0u), "s"(p.ws), "v"(l16), "s"(p.wr), "s"(dst) : "memory");
            glds16(lane < 8 ? p.wr + 512 + lane * 8 : p.gw + (lane - 8) * 8, dst + 1024, false);
            asm volatile("s_mov_b32 m0, %2\n\ts_nop 0\n\tglobal_load_lds_dwordx4 %0, %1" :: "v"(896 + sub * 16), "s"(p.gw), "s"(dst + 2048) : "memory");
        }
        __builtin_amdgcn_s_barrier();
        STAMP(1);
        if (XPF) {
            const int id = min((slot * 4 + hw) * 64 + lane, 4095);
            u32 keep;
            asm volatile("s_mov_b32 %0, m0\n\ts_mov_b32 m0, %3\n\ts_nop 0\n\tglobal_load_lds_dword %1, %2\n\ts_mov_b32 m0, %0" : "=&s"(keep) : "v"((u32)((id >> 1) * 1152 + ((id & 1) ? 1024 : xcd * 128))), "s"(p.x), "s"((u32)(uintptr_t)(smem + LDS_DUMMY)) : "memory");
        }
        v2u uu[8];
        const u32 uo0 = (u32)((256 * hw + 4 * lane) * D + chbase) * 2, uo1 = uo0 + 1024 * D * 2;
#define LD_U(i) asm volatile("global_load_dwordx2 %0, %1, %2 offset:%3" : "=v"(uu[i]) : "v"((i) < 4 ? uo0 : uo1), "s"(p.x), "n"(((i) & 3) * D * 2))
#pragma unroll
        for (int i = 0; i < NB1; ++i) LD_U(i);
        *(LAS v4u*)(smem + LDS_TAP + ht * 16) = z4; *(LAS v4u*)(smem + LDS_TAP + 4096 + ht * 16) = z4;
        *(LAS v4u*)(smem + LDS_TAP + 8192 + ht * 16 + (ht >> 7) * (LDS_U - LDS_TAP - 8192 + ((ht >> 5) & 3) * 4608 + (ht & 31) * 16 - ht * 16)) = z4;
        u32 ctl = (u32)(uintptr_t)(smem + LDS_CTL), one = 1u, cty = ctl + 64 * hw + (lane & 3) * 16;
        const u32 late = (u32)((127 - cw) >> 31), pa_ = OFF_RG + hline(p.ws, cw & 127) + hw * 32, pk_ = pa_ ^ ((pa_ ^ (OFF_CI + ((cw - 128) * 4 + hw) * 16)) & late);
        u32 po = pk_ + (lane >> 1) + (sub != 0) * (OFF_G + hline(p.ws, ((lane - 1) & 3) * 64 + ((cw & 127) >> 1)) + ((4 * cw + hw) & 7) * 16 - pk_ - (lane >> 1));
        const u64 pmask = 1ull | ((u64)(~late & 1u) << 32) | (~late & ((1u << (1 + REPG)) - 1));
        u32 nrow = 0xffffu - (rrow + (lane >> 4));
        u32 go = OFF_G + hline(p.ws, (cw % REPG) * 64 + 8 * hw + (lane >> 3)) + (lane & 7) * 16, go2 = OFF_G + hline(p.ws, (cw % REPG) * 64 + 32 + 8 * hw + (lane >> 3)) + (lane & 7) * 16;
        LAS char* up = smem + LDS_U + (256 + 256 * hw + 4 * lane) * 2;
        LAS char* yp = smem + CH_Y + hw * 1152 + (lane + (lane >> 3)) * 16;
        LAS char* rw = smem + LDS_RW + hw * 3072 + sub * 16;
        asm volatile("" : "+v"(ctl), "+v"(one), "+v"(cty), "+v"(po), "+v"(go), "+v"(go2), "+v"(up), "+v"(yp), "+v"(rw), "+v"(nrow));
        u32 wt[3];
#pragma unroll
        for (int k = 0; k < 3; ++k) {
            const u32 i = (36 * k + slot * 2 + (hw & 1)) * 48 + min(lane, 47), kk = i / 9u, rw_ = kk / 9u;
            wt[k] = ((xcd * 9u + kk - 9u * rw_) * 64u + (rw_ >> 1) + 32u * (rw_ & 1)) * 1152u + (i - 9u * kk) * 128u;
            asm volatile("" : "+v"(wt[k]));
        }
        BAR();
        __builtin_amdgcn_s_setprio(3);
        u32 tag;
        {
            asm volatile("s_waitcnt vmcnt(%6)" : "+v"(xr[0]), "+v"(xr[1]), "+v"(xr[2]), "+v"(xr[3]), "+v"(xr[4]), "+v"(tagv) : "n"(NB1 + XPF) : "memory");
            STAMP(2);
            tag = __builtin_amdgcn_readfirstlane(tagv) + 1u;
            if (sub >= 8) xr[4] = z4;
            float r = 0.f, s = 0.f;
#pragma unroll
            for (int j = 0; j < 5; ++j) {
                const v4u a = *(LAS v4u*)(rw + j * 256), g = *(LAS v4u*)(rw + 1152 + j * 256);
#pragma unroll
                for (int e = 0; e < 4; ++e) {
                    const u32 xe = xr[j][e], ae = a[e], ge = g[e];
                    r = __builtin_amdgcn_fdot2_f32_bf16(__builtin_bit_cast(bf16x2_t, xe), __builtin_bit_cast(bf16x2_t, ae), r, false);
                    s = __builtin_amdgcn_fdot2_f32_bf16(__builtin_bit_cast(bf16x2_t, xe), __builtin_bit_cast(bf16x2_t, ge), s, false);
                }
            }
            r = dpp_add<0x128, 0xf>(r); s = dpp_add<0x128, 0xf>(s); r = dpp_add<0x124, 0xf>(r); s = dpp_add<0x124, 0xf>(s);
            r = dpp_add<0x122, 0xf>(r); s = dpp_add<0x122, 0xf>(s); r = dpp_add<0x121, 0xf>(r); s = dpp_add<0x121, 0xf>(s);
            const u32 pay = make_payload(r, sigmoidf_(rbf(s))), kk = (pay & 0xffff0000u) | nrow;
            const u32 p0 = (u32)__builtin_amdgcn_readlane((int)pay, 0), p1 = (u32)__builtin_amdgcn_readlane((int)pay, 16), p2 = (u32)__builtin_amdgcn_readlane((int)pay, 32), p3 = (u32)__builtin_amdgcn_readlane((int)pay, 48);
            const auto sw = __builtin_amdgcn_permlane16_swap(kk, kk, false, false);
            const u32 g01 = (p0 & 0xffffu) | (p1 << 16), g23 = (p2 & 0xffffu) | (p3 << 16);
            const v4u pv = {sub ? g01 : sw[0], tag, sub ? g23 : sw[1], tag};
            asm volatile("s_mov_b64 exec, %3\n\tglobal_store_dwordx4 %0, %1, %2 sc1\n\ts_mov_b64 exec, -1\n\ts_nop 0" :: "v"(po), "v"(pv), "s"(p.ws), "s"(pmask) : "memory");
        }
        STAMP(3);
        __builtin_amdgcn_s_setprio(HPRIO);
        *(LAS u32*)(smem + LDS_CTL + C_TAG) = tag;
        LDS_ADD1(ctl, one, C_PUB);
        LWAIT(ctl, C_PUB, 4);
#pragma unroll
        for (int i = NB1; i < 4; ++i) LD_U(i);
        LDS_ADD1(ctl, one, C_PUB);
        LWAIT(ctl, C_PUB, 8);
        v4u gq[2];
#define POLL_G "global_load_dwordx4 v[100:103], %[go], %[ws] sc1\n\tglobal_load_dwordx4 v[104:107], %[go2], %[ws] sc1\n\t"
#define LD_U4 "global_load_dwordx2 %[u4], %[uo], %[x]\n\tglobal_load_dwordx2 %[u5], %[uo], %[x] offset:1152\n\tglobal_load_dwordx2 %[u6], %[uo], %[x] offset:2304\n\tglobal_load_dwordx2 %[u7], %[uo], %[x] offset:3456\n\t"
        asm volatile("" : "=&{v[100:103]}"(gq[0]), "=&{v[104:107]}"(gq[1]), "=&v"(uu[4]), "=&v"(uu[5]), "=&v"(uu[6]), "=&v"(uu[7]));
        asm volatile("s_cmp_ge_u32 %[hw], 2\n\ts_cbranch_scc1 1f\n\t" LD_U4 POLL_G "s_waitcnt vmcnt(6)\n\ts_branch 2f\n1:\n\ts_waitcnt vmcnt(0)\n2:"
                     : "+{v[100:103]}"(gq[0]), "+{v[104:107]}"(gq[1]), [u4] "+v"(uu[4]), [u5] "+v"(uu[5]), [u6] "+v"(uu[6]), [u7] "+v"(uu[7]), "+v"(uu[0]), "+v"(uu[1]), "+v"(uu[2]), "+v"(uu[3])
                     : [go] "v"(go), [go2] "v"(go2), [ws] "s"(p.ws), [uo] "v"(uo1), [x] "s"(p.x), [hw] "s"(hw) : "memory", "scc");
        STAMP(4);
        u32 ct2 = ctl + (hw & 2) * 2;
        auto stage = [&](int g) __attribute__((always_inline)) {
            const v2u* u4 = uu + 4 * g;
            LAS char* d = up + g * 2048;
            *(LAS v2u*)(d) = (v2u){__builtin_amdgcn_perm(u4[1].x, u4[0].x, 0x05040100u), __builtin_amdgcn_perm(u4[3].x, u4[2].x, 0x05040100u)};
            *(LAS v2u*)(d + 4608) = (v2u){__builtin_amdgcn_perm(u4[1].x, u4[0].x, 0x07060302u), __builtin_amdgcn_perm(u4[3].x, u4[2].x, 0x07060302u)};
            *(LAS v2u*)(d + 2 * 4608) = (v2u){__builtin_amdgcn_perm(u4[1].y, u4[0].y, 0x05040100u), __builtin_amdgcn_perm(u4[3].y, u4[2].y, 0x05040100u)};
            *(LAS v2u*)(d + 3 * 4608) = (v2u){__builtin_amdgcn_perm(u4[1].y, u4[0].y, 0x07060302u), __builtin_amdgcn_perm(u4[3].y, u4[2].y, 0x07060302u)};
            if (g) LDS_ADD1(ct2, one, C_ST2); else LDS_ADD1(ctl, one, C_STG);
        };
        stage(0);
        STAMP(5);
        asm volatile("s_cmp_lt_u32 %[hw], 2\n\ts_cbranch_scc1 1f\n\t" POLL_G LD_U4 "\n1:"
                     : "+{v[100:103]}"(gq[0]), "+{v[104:107]}"(gq[1]), [u4] "+v"(uu[4]), [u5] "+v"(uu[5]), [u6] "+v"(uu[6]), [u7] "+v"(uu[7])
                     : [go] "v"(go), [go2] "v"(go2), [ws] "s"(p.ws), [uo] "v"(uo1), [x] "s"(p.x), [hw] "s"(hw) : "memory", "scc");
        if (hw < 2) {
            asm volatile("s_waitcnt vmcnt(2)" : "+v"(uu[4]), "+v"(uu[5]), "+v"(uu[6]), "+v"(uu[7]));
            stage(1);
            STAMP(6);
        }
        {
            u32 c_;
            asm volatile("s_mov_b32 %[c], 0x80000\n\ts_waitcnt vmcnt(4)\n\ts_cmp_ge_u32 %[hw], 2\n\ts_cbranch_scc1 1f\n\ts_waitcnt vmcnt(0)\n1:\n\tv_cmpx_eq_u32 %[tag], v101\n\tv_cmpx_eq_u32 %[tag], v103\n\tv_cmpx_eq_u32 %[tag], v105\n\tv_cmpx_eq_u32 %[tag], v107\n\t"
                         "s_cmp_eq_u64 exec, -1\n\ts_mov_b64 exec, -1\n\ts_cbranch_scc1 2f\n\ts_sleep 4\n\t" POLL_G "s_waitcnt vmcnt(0)\n\ts_sub_u32 %[c], %[c], 1\n\ts_cbranch_scc0 1b\n2:\n\ts_nop 4"
                         : "+{v[100:103]}"(gq[0]), "+{v[104:107]}"(gq[1]), [c] "=&s"(c_) : [go] "v"(go), [go2] "v"(go2), [ws] "s"(p.ws), [tag] "s"(tag), [hw] "s"(hw) : "memory", "scc", "vcc");
        }
        STAMP(7);
        *(LAS v2u*)(smem + LDS_G + hw * 3072 + lane * 8) = (v2u){gq[0].x, gq[0].z}; *(LAS v2u*)(smem + LDS_G + hw * 3072 + 512 + lane * 8) = (v2u){gq[1].x, gq[1].z};
        if (hw < 2) {
            asm volatile("s_mov_b32 m0, %4\n\ts_nop 0\n\tglobal_load_lds_dword %0, %3\n\tglobal_load_lds_dword %1, %3\n\tglobal_load_lds_dword %2, %3" :: "v"(wt[0]), "v"(wt[1]), "v"(wt[2]), "s"(p.wd), "n"(LDS_DUMMY) : "memory");
        }
#pragma unroll
        for (int g = 0; g < 2; ++g) {
            if (g == 1 && hw >= 2) {
                asm volatile("s_waitcnt vmcnt(4)" : "+v"(uu[4]), "+v"(uu[5]), "+v"(uu[6]), "+v"(uu[7]));
                stage(1);
                STAMP(6);
            }
            const v2u* u4 = uu + 4 * g;
            const float gr[4] = {bflo(gq[g].x), bfhi(gq[g].x), bflo(gq[g].z), bfhi(gq[g].z)};
            float o[4][4];
#pragma unroll
            for (int r = 0; r < 4; ++r) {
                const float xv[4] = {bflo(u4[r].x), bfhi(u4[r].x), bflo(u4[r].y), bfhi(u4[r].y)};
#pragma unroll
                for (int c = 0; c < 4; ++c) asm("v_fma_f32 %0, -%1, %2, %2" : "=v"(o[r][c]) : "v"(gr[r]), "v"(xv[c]));
                asm volatile("" : "+v"(o[r][0]), "+v"(o[r][1]), "+v"(o[r][2]), "+v"(o[r][3]));
            }
            {
                u32 t_, c_;
                asm volatile("s_mov_b32 %1, 0x400000\n1:\n\tds_read_b32 %0, %2 offset:%3\n\ts_waitcnt lgkmcnt(0)\n\tv_cmp_eq_u32 vcc, 0, %0\n\ts_cmp_eq_u64 vcc, 0\n\ts_cbranch_scc1 2f\n\ts_sleep %4\n\ts_sub_u32 %1, %1, 1\n\ts_cbranch_scc0 1b\n2:"
                             : "=&v"(t_), "=&s"(c_) : "v"(cty), "n"(C_Y + 256 * g), "n"(YSLEEP) : "memory", "scc", "vcc");
            }
            if (g == 0) STAMP(11);
            v4f y[4];
#pragma unroll
            for (int c = 0; c < 4; ++c) y[c] = *(LAS v4f*)(yp + c * KCH + g * 4608);
            SBAR();
#define ST_O(i, v) asm volatile("global_store_dwordx2 %0, %1, %2 offset:%3" :: "v"(g ? uo1 : uo0), "v"(v), "s"(p.out), "n"((i) * D * 2) : "memory")
#pragma unroll
            for (int r = 0; r < 4; ++r) {
#pragma unroll
                for (int c = 0; c < 4; ++c) { const float yv = y[c][r]; asm("v_fmac_f32 %0, %1, %2" : "+v"(o[r][c]) : "v"(gr[r]), "v"(yv)); }
                ST_O(r, ((v2u){cvtpk(o[r][0], o[r][1]), cvtpk(o[r][2], o[r][3])}));
            }
            STAMP(8 + g);
        }
        asm volatile("s_waitcnt vmcnt(0)" ::: "memory");
        LDS_ADD1(ctl, one, C_ACK);
        STAMP(10);
        ROLE_END();
    }
    else
    {
        ROLE_BEGIN(0);
        const int ch = wave;
        const int dch = chbase + ch, pi = dch * NST + lane * 2;
        const u32 are = __builtin_nontemporal_load((const u32*)(p.Are + pi)), aim = __builtin_nontemporal_load((const u32*)(p.Aim + pi)), bre = __builtin_nontemporal_load((const u32*)(p.Bre + pi));
        const u32 bim = __builtin_nontemporal_load((const u32*)(p.Bim + pi)), cre = __builtin_nontemporal_load((const u32*)(p.Cre + pi)), cim = __builtin_nontemporal_load((const u32*)(p.Cim + pi));
        const u16 ldt = p.logdt[dch], dsk = p.dskip[dch];
        SBAR();
        __builtin_amdgcn_s_barrier();
        SBAR();
        LAS char* kb = smem + ch * KCH;
        const int r32 = lane & 31, h = lane >> 5, q = lane >> 4, n = lane & 15;
        const int ai = 4 * (r32 >> 3) + (r32 & 3), hh = (r32 >> 2) & 1;
        const u64 tsel = ai < 8 ? (hh ? KROW_1LO : KROW_0LO) : (hh ? KROW_1HI : KROW_0HI);
        const int o = (int)((tsel >> (8 * (ai & 7))) & 0xff);
        LAS char* ap = kb + o * STG_ROW + h * 16;
        LAS char* bp = kb + r32 * STG_ROW + h * 16;
        u32 sg[14];
#pragma unroll
        for (int k = 0; k < 14; ++k) { sg[k] = (u32)(uintptr_t)(kb + lane * 8 + k * 4 * STG_ROW); asm volatile("" : "+v"(sg[k])); }
        bf16x8 b5 = band(((0x1fu << 6) << n) >> (8 * q), 0x3f803f80u), b3 = band(((0x7u << 11) << n) >> (8 * q), 0x3f803f80u);
        bf16x8 b9 = band(((0x1ffu << 4) << n) >> (8 * q), 0x3f803f80u), b7 = band(((0x7fu << 5) << n) >> (8 * q), 0x3f803f80u);
        bf16x8 pat = band(((0x3u << 12) << n) >> (8 * q), 0xffffffffu);
        LAS char* k0 = kb + 32 + lane * 16;
        LAS char* kt = kb + (lane & 3) * KCOPY + (lane < 16 ? 32 + 1024 + (lane >> 2) * 16 : ((lane >> 2) & 1) * 16);
        LAS char* a0 = kb + ((lane >> 2) & 3) * KCOPY + (28 - 8 * q + 4 * (lane & 3)) * 2;
        LAS char* bb = smem + LDS_U + ch * 4608 + (16 * n + 8 * q - 16) * 2;
        LAS char* yb = kb + CH_Y + (4 * n + q + (n >> 1)) * 16;
        LAS char* tp = smem + LDS_TAP + ch * 2304 + ((n & 1) ? 13 + n - 4 * q : 96 + 14 + n - 4 * q) * 4;
        LAS char* ta = smem + LDS_TAP + ch * 2304 + (h ? 0 : 768) + 64 + 2 * r32;
        LAS char* tb = smem + LDS_TAP + ch * 2304 + 1600 + 64 * h + 2 * r32;
        u32 ctl = (u32)(uintptr_t)(smem + LDS_CTL), ctc = ctl + C_Y + ch * 16 + (lane & 15), one = 1u, so = OFF_SEL + (8 + xcd) * 256 + (lane & 7) * 8;
        u32 m01 = lane < 2 ? ~0u : 0u, m2 = lane == 2 ? ~0u : 0u, m3 = lane == 3 ? ~0u : 0u, m0_ = lane == 0 ? ~0u : 0u;
        asm volatile("" : "+v"(b5), "+v"(b3), "+v"(b9), "+v"(b7), "+v"(pat), "+v"(ap), "+v"(bp), "+v"(m01), "+v"(m2), "+v"(m3), "+v"(m0_));
        asm volatile("" : "+v"(k0), "+v"(kt), "+v"(a0), "+v"(bb), "+v"(yb), "+v"(tp), "+v"(ta), "+v"(tb), "+v"(ctl), "+v"(ctc), "+v"(one), "+v"(so));
        __builtin_amdgcn_s_barrier();
        SBAR();
        const float dt = rbf(expf(bf2f(ldt)));
        STAMP(1);
        v16f acc;
        u32 stg;
        float kscale;
        {
            const v2f a2 = (v2f){bflo(are), bfhi(are)} * dt * 1.44269504088896341f;
            const v2f brev = (v2f){bflo(aim), bfhi(aim)} * dt * 0.15915494309189535f;
            const cf2 Bc = {{dt * bflo(bre), dt * bflo(bim)}, {dt * bfhi(bre), dt * bfhi(bim)}};
            const cf2 Cc = {{bflo(cre), bflo(cim)}, {bfhi(cre), bfhi(cim)}};
            cf2 W = cmul2(Cc, Bc);
            u32 mx = max(max(__float_as_uint(W.a.x) & 0x7f800000u, __float_as_uint(W.a.y) & 0x7f800000u), max(__float_as_uint(W.b.x) & 0x7f800000u, __float_as_uint(W.b.y) & 0x7f800000u));
            mx = dpp_umax<0x111, 0xf>(mx); mx = dpp_umax<0x112, 0xf>(mx); mx = dpp_umax<0x114, 0xf>(mx); mx = dpp_umax<0x118, 0xf>(mx);
            mx = dpp_umax<0x142, 0xa>(mx); mx = dpp_umax<0x143, 0xc>(mx);
            mx = min(max((u32)__builtin_amdgcn_readlane((int)mx, 63), 0x00800000u), 0x7e800000u);
            kscale = __uint_as_float(mx);
            const float inv = __uint_as_float(0x7f000000u - mx);
            W.a *= inv; W.b *= inv;
            const cf2 E1 = cexp2(a2, brev, 1.f), E8 = cexp2(a2, brev, 8.f), E256 = cexp2(a2, brev, 256.f);
            asm volatile("s_nop 0\n\t" KGEN_ASM :: "{v[48:49]}"(W.a), "{v[50:51]}"(W.b), "{v[52:53]}"(E1.a), "{v[54:55]}"(E1.b), "{v[56:57]}"(E8.a), "{v[58:59]}"(E8.b), "{v[60:61]}"(E256.a), "{v[62:63]}"(E256.b),
                         [b0] "v"(sg[0]), [b1] "v"(sg[1]), [b2] "v"(sg[2]), [b3] "v"(sg[3]), [b4] "v"(sg[4]), [b5] "v"(sg[5]), [b6] "v"(sg[6]), [b7] "v"(sg[7]), [b8] "v"(sg[8]), [b9] "v"(sg[9]),
                         [b10] "v"(sg[10]), [b11] "v"(sg[11]), [b12] "v"(sg[12]), [b13] "v"(sg[13]) : "memory", KGEN_CLOBBER);
            STAMP(2);
            asm volatile(KMFMA_ASM : "=&{v[48:63]}"(acc), [stg] "=&v"(stg) : [ap] "v"(ap), [bp] "v"(bp), [ctl] "v"(ctl) : "memory", KMFMA_CLOBBER);
            acc *= kscale;
        }
        STAMP(3);
        acc[0] += __uint_as_float(((u32)dsk << 16) & m0_);
        u32 t78 = cvtpk(acc[7], acc[8]), t9a = cvtpk(acc[9], acc[10]);
        {
            const auto s0 = __builtin_amdgcn_permlane32_swap(cvtpk(acc[0], acc[1]), cvtpk(acc[4], acc[4]), false, false);
            const auto s1 = __builtin_amdgcn_permlane32_swap(cvtpk(acc[2], acc[3]), cvtpk(acc[5], acc[6]), false, false);
            const v4u ww = {s0[0], s1[0], s0[1], s1[1]};
            const u32 pz = (u32)__builtin_amdgcn_update_dpp(0, (int)ww.z, 0x138, 0xf, 0xf, true), pw = (u32)__builtin_amdgcn_update_dpp(0, (int)ww.w, 0x138, 0xf, 0xf, true);
            const v4u c1 = {__builtin_amdgcn_alignbit(ww.x, pw, 16), __builtin_amdgcn_alignbit(ww.y, ww.x, 16), __builtin_amdgcn_alignbit(ww.z, ww.y, 16), __builtin_amdgcn_alignbit(ww.w, ww.z, 16)};
            *(LAS v4u*)(k0) = ww;
            *(LAS v4u*)(k0 + KCOPY) = c1;
            *(LAS v4u*)(k0 + 2 * KCOPY) = (v4u){pw, ww.x, ww.y, ww.z};
            *(LAS v4u*)(k0 + 3 * KCOPY) = (v4u){__builtin_amdgcn_alignbit(pw, pz, 16), c1.x, c1.y, c1.z};
            const u32 w0 = (u32)__builtin_amdgcn_readlane((int)ww.z, 63), w1 = (u32)__builtin_amdgcn_readlane((int)ww.w, 63);
            const u32 w3 = w1 >> 16, w2 = w3 * 0x10001u, w10 = (w0 >> 16) | (w1 << 16);
            *(LAS v4u*)kt = (v4u){(w2 & m01) | (w1 & m2) | (w10 & m3), (w3 & m0_) | (w2 & ((m01 ^ m0_) | m2 | m3)), (w3 & m2) | (w2 & m3), 0u};
        }
        asm volatile("" ::: "memory");
        pat = __builtin_bit_cast(bf16x8, __builtin_bit_cast(v4u, pat) & (((u32)__builtin_amdgcn_readlane((int)t78, 63) >> 16) * 0x10001u));
#define POLL_SEL "global_load_dwordx2 v[240:241], %[so], %[ws] sc1\n\t"
        v2u sq;
        const v4f zero4 = {0.f, 0.f, 0.f, 0.f};
        if (MPRIO) __builtin_amdgcn_s_setprio(MPRIO);
        STAMP(4);
        {
            u32 s_, c_;
            asm volatile(CONV_ASM : "=&{v[240:241]}"(sq), [s] "=&s"(s_), [c] "=&s"(c_)
                         : "{v[4:7]}"(b5), "{v[8:11]}"(b3), "{v[12:15]}"(b9), "{v[16:19]}"(b7), "{v[20:23]}"(pat), [a0] "v"(a0), [bb] "v"(bb), [yb] "v"(yb), [tp] "v"(tp), [ta] "v"(ta), [tb] "v"(tb),
                           [ctl] "v"(ctl), [ctc] "v"(ctc), [one] "v"(one), [so] "v"(so), [t78] "v"(t78), [t9a] "v"(t9a), [stg] "v"(stg), [ws] "s"(p.ws) : "memory", "scc", CONV_CLOBBER);
        }
        STAMP(9);
        asm volatile("s_cmp_eq_u32 %0, 0\n\ts_cbranch_scc1 1f\n\ts_endpgm\n1:" :: "s"(wave) : "memory", "scc");
        {
            const u32 tag = __builtin_amdgcn_readfirstlane(*(LAS u32*)(smem + LDS_CTL + C_TAG));
            u32 c_;
            u32 pb[9];
#pragma unroll
            for (int k = 0; k < 9; ++k) { pb[k] = OFF_PART + cw * PP + hline(p.ws, 8 * k + (lane >> 3)) + (lane & 7) * 16; asm volatile("" : "+v"(pb[k])); }
            {
                u32 h_;
                asm volatile("s_mov_b32 %[c], 64\n1:\n\ts_load_dword %[h], %[a], 0x4 glc\n\ts_waitcnt lgkmcnt(0)\n\ts_cmp_eq_u32 %[h], %[tag]\n\ts_cbranch_scc1 2f\n\ts_sub_u32 %[c], %[c], 1\n\ts_cbranch_scc0 1b\n2:"
                             : [h] "=&s"(h_), [c] "=&s"(c_) : [a] "s"(p.ws + (OFF_PART + cw * PP + hline(p.ws, cw >> 1))), [tag] "s"(tag) : "memory", "scc");
            }
            STAMP(10);
            typedef __attribute__((ext_vector_type(32))) u32 v32u;
            v32u ra, rb; v4u rc, rd;
            asm volatile("global_load_dwordx4 v[64:67], %[pb0], %[ws] sc1\n\tglobal_load_dwordx4 v[68:71], %[pb1], %[ws] sc1\n\tglobal_load_dwordx4 v[72:75], %[pb2], %[ws] sc1\n\tglobal_load_dwordx4 v[76:79], %[pb3], %[ws] sc1\n\tglobal_load_dwordx4 v[80:83], %[pb4], %[ws] sc1\n\tglobal_load_dwordx4 v[84:87], %[pb5], %[ws] sc1\n\tglobal_load_dwordx4 v[88:91], %[pb6], %[ws] sc1\n\tglobal_load_dwordx4 v[92:95], %[pb7], %[ws] sc1\n\tglobal_load_dwordx4 v[96:99], %[pb8], %[ws] sc1\n\t" : "=&{v[64:95]}"(ra), "=&{v[96:99]}"(rc) : [pb0] "v"(pb[0]), [pb1] "v"(pb[1]), [pb2] "v"(pb[2]), [pb3] "v"(pb[3]), [pb4] "v"(pb[4]), [pb5] "v"(pb[5]), [pb6] "v"(pb[6]), [pb7] "v"(pb[7]), [pb8] "v"(pb[8]), [ws] "s"(p.ws) : "memory");
            asm volatile("s_mov_b32 %[c], 0x80000\n\ts_waitcnt vmcnt(9)\n1:\n\tv_cmpx_eq_u32 %[tag], v241\n\t"
                         "s_cmp_eq_u64 exec, -1\n\ts_mov_b64 exec, -1\n\ts_cbranch_scc1 2f\n\t" POLL_SEL "s_waitcnt vmcnt(0)\n\ts_sub_u32 %[c], %[c], 1\n\ts_cbranch_scc0 1b\n2:\n\ts_nop 4"
                         : "+{v[240:241]}"(sq), [c] "=&s"(c_) : [so] "v"(so), [ws] "s"(p.ws), [tag] "s"(tag) : "memory", "scc", "vcc");
            const int t = (int)(0xffffu - (sq.x & 0xffffu)) & 2047;
            const float rs = rbf(sigmoidf_(key_to_r(sq.x)));
            LWAIT(ctl, C_ACK, 4);
            const float g = bf2f(*(LAS u16*)(smem + LDS_G + ((t >> 8) & 3) * 3072 + (t >> 10) * 512 + (t & 255) * 2));
            const int uq = (t & 255) >> 2;
            float xv[4], av[4];
#pragma unroll
            for (int c = 0; c < 4; ++c) {
                xv[c] = bf2f(*(LAS u16*)(smem + LDS_U + c * 4608 + (256 + t) * 2));
                av[c] = g * *(LAS float*)(smem + c * KCH + CH_Y + (t >> 8) * 1152 + (uq + (uq >> 3)) * 16 + (t & 3) * 4);
            }
            const u32 e1 = n < 4 ? ((n & 1) ? 0x3f800000u : 0x00003f80u) : 0u;
            bf16x8 sel4 = __builtin_bit_cast(bf16x8, (n & 2) ? (v4u){0u, 0u, e1, e1} : (v4u){e1, e1, 0u, 0u});
            u32 oo = (u32)(t * D + chbase) * 2;
            float omg = 1.f - g;
            asm volatile("" : "+v"(sel4), "+v"(oo), "+v"(omg), "+v"(xv[0]), "+v"(xv[1]), "+v"(xv[2]), "+v"(xv[3]), "+v"(av[0]), "+v"(av[1]), "+v"(av[2]), "+v"(av[3]));
            v4f s0, s1;
            asm volatile("s_mov_b32 %[c], 0x40000\n\tglobal_load_dwordx4 v[100:103], %[pb0], %[ws] sc1\n\tglobal_load_dwordx4 v[104:107], %[pb1], %[ws] sc1\n\tglobal_load_dwordx4 v[108:111], %[pb2], %[ws] sc1\n\tglobal_load_dwordx4 v[112:115], %[pb3], %[ws] sc1\n\tglobal_load_dwordx4 v[116:119], %[pb4], %[ws] sc1\n\tglobal_load_dwordx4 v[120:123], %[pb5], %[ws] sc1\n\tglobal_load_dwordx4 v[124:127], %[pb6], %[ws] sc1\n\tglobal_load_dwordx4 v[128:131], %[pb7], %[ws] sc1\n\tglobal_load_dwordx4 v[132:135], %[pb8], %[ws] sc1\n\t"
                         "1:\n\ts_waitcnt vmcnt(9)\n\tv_cmpx_eq_u32 %[tag], v65\n\tv_cmpx_eq_u32 %[tag], v67\n\tv_cmpx_eq_u32 %[tag], v69\n\tv_cmpx_eq_u32 %[tag], v71\n\tv_cmpx_eq_u32 %[tag], v73\n\tv_cmpx_eq_u32 %[tag], v75\n\tv_cmpx_eq_u32 %[tag], v77\n\tv_cmpx_eq_u32 %[tag], v79\n\tv_cmpx_eq_u32 %[tag], v81\n\tv_cmpx_eq_u32 %[tag], v83\n\tv_cmpx_eq_u32 %[tag], v85\n\tv_cmpx_eq_u32 %[tag], v87\n\tv_cmpx_eq_u32 %[tag], v89\n\tv_cmpx_eq_u32 %[tag], v91\n\tv_cmpx_eq_u32 %[tag], v93\n\tv_cmpx_eq_u32 %[tag], v95\n\tv_cmpx_eq_u32 %[tag], v97\n\tv_cmpx_eq_u32 %[tag], v99\n\ts_cmp_eq_u64 exec, -1\n\ts_mov_b64 exec, -1\n\ts_cbranch_scc1 3f\n\tglobal_load_dwordx4 v[64:67], %[pb0], %[ws] sc1\n\tglobal_load_dwordx4 v[68:71], %[pb1], %[ws] sc1\n\tglobal_load_dwordx4 v[72:75], %[pb2], %[ws] sc1\n\tglobal_load_dwordx4 v[76:79], %[pb3], %[ws] sc1\n\tglobal_load_dwordx4 v[80:83], %[pb4], %[ws] sc1\n\tglobal_load_dwordx4 v[84:87], %[pb5], %[ws] sc1\n\tglobal_load_dwordx4 v[88:91], %[pb6], %[ws] sc1\n\tglobal_load_dwordx4 v[92:95], %[pb7], %[ws] sc1\n\tglobal_load_dwordx4 v[96:99], %[pb8], %[ws] sc1\n\t"
                         "s_waitcnt vmcnt(9)\n\tv_cmpx_eq_u32 %[tag], v101\n\tv_cmpx_eq_u32 %[tag], v103\n\tv_cmpx_eq_u32 %[tag], v105\n\tv_cmpx_eq_u32 %[tag], v107\n\tv_cmpx_eq_u32 %[tag], v109\n\tv_cmpx_eq_u32 %[tag], v111\n\tv_cmpx_eq_u32 %[tag], v113\n\tv_cmpx_eq_u32 %[tag], v115\n\tv_cmpx_eq_u32 %[tag], v117\n\tv_cmpx_eq_u32 %[tag], v119\n\tv_cmpx_eq_u32 %[tag], v121\n\tv_cmpx_eq_u32 %[tag], v123\n\tv_cmpx_eq_u32 %[tag], v125\n\tv_cmpx_eq_u32 %[tag], v127\n\tv_cmpx_eq_u32 %[tag], v129\n\tv_cmpx_eq_u32 %[tag], v131\n\tv_cmpx_eq_u32 %[tag], v133\n\tv_cmpx_eq_u32 %[tag], v135\n\ts_cmp_eq_u64 exec, -1\n\ts_mov_b64 exec, -1\n\ts_cbranch_scc1 2f\n\tglobal_load_dwordx4 v[100:103], %[pb0], %[ws] sc1\n\tglobal_load_dwordx4 v[104:107], %[pb1], %[ws] sc1\n\tglobal_load_dwordx4 v[108:111], %[pb2], %[ws] sc1\n\tglobal_load_dwordx4 v[112:115], %[pb3], %[ws] sc1\n\tglobal_load_dwordx4 v[116:119], %[pb4], %[ws] sc1\n\tglobal_load_dwordx4 v[120:123], %[pb5], %[ws] sc1\n\tglobal_load_dwordx4 v[124:127], %[pb6], %[ws] sc1\n\tglobal_load_dwordx4 v[128:131], %[pb7], %[ws] sc1\n\tglobal_load_dwordx4 v[132:135], %[pb8], %[ws] sc1\n\t"
                         "s_sub_u32 %[c], %[c], 1\n\ts_cbranch_scc0 1b\n\ts_waitcnt vmcnt(0)\n"
                         "2:\n\tv_mov_b32 v101, v104\n\tv_mov_b32 v103, v106\n\tv_mov_b32 v109, v112\n\tv_mov_b32 v111, v114\n\tv_mov_b32 v117, v120\n\tv_mov_b32 v119, v122\n\tv_mov_b32 v125, v128\n\tv_mov_b32 v127, v130\n\tv_mov_b32 v133, 0\n\tv_mov_b32 v135, 0\n\tv_mfma_f32_16x16x32_bf16 v[136:139], %[sel], v[100:103], 0\n\tv_mfma_f32_16x16x32_bf16 v[140:143], %[sel], v[108:111], 0\n\tv_mfma_f32_16x16x32_bf16 v[136:139], %[sel], v[116:119], v[136:139]\n\tv_mfma_f32_16x16x32_bf16 v[140:143], %[sel], v[124:127], v[140:143]\n\tv_mfma_f32_16x16x32_bf16 v[136:139], %[sel], v[132:135], v[136:139]\n\ts_branch 4f\n"
                         "3:\n\tv_mov_b32 v65, v68\n\tv_mov_b32 v67, v70\n\tv_mov_b32 v73, v76\n\tv_mov_b32 v75, v78\n\tv_mov_b32 v81, v84\n\tv_mov_b32 v83, v86\n\tv_mov_b32 v89, v92\n\tv_mov_b32 v91, v94\n\tv_mov_b32 v97, 0\n\tv_mov_b32 v99, 0\n\tv_mfma_f32_16x16x32_bf16 v[136:139], %[sel], v[64:67], 0\n\tv_mfma_f32_16x16x32_bf16 v[140:143], %[sel], v[72:75], 0\n\tv_mfma_f32_16x16x32_bf16 v[136:139], %[sel], v[80:83], v[136:139]\n\tv_mfma_f32_16x16x32_bf16 v[140:143], %[sel], v[88:91], v[140:143]\n\tv_mfma_f32_16x16x32_bf16 v[136:139], %[sel], v[96:99], v[136:139]\n\t\n4:\n\ts_nop 7"
                         : "+{v[64:95]}"(ra), "+{v[96:99]}"(rc), "=&{v[100:131]}"(rb), "=&{v[132:135]}"(rd), "=&{v[136:139]}"(s0), "=&{v[140:143]}"(s1), [c] "=&s"(c_)
                         : [pb0] "v"(pb[0]), [pb1] "v"(pb[1]), [pb2] "v"(pb[2]), [pb3] "v"(pb[3]), [pb4] "v"(pb[4]), [pb5] "v"(pb[5]), [pb6] "v"(pb[6]), [pb7] "v"(pb[7]), [pb8] "v"(pb[8]), [ws] "s"(p.ws), [tag] "s"(tag), [sel] "v"(sel4) : "memory", "scc", "vcc");
            STAMP(11);
            float o[4];
#pragma unroll
            for (int c = 0; c < 4; ++c) o[c] = fmaf(omg, xv[c] + rbf(rbf(dpp_add<0x128, 0xf>(s0[c] + s1[c])) * rs), av[c]);
            asm volatile("s_mov_b64 exec, 0xff\n\tglobal_store_dwordx2 %0, %1, %2\n\ts_mov_b64 exec, -1" :: "v"(oo), "v"((v2u){cvtpk(o[0], o[1]), cvtpk(o[2], o[3])}), "s"(p.out) : "memory");
            STAMP(12);
            asm volatile("s_waitcnt vmcnt(0)" :: "v"(ra), "v"(rb), "v"(rc), "v"(rd) : "memory");
        }
        ROLE_END();
    }
    }
}

extern "C" void kernel_launch(void* const* d_in, const int* in_sizes, int n_in,
                              void* d_out, int out_size, void* d_ws, size_t ws_size,
                              hipStream_t stream) {
    Params p;
    p.x = (const u16*)d_in[0]; p.Are = (const u16*)d_in[1]; p.Aim = (const u16*)d_in[2]; p.Bre = (const u16*)d_in[3];
    p.Bim = (const u16*)d_in[4]; p.Cre = (const u16*)d_in[5]; p.Cim = (const u16*)d_in[6]; p.logdt = (const u16*)d_in[7];
    p.dskip = (const u16*)d_in[8]; p.wr = (const u16*)d_in[10]; p.wg = (const u16*)d_in[11]; p.wu = (const u16*)d_in[12];
    p.wd = (const u16*)d_in[13]; p.gw = (const u16*)d_in[14]; p.out = (u16*)d_out; p.ws = (char*)d_ws;
    (void)hipFuncSetAttribute((const void*)fused_kernel, hipFuncAttributeMaxDynamicSharedMemorySize, LDS_TOTAL);
    fused_kernel<<<256, 512, LDS_TOTAL, stream>>>(p);
}
```

```cpp
#include <hip/hip_runtime.h>
#include <stdint.h>

typedef unsigned short u16;
typedef unsigned int u32;
typedef unsigned long long u64;
typedef __attribute__((ext_vector_type(4))) u32 v4u;
typedef __attribute__((ext_vector_type(2))) u32 v2u;
typedef __attribute__((ext_vector_type(2))) float v2f;
typedef __attribute__((ext_vector_type(4))) float v4f;
typedef __attribute__((ext_vector_type(16))) float v16f;
typedef __attribute__((ext_vector_type(8))) short bf16x8;
typedef __attribute__((ext_vector_type(4))) short s16x4;
typedef __attribute__((ext_vector_type(2))) __bf16 bf16x2_t;
typedef __attribute__((ext_vector_type(2))) _Float16 f16x2_t;
typedef __attribute__((ext_vector_type(8))) _Float16 f16x8;
#define LAS __attribute__((address_space(3)))
#define GAS __attribute__((address_space(1)))
#define RLX_AGENT __ATOMIC_RELAXED, __HIP_MEMORY_SCOPE_AGENT
#define SBAR() __builtin_amdgcn_sched_barrier(0)
#define BAR() asm volatile("s_waitcnt lgkmcnt(0)\n\ts_barrier" ::: "memory")
#define WSR() __builtin_amdgcn_make_buffer_rsrc(p.ws, 0, 0x7fffffff, 0x00020000)
#define REREAD() asm volatile("" ::: "memory")

#define WNT " nt"
#define CODE_KB 25
#define NB1 2
#define REPG 2
#define PFG0 480
#define FDELAY 0
#define PFR0 16
#define SELPF 0
#define SELWAIT 50
#define MPRIO 0
#define YSLEEP 1
#define HPRIO 0
#define K2 4
#define TOUCH 0
#define XT 0
#define WPF 1
#define K1 6
#define PK 2
#define WT 0
#define XPF 0

constexpr int L = 2048, D = 576, NST = 128, F = 4608;
constexpr int NFFN = 72;
constexpr u32 OFF_RG = 4096;
constexpr u32 OFF_G = OFF_RG + 20480;
constexpr u32 OFF_SEL = OFF_G + REPG * 10240;
constexpr u32 OFF_CI = OFF_SEL + 16 * 256;
constexpr u32 OFF_PART = OFF_CI + 4096;
constexpr u32 PP = 11 * 1024;
constexpr u32 SPIN_CAP = 1u << 19;

#define STAMP(i)
#define STAMP0_FIX()
#define STAMP_FLUSH()

struct Params {
    const u16 *x, *Are, *Aim, *Bre, *Bim, *Cre, *Cim, *logdt, *dskip, *wr, *wg, *wu, *wd, *gw;
    u16* out;
    char* ws;
};

__device__ __forceinline__ float bflo(u32 w) { return __uint_as_float(w << 16); }
__device__ __forceinline__ float bfhi(u32 w) { return __uint_as_float(w & 0xffff0000u); }
__device__ __forceinline__ float bf2f(u16 h) { return __uint_as_float((u32)h << 16); }
__device__ __forceinline__ u32 cvtpk(float lo, float hi) { v2f v = {lo, hi}; bf16x2_t r = __builtin_convertvector(v, bf16x2_t); return __builtin_bit_cast(u32, r); }
__device__ __forceinline__ u32 cvth(v2f v) { return __builtin_bit_cast(u32, __builtin_convertvector(v, f16x2_t)); }
__device__ __forceinline__ float rbf(float f) { return bflo(cvtpk(f, f)); }
__device__ __forceinline__ float sigmoidf_(float s) { return __builtin_amdgcn_rcpf(1.f + __expf(-s)); }

template <int CTRL, int RM> __device__ __forceinline__ float dpp_add(float v) {
    return v + __builtin_bit_cast(float, __builtin_amdgcn_update_dpp(0, __builtin_bit_cast(int, v), CTRL, RM, 0xf, true));
}
__device__ __forceinline__ float wave_sum63(float v) {
    v = dpp_add<0x111, 0xf>(v); v = dpp_add<0x112, 0xf>(v); v = dpp_add<0x114, 0xf>(v); v = dpp_add<0x118, 0xf>(v);
    v = dpp_add<0x142, 0xa>(v); v = dpp_add<0x143, 0xc>(v);
    return v;
}
template <int CTRL, int RM> __device__ __forceinline__ u32 dpp_umax(u32 v) {
    u32 o = (u32)__builtin_amdgcn_update_dpp(0, (int)v, CTRL, RM, 0xf, true);
    return v > o ? v : o;
}

typedef v2f cf;
__device__ __forceinline__ cf cmul(cf a, cf b) { cf t = a.yy * (cf){-b.y, b.x}; return __builtin_elementwise_fma(a.xx, b, t); }
__device__ __forceinline__ cf cexp_(float a2, float brev, float rho) {
    float m = __builtin_amdgcn_exp2f(a2 * rho);
    float ph = __builtin_amdgcn_fractf(brev * rho);
    cf c; c.x = m * __builtin_amdgcn_cosf(ph); c.y = m * __builtin_amdgcn_sinf(ph); return c;
}

struct cf2 { cf a, b; };
__device__ __forceinline__ cf2 cmul2(cf2 x, cf2 y) { return {cmul(x.a, y.a), cmul(x.b, y.b)}; }
__device__ __forceinline__ cf2 cexp2(v2f a2, v2f brev, float rho) { return {cexp_(a2.x, brev.x, rho), cexp_(a2.y, brev.y, rho)}; }

__device__ __forceinline__ u32 make_payload(float r, float g) {
    const u32 w = cvtpk(g, r);
    return w ^ (((u32)((int)w >> 31) & 0x7fff0000u) | 0x80000000u);
}
__device__ __forceinline__ u32 make_key(u32 pay, u32 t) { return (pay & 0xffff0000u) | (0xffffu - t); }
__device__ __forceinline__ float key_to_r(u32 k) {
    u32 hi = k & 0xffff0000u;
    return __uint_as_float((hi & 0x80000000u) ? (hi ^ 0x80000000u) : (~hi & 0xffff0000u));
}

constexpr int STG_ROW = 528;
constexpr int NA = 21;
constexpr int KCH = (NA + 32) * STG_ROW;
constexpr int KCOPY = 1344;
constexpr int CH_Y = 4 * KCOPY;
constexpr int LDS_U = 4 * KCH;
constexpr int LDS_TAP = LDS_U + 4 * 4608;
constexpr int LDS_CTL = LDS_TAP + 4 * 2304;
constexpr int C_TAG = 0, C_STG = 4, C_PUB = 8, C_ACK = 12, C_ST2 = 20, C_Y = 64;
constexpr int LDS_RW = LDS_CTL + 1024;
constexpr int LDS_G = LDS_RW;
constexpr int LDS_DUMMY = LDS_RW + 4 * 3072;
constexpr int LDS_TOTAL = LDS_DUMMY + 1024;
static_assert(CH_Y + 8 * 1152 <= KCH && LDS_TOTAL <= 160 * 1024, "lds");
constexpr int WDP = 1184;
constexpr int XSP = 1168;
constexpr int FL_XS = 144 * 1024;
constexpr int FL_H = FL_XS + 8 * XSP;
constexpr int FL_Z = FL_H + 8 * 144;
constexpr int FL_TAG = FL_Z + 256;
constexpr int LDS_ALLOC = FL_TAG + 64 > LDS_TOTAL ? FL_TAG + 64 : LDS_TOTAL;
static_assert(LDS_ALLOC <= 160 * 1024, "lds");

__device__ __forceinline__ int sum_rows(int v) {
    auto a = __builtin_amdgcn_permlane16_swap((u32)v, (u32)v, false, false);
    v = (int)(a[0] + a[1]);
    auto b = __builtin_amdgcn_permlane32_swap((u32)v, (u32)v, false, false);
    return (int)(b[0] + b[1]);
}
__device__ __forceinline__ float sum_rows_f(float v) {
    auto a = __builtin_amdgcn_permlane16_swap(__float_as_uint(v), __float_as_uint(v), false, false);
    v = __uint_as_float(a[0]) + __uint_as_float(a[1]);
    auto b = __builtin_amdgcn_permlane32_swap(__float_as_uint(v), __float_as_uint(v), false, false);
    return __uint_as_float(b[0]) + __uint_as_float(b[1]);
}
__device__ __forceinline__ u32 top8_d(LAS char* t8, int lane, int n) {
    LAS u32* cand = (LAS u32*)(t8 + 256);
    u32 res = 0;
    _Pragma("unroll 1") for (int i0 = 0; i0 < n; i0 += 64) {
        const int i = i0 + lane;
        const u32 k = i < n ? cand[i] : 0u;
        int rk = 0;
        _Pragma("unroll 1") for (int j0 = 0; j0 < n; j0 += 16) {
            v4u c[4];
#pragma unroll
            for (int e = 0; e < 4; ++e) c[e] = *(LAS v4u*)(cand + j0 + 4 * e);
#pragma unroll
            for (int e = 0; e < 16; ++e) rk += (c[e >> 2][e & 3] > k) & (j0 + e < n);
        }
        const bool hit = i < n && rk < 8;
        const u32 got = (u32)__builtin_amdgcn_ds_permute((hit ? rk : 63) * 4, (int)(hit ? k : 0u));
        if (lane < 8) res |= got;
    }
    return res;
}

__device__ __forceinline__ u32 lds_peek(LAS char* word) { return __hip_atomic_load((LAS u32*)word, __ATOMIC_RELAXED, __HIP_MEMORY_SCOPE_WORKGROUP); }
#define LWAIT_BODY "1:\n\tds_read_b32 %0, %3 offset:%4\n\ts_waitcnt lgkmcnt(0)\n\tv_readfirstlane_b32 %1, %0\n\ts_cmp_ge_u32 %1, %5\n\ts_cbranch_scc1 2f\n\ts_sleep 1\n\ts_sub_u32 %2, %2, 1\n\ts_cbranch_scc0 1b\n2:"
#define LSPIN(addr, off, want) do { u32 t_, s_, c_; asm volatile("s_mov_b32 %2, 0x800000\n1:\n\tds_read_b32 %0, %3 offset:%4\n\ts_waitcnt lgkmcnt(0)\n\tv_readfirstlane_b32 %1, %0\n\ts_cmp_ge_u32 %1, %5\n\ts_cbranch_scc1 2f\n\ts_sub_u32 %2, %2, 1\n\ts_cbranch_scc0 1b\n2:" : "=&v"(t_), "=&s"(s_), "=&s"(c_) : "v"(addr), "n"(off), "n"(want) : "memory", "scc"); } while (0)
#define LWAIT(addr, off, want) do { u32 t_, s_, c_; asm volatile("s_mov_b32 %2, 0x400000\n" LWAIT_BODY : "=&v"(t_), "=&s"(s_), "=&s"(c_) : "v"(addr), "n"(off), "n"(want) : "memory", "scc"); } while (0)
#define LWAIT_PEEK(peek, addr, off, want) do { u32 t_, s_, c_; asm volatile("s_nop 0\n\tv_readfirstlane_b32 %1, %6\n\ts_mov_b32 %2, 0x400000\n\ts_cmp_ge_u32 %1, %5\n\ts_cbranch_scc1 2f\n" LWAIT_BODY : "=&v"(t_), "=&s"(s_), "=&s"(c_) : "v"(addr), "n"(off), "n"(want), "v"(peek) : "memory", "scc"); } while (0)
#define LDS_ADD1(addr, one, off) asm volatile("s_mov_b64 exec, 1\n\tds_add_u32 %0, %1 offset:%2\n\ts_mov_b64 exec, -1" :: "v"(addr), "v"(one), "n"(off) : "memory")
#define LDS_SETB(addr, one, off) asm volatile("s_mov_b64 exec, 1\n\tds_write_b8 %0, %1 offset:%2\n\ts_mov_b64 exec, -1" :: "v"(addr), "v"(one), "n"(off) : "memory")
__device__ __forceinline__ void glds16(const void* g, u32 lds_dst, bool nt) {
    u32 keep;
    if (nt) asm volatile("s_mov_b32 %0, m0\n\ts_mov_b32 m0, %2\n\ts_nop 0\n\tglobal_load_lds_dwordx4 %1, off" WNT "\n\ts_mov_b32 m0, %0" : "=&s"(keep) : "v"(g), "s"(lds_dst) : "memory");
    else asm volatile("s_mov_b32 %0, m0\n\ts_mov_b32 m0, %2\n\ts_nop 0\n\tglobal_load_lds_dwordx4 %1, off\n\ts_mov_b32 m0, %0" : "=&s"(keep) : "v"(g), "s"(lds_dst) : "memory");
}
__device__ __forceinline__ bf16x8 band(u32 bits, u32 val) {
    v4u r;
#pragma unroll
    for (int j = 0; j < 4; ++j) r[j] = __builtin_amdgcn_perm((u32)__builtin_amdgcn_sbfe((int)bits, 2 * j + 1, 1), (u32)__builtin_amdgcn_sbfe((int)bits, 2 * j, 1), 0x05040100u) & val;
    return __builtin_bit_cast(bf16x8, r);
}

__device__ __forceinline__ u32 hline(const char* ws, u32 i) { const u32 q = i / 7u, r = i - 7u * q; return (8u * q + r + (r >= ((3u - (u32)((uintptr_t)ws >> 7)) & 7u))) * 128u; }
__device__ __forceinline__ u32 wd_line(u32 xcd, u32 i) {
    const u32 k = i / 9u, r = k / 9u;
    return ((xcd * 9u + k - 9u * r) * 64u + (r >> 1) + 32u * (r & 1)) * 1152u + (i - 9u * k) * 128u;
}
__device__ __forceinline__ bool touch(const Params& p, int xcd, int e, int lane, u32 dst) {
    const int wa = xcd * 9 + ((3 - (int)(((uintptr_t)p.wg >> 7) & 7) - xcd * 9) & 7), two = wa == xcd * 9;
    const int g0 = two ? 576 : PFG0, na = (g0 * 2) << two, nb = (576 - g0) * 18, nc = WPF * 1728;
    if (e * 64 >= na + nb + nc) return false;
    const u32 i = min(e * 64 + lane, na + nb + nc - 1);
    const u32 j = i - na, k = j / 9u;
    const bool first = i < (u32)na, last = i >= (u32)(na + nb);
    const u32 seg = first ? i >> two : k + 2 * g0, col = first ? (wa + 8 * (i & two)) * 128u : xcd * 1152u + (j - 9u * k) * 128u;
    const char* a = (const char*)(last ? p.wd : ((seg & 1) ? p.wu : p.wg)) + (last ? wd_line(xcd, j - nb) : (seg >> 1) * (F * 2) + col);
    asm volatile("s_mov_b32 m0, %1\n\ts_nop 0\n\tglobal_load_lds_dword %0, off" :: "v"(a), "s"(dst) : "memory");
    return true;
}

#define KGEN_ASM ".p2align 3\n\tv_mov_b32 v64, 0x3c00\n\tv_mov_b32 v65, 0x3c00\n\tv_pk_mul_f32 v[66:67], v[56:57], v[56:57] op_sel:[1,1] op_sel_hi:[1,0] neg_lo:[0,1]\n\tv_pk_mul_f32 v[68:69], v[58:59], v[58:59] op_sel:[1,1] op_sel_hi:[1,0] neg_lo:[0,1]\n\tv_pk_mul_f32 v[70:71], v[52:53], v[52:53] op_sel:[1,1] op_sel_hi:[1,0] neg_lo:[0,1]\n\tv_pk_mul_f32 v[72:73], v[54:55], v[54:55] op_sel:[1,1] op_sel_hi:[1,0] neg_lo:[0,1]\n\tv_pk_fma_f32 v[66:67], v[56:57], v[56:57], v[66:67] op_sel:[0,0,0] op_sel_hi:[0,1,1]\n\tv_pk_fma_f32 v[68:69], v[58:59], v[58:59], v[68:69] op_sel:[0,0,0] op_sel_hi:[0,1,1]\n\tv_pk_fma_f32 v[70:71], v[52:53], v[52:53], v[70:71] op_sel:[0,0,0] op_sel_hi:[0,1,1]\n\tv_pk_fma_f32 v[72:73], v[54:55], v[54:55], v[72:73] op_sel:[0,0,0] op_sel_hi:[0,1,1]\n\tv_cvt_pk_f16_f32 v74, v56, -v57\n\tv_cvt_pk_f16_f32 v75, v58, -v59\n\tv_cvt_pk_f16_f32 v76, v48, v49\n\tv_cvt_pk_f16_f32 v77, v50, v51\n\tv_pk_mul_f32 v[78:79], v[48:49], v[60:61] op_sel:[1,1] op_sel_hi:[1,0] neg_lo:[0,1]\n\tv_pk_mul_f32 v[80:81], v[50:51], v[62:63] op_sel:[1,1] op_sel_hi:[1,0] neg_lo:[0,1]\n\tv_pk_mul_f32 v[82:83], v[48:49], v[52:53] op_sel:[1,1] op_sel_hi:[1,0] neg_lo:[0,1]\n\tv_pk_mul_f32 v[84:85], v[50:51], v[54:55] op_sel:[1,1] op_sel_hi:[1,0] neg_lo:[0,1]\n\tds_write2_b64 %[b0], v[64:65], v[74:75] offset0:0 offset1:66\n\tv_pk_fma_f32 v[78:79], v[48:49], v[60:61], v[78:79] op_sel:[0,0,0] op_sel_hi:[0,1,1]\n\tv_pk_fma_f32 v[80:81], v[50:51], v[62:63], v[80:81] op_sel:[0,0,0] op_sel_hi:[0,1,1]\n\tv_pk_fma_f32 v[82:83], v[48:49], v[52:53], v[82:83] op_sel:[0,0,0] op_sel_hi:[0,1,1]\n\tv_pk_fma_f32 v[84:85], v[50:51], v[54:55], v[84:85] op_sel:[0,0,0] op_sel_hi:[0,1,1]\n\tv_cvt_pk_f16_f32 v86, v66, -v67\n\tv_cvt_pk_f16_f32 v87, v68, -v69\n\tv_pk_mul_f32 v[88:89], v[66:67], v[66:67] op_sel:[1,1] op_sel_hi:[1,0] neg_lo:[0,1]\n\tv_pk_mul_f32 v[90:91], v[68:69], v[68:69] op_sel:[1,1] op_sel_hi:[1,0] neg_lo:[0,1]\n\tv_pk_mul_f32 v[92:93], v[66:67], v[56:57] op_sel:[1,1] op_sel_hi:[1,0] neg_lo:[0,1]\n\tv_pk_mul_f32 v[94:95], v[68:69], v[58:59] op_sel:[1,1] op_sel_hi:[1,0] neg_lo:[0,1]\n\tv_pk_fma_f32 v[88:89], v[66:67], v[66:67], v[88:89] op_sel:[0,0,0] op_sel_hi:[0,1,1]\n\tv_pk_fma_f32 v[90:91], v[68:69], v[68:69], v[90:91] op_sel:[0,0,0] op_sel_hi:[0,1,1]\n\tv_pk_fma_f32 v[92:93], v[66:67], v[56:57], v[92:93] op_sel:[0,0,0] op_sel_hi:[0,1,1]\n\tv_pk_fma_f32 v[94:95], v[68:69], v[58:59], v[94:95] op_sel:[0,0,0] op_sel_hi:[0,1,1]\n\tv_cvt_pk_f16_f32 v64, v78, v79\n\tv_cvt_pk_f16_f32 v65, v80, v81\n\tv_cvt_pk_f16_f32 v96, v82, v83\n\tv_cvt_pk_f16_f32 v97, v84, v85\n\tv_pk_mul_f32 v[98:99], v[70:71], v[70:71] op_sel:[1,1] op_sel_hi:[1,0] neg_lo:[0,1]\n\tv_pk_mul_f32 v[100:101], v[72:73], v[72:73] op_sel:[1,1] op_sel_hi:[1,0] neg_lo:[0,1]\n\tv_pk_mul_f32 v[102:103], v[78:79], v[56:57] op_sel:[1,1] op_sel_hi:[1,0] neg_lo:[0,1]\n\tv_pk_mul_f32 v[104:105], v[80:81], v[58:59] op_sel:[1,1] op_sel_hi:[1,0] neg_lo:[0,1]\n\tds_write2_b64 %[b8], v[76:77], v[96:97] offset0:0 offset1:66\n\tv_pk_fma_f32 v[98:99], v[70:71], v[70:71], v[98:99] op_sel:[0,0,0] op_sel_hi:[0,1,1]\n\tv_pk_fma_f32 v[100:101], v[72:73], v[72:73], v[100:101] op_sel:[0,0,0] op_sel_hi:[0,1,1]\n\tv_pk_fma_f32 v[102:103], v[78:79], v[56:57], v[102:103] op_sel:[0,0,0] op_sel_hi:[0,1,1]\n\tv_pk_fma_f32 v[104:105], v[80:81], v[58:59], v[104:105] op_sel:[0,0,0] op_sel_hi:[0,1,1]\n\tv_cvt_pk_f16_f32 v106, v88, -v89\n\tv_cvt_pk_f16_f32 v107, v90, -v91\n\tv_cvt_pk_f16_f32 v108, v92, -v93\n\tv_cvt_pk_f16_f32 v109, v94, -v95\n\tv_pk_mul_f32 v[110:111], v[78:79], v[70:71] op_sel:[1,1] op_sel_hi:[1,0] neg_lo:[0,1]\n\tv_pk_mul_f32 v[112:113], v[80:81], v[72:73] op_sel:[1,1] op_sel_hi:[1,0] neg_lo:[0,1]\n\tv_pk_mul_f32 v[114:115], v[48:49], v[70:71] op_sel:[1,1] op_sel_hi:[1,0] neg_lo:[0,1]\n\tv_pk_mul_f32 v[116:117], v[50:51], v[72:73] op_sel:[1,1] op_sel_hi:[1,0] neg_lo:[0,1]\n\tds_write2_b64 %[b0], v[86:87], v[108:109] offset0:132 offset1:198\n\tv_pk_fma_f32 v[110:111], v[78:79], v[70:71], v[110:111] op_sel:[0,0,0] op_sel_hi:[0,1,1]\n\tv_pk_fma_f32 v[112:113], v[80:81], v[72:73], v[112:113] op_sel:[0,0,0] op_sel_hi:[0,1,1]\n\tv_pk_fma_f32 v[114:115], v[48:49], v[70:71], v[114:115] op_sel:[0,0,0] op_sel_hi:[0,1,1]\n\tv_pk_fma_f32 v[116:117], v[50:51], v[72:73], v[116:117] op_sel:[0,0,0] op_sel_hi:[0,1,1]\n\tv_cvt_pk_f16_f32 v76, v102, v103\n\tv_cvt_pk_f16_f32 v77, v104, v105\n\tv_pk_mul_f32 v[96:97], v[82:83], v[70:71] op_sel:[1,1] op_sel_hi:[1,0] neg_lo:[0,1]\n\tv_pk_mul_f32 v[118:119], v[84:85], v[72:73] op_sel:[1,1] op_sel_hi:[1,0] neg_lo:[0,1]\n\tv_pk_mul_f32 v[120:121], v[88:89], v[88:89] op_sel:[1,1] op_sel_hi:[1,0] neg_lo:[0,1]\n\tv_pk_mul_f32 v[122:123], v[90:91], v[90:91] op_sel:[1,1] op_sel_hi:[1,0] neg_lo:[0,1]\n\tv_pk_fma_f32 v[96:97], v[82:83], v[70:71], v[96:97] op_sel:[0,0,0] op_sel_hi:[0,1,1]\n\tv_pk_fma_f32 v[118:119], v[84:85], v[72:73], v[118:119] op_sel:[0,0,0] op_sel_hi:[0,1,1]\n\tv_pk_fma_f32 v[120:121], v[88:89], v[88:89], v[120:121] op_sel:[0,0,0] op_sel_hi:[0,1,1]\n\tv_pk_fma_f32 v[122:123], v[90:91], v[90:91], v[122:123] op_sel:[0,0,0] op_sel_hi:[0,1,1]\n\tv_cvt_pk_f16_f32 v124, v110, v111\n\tv_cvt_pk_f16_f32 v125, v112, v113\n\tv_cvt_pk_f16_f32 v126, v114, v115\n\tv_cvt_pk_f16_f32 v127, v116, v117\n\tv_pk_mul_f32 v[70:71], v[88:89], v[56:57] op_sel:[1,1] op_sel_hi:[1,0] neg_lo:[0,1]\n\tv_pk_mul_f32 v[72:73], v[90:91], v[58:59] op_sel:[1,1] op_sel_hi:[1,0] neg_lo:[0,1]\n\tv_pk_mul_f32 v[128:129], v[102:103], v[60:61] op_sel:[1,1] op_sel_hi:[1,0] neg_lo:[0,1]\n\tv_pk_mul_f32 v[130:131], v[104:105], v[62:63] op_sel:[1,1] op_sel_hi:[1,0] neg_lo:[0,1]\n\tds_write2_b64 %[b11], v[64:65], v[124:125] offset0:0 offset1:66\n\tv_pk_fma_f32 v[70:71], v[88:89], v[56:57], v[70:71] op_sel:[0,0,0] op_sel_hi:[0,1,1]\n\tv_pk_fma_f32 v[72:73], v[90:91], v[58:59], v[72:73] op_sel:[0,0,0] op_sel_hi:[0,1,1]\n\tv_pk_fma_f32 v[128:129], v[102:103], v[60:61], v[128:129] op_sel:[0,0,0] op_sel_hi:[0,1,1]\n\tv_pk_fma_f32 v[130:131], v[104:105], v[62:63], v[130:131] op_sel:[0,0,0] op_sel_hi:[0,1,1]\n\tv_cvt_pk_f16_f32 v132, v96, v97\n\tv_cvt_pk_f16_f32 v133, v118, v119\n\tv_cvt_pk_f16_f32 v134, v120, -v121\n\tv_cvt_pk_f16_f32 v135, v122, -v123\n\tv_pk_mul_f32 v[102:103], v[78:79], v[98:99] op_sel:[1,1] op_sel_hi:[1,0] neg_lo:[0,1]\n\tv_pk_mul_f32 v[104:105], v[80:81], v[100:101] op_sel:[1,1] op_sel_hi:[1,0] neg_lo:[0,1]\n\tv_pk_mul_f32 v[136:137], v[88:89], v[66:67] op_sel:[1,1] op_sel_hi:[1,0] neg_lo:[0,1]\n\tv_pk_mul_f32 v[138:139], v[90:91], v[68:69] op_sel:[1,1] op_sel_hi:[1,0] neg_lo:[0,1]\n\tds_write2_b64 %[b8], v[126:127], v[132:133] offset0:132 offset1:198\n\tv_pk_fma_f32 v[102:103], v[78:79], v[98:99], v[102:103] op_sel:[0,0,0] op_sel_hi:[0,1,1]\n\tv_pk_fma_f32 v[104:105], v[80:81], v[100:101], v[104:105] op_sel:[0,0,0] op_sel_hi:[0,1,1]\n\tv_pk_fma_f32 v[136:137], v[88:89], v[66:67], v[136:137] op_sel:[0,0,0] op_sel_hi:[0,1,1]\n\tv_pk_fma_f32 v[138:139], v[90:91], v[68:69], v[138:139] op_sel:[0,0,0] op_sel_hi:[0,1,1]\n\tv_cvt_pk_f16_f32 v64, v70, -v71\n\tv_cvt_pk_f16_f32 v65, v72, -v73\n\tv_cvt_pk_f16_f32 v124, v128, v129\n\tv_cvt_pk_f16_f32 v125, v130, v131\n\tv_pk_mul_f32 v[78:79], v[88:89], v[92:93] op_sel:[1,1] op_sel_hi:[1,0] neg_lo:[0,1]\n\tv_pk_mul_f32 v[80:81], v[90:91], v[94:95] op_sel:[1,1] op_sel_hi:[1,0] neg_lo:[0,1]\n\tv_pk_mul_f32 v[140:141], v[120:121], v[120:121] op_sel:[1,1] op_sel_hi:[1,0] neg_lo:[0,1]\n\tv_pk_mul_f32 v[142:143], v[122:123], v[122:123] op_sel:[1,1] op_sel_hi:[1,0] neg_lo:[0,1]\n\tds_write2_b64 %[b1], v[106:107], v[64:65] offset0:0 offset1:66\n\tv_pk_fma_f32 v[78:79], v[88:89], v[92:93], v[78:79] op_sel:[0,0,0] op_sel_hi:[0,1,1]\n\tv_pk_fma_f32 v[80:81], v[90:91], v[94:95], v[80:81] op_sel:[0,0,0] op_sel_hi:[0,1,1]\n\tv_pk_fma_f32 v[140:141], v[120:121], v[120:121], v[140:141] op_sel:[0,0,0] op_sel_hi:[0,1,1]\n\tv_pk_fma_f32 v[142:143], v[122:123], v[122:123], v[142:143] op_sel:[0,0,0] op_sel_hi:[0,1,1]\n\tv_cvt_pk_f16_f32 v126, v102, v103\n\tv_cvt_pk_f16_f32 v127, v104, v105\n\tv_cvt_pk_f16_f32 v132, v136, -v137\n\tv_cvt_pk_f16_f32 v133, v138, -v139\n\tv_pk_mul_f32 v[144:145], v[120:121], v[56:57] op_sel:[1,1] op_sel_hi:[1,0] neg_lo:[0,1]\n\tv_pk_mul_f32 v[146:147], v[122:123], v[58:59] op_sel:[1,1] op_sel_hi:[1,0] neg_lo:[0,1]\n\tv_pk_mul_f32 v[148:149], v[128:129], v[60:61] op_sel:[1,1] op_sel_hi:[1,0] neg_lo:[0,1]\n\tv_pk_mul_f32 v[150:151], v[130:131], v[62:63] op_sel:[1,1] op_sel_hi:[1,0] neg_lo:[0,1]\n\tds_write2_b64 %[b9], v[76:77], v[124:125] offset0:0 offset1:66\n\tv_pk_fma_f32 v[144:145], v[120:121], v[56:57], v[144:145] op_sel:[0,0,0] op_sel_hi:[0,1,1]\n\tv_pk_fma_f32 v[146:147], v[122:123], v[58:59], v[146:147] op_sel:[0,0,0] op_sel_hi:[0,1,1]\n\tv_pk_fma_f32 v[148:149], v[128:129], v[60:61], v[148:149] op_sel:[0,0,0] op_sel_hi:[0,1,1]\n\tv_pk_fma_f32 v[150:151], v[130:131], v[62:63], v[150:151] op_sel:[0,0,0] op_sel_hi:[0,1,1]\n\tv_cvt_pk_f16_f32 v152, v78, -v79\n\tv_cvt_pk_f16_f32 v153, v80, -v81\n\tv_cvt_pk_f16_f32 v154, v140, -v141\n\tv_cvt_pk_f16_f32 v155, v142, -v143\n\tv_pk_mul_f32 v[128:129], v[102:103], v[60:61] op_sel:[1,1] op_sel_hi:[1,0] neg_lo:[0,1]\n\tv_pk_mul_f32 v[130:131], v[104:105], v[62:63] op_sel:[1,1] op_sel_hi:[1,0] neg_lo:[0,1]\n\tv_pk_mul_f32 v[140:141], v[110:111], v[98:99] op_sel:[1,1] op_sel_hi:[1,0] neg_lo:[0,1]\n\tv_pk_mul_f32 v[142:143], v[112:113], v[100:101] op_sel:[1,1] op_sel_hi:[1,0] neg_lo:[0,1]\n\tds_write2_b64 %[b1], v[132:133], v[152:153] offset0:132 offset1:198\n\tv_pk_fma_f32 v[128:129], v[102:103], v[60:61], v[128:129] op_sel:[0,0,0] op_sel_hi:[0,1,1]\n\tv_pk_fma_f32 v[130:131], v[104:105], v[62:63], v[130:131] op_sel:[0,0,0] op_sel_hi:[0,1,1]\n\tv_pk_fma_f32 v[140:141], v[110:111], v[98:99], v[140:141] op_sel:[0,0,0] op_sel_hi:[0,1,1]\n\tv_pk_fma_f32 v[142:143], v[112:113], v[100:101], v[142:143] op_sel:[0,0,0] op_sel_hi:[0,1,1]\n\tv_cvt_pk_f16_f32 v76, v144, -v145\n\tv_cvt_pk_f16_f32 v77, v146, -v147\n\tv_cvt_pk_f16_f32 v124, v148, v149\n\tv_cvt_pk_f16_f32 v125, v150, v151\n\tv_pk_mul_f32 v[102:103], v[120:121], v[66:67] op_sel:[1,1] op_sel_hi:[1,0] neg_lo:[0,1]\n\tv_pk_mul_f32 v[104:105], v[122:123], v[68:69] op_sel:[1,1] op_sel_hi:[1,0] neg_lo:[0,1]\n\tv_pk_mul_f32 v[110:111], v[120:121], v[92:93] op_sel:[1,1] op_sel_hi:[1,0] neg_lo:[0,1]\n\tv_pk_mul_f32 v[112:113], v[122:123], v[94:95] op_sel:[1,1] op_sel_hi:[1,0] neg_lo:[0,1]\n\tds_write2_b64 %[b2], v[134:135], v[76:77] offset0:0 offset1:66\n\tv_pk_fma_f32 v[102:103], v[120:121], v[66:67], v[102:103] op_sel:[0,0,0] op_sel_hi:[0,1,1]\n\tv_pk_fma_f32 v[104:105], v[122:123], v[68:69], v[104:105] op_sel:[0,0,0] op_sel_hi:[0,1,1]\n\tv_pk_fma_f32 v[110:111], v[120:121], v[92:93], v[110:111] op_sel:[0,0,0] op_sel_hi:[0,1,1]\n\tv_pk_fma_f32 v[112:113], v[122:123], v[94:95], v[112:113] op_sel:[0,0,0] op_sel_hi:[0,1,1]\n\tv_cvt_pk_f16_f32 v144, v128, v129\n\tv_cvt_pk_f16_f32 v145, v130, v131\n\tv_cvt_pk_f16_f32 v146, v140, v141\n\tv_cvt_pk_f16_f32 v147, v142, v143\n\tv_pk_mul_f32 v[66:67], v[148:149], v[60:61] op_sel:[1,1] op_sel_hi:[1,0] neg_lo:[0,1]\n\tv_pk_mul_f32 v[68:69], v[150:151], v[62:63] op_sel:[1,1] op_sel_hi:[1,0] neg_lo:[0,1]\n\tv_pk_mul_f32 v[92:93], v[128:129], v[60:61] op_sel:[1,1] op_sel_hi:[1,0] neg_lo:[0,1]\n\tv_pk_mul_f32 v[94:95], v[130:131], v[62:63] op_sel:[1,1] op_sel_hi:[1,0] neg_lo:[0,1]\n\tds_write2_b64 %[b9], v[124:125], v[146:147] offset0:132 offset1:198\n\tv_pk_fma_f32 v[66:67], v[148:149], v[60:61], v[66:67] op_sel:[0,0,0] op_sel_hi:[0,1,1]\n\tv_pk_fma_f32 v[68:69], v[150:151], v[62:63], v[68:69] op_sel:[0,0,0] op_sel_hi:[0,1,1]\n\tv_pk_fma_f32 v[92:93], v[128:129], v[60:61], v[92:93] op_sel:[0,0,0] op_sel_hi:[0,1,1]\n\tv_pk_fma_f32 v[94:95], v[130:131], v[62:63], v[94:95] op_sel:[0,0,0] op_sel_hi:[0,1,1]\n\tv_cvt_pk_f16_f32 v140, v102, -v103\n\tv_cvt_pk_f16_f32 v141, v104, -v105\n\tv_cvt_pk_f16_f32 v142, v110, -v111\n\tv_cvt_pk_f16_f32 v143, v112, -v113\n\tv_pk_mul_f32 v[102:103], v[120:121], v[88:89] op_sel:[1,1] op_sel_hi:[1,0] neg_lo:[0,1]\n\tv_pk_mul_f32 v[104:105], v[122:123], v[90:91] op_sel:[1,1] op_sel_hi:[1,0] neg_lo:[0,1]\n\tv_pk_mul_f32 v[110:111], v[120:121], v[70:71] op_sel:[1,1] op_sel_hi:[1,0] neg_lo:[0,1]\n\tv_pk_mul_f32 v[112:113], v[122:123], v[72:73] op_sel:[1,1] op_sel_hi:[1,0] neg_lo:[0,1]\n\tds_write2_b64 %[b2], v[140:141], v[142:143] offset0:132 offset1:198\n\tv_pk_fma_f32 v[102:103], v[120:121], v[88:89], v[102:103] op_sel:[0,0,0] op_sel_hi:[0,1,1]\n\tv_pk_fma_f32 v[104:105], v[122:123], v[90:91], v[104:105] op_sel:[0,0,0] op_sel_hi:[0,1,1]\n\tv_pk_fma_f32 v[110:111], v[120:121], v[70:71], v[110:111] op_sel:[0,0,0] op_sel_hi:[0,1,1]\n\tv_pk_fma_f32 v[112:113], v[122:123], v[72:73], v[112:113] op_sel:[0,0,0] op_sel_hi:[0,1,1]\n\tv_cvt_pk_f16_f32 v124, v66, v67\n\tv_cvt_pk_f16_f32 v125, v68, v69\n\tv_cvt_pk_f16_f32 v128, v92, v93\n\tv_cvt_pk_f16_f32 v129, v94, v95\n\tv_pk_mul_f32 v[70:71], v[48:49], v[98:99] op_sel:[1,1] op_sel_hi:[1,0] neg_lo:[0,1]\n\tv_pk_mul_f32 v[72:73], v[50:51], v[100:101] op_sel:[1,1] op_sel_hi:[1,0] neg_lo:[0,1]\n\tv_pk_mul_f32 v[88:89], v[82:83], v[98:99] op_sel:[1,1] op_sel_hi:[1,0] neg_lo:[0,1]\n\tv_pk_mul_f32 v[90:91], v[84:85], v[100:101] op_sel:[1,1] op_sel_hi:[1,0] neg_lo:[0,1]\n\tds_write2_b64 %[b10], v[126:127], v[144:145] offset0:132 offset1:198\n\tv_pk_fma_f32 v[70:71], v[48:49], v[98:99], v[70:71] op_sel:[0,0,0] op_sel_hi:[0,1,1]\n\tv_pk_fma_f32 v[72:73], v[50:51], v[100:101], v[72:73] op_sel:[0,0,0] op_sel_hi:[0,1,1]\n\tv_pk_fma_f32 v[88:89], v[82:83], v[98:99], v[88:89] op_sel:[0,0,0] op_sel_hi:[0,1,1]\n\tv_pk_fma_f32 v[90:91], v[84:85], v[100:101], v[90:91] op_sel:[0,0,0] op_sel_hi:[0,1,1]\n\tv_cvt_pk_f16_f32 v92, v102, -v103\n\tv_cvt_pk_f16_f32 v93, v104, -v105\n\tv_cvt_pk_f16_f32 v94, v110, -v111\n\tv_cvt_pk_f16_f32 v95, v112, -v113\n\tv_pk_mul_f32 v[82:83], v[66:67], v[60:61] op_sel:[1,1] op_sel_hi:[1,0] neg_lo:[0,1]\n\tv_pk_mul_f32 v[84:85], v[68:69], v[62:63] op_sel:[1,1] op_sel_hi:[1,0] neg_lo:[0,1]\n\tv_pk_mul_f32 v[102:103], v[120:121], v[136:137] op_sel:[1,1] op_sel_hi:[1,0] neg_lo:[0,1]\n\tv_pk_mul_f32 v[104:105], v[122:123], v[138:139] op_sel:[1,1] op_sel_hi:[1,0] neg_lo:[0,1]\n\tds_write2_b64 %[b3], v[92:93], v[94:95] offset0:0 offset1:66\n\tv_pk_fma_f32 v[82:83], v[66:67], v[60:61], v[82:83] op_sel:[0,0,0] op_sel_hi:[0,1,1]\n\tv_pk_fma_f32 v[84:85], v[68:69], v[62:63], v[84:85] op_sel:[0,0,0] op_sel_hi:[0,1,1]\n\tv_pk_fma_f32 v[102:103], v[120:121], v[136:137], v[102:103] op_sel:[0,0,0] op_sel_hi:[0,1,1]\n\tv_pk_fma_f32 v[104:105], v[122:123], v[138:139], v[104:105] op_sel:[0,0,0] op_sel_hi:[0,1,1]\n\tv_cvt_pk_f16_f32 v110, v70, v71\n\tv_cvt_pk_f16_f32 v111, v72, v73\n\tv_cvt_pk_f16_f32 v112, v88, v89\n\tv_cvt_pk_f16_f32 v113, v90, v91\n\tv_pk_mul_f32 v[66:67], v[120:121], v[78:79] op_sel:[1,1] op_sel_hi:[1,0] neg_lo:[0,1]\n\tv_pk_mul_f32 v[68:69], v[122:123], v[80:81] op_sel:[1,1] op_sel_hi:[1,0] neg_lo:[0,1]\n\tv_pk_mul_f32 v[70:71], v[114:115], v[98:99] op_sel:[1,1] op_sel_hi:[1,0] neg_lo:[0,1]\n\tv_pk_mul_f32 v[72:73], v[116:117], v[100:101] op_sel:[1,1] op_sel_hi:[1,0] neg_lo:[0,1]\n\tds_write2_b64 %[b11], v[110:111], v[112:113] offset0:132 offset1:198\n\tv_pk_fma_f32 v[66:67], v[120:121], v[78:79], v[66:67] op_sel:[0,0,0] op_sel_hi:[0,1,1]\n\tv_pk_fma_f32 v[68:69], v[122:123], v[80:81], v[68:69] op_sel:[0,0,0] op_sel_hi:[0,1,1]\n\tv_pk_fma_f32 v[70:71], v[114:115], v[98:99], v[70:71] op_sel:[0,0,0] op_sel_hi:[0,1,1]\n\tv_pk_fma_f32 v[72:73], v[116:117], v[100:101], v[72:73] op_sel:[0,0,0] op_sel_hi:[0,1,1]\n\tv_cvt_pk_f16_f32 v88, v82, v83\n\tv_cvt_pk_f16_f32 v89, v84, v85\n\tv_cvt_pk_f16_f32 v90, v102, -v103\n\tv_cvt_pk_f16_f32 v91, v104, -v105\n\tv_pk_mul_f32 v[78:79], v[96:97], v[98:99] op_sel:[1,1] op_sel_hi:[1,0] neg_lo:[0,1]\n\tv_pk_mul_f32 v[80:81], v[118:119], v[100:101] op_sel:[1,1] op_sel_hi:[1,0] neg_lo:[0,1]\n\tv_pk_mul_f32 v[102:103], v[82:83], v[60:61] op_sel:[1,1] op_sel_hi:[1,0] neg_lo:[0,1]\n\tv_pk_mul_f32 v[104:105], v[84:85], v[62:63] op_sel:[1,1] op_sel_hi:[1,0] neg_lo:[0,1]\n\tds_write2_b64 %[b12], v[128:129], v[124:125] offset0:132 offset1:198\n\tv_pk_fma_f32 v[78:79], v[96:97], v[98:99], v[78:79] op_sel:[0,0,0] op_sel_hi:[0,1,1]\n\tv_pk_fma_f32 v[80:81], v[118:119], v[100:101], v[80:81] op_sel:[0,0,0] op_sel_hi:[0,1,1]\n\tv_pk_fma_f32 v[102:103], v[82:83], v[60:61], v[102:103] op_sel:[0,0,0] op_sel_hi:[0,1,1]\n\tv_pk_fma_f32 v[104:105], v[84:85], v[62:63], v[104:105] op_sel:[0,0,0] op_sel_hi:[0,1,1]\n\tv_cvt_pk_f16_f32 v110, v66, -v67\n\tv_cvt_pk_f16_f32 v111, v68, -v69\n\tv_cvt_pk_f16_f32 v112, v70, v71\n\tv_cvt_pk_f16_f32 v113, v72, v73\n\tv_pk_mul_f16 v66, v154, v74 op_sel:[1,1] op_sel_hi:[1,0] neg_lo:[0,1]\n\tv_pk_mul_f16 v67, v155, v75 op_sel:[1,1] op_sel_hi:[1,0] neg_lo:[0,1]\n\tv_pk_mul_f16 v68, v154, v86 op_sel:[1,1] op_sel_hi:[1,0] neg_lo:[0,1]\n\tv_pk_mul_f16 v69, v155, v87 op_sel:[1,1] op_sel_hi:[1,0] neg_lo:[0,1]\n\tds_write2_b64 %[b3], v[90:91], v[110:111] offset0:132 offset1:198\n\tv_pk_fma_f16 v66, v154, v74, v66 op_sel:[0,0,0] op_sel_hi:[0,1,1]\n\tv_pk_fma_f16 v67, v155, v75, v67 op_sel:[0,0,0] op_sel_hi:[0,1,1]\n\tv_pk_fma_f16 v68, v154, v86, v68 op_sel:[0,0,0] op_sel_hi:[0,1,1]\n\tv_pk_fma_f16 v69, v155, v87, v69 op_sel:[0,0,0] op_sel_hi:[0,1,1]\n\tv_cvt_pk_f16_f32 v70, v78, v79\n\tv_cvt_pk_f16_f32 v71, v80, v81\n\tv_cvt_pk_f16_f32 v72, v102, v103\n\tv_cvt_pk_f16_f32 v73, v104, v105\n\tv_pk_mul_f16 v78, v154, v108 op_sel:[1,1] op_sel_hi:[1,0] neg_lo:[0,1]\n\tv_pk_mul_f16 v79, v155, v109 op_sel:[1,1] op_sel_hi:[1,0] neg_lo:[0,1]\n\tv_pk_mul_f32 v[80:81], v[102:103], v[60:61] op_sel:[1,1] op_sel_hi:[1,0] neg_lo:[0,1]\n\tv_pk_mul_f32 v[82:83], v[104:105], v[62:63] op_sel:[1,1] op_sel_hi:[1,0] neg_lo:[0,1]\n\tds_write2_b64 %[b4], v[154:155], v[66:67] offset0:0 offset1:66\n\tv_pk_fma_f16 v78, v154, v108, v78 op_sel:[0,0,0] op_sel_hi:[0,1,1]\n\tv_pk_fma_f16 v79, v155, v109, v79 op_sel:[0,0,0] op_sel_hi:[0,1,1]\n\tv_pk_fma_f32 v[80:81], v[102:103], v[60:61], v[80:81] op_sel:[0,0,0] op_sel_hi:[0,1,1]\n\tv_pk_fma_f32 v[82:83], v[104:105], v[62:63], v[82:83] op_sel:[0,0,0] op_sel_hi:[0,1,1]\n\tv_pk_mul_f16 v74, v154, v106 op_sel:[1,1] op_sel_hi:[1,0] neg_lo:[0,1]\n\tv_pk_mul_f16 v75, v155, v107 op_sel:[1,1] op_sel_hi:[1,0] neg_lo:[0,1]\n\tv_pk_mul_f16 v84, v154, v64 op_sel:[1,1] op_sel_hi:[1,0] neg_lo:[0,1]\n\tv_pk_mul_f16 v85, v155, v65 op_sel:[1,1] op_sel_hi:[1,0] neg_lo:[0,1]\n\tds_write2_b64 %[b4], v[68:69], v[78:79] offset0:132 offset1:198\n\tv_pk_fma_f16 v74, v154, v106, v74 op_sel:[0,0,0] op_sel_hi:[0,1,1]\n\tv_pk_fma_f16 v75, v155, v107, v75 op_sel:[0,0,0] op_sel_hi:[0,1,1]\n\tv_pk_fma_f16 v84, v154, v64, v84 op_sel:[0,0,0] op_sel_hi:[0,1,1]\n\tv_pk_fma_f16 v85, v155, v65, v85 op_sel:[0,0,0] op_sel_hi:[0,1,1]\n\tv_cvt_pk_f16_f32 v66, v80, v81\n\tv_cvt_pk_f16_f32 v67, v82, v83\n\tv_pk_mul_f16 v80, v154, v132 op_sel:[1,1] op_sel_hi:[1,0] neg_lo:[0,1]\n\tv_pk_mul_f16 v81, v155, v133 op_sel:[1,1] op_sel_hi:[1,0] neg_lo:[0,1]\n\tv_pk_mul_f16 v82, v154, v152 op_sel:[1,1] op_sel_hi:[1,0] neg_lo:[0,1]\n\tv_pk_mul_f16 v83, v155, v153 op_sel:[1,1] op_sel_hi:[1,0] neg_lo:[0,1]\n\tds_write2_b64 %[b5], v[74:75], v[84:85] offset0:0 offset1:66\n\tv_pk_fma_f16 v80, v154, v132, v80 op_sel:[0,0,0] op_sel_hi:[0,1,1]\n\tv_pk_fma_f16 v81, v155, v133, v81 op_sel:[0,0,0] op_sel_hi:[0,1,1]\n\tv_pk_fma_f16 v82, v154, v152, v82 op_sel:[0,0,0] op_sel_hi:[0,1,1]\n\tv_pk_fma_f16 v83, v155, v153, v83 op_sel:[0,0,0] op_sel_hi:[0,1,1]\n\tv_pk_mul_f16 v64, v154, v134 op_sel:[1,1] op_sel_hi:[1,0] neg_lo:[0,1]\n\tv_pk_mul_f16 v65, v155, v135 op_sel:[1,1] op_sel_hi:[1,0] neg_lo:[0,1]\n\tv_pk_mul_f16 v68, v154, v76 op_sel:[1,1] op_sel_hi:[1,0] neg_lo:[0,1]\n\tv_pk_mul_f16 v69, v155, v77 op_sel:[1,1] op_sel_hi:[1,0] neg_lo:[0,1]\n\tds_write2_b64 %[b5], v[80:81], v[82:83] offset0:132 offset1:198\n\tv_pk_fma_f16 v64, v154, v134, v64 op_sel:[0,0,0] op_sel_hi:[0,1,1]\n\tv_pk_fma_f16 v65, v155, v135, v65 op_sel:[0,0,0] op_sel_hi:[0,1,1]\n\tv_pk_fma_f16 v68, v154, v76, v68 op_sel:[0,0,0] op_sel_hi:[0,1,1]\n\tv_pk_fma_f16 v69, v155, v77, v69 op_sel:[0,0,0] op_sel_hi:[0,1,1]\n\tv_pk_mul_f16 v74, v154, v140 op_sel:[1,1] op_sel_hi:[1,0] neg_lo:[0,1]\n\tv_pk_mul_f16 v75, v155, v141 op_sel:[1,1] op_sel_hi:[1,0] neg_lo:[0,1]\n\tv_pk_mul_f16 v78, v154, v142 op_sel:[1,1] op_sel_hi:[1,0] neg_lo:[0,1]\n\tv_pk_mul_f16 v79, v155, v143 op_sel:[1,1] op_sel_hi:[1,0] neg_lo:[0,1]\n\tds_write2_b64 %[b6], v[64:65], v[68:69] offset0:0 offset1:66\n\tv_pk_fma_f16 v74, v154, v140, v74 op_sel:[0,0,0] op_sel_hi:[0,1,1]\n\tv_pk_fma_f16 v75, v155, v141, v75 op_sel:[0,0,0] op_sel_hi:[0,1,1]\n\tv_pk_fma_f16 v78, v154, v142, v78 op_sel:[0,0,0] op_sel_hi:[0,1,1]\n\tv_pk_fma_f16 v79, v155, v143, v79 op_sel:[0,0,0] op_sel_hi:[0,1,1]\n\tv_pk_mul_f16 v76, v154, v92 op_sel:[1,1] op_sel_hi:[1,0] neg_lo:[0,1]\n\tv_pk_mul_f16 v77, v155, v93 op_sel:[1,1] op_sel_hi:[1,0] neg_lo:[0,1]\n\tv_pk_mul_f16 v80, v154, v94 op_sel:[1,1] op_sel_hi:[1,0] neg_lo:[0,1]\n\tv_pk_mul_f16 v81, v155, v95 op_sel:[1,1] op_sel_hi:[1,0] neg_lo:[0,1]\n\tds_write2_b64 %[b6], v[74:75], v[78:79] offset0:132 offset1:198\n\tv_pk_fma_f16 v76, v154, v92, v76 op_sel:[0,0,0] op_sel_hi:[0,1,1]\n\tv_pk_fma_f16 v77, v155, v93, v77 op_sel:[0,0,0] op_sel_hi:[0,1,1]\n\tv_pk_fma_f16 v80, v154, v94, v80 op_sel:[0,0,0] op_sel_hi:[0,1,1]\n\tv_pk_fma_f16 v81, v155, v95, v81 op_sel:[0,0,0] op_sel_hi:[0,1,1]\n\tv_pk_mul_f16 v64, v154, v90 op_sel:[1,1] op_sel_hi:[1,0] neg_lo:[0,1]\n\tv_pk_mul_f16 v65, v155, v91 op_sel:[1,1] op_sel_hi:[1,0] neg_lo:[0,1]\n\tv_pk_mul_f16 v68, v154, v110 op_sel:[1,1] op_sel_hi:[1,0] neg_lo:[0,1]\n\tv_pk_mul_f16 v69, v155, v111 op_sel:[1,1] op_sel_hi:[1,0] neg_lo:[0,1]\n\tds_write2_b64 %[b7], v[76:77], v[80:81] offset0:0 offset1:66\n\tv_pk_fma_f16 v64, v154, v90, v64 op_sel:[0,0,0] op_sel_hi:[0,1,1]\n\tv_pk_fma_f16 v65, v155, v91, v65 op_sel:[0,0,0] op_sel_hi:[0,1,1]\n\tv_pk_fma_f16 v68, v154, v110, v68 op_sel:[0,0,0] op_sel_hi:[0,1,1]\n\tv_pk_fma_f16 v69, v155, v111, v69 op_sel:[0,0,0] op_sel_hi:[0,1,1]\n\ts_nop 0\n\tds_write2_b64 %[b7], v[64:65], v[68:69] offset0:132 offset1:198\n\tds_write2_b64 %[b10], v[88:89], v[66:67] offset0:0 offset1:66\n\tds_write2_b64 %[b12], v[112:113], v[70:71] offset0:0 offset1:66\n\tds_write_b64 %[b13], v[72:73] offset:0"
#define KGEN_CLOBBER "v64", "v65", "v66", "v67", "v68", "v69", "v70", "v71", "v72", "v73", "v74", "v75", "v76", "v77", "v78", "v79", "v80", "v81", "v82", "v83", "v84", "v85", "v86", "v87", "v88", "v89", "v90", "v91", "v92", "v93", "v94", "v95", "v96", "v97", "v98", "v99", "v100", "v101", "v102", "v103", "v104", "v105", "v106", "v107", "v108", "v109", "v110", "v111", "v112", "v113", "v114", "v115", "v116", "v117", "v118", "v119", "v120", "v121", "v122", "v123", "v124", "v125", "v126", "v127", "v128", "v129", "v130", "v131", "v132", "v133", "v134", "v135", "v136", "v137", "v138", "v139", "v140", "v141", "v142", "v143", "v144", "v145", "v146", "v147", "v148", "v149", "v150", "v151", "v152", "v153", "v154", "v155"
#define KROW_0LO 0x2b2a2d2c23222120ull
#define KROW_0HI 0x202020202b343332ull
#define KROW_1LO 0x2524272a31302f2eull
#define KROW_1HI 0x2b2b2b2b20292826ull
#define KMFMA_ASM "ds_read_b128 v[64:67], %[ap] offset:0\n\tds_read_b128 v[68:71], %[bp] offset:0\n\tds_read_b128 v[72:75], %[ap] offset:32\n\tds_read_b128 v[76:79], %[bp] offset:32\n\tds_read_b128 v[80:83], %[ap] offset:64\n\tds_read_b128 v[84:87], %[bp] offset:64\n\tds_read_b128 v[88:91], %[ap] offset:96\n\tds_read_b128 v[92:95], %[bp] offset:96\n\ts_waitcnt lgkmcnt(6)\n\tv_mfma_f32_32x32x16_f16 v[48:63], v[64:67], v[68:71], 0\n\tds_read_b128 v[96:99], %[ap] offset:128\n\tds_read_b128 v[100:103], %[bp] offset:128\n\ts_waitcnt lgkmcnt(6)\n\tv_mfma_f32_32x32x16_f16 v[48:63], v[72:75], v[76:79], v[48:63]\n\tds_read_b128 v[104:107], %[ap] offset:160\n\tds_read_b128 v[108:111], %[bp] offset:160\n\ts_waitcnt lgkmcnt(6)\n\tv_mfma_f32_32x32x16_f16 v[48:63], v[80:83], v[84:87], v[48:63]\n\tds_read_b128 v[64:67], %[ap] offset:192\n\tds_read_b128 v[68:71], %[bp] offset:192\n\ts_waitcnt lgkmcnt(6)\n\tv_mfma_f32_32x32x16_f16 v[48:63], v[88:91], v[92:95], v[48:63]\n\tds_read_b128 v[72:75], %[ap] offset:224\n\tds_read_b128 v[76:79], %[bp] offset:224\n\ts_waitcnt lgkmcnt(6)\n\tv_mfma_f32_32x32x16_f16 v[48:63], v[96:99], v[100:103], v[48:63]\n\tds_read_b128 v[80:83], %[ap] offset:256\n\tds_read_b128 v[84:87], %[bp] offset:256\n\ts_waitcnt lgkmcnt(6)\n\tv_mfma_f32_32x32x16_f16 v[48:63], v[104:107], v[108:111], v[48:63]\n\tds_read_b128 v[88:91], %[ap] offset:288\n\tds_read_b128 v[92:95], %[bp] offset:288\n\ts_waitcnt lgkmcnt(6)\n\tv_mfma_f32_32x32x16_f16 v[48:63], v[64:67], v[68:71], v[48:63]\n\tds_read_b128 v[96:99], %[ap] offset:320\n\tds_read_b128 v[100:103], %[bp] offset:320\n\ts_waitcnt lgkmcnt(6)\n\tv_mfma_f32_32x32x16_f16 v[48:63], v[72:75], v[76:79], v[48:63]\n\tds_read_b128 v[104:107], %[ap] offset:352\n\tds_read_b128 v[108:111], %[bp] offset:352\n\ts_waitcnt lgkmcnt(6)\n\tv_mfma_f32_32x32x16_f16 v[48:63], v[80:83], v[84:87], v[48:63]\n\tds_read_b128 v[64:67], %[ap] offset:384\n\tds_read_b128 v[68:71], %[bp] offset:384\n\ts_waitcnt lgkmcnt(6)\n\tv_mfma_f32_32x32x16_f16 v[48:63], v[88:91], v[92:95], v[48:63]\n\tds_read_b128 v[72:75], %[ap] offset:416\n\tds_read_b128 v[76:79], %[bp] offset:416\n\ts_waitcnt lgkmcnt(6)\n\tv_mfma_f32_32x32x16_f16 v[48:63], v[96:99], v[100:103], v[48:63]\n\tds_read_b128 v[80:83], %[ap] offset:448\n\tds_read_b128 v[84:87], %[bp] offset:448\n\ts_waitcnt lgkmcnt(6)\n\tv_mfma_f32_32x32x16_f16 v[48:63], v[104:107], v[108:111], v[48:63]\n\tds_read_b128 v[88:91], %[ap] offset:480\n\tds_read_b128 v[92:95], %[bp] offset:480\n\ts_waitcnt lgkmcnt(6)\n\tv_mfma_f32_32x32x16_f16 v[48:63], v[64:67], v[68:71], v[48:63]\n\tds_read_b32 %[stg], %[ctl] offset:4\n\ts_waitcnt lgkmcnt(4)\n\tv_mfma_f32_32x32x16_f16 v[48:63], v[72:75], v[76:79], v[48:63]\n\ts_waitcnt lgkmcnt(2)\n\tv_mfma_f32_32x32x16_f16 v[48:63], v[80:83], v[84:87], v[48:63]\n\ts_waitcnt lgkmcnt(0)\n\tv_mfma_f32_32x32x16_f16 v[48:63], v[88:91], v[92:95], v[48:63]\n\ts_nop 7\n\ts_nop 3\n\ts_waitcnt lgkmcnt(0)"
#define KMFMA_CLOBBER "v64", "v65", "v66", "v67", "v68", "v69", "v70", "v71", "v72", "v73", "v74", "v75", "v76", "v77", "v78", "v79", "v80", "v81", "v82", "v83", "v84", "v85", "v86", "v87", "v88", "v89", "v90", "v91", "v92", "v93", "v94", "v95", "v96", "v97", "v98", "v99", "v100", "v101", "v102", "v103", "v104", "v105", "v106", "v107", "v108", "v109", "v110", "v111"
#define CONV_ASM ".p2align 3\n\tds_read_b64_tr_b16 v[52:53], %[a0] offset:456\n\tds_read_b64_tr_b16 v[54:55], %[a0] offset:448\n\tds_read_b64_tr_b16 v[84:85], %[a0] offset:968\n\tds_read_b64_tr_b16 v[86:87], %[a0] offset:960\n\tds_read_b64_tr_b16 v[48:49], %[a0] offset:392\n\tds_read_b64_tr_b16 v[50:51], %[a0] offset:384\n\tds_read_b64_tr_b16 v[80:81], %[a0] offset:904\n\tds_read_b64_tr_b16 v[82:83], %[a0] offset:896\n\tds_read_b64_tr_b16 v[44:45], %[a0] offset:328\n\tds_read_b64_tr_b16 v[46:47], %[a0] offset:320\n\tds_read_b64_tr_b16 v[76:77], %[a0] offset:840\n\tds_read_b64_tr_b16 v[78:79], %[a0] offset:832\n\tv_readfirstlane_b32 %[s], %[stg]\n\ts_mov_b32 %[c], 0x400000\n\ts_cmp_ge_u32 %[s], 4\n\ts_cbranch_scc1 2f\n\t1:\n\tds_read_b32 v216, %[ctl] offset:4\n\ts_waitcnt lgkmcnt(0)\n\tv_readfirstlane_b32 %[s], v216\n\ts_cmp_ge_u32 %[s], 4\n\ts_cbranch_scc1 2f\n\ts_sleep 1\n\ts_sub_u32 %[c], %[c], 1\n\ts_cbranch_scc0 1b\n\t.p2align 3\n\t2:\n\tds_read_b128 v[164:167], %[bb] offset:64\n\tds_read_b128 v[168:171], %[bb] offset:128\n\tds_read_b128 v[172:175], %[bb] offset:192\n\tds_read_b64_tr_b16 v[40:41], %[a0] offset:264\n\tds_read_b64_tr_b16 v[42:43], %[a0] offset:256\n\tds_read_b128 v[176:179], %[bb] offset:256\n\ts_waitcnt lgkmcnt(5)\n\ts_nop 0\n\tv_mfma_f32_16x16x32_bf16 v[132:135], v[52:55], v[164:167], 0\n\tds_read_b64_tr_b16 v[72:73], %[a0] offset:776\n\tds_read_b64_tr_b16 v[74:75], %[a0] offset:768\n\tds_read_b64_tr_b16 v[36:37], %[a0] offset:200\n\tv_mfma_f32_16x16x32_bf16 v[136:139], v[84:87], v[164:167], 0\n\tds_read_b64_tr_b16 v[38:39], %[a0] offset:192\n\ts_waitcnt lgkmcnt(8)\n\ts_nop 0\n\tv_mfma_f32_16x16x32_bf16 v[132:135], v[48:51], v[168:171], v[132:135]\n\tds_read_b128 v[180:183], %[bb] offset:320\n\tds_read_b64_tr_b16 v[68:69], %[a0] offset:712\n\tds_read_b64_tr_b16 v[70:71], %[a0] offset:704\n\tv_mfma_f32_16x16x32_bf16 v[136:139], v[80:83], v[168:171], v[136:139]\n\tds_read_b64_tr_b16 v[32:33], %[a0] offset:136\n\ts_waitcnt lgkmcnt(8)\n\ts_nop 0\n\tv_mfma_f32_16x16x32_bf16 v[132:135], v[44:47], v[172:175], v[132:135]\n\tds_read_b64_tr_b16 v[34:35], %[a0] offset:128\n\tds_read_b128 v[184:187], %[bb] offset:384\n\tds_read_b64_tr_b16 v[64:65], %[a0] offset:648\n\tv_mfma_f32_16x16x32_bf16 v[136:139], v[76:79], v[172:175], v[136:139]\n\tds_read_b64_tr_b16 v[66:67], %[a0] offset:640\n\tds_read_b64_tr_b16 v[28:29], %[a0] offset:72\n\tds_read_b64_tr_b16 v[30:31], %[a0] offset:64\n\tv_mfma_f32_16x16x32_bf16 v[132:135], v[40:43], v[176:179], v[132:135]\n\tds_read_b128 v[164:167], %[bb] offset:448\n\ts_waitcnt lgkmcnt(8)\n\ts_nop 0\n\tv_mfma_f32_16x16x32_bf16 v[136:139], v[72:75], v[176:179], v[136:139]\n\tds_read_b64_tr_b16 v[60:61], %[a0] offset:584\n\tds_read_b64_tr_b16 v[62:63], %[a0] offset:576\n\tds_read_b64_tr_b16 v[24:25], %[a0] offset:8\n\tv_mfma_f32_16x16x32_bf16 v[132:135], v[36:39], v[180:183], v[132:135]\n\tds_read_b64_tr_b16 v[26:27], %[a0] offset:0\n\tds_read_b128 v[168:171], %[bb] offset:512\n\tds_read_b64_tr_b16 v[56:57], %[a0] offset:520\n\tv_mfma_f32_16x16x32_bf16 v[136:139], v[68:71], v[180:183], v[136:139]\n\tds_read_b64_tr_b16 v[58:59], %[a0] offset:512\n\ts_waitcnt lgkmcnt(8)\n\ts_nop 0\n\tv_mfma_f32_16x16x32_bf16 v[132:135], v[32:35], v[184:187], v[132:135]\n\tds_read_b64_tr_b16 v[88:89], %[a0] offset:1032\n\tds_read_b64_tr_b16 v[90:91], %[a0] offset:1024\n\tds_read_b128 v[188:191], %[bb] offset:496\n\tv_mfma_f32_16x16x32_bf16 v[136:139], v[64:67], v[184:187], v[136:139]\n\tds_read_b128 v[172:175], %[bb] offset:576\n\ts_waitcnt lgkmcnt(8)\n\ts_nop 0\n\tv_mfma_f32_16x16x32_bf16 v[132:135], v[28:31], v[164:167], v[132:135]\n\tds_read_b128 v[176:179], %[bb] offset:640\n\tv_mfma_f32_16x16x32_bf16 v[136:139], v[60:63], v[164:167], v[136:139]\n\ts_waitcnt lgkmcnt(5)\n\ts_nop 0\n\tv_mfma_f32_16x16x32_bf16 v[132:135], v[24:27], v[168:171], v[132:135]\n\tds_write_b16 %[ta], %[t78]\n\tds_read_b128 v[180:183], %[bb] offset:704\n\tv_mfma_f32_16x16x32_bf16 v[136:139], v[56:59], v[168:171], v[136:139]\n\ts_waitcnt lgkmcnt(3)\n\ts_nop 0\n\tv_mfma_f32_16x16x32_bf16 v[140:143], v[88:91], v[168:171], 0\n\tds_write_b16_d16_hi %[ta], %[t78] offset:64\n\tds_read_b128 v[184:187], %[bb] offset:768\n\tv_mfma_f32_16x16x32_bf16 v[204:207], v[188:191], v[4:7], 0\n\tds_write_b128 %[yb], v[132:135] offset:0\n\tv_mfma_f32_16x16x32_bf16 v[218:221], v[168:171], v[4:7], 0\n\tds_write_b16 %[ta], %[t78] offset:388\n\tds_read_b128 v[164:167], %[bb] offset:832\n\tv_mfma_f32_16x16x32_bf16 v[208:211], v[188:191], v[8:11], 0\n\tds_write_b8 %[ctc], %[one] offset:0\n\tds_write_b16_d16_hi %[ta], %[t78] offset:452\n\tv_mfma_f32_16x16x32_bf16 v[222:225], v[168:171], v[8:11], 0\n\tds_write_b16 %[tb], %[t9a]\n\tv_mfma_f32_16x16x32_bf16 v[212:215], v[188:191], v[12:15], 0\n\tds_write_b16_d16_hi %[tb], %[t9a] offset:128\n\tv_cvt_pk_bf16_f32 v192, v221, v207\n\tv_mfma_f32_16x16x32_bf16 v[226:229], v[168:171], v[16:19], 0\n\tds_write_b16 %[tb], %[t9a] offset:388\n\tv_cvt_pk_bf16_f32 v193, v220, v206\n\tv_mfma_f32_16x16x32_bf16 v[148:151], v[20:23], v[168:171], 0\n\tds_write_b16_d16_hi %[tb], %[t9a] offset:516\n\tds_read_b128 v[168:171], %[bb] offset:896\n\tv_mfma_f32_16x16x32_bf16 v[136:139], v[52:55], v[172:175], v[136:139]\n\tds_read_b64 v[92:93], %[tp] offset:0\n\tds_read_b64 v[94:95], %[tp] offset:8\n\tds_read_b64 v[96:97], %[tp] offset:64\n\tv_mfma_f32_16x16x32_bf16 v[140:143], v[84:87], v[172:175], v[140:143]\n\tds_read_b128 v[172:175], %[bb] offset:960\n\ts_waitcnt lgkmcnt(7)\n\ts_nop 0\n\tv_mfma_f32_16x16x32_bf16 v[136:139], v[48:51], v[176:179], v[136:139]\n\tds_read_b64 v[98:99], %[tp] offset:72\n\tds_read_b64 v[100:101], %[tp] offset:128\n\tds_read_b64 v[102:103], %[tp] offset:136\n\tv_mfma_f32_16x16x32_bf16 v[140:143], v[80:83], v[176:179], v[140:143]\n\tds_read_b128 v[176:179], %[bb] offset:1024\n\tds_read_b64 v[104:105], %[tp] offset:768\n\tds_read_b64 v[106:107], %[tp] offset:776\n\tv_mfma_f32_16x16x32_bf16 v[136:139], v[44:47], v[180:183], v[136:139]\n\tds_read_b64 v[108:109], %[tp] offset:832\n\tds_read_b64 v[110:111], %[tp] offset:840\n\tds_read_b64 v[112:113], %[tp] offset:896\n\tv_mfma_f32_16x16x32_bf16 v[140:143], v[76:79], v[180:183], v[140:143]\n\tds_read_b64 v[114:115], %[tp] offset:904\n\tds_read_b64 v[116:117], %[tp] offset:1536\n\tds_read_b64 v[118:119], %[tp] offset:1544\n\tv_mfma_f32_16x16x32_bf16 v[136:139], v[40:43], v[184:187], v[136:139]\n\tds_read_b64 v[120:121], %[tp] offset:1600\n\tds_read_b64 v[122:123], %[tp] offset:1608\n\tv_cvt_pk_bf16_f32 v194, v219, v205\n\tv_mfma_f32_16x16x32_bf16 v[140:143], v[72:75], v[184:187], v[140:143]\n\tv_cvt_pk_bf16_f32 v195, v218, v204\n\tds_read_b64 v[124:125], %[tp] offset:1664\n\tds_read_b64 v[126:127], %[tp] offset:1672\n\tv_mfma_f32_16x16x32_bf16 v[136:139], v[36:39], v[164:167], v[136:139]\n\tds_read_b64 v[128:129], %[tp] offset:1728\n\tds_read_b64 v[130:131], %[tp] offset:1736\n\tv_cvt_pk_bf16_f32 v196, v225, v211\n\tv_mfma_f32_16x16x32_bf16 v[140:143], v[68:71], v[164:167], v[140:143]\n\tv_cvt_pk_bf16_f32 v197, v224, v210\n\ts_waitcnt lgkmcnt(6)\n\ts_nop 0\n\tv_mfma_f32_16x16x32_bf16 v[136:139], v[32:35], v[168:171], v[136:139]\n\tv_cvt_pk_bf16_f32 v198, v223, v209\n\tv_mfma_f32_16x16x32_bf16 v[140:143], v[64:67], v[168:171], v[140:143]\n\tv_cvt_pk_bf16_f32 v199, v222, v208\n\tv_mfma_f32_16x16x32_bf16 v[136:139], v[28:31], v[172:175], v[136:139]\n\tv_cvt_pk_bf16_f32 v200, v229, v215\n\tv_mfma_f32_16x16x32_bf16 v[140:143], v[60:63], v[172:175], v[140:143]\n\tds_read_b128 v[188:191], %[bb] offset:1008\n\tv_cvt_pk_bf16_f32 v201, v228, v214\n\tv_mfma_f32_16x16x32_bf16 v[136:139], v[24:27], v[176:179], v[136:139]\n\tv_cvt_pk_bf16_f32 v202, v227, v213\n\tv_mfma_f32_16x16x32_bf16 v[140:143], v[56:59], v[176:179], v[140:143]\n\tv_cvt_pk_bf16_f32 v203, v226, v212\n\tv_mfma_f32_16x16x32_bf16 v[144:147], v[88:91], v[176:179], 0\n\tv_mfma_f32_16x16x32_bf16 v[140:143], v[192:195], v[92:95], v[140:143]\n\tv_mfma_f32_16x16x32_bf16 v[144:147], v[192:195], v[96:99], v[144:147]\n\tv_mfma_f32_16x16x32_bf16 v[148:151], v[192:195], v[100:103], v[148:151]\n\tv_mfma_f32_16x16x32_bf16 v[140:143], v[196:199], v[104:107], v[140:143]\n\tds_write_b128 %[yb], v[136:139] offset:1152\n\tds_read_b128 v[180:183], %[bb] offset:1088\n\tv_mfma_f32_16x16x32_bf16 v[144:147], v[196:199], v[108:111], v[144:147]\n\tv_mfma_f32_16x16x32_bf16 v[148:151], v[196:199], v[112:115], v[148:151]\n\tds_write_b8 %[ctc], %[one] offset:64\n\tds_read_b128 v[184:187], %[bb] offset:1152\n\tv_mfma_f32_16x16x32_bf16 v[148:151], v[200:203], v[116:119], v[148:151]\n\ts_waitcnt lgkmcnt(4)\n\ts_nop 0\n\tv_mfma_f32_16x16x32_bf16 v[152:155], v[200:203], v[120:123], 0\n\tds_read_b128 v[164:167], %[bb] offset:1216\n\tv_mfma_f32_16x16x32_bf16 v[156:159], v[200:203], v[124:127], 0\n\tv_mfma_f32_16x16x32_bf16 v[160:163], v[200:203], v[128:131], 0\n\tds_read_b128 v[168:171], %[bb] offset:1280\n\tv_mfma_f32_16x16x32_bf16 v[204:207], v[188:191], v[4:7], 0\n\tv_mfma_f32_16x16x32_bf16 v[218:221], v[176:179], v[4:7], 0\n\tds_read_b128 v[172:175], %[bb] offset:1344\n\tv_mfma_f32_16x16x32_bf16 v[208:211], v[188:191], v[8:11], 0\n\tv_mfma_f32_16x16x32_bf16 v[222:225], v[176:179], v[8:11], 0\n\tv_mfma_f32_16x16x32_bf16 v[212:215], v[188:191], v[12:15], 0\n\tv_mfma_f32_16x16x32_bf16 v[226:229], v[176:179], v[16:19], 0\n\tv_mfma_f32_16x16x32_bf16 v[152:155], v[20:23], v[176:179], v[152:155]\n\ts_waitcnt lgkmcnt(0)\n\ts_nop 0\n\tv_mfma_f32_16x16x32_bf16 v[140:143], v[52:55], v[180:183], v[140:143]\n\tds_read_b128 v[176:179], %[bb] offset:1408\n\tv_cvt_pk_bf16_f32 v192, v221, v207\n\tv_mfma_f32_16x16x32_bf16 v[144:147], v[84:87], v[180:183], v[144:147]\n\tds_read_b128 v[180:183], %[bb] offset:1472\n\tv_cvt_pk_bf16_f32 v193, v220, v206\n\tv_mfma_f32_16x16x32_bf16 v[140:143], v[48:51], v[184:187], v[140:143]\n\tv_cvt_pk_bf16_f32 v194, v219, v205\n\tv_mfma_f32_16x16x32_bf16 v[144:147], v[80:83], v[184:187], v[144:147]\n\tds_read_b128 v[184:187], %[bb] offset:1536\n\tv_cvt_pk_bf16_f32 v195, v218, v204\n\tv_mfma_f32_16x16x32_bf16 v[140:143], v[44:47], v[164:167], v[140:143]\n\tv_cvt_pk_bf16_f32 v196, v225, v211\n\tv_mfma_f32_16x16x32_bf16 v[144:147], v[76:79], v[164:167], v[144:147]\n\tv_cvt_pk_bf16_f32 v197, v224, v210\n\tv_mfma_f32_16x16x32_bf16 v[140:143], v[40:43], v[168:171], v[140:143]\n\tv_cvt_pk_bf16_f32 v198, v223, v209\n\tv_mfma_f32_16x16x32_bf16 v[144:147], v[72:75], v[168:171], v[144:147]\n\tv_cvt_pk_bf16_f32 v199, v222, v208\n\tv_mfma_f32_16x16x32_bf16 v[140:143], v[36:39], v[172:175], v[140:143]\n\tv_cvt_pk_bf16_f32 v200, v229, v215\n\tv_mfma_f32_16x16x32_bf16 v[144:147], v[68:71], v[172:175], v[144:147]\n\ts_waitcnt lgkmcnt(0)\n\ts_nop 0\n\tv_mfma_f32_16x16x32_bf16 v[140:143], v[32:35], v[176:179], v[140:143]\n\tv_cvt_pk_bf16_f32 v201, v228, v214\n\tv_mfma_f32_16x16x32_bf16 v[144:147], v[64:67], v[176:179], v[144:147]\n\tv_cvt_pk_bf16_f32 v202, v227, v213\n\tv_mfma_f32_16x16x32_bf16 v[140:143], v[28:31], v[180:183], v[140:143]\n\tds_read_b128 v[188:191], %[bb] offset:1520\n\tv_cvt_pk_bf16_f32 v203, v226, v212\n\tv_mfma_f32_16x16x32_bf16 v[144:147], v[60:63], v[180:183], v[144:147]\n\tv_mfma_f32_16x16x32_bf16 v[140:143], v[24:27], v[184:187], v[140:143]\n\tv_mfma_f32_16x16x32_bf16 v[144:147], v[56:59], v[184:187], v[144:147]\n\tv_mfma_f32_16x16x32_bf16 v[148:151], v[88:91], v[184:187], v[148:151]\n\tv_mfma_f32_16x16x32_bf16 v[144:147], v[192:195], v[92:95], v[144:147]\n\tv_mfma_f32_16x16x32_bf16 v[148:151], v[192:195], v[96:99], v[148:151]\n\tv_mfma_f32_16x16x32_bf16 v[152:155], v[192:195], v[100:103], v[152:155]\n\tds_read_b128 v[164:167], %[bb] offset:1600\n\tv_mfma_f32_16x16x32_bf16 v[144:147], v[196:199], v[104:107], v[144:147]\n\tv_mfma_f32_16x16x32_bf16 v[148:151], v[196:199], v[108:111], v[148:151]\n\tds_write_b128 %[yb], v[140:143] offset:2304\n\tv_mfma_f32_16x16x32_bf16 v[152:155], v[196:199], v[112:115], v[152:155]\n\tds_read_b128 v[168:171], %[bb] offset:1664\n\tv_mfma_f32_16x16x32_bf16 v[152:155], v[200:203], v[116:119], v[152:155]\n\tds_write_b8 %[ctc], %[one] offset:128\n\tds_read_b128 v[172:175], %[bb] offset:1728\n\tv_mfma_f32_16x16x32_bf16 v[156:159], v[200:203], v[120:123], v[156:159]\n\tv_mfma_f32_16x16x32_bf16 v[160:163], v[200:203], v[124:127], v[160:163]\n\ts_waitcnt lgkmcnt(3)\n\ts_nop 0\n\tv_mfma_f32_16x16x32_bf16 v[204:207], v[188:191], v[4:7], 0\n\tds_read_b128 v[176:179], %[bb] offset:1792\n\tv_mfma_f32_16x16x32_bf16 v[218:221], v[184:187], v[4:7], 0\n\tds_read_b128 v[180:183], %[bb] offset:1856\n\tv_mfma_f32_16x16x32_bf16 v[208:211], v[188:191], v[8:11], 0\n\tv_mfma_f32_16x16x32_bf16 v[222:225], v[184:187], v[8:11], 0\n\tv_mfma_f32_16x16x32_bf16 v[212:215], v[188:191], v[12:15], 0\n\tv_mfma_f32_16x16x32_bf16 v[226:229], v[184:187], v[16:19], 0\n\tv_mfma_f32_16x16x32_bf16 v[156:159], v[20:23], v[184:187], v[156:159]\n\tds_read_b128 v[184:187], %[bb] offset:1920\n\tv_mfma_f32_16x16x32_bf16 v[144:147], v[52:55], v[164:167], v[144:147]\n\tv_cvt_pk_bf16_f32 v192, v221, v207\n\tv_mfma_f32_16x16x32_bf16 v[148:151], v[84:87], v[164:167], v[148:151]\n\ts_waitcnt lgkmcnt(1)\n\ts_nop 0\n\tv_mfma_f32_16x16x32_bf16 v[144:147], v[48:51], v[168:171], v[144:147]\n\tds_read_b128 v[164:167], %[bb] offset:1984\n\tv_cvt_pk_bf16_f32 v193, v220, v206\n\tv_mfma_f32_16x16x32_bf16 v[148:151], v[80:83], v[168:171], v[148:151]\n\tds_read_b128 v[168:171], %[bb] offset:2048\n\tv_cvt_pk_bf16_f32 v194, v219, v205\n\tv_mfma_f32_16x16x32_bf16 v[144:147], v[44:47], v[172:175], v[144:147]\n\tds_read_b128 v[188:191], %[bb] offset:2032\n\tv_cvt_pk_bf16_f32 v195, v218, v204\n\tv_mfma_f32_16x16x32_bf16 v[148:151], v[76:79], v[172:175], v[148:151]\n\tv_cvt_pk_bf16_f32 v196, v225, v211\n\tv_mfma_f32_16x16x32_bf16 v[144:147], v[40:43], v[176:179], v[144:147]\n\tv_cvt_pk_bf16_f32 v197, v224, v210\n\tv_mfma_f32_16x16x32_bf16 v[148:151], v[72:75], v[176:179], v[148:151]\n\tv_cvt_pk_bf16_f32 v198, v223, v209\n\tv_mfma_f32_16x16x32_bf16 v[144:147], v[36:39], v[180:183], v[144:147]\n\tv_cvt_pk_bf16_f32 v199, v222, v208\n\tv_mfma_f32_16x16x32_bf16 v[148:151], v[68:71], v[180:183], v[148:151]\n\ts_waitcnt lgkmcnt(0)\n\ts_nop 0\n\tv_mfma_f32_16x16x32_bf16 v[144:147], v[32:35], v[184:187], v[144:147]\n\tv_cvt_pk_bf16_f32 v200, v229, v215\n\tv_mfma_f32_16x16x32_bf16 v[148:151], v[64:67], v[184:187], v[148:151]\n\tv_cvt_pk_bf16_f32 v201, v228, v214\n\tv_mfma_f32_16x16x32_bf16 v[144:147], v[28:31], v[164:167], v[144:147]\n\tv_cvt_pk_bf16_f32 v202, v227, v213\n\tv_mfma_f32_16x16x32_bf16 v[148:151], v[60:63], v[164:167], v[148:151]\n\tv_cvt_pk_bf16_f32 v203, v226, v212\n\tv_mfma_f32_16x16x32_bf16 v[144:147], v[24:27], v[168:171], v[144:147]\n\tv_mfma_f32_16x16x32_bf16 v[148:151], v[56:59], v[168:171], v[148:151]\n\tv_mfma_f32_16x16x32_bf16 v[152:155], v[88:91], v[168:171], v[152:155]\n\tv_mfma_f32_16x16x32_bf16 v[204:207], v[188:191], v[4:7], 0\n\tv_mfma_f32_16x16x32_bf16 v[218:221], v[168:171], v[4:7], 0\n\tv_mfma_f32_16x16x32_bf16 v[208:211], v[188:191], v[8:11], 0\n\tv_mfma_f32_16x16x32_bf16 v[222:225], v[168:171], v[8:11], 0\n\tv_mfma_f32_16x16x32_bf16 v[212:215], v[188:191], v[12:15], 0\n\tv_mfma_f32_16x16x32_bf16 v[226:229], v[168:171], v[16:19], 0\n\tds_write_b128 %[yb], v[144:147] offset:3456\n\tv_mfma_f32_16x16x32_bf16 v[160:163], v[20:23], v[168:171], v[160:163]\n\tds_read_b32 v216, %[ctl] offset:20\n\tv_mfma_f32_16x16x32_bf16 v[148:151], v[192:195], v[92:95], v[148:151]\n\tds_write_b8 %[ctc], %[one] offset:192\n\tv_mfma_f32_16x16x32_bf16 v[152:155], v[192:195], v[96:99], v[152:155]\n\tv_mfma_f32_16x16x32_bf16 v[156:159], v[192:195], v[100:103], v[156:159]\n\tv_mfma_f32_16x16x32_bf16 v[148:151], v[196:199], v[104:107], v[148:151]\n\tv_cvt_pk_bf16_f32 v192, v221, v207\n\tv_mfma_f32_16x16x32_bf16 v[152:155], v[196:199], v[108:111], v[152:155]\n\tv_cvt_pk_bf16_f32 v193, v220, v206\n\tv_mfma_f32_16x16x32_bf16 v[156:159], v[196:199], v[112:115], v[156:159]\n\tv_cvt_pk_bf16_f32 v194, v219, v205\n\tv_mfma_f32_16x16x32_bf16 v[156:159], v[200:203], v[116:119], v[156:159]\n\tv_cvt_pk_bf16_f32 v195, v218, v204\n\tv_mfma_f32_16x16x32_bf16 v[160:163], v[200:203], v[120:123], v[160:163]\n\ts_waitcnt lgkmcnt(0)\n\ts_nop 0\n\tv_readfirstlane_b32 %[s], v216\n\ts_mov_b32 %[c], 0x400000\n\ts_cmp_ge_u32 %[s], 2\n\ts_cbranch_scc1 2f\n\t1:\n\tds_read_b32 v216, %[ctl] offset:20\n\ts_waitcnt lgkmcnt(0)\n\tv_readfirstlane_b32 %[s], v216\n\ts_cmp_ge_u32 %[s], 2\n\ts_cbranch_scc1 2f\n\ts_sleep 1\n\ts_sub_u32 %[c], %[c], 1\n\ts_cbranch_scc0 1b\n\t.p2align 3\n\t2:\n\tv_mfma_f32_16x16x32_bf16 v[152:155], v[192:195], v[92:95], v[152:155]\n\tds_read_b128 v[172:175], %[bb] offset:2112\n\tv_cvt_pk_bf16_f32 v196, v225, v211\n\tv_mfma_f32_16x16x32_bf16 v[156:159], v[192:195], v[96:99], v[156:159]\n\tds_read_b128 v[176:179], %[bb] offset:2176\n\tv_cvt_pk_bf16_f32 v197, v224, v210\n\tv_mfma_f32_16x16x32_bf16 v[160:163], v[192:195], v[100:103], v[160:163]\n\tv_cvt_pk_bf16_f32 v198, v223, v209\n\tds_read_b128 v[180:183], %[bb] offset:2240\n\tv_cvt_pk_bf16_f32 v199, v222, v208\n\ts_nop 1\n\ts_nop 0\n\tv_mfma_f32_16x16x32_bf16 v[152:155], v[196:199], v[104:107], v[152:155]\n\tds_read_b128 v[184:187], %[bb] offset:2304\n\tv_cvt_pk_bf16_f32 v200, v229, v215\n\tv_mfma_f32_16x16x32_bf16 v[156:159], v[196:199], v[108:111], v[156:159]\n\tv_cvt_pk_bf16_f32 v201, v228, v214\n\tds_read_b128 v[164:167], %[bb] offset:2368\n\tv_mfma_f32_16x16x32_bf16 v[160:163], v[196:199], v[112:115], v[160:163]\n\tv_cvt_pk_bf16_f32 v202, v227, v213\n\tds_read_b128 v[168:171], %[bb] offset:2432\n\tv_cvt_pk_bf16_f32 v203, v226, v212\n\ts_nop 1\n\ts_nop 0\n\tv_mfma_f32_16x16x32_bf16 v[160:163], v[200:203], v[116:119], v[160:163]\n\ts_waitcnt lgkmcnt(3)\n\ts_nop 0\n\tv_mfma_f32_16x16x32_bf16 v[148:151], v[52:55], v[172:175], v[148:151]\n\tv_mfma_f32_16x16x32_bf16 v[152:155], v[84:87], v[172:175], v[152:155]\n\tds_read_b128 v[172:175], %[bb] offset:2496\n\tv_mfma_f32_16x16x32_bf16 v[148:151], v[48:51], v[176:179], v[148:151]\n\tv_mfma_f32_16x16x32_bf16 v[152:155], v[80:83], v[176:179], v[152:155]\n\tds_read_b128 v[176:179], %[bb] offset:2560\n\tv_mfma_f32_16x16x32_bf16 v[148:151], v[44:47], v[180:183], v[148:151]\n\tds_read_b128 v[188:191], %[bb] offset:2544\n\tv_mfma_f32_16x16x32_bf16 v[152:155], v[76:79], v[180:183], v[152:155]\n\ts_waitcnt lgkmcnt(2)\n\ts_nop 0\n\tv_mfma_f32_16x16x32_bf16 v[148:151], v[40:43], v[184:187], v[148:151]\n\tv_mfma_f32_16x16x32_bf16 v[152:155], v[72:75], v[184:187], v[152:155]\n\tds_read_b128 v[180:183], %[bb] offset:2624\n\tv_mfma_f32_16x16x32_bf16 v[148:151], v[36:39], v[164:167], v[148:151]\n\tv_mfma_f32_16x16x32_bf16 v[152:155], v[68:71], v[164:167], v[152:155]\n\tds_read_b128 v[184:187], %[bb] offset:2688\n\tv_mfma_f32_16x16x32_bf16 v[148:151], v[32:35], v[168:171], v[148:151]\n\tv_mfma_f32_16x16x32_bf16 v[152:155], v[64:67], v[168:171], v[152:155]\n\tds_read_b128 v[164:167], %[bb] offset:2752\n\tv_mfma_f32_16x16x32_bf16 v[148:151], v[28:31], v[172:175], v[148:151]\n\tv_mfma_f32_16x16x32_bf16 v[152:155], v[60:63], v[172:175], v[152:155]\n\ts_waitcnt lgkmcnt(1)\n\ts_nop 0\n\tv_mfma_f32_16x16x32_bf16 v[148:151], v[24:27], v[176:179], v[148:151]\n\tds_read_b128 v[168:171], %[bb] offset:2816\n\tv_mfma_f32_16x16x32_bf16 v[152:155], v[56:59], v[176:179], v[152:155]\n\tds_read_b128 v[172:175], %[bb] offset:2880\n\tv_mfma_f32_16x16x32_bf16 v[156:159], v[88:91], v[176:179], v[156:159]\n\tv_mfma_f32_16x16x32_bf16 v[204:207], v[188:191], v[4:7], 0\n\tv_mfma_f32_16x16x32_bf16 v[218:221], v[176:179], v[4:7], 0\n\tv_mfma_f32_16x16x32_bf16 v[208:211], v[188:191], v[8:11], 0\n\tv_mfma_f32_16x16x32_bf16 v[222:225], v[176:179], v[8:11], 0\n\tds_write_b128 %[yb], v[148:151] offset:4608\n\tv_mfma_f32_16x16x32_bf16 v[152:155], v[52:55], v[180:183], v[152:155]\n\tds_read_b128 v[176:179], %[bb] offset:2944\n\tv_mfma_f32_16x16x32_bf16 v[156:159], v[84:87], v[180:183], v[156:159]\n\tds_write_b8 %[ctc], %[one] offset:256\n\tds_read_b128 v[180:183], %[bb] offset:3008\n\tv_mfma_f32_16x16x32_bf16 v[152:155], v[48:51], v[184:187], v[152:155]\n\tv_cvt_pk_bf16_f32 v192, v221, v207\n\tv_mfma_f32_16x16x32_bf16 v[156:159], v[80:83], v[184:187], v[156:159]\n\ts_waitcnt lgkmcnt(3)\n\ts_nop 0\n\tv_mfma_f32_16x16x32_bf16 v[152:155], v[44:47], v[164:167], v[152:155]\n\tds_read_b128 v[184:187], %[bb] offset:3072\n\tv_cvt_pk_bf16_f32 v193, v220, v206\n\tv_mfma_f32_16x16x32_bf16 v[156:159], v[76:79], v[164:167], v[156:159]\n\tv_cvt_pk_bf16_f32 v194, v219, v205\n\tv_mfma_f32_16x16x32_bf16 v[152:155], v[40:43], v[168:171], v[152:155]\n\tv_cvt_pk_bf16_f32 v195, v218, v204\n\tv_mfma_f32_16x16x32_bf16 v[156:159], v[72:75], v[168:171], v[156:159]\n\tds_read_b128 v[188:191], %[bb] offset:3056\n\tv_cvt_pk_bf16_f32 v196, v225, v211\n\tv_mfma_f32_16x16x32_bf16 v[152:155], v[36:39], v[172:175], v[152:155]\n\tv_cvt_pk_bf16_f32 v197, v224, v210\n\tv_mfma_f32_16x16x32_bf16 v[156:159], v[68:71], v[172:175], v[156:159]\n\ts_waitcnt lgkmcnt(1)\n\ts_nop 0\n\tv_mfma_f32_16x16x32_bf16 v[152:155], v[32:35], v[176:179], v[152:155]\n\tds_read_b32 v216, %[ctl] offset:24\n\tv_cvt_pk_bf16_f32 v198, v223, v209\n\tv_mfma_f32_16x16x32_bf16 v[156:159], v[64:67], v[176:179], v[156:159]\n\tv_cvt_pk_bf16_f32 v199, v222, v208\n\tv_mfma_f32_16x16x32_bf16 v[152:155], v[28:31], v[180:183], v[152:155]\n\tv_mfma_f32_16x16x32_bf16 v[156:159], v[60:63], v[180:183], v[156:159]\n\tv_mfma_f32_16x16x32_bf16 v[152:155], v[24:27], v[184:187], v[152:155]\n\tv_mfma_f32_16x16x32_bf16 v[156:159], v[56:59], v[184:187], v[156:159]\n\tv_mfma_f32_16x16x32_bf16 v[160:163], v[88:91], v[184:187], v[160:163]\n\tv_mfma_f32_16x16x32_bf16 v[156:159], v[192:195], v[92:95], v[156:159]\n\ts_waitcnt lgkmcnt(0)\n\ts_nop 0\n\tv_readfirstlane_b32 %[s], v216\n\ts_mov_b32 %[c], 0x400000\n\ts_cmp_ge_u32 %[s], 2\n\ts_cbranch_scc1 2f\n\t1:\n\tds_read_b32 v216, %[ctl] offset:24\n\ts_waitcnt lgkmcnt(0)\n\tv_readfirstlane_b32 %[s], v216\n\ts_cmp_ge_u32 %[s], 2\n\ts_cbranch_scc1 2f\n\ts_sleep 1\n\ts_sub_u32 %[c], %[c], 1\n\ts_cbranch_scc0 1b\n\t.p2align 3\n\t2:\n\tv_mfma_f32_16x16x32_bf16 v[160:163], v[192:195], v[96:99], v[160:163]\n\tds_write_b128 %[yb], v[152:155] offset:5760\n\tv_mfma_f32_16x16x32_bf16 v[156:159], v[196:199], v[104:107], v[156:159]\n\tds_read_b128 v[164:167], %[bb] offset:3136\n\tv_mfma_f32_16x16x32_bf16 v[160:163], v[196:199], v[108:111], v[160:163]\n\tds_write_b8 %[ctc], %[one] offset:320\n\tds_read_b128 v[168:171], %[bb] offset:3200\n\tv_mfma_f32_16x16x32_bf16 v[204:207], v[188:191], v[4:7], 0\n\tds_read_b128 v[172:175], %[bb] offset:3264\n\tv_mfma_f32_16x16x32_bf16 v[218:221], v[184:187], v[4:7], 0\n\tds_read_b128 v[176:179], %[bb] offset:3328\n\tv_mfma_f32_16x16x32_bf16 v[208:211], v[188:191], v[8:11], 0\n\tds_read_b128 v[180:183], %[bb] offset:3392\n\tv_mfma_f32_16x16x32_bf16 v[222:225], v[184:187], v[8:11], 0\n\ts_waitcnt lgkmcnt(3)\n\ts_nop 0\n\tv_mfma_f32_16x16x32_bf16 v[156:159], v[52:55], v[164:167], v[156:159]\n\tds_read_b128 v[184:187], %[bb] offset:3456\n\tv_cvt_pk_bf16_f32 v192, v221, v207\n\tv_mfma_f32_16x16x32_bf16 v[160:163], v[84:87], v[164:167], v[160:163]\n\tds_read_b128 v[164:167], %[bb] offset:3520\n\tv_cvt_pk_bf16_f32 v193, v220, v206\n\tv_mfma_f32_16x16x32_bf16 v[156:159], v[48:51], v[168:171], v[156:159]\n\tv_cvt_pk_bf16_f32 v194, v219, v205\n\tv_mfma_f32_16x16x32_bf16 v[160:163], v[80:83], v[168:171], v[160:163]\n\ts_waitcnt lgkmcnt(2)\n\ts_nop 0\n\tv_mfma_f32_16x16x32_bf16 v[156:159], v[44:47], v[172:175], v[156:159]\n\tds_read_b128 v[168:171], %[bb] offset:3584\n\tv_cvt_pk_bf16_f32 v195, v218, v204\n\tv_mfma_f32_16x16x32_bf16 v[160:163], v[76:79], v[172:175], v[160:163]\n\tds_read_b128 v[172:175], %[bb] offset:3648\n\tv_cvt_pk_bf16_f32 v196, v225, v211\n\tv_mfma_f32_16x16x32_bf16 v[156:159], v[40:43], v[176:179], v[156:159]\n\tglobal_load_dwordx2 v[240:241], %[so], %[ws] sc1\n\tv_cvt_pk_bf16_f32 v197, v224, v210\n\tv_mfma_f32_16x16x32_bf16 v[160:163], v[72:75], v[176:179], v[160:163]\n\tds_read_b128 v[176:179], %[bb] offset:3712\n\tv_cvt_pk_bf16_f32 v198, v223, v209\n\tv_mfma_f32_16x16x32_bf16 v[156:159], v[36:39], v[180:183], v[156:159]\n\tv_cvt_pk_bf16_f32 v199, v222, v208\n\tv_mfma_f32_16x16x32_bf16 v[160:163], v[68:71], v[180:183], v[160:163]\n\ts_waitcnt lgkmcnt(1)\n\ts_nop 0\n\tv_mfma_f32_16x16x32_bf16 v[156:159], v[32:35], v[184:187], v[156:159]\n\tds_read_b128 v[180:183], %[bb] offset:3776\n\tv_mfma_f32_16x16x32_bf16 v[160:163], v[64:67], v[184:187], v[160:163]\n\tds_read_b128 v[184:187], %[bb] offset:3840\n\tv_mfma_f32_16x16x32_bf16 v[156:159], v[28:31], v[164:167], v[156:159]\n\tv_mfma_f32_16x16x32_bf16 v[160:163], v[60:63], v[164:167], v[160:163]\n\tds_read_b128 v[164:167], %[bb] offset:3904\n\tv_mfma_f32_16x16x32_bf16 v[156:159], v[24:27], v[168:171], v[156:159]\n\tv_mfma_f32_16x16x32_bf16 v[160:163], v[56:59], v[168:171], v[160:163]\n\tds_read_b128 v[168:171], %[bb] offset:3968\n\tv_mfma_f32_16x16x32_bf16 v[160:163], v[192:195], v[92:95], v[160:163]\n\tv_mfma_f32_16x16x32_bf16 v[160:163], v[196:199], v[104:107], v[160:163]\n\tv_mfma_f32_16x16x32_bf16 v[160:163], v[52:55], v[172:175], v[160:163]\n\ts_waitcnt lgkmcnt(1)\n\ts_nop 0\n\tv_mfma_f32_16x16x32_bf16 v[160:163], v[48:51], v[176:179], v[160:163]\n\tds_write_b128 %[yb], v[156:159] offset:6912\n\tv_mfma_f32_16x16x32_bf16 v[160:163], v[44:47], v[180:183], v[160:163]\n\tds_read_b128 v[172:175], %[bb] offset:4032\n\tv_mfma_f32_16x16x32_bf16 v[160:163], v[40:43], v[184:187], v[160:163]\n\tds_write_b8 %[ctc], %[one] offset:384\n\tds_read_b128 v[176:179], %[bb] offset:4096\n\tv_mfma_f32_16x16x32_bf16 v[160:163], v[36:39], v[164:167], v[160:163]\n\ts_waitcnt lgkmcnt(4)\n\ts_nop 0\n\tv_mfma_f32_16x16x32_bf16 v[160:163], v[32:35], v[168:171], v[160:163]\n\ts_waitcnt lgkmcnt(2)\n\ts_nop 0\n\tv_mfma_f32_16x16x32_bf16 v[160:163], v[28:31], v[172:175], v[160:163]\n\ts_waitcnt lgkmcnt(0)\n\ts_nop 0\n\tv_mfma_f32_16x16x32_bf16 v[160:163], v[24:27], v[176:179], v[160:163]\n\ts_nop 7\n\tds_write_b128 %[yb], v[160:163] offset:8064\n\tds_write_b8 %[ctc], %[one] offset:448"
#define CONV_CLOBBER "v24", "v25", "v26", "v27", "v28", "v29", "v30", "v31", "v32", "v33", "v34", "v35", "v36", "v37", "v38", "v39", "v40", "v41", "v42", "v43", "v44", "v45", "v46", "v47", "v48", "v49", "v50", "v51", "v52", "v53", "v54", "v55", "v56", "v57", "v58", "v59", "v60", "v61", "v62", "v63", "v64", "v65", "v66", "v67", "v68", "v69", "v70", "v71", "v72", "v73", "v74", "v75", "v76", "v77", "v78", "v79", "v80", "v81", "v82", "v83", "v84", "v85", "v86", "v87", "v88", "v89", "v90", "v91", "v92", "v93", "v94", "v95", "v96", "v97", "v98", "v99", "v100", "v101", "v102", "v103", "v104", "v105", "v106", "v107", "v108", "v109", "v110", "v111", "v112", "v113", "v114", "v115", "v116", "v117", "v118", "v119", "v120", "v121", "v122", "v123", "v124", "v125", "v126", "v127", "v128", "v129", "v130", "v131", "v132", "v133", "v134", "v135", "v136", "v137", "v138", "v139", "v140", "v141", "v142", "v143", "v144", "v145", "v146", "v147", "v148", "v149", "v150", "v151", "v152", "v153", "v154", "v155", "v156", "v157", "v158", "v159", "v160", "v161", "v162", "v163", "v164", "v165", "v166", "v167", "v168", "v169", "v170", "v171", "v172", "v173", "v174", "v175", "v176", "v177", "v178", "v179", "v180", "v181", "v182", "v183", "v184", "v185", "v186", "v187", "v188", "v189", "v190", "v191", "v192", "v193", "v194", "v195", "v196", "v197", "v198", "v199", "v200", "v201", "v202", "v203", "v204", "v205", "v206", "v207", "v208", "v209", "v210", "v211", "v212", "v213", "v214", "v215", "v216", "v217", "v218", "v219", "v220", "v221", "v222", "v223", "v224", "v225", "v226", "v227", "v228", "v229"

typedef __attribute__((address_space(4))) const Params* KArg;
#define ROLE_BEGIN(N) KArg kp; int b, tid; \
    asm volatile("fk_role" #N ":\n\tv_mov_b32 %0, v0" : "=&v"(tid) : "{s[0:1]}"(kp0), "{s2}"(b0), "{v0}"(tid0) : "memory"); \
    asm volatile("s_mov_b64 %0, s[0:1]" : "=&s"(kp) : "{s[0:1]}"(kp0), "{s2}"(b0)); asm volatile("s_mov_b32 %0, s2" : "=&s"(b) : "{s2}"(b0)); \
    const int lane = tid & 63, wave = __builtin_amdgcn_readfirstlane(tid >> 6), xcd = b & 7, slot = b >> 3; \
    const int cw = slot < 16 ? xcd * 16 + slot : 112 + xcd * 2 + slot, chbase = cw * 4, w = xcd * 9 + (slot - 18); (void)chbase; (void)w; (void)lane; \
    const Params p = {kp->x, kp->Are, kp->Aim, kp->Bre, kp->Bim, kp->Cre, kp->Cim, kp->logdt, kp->dskip, kp->wr, kp->wg, kp->wu, kp->wd, kp->gw, kp->out, kp->ws}; \
    asm volatile("s_waitcnt lgkmcnt(0)"); STAMP0_FIX()
#define ROLE_END() do { STAMP_FLUSH(); __builtin_amdgcn_s_waitcnt(0x0070); asm volatile("s_endpgm" ::: "memory"); } while (0)
#define MF(A_, B_, C_) __builtin_amdgcn_mfma_f32_16x16x32_bf16(A_, B_, C_, 0, 0, 0)
#define GRP(mask, n) __builtin_amdgcn_sched_group_barrier(mask, n, 0)

#define FFN_FILL() \
    if (FDELAY) __builtin_amdgcn_s_sleep(FDELAY); \
    { const char* gsrc = (const char*)((wave & 1) ? p.wu : p.wg) + w * 128 + (wave >> 1) * (8 * F * 2); \
      const u32 gof = (lane >> 3) * (F * 2) + ((lane & 7) ^ (((lane >> 3) & 2) | ((wave & 2) << 1))) * 16; \
      _Pragma("unroll") for (int i = 0; i < 18; ++i) { \
          asm volatile("s_mov_b32 m0, %2\n\ts_nop 0\n\tglobal_load_lds_dwordx4 %0, %1" WNT :: "v"(gof), "s"(gsrc + i * (32 * F * 2)), "s"((8 * i + wave) * 1024) : "memory"); \
          if (i == 5 || i == 11) __builtin_amdgcn_s_barrier(); } } \
    STAMP(11)
#define FFN_WDFRAG(k, pr) do { LAS char* t_ = ta + (pr) * 64; \
    _Pragma("unroll") for (int e = 0; e < 8; ++e) a[k][e] = __builtin_amdgcn_ds_read_tr16_b64_v4i16((LAS s16x4*)(t_ + (e >> 2) * 32 + (e & 3) * 16 * WDP)); } while (0)
#define FFN_DOWN(k, pr) do { v4f y_ = MF(__builtin_shufflevector(a[k][0], a[k][1], 0, 1, 2, 3, 4, 5, 6, 7), hf[0], ((v4f){0.f, 0.f, 0.f, 0.f})); \
    y_ = MF(__builtin_shufflevector(a[k][4], a[k][5], 0, 1, 2, 3, 4, 5, 6, 7), hf[1], y_); \
    y_ = MF(__builtin_shufflevector(a[k][2], a[k][3], 0, 1, 2, 3, 4, 5, 6, 7), hf[2], y_); \
    ya[k] = MF(__builtin_shufflevector(a[k][6], a[k][7], 0, 1, 2, 3, 4, 5, 6, 7), hf[3], y_); } while (0)
#define FFN_ADDR() const int n = lane & 15, q = lane >> 4; \
    LAS char* ta = smem + (lane >> 2) * WDP + (lane & 3) * 8; \
    LAS char* hrow = smem + FL_H + (n & 7) * 144 + q * 16; \
    LAS char* ha = n < 8 ? hrow : smem + FL_Z + q * 16; \
    LAS char* hb = n < 8 ? smem + FL_Z + q * 16 : hrow; \
    u32 obase = OFF_PART + (4 * (n >> 3) + q) * PP + hline(p.ws, w) + (n & 7) * 16, ctl = (u32)(uintptr_t)(smem + FL_TAG); \
    asm volatile("" : "+v"(ta), "+v"(ha), "+v"(hb), "+v"(obase), "+v"(ctl))
#define FFN_HFRAG() const bf16x8 hf[4] = {*(LAS bf16x8*)(ha), *(LAS bf16x8*)(hb), *(LAS bf16x8*)(ha + 64), *(LAS bf16x8*)(hb + 64)}
#define FFN_PUB(k, pr) __builtin_amdgcn_raw_buffer_store_b128((v4u){cvtpk(ya[k][0], ya[k][1]), tag, cvtpk(ya[k][2], ya[k][3]), tag}, WSR(), obase + (pr) * (8 * PP), 0, 16)

__global__ __launch_bounds__(512, 2) void fused_kernel(Params p_unused) {
    extern __shared__ __attribute__((aligned(16))) char smem_[];
    LAS char* smem = (LAS char*)smem_;
    const KArg kp0 = (KArg)__builtin_amdgcn_kernarg_segment_ptr();
    const int tid0 = threadIdx.x, b0 = blockIdx.x;
    asm volatile("s_load_dword s94, s[0:1], 0x0\n\ts_load_dword s95, s[0:1], 0x40\n\tv_readfirstlane_b32 s92, v0\n\t"
                 "s_cmp_ge_u32 s2, 32\n\ts_cbranch_scc1 1f\n\t"
                 "s_getpc_b64 s[90:91]\n\ts_and_b32 s90, s90, 0xffffff00\n\ts_and_b32 s93, s2, -8\n\ts_lshr_b32 s89, s92, 6\n\ts_add_u32 s93, s93, s89\n\ts_min_u32 s93, s93, %3\n\ts_lshl_b32 s93, s93, 10\n\t"
                 "v_and_b32 v1, 63, v0\n\tv_lshlrev_b32 v1, 4, v1\n\tv_add_u32 v1, s93, v1\n\ts_mov_b32 m0, %4\n\ts_nop 0\n\tglobal_load_lds_dwordx4 v1, s[90:91]\n"
                 "1:\n\ts_cmp_lt_u32 s2, 144\n\ts_cbranch_scc0 2f\n\ts_cmp_lt_u32 s92, 256\n\ts_cbranch_scc1 fk_role0\n\ts_branch fk_role1\n"
                 "2:\n\ts_cmp_lt_u32 s2, 216\n\ts_cbranch_scc0 3f\n\ts_cmp_lt_u32 s92, 256\n\ts_cbranch_scc1 fk_role2\n\ts_branch fk_role3\n"
                 "3:\n\ts_cmp_lt_u32 s2, 224\n\ts_cbranch_scc1 fk_role4\n\ts_branch fk_role5"
                 :: "{s[0:1]}"(kp0), "{s2}"(b0), "{v0}"(tid0), "n"(CODE_KB - 1), "n"(LDS_DUMMY) : "memory", "scc", "s89", "s90", "s91", "s92", "s93", "s94", "s95", "v1");
    const int slot = b0 >> 3, wave = __builtin_amdgcn_readfirstlane(tid0 >> 6);
    if (slot >= 18) {
    if (slot >= 27 || wave >= 4) {
    if (slot < 28) {
    if (slot == 27)
    {
        ROLE_BEGIN(4);
        const u32 tag = __builtin_amdgcn_readfirstlane(__hip_atomic_load((GAS u32*)p.ws, RLX_AGENT)) + 1u;
        if (tid < 128) ((LAS u32*)smem)[tid] = 0;
        u32 ga = (u32)(uintptr_t)(smem + (lane & 15) * 4), gv = (u32)(uintptr_t)(smem + (lane >> 2) * 4), gq_ = (u32)(uintptr_t)(smem + (lane & 3) * 16), zero = (u32)(uintptr_t)smem;
        u32 ro = OFF_RG + hline(p.ws, tid >> 3) + (tid & 7) * 16, ro2 = OFF_RG + hline(p.ws, 64 + (tid >> 3)) + (tid & 7) * 16, so = OFF_SEL + ((lane & 1) * 8 + xcd) * 256, xo = (lane & 3) * 128;
        asm volatile("" : "+v"(ga), "+v"(gv), "+v"(gq_), "+v"(zero), "+v"(ro), "+v"(ro2), "+v"(so), "+v"(xo));
        BAR();
        __builtin_amdgcn_s_sleep(SELWAIT);
        v4u qa, qb;
        u32 c_;
#define POLL_A "global_load_dwordx4 v[20:23], %[ro], %[ws] sc1\n\tglobal_load_dwordx4 v[24:27], %[r2], %[ws] sc1\n\t"
#define POLL_B "global_load_dwordx4 v[28:31], %[ro], %[ws] sc1\n\tglobal_load_dwordx4 v[32:35], %[r2], %[ws] sc1\n\t"
        asm volatile("s_mov_b32 %[c], 0x40000\n\t" POLL_A "s_sleep 8\n\t" POLL_B
                     "1:\n\ts_waitcnt vmcnt(2)\n\tv_cmpx_eq_u32 %[tag], v21\n\tv_cmpx_eq_u32 %[tag], v23\n\tv_cmpx_eq_u32 %[tag], v25\n\tv_cmpx_eq_u32 %[tag], v27\n\t"
                     "s_cmp_eq_u64 exec, -1\n\ts_mov_b64 exec, -1\n\ts_cbranch_scc1 3f\n\t" POLL_A
                     "s_waitcnt vmcnt(2)\n\tv_cmpx_eq_u32 %[tag], v29\n\tv_cmpx_eq_u32 %[tag], v31\n\tv_cmpx_eq_u32 %[tag], v33\n\tv_cmpx_eq_u32 %[tag], v35\n\t"
                     "s_cmp_eq_u64 exec, -1\n\ts_mov_b64 exec, -1\n\ts_cbranch_scc1 2f\n\t" POLL_B
                     "s_sub_u32 %[c], %[c], 1\n\ts_cbranch_scc0 1b\n\ts_waitcnt vmcnt(0)\n"
                     "2:\n\tv_mov_b32 v36, v28\n\tv_mov_b32 v37, v30\n\tv_mov_b32 v38, v32\n\tv_mov_b32 v39, v34\n\ts_branch 4f\n"
                     "3:\n\tv_mov_b32 v36, v20\n\tv_mov_b32 v37, v22\n\tv_mov_b32 v38, v24\n\tv_mov_b32 v39, v26\n"
                     "4:"
                     : "=&{v[36:39]}"(qa), [c] "=&s"(c_) : [ro] "v"(ro), [r2] "v"(ro2), [ws] "s"(p.ws), [tag] "s"(tag)
                     : "memory", "scc", "vcc", "v20", "v21", "v22", "v23", "v24", "v25", "v26", "v27", "v28", "v29", "v30", "v31", "v32", "v33", "v34", "v35");
        asm volatile("" : "=&{v[20:23]}"(qb));
        v4u qc, qd, qe;
        asm volatile("" : "=&{v[24:27]}"(qc), "=&{v[28:31]}"(qd), "=&{v[32:35]}"(qe));
        STAMP(2);
        u32 rank, tau, cnt;
        u64 m0_, m1_, m2_, m3_;
        {
            u32 v, nv; v4u q4;
            asm volatile("ds_max_u32 %0, %1\n\ts_waitcnt lgkmcnt(0)\n\ts_barrier" :: "v"(ga), "v"(max(max(qa.x, qa.y), max(qa.z, qa.w))) : "memory");
#define RANK16(off) asm volatile("ds_read_b32 %[v], %[gv] offset:" #off "\n\tds_read_b128 v[40:43], %[gq] offset:" #off "\n\tds_read_b32 %[n], %[z] offset:64\n\tv_mov_b32 %[r], 0\n\ts_waitcnt lgkmcnt(0)\n\t" \
                         "v_cmp_gt_u32 %[a], v40, %[v]\n\tv_cmp_gt_u32 %[b], v41, %[v]\n\tv_cmp_gt_u32 %[c], v42, %[v]\n\tv_cmp_gt_u32 %[d], v43, %[v]\n\t" \
                         "v_addc_co_u32 %[r], vcc, 0, %[r], %[a]\n\tv_addc_co_u32 %[r], vcc, 0, %[r], %[b]\n\tv_addc_co_u32 %[r], vcc, 0, %[r], %[c]\n\tv_addc_co_u32 %[r], vcc, 0, %[r], %[d]\n\ts_nop 1\n\t" \
                         "v_add_u32_dpp %[r], %[r], %[r] quad_perm:[1,0,3,2] row_mask:0xf bank_mask:0xf\n\ts_nop 1\n\tv_add_u32_dpp %[r], %[r], %[r] quad_perm:[2,3,0,1] row_mask:0xf bank_mask:0xf\n\ts_nop 0" \
                         : [v] "=&v"(v), [r] "=&v"(rank), [n] "=&v"(nv), "=&{v[40:43]}"(q4), [a] "=&s"(m0_), [b] "=&s"(m1_), [c] "=&s"(m2_), [d] "=&s"(m3_) : [gv] "v"(gv), [gq] "v"(gq_), [z] "v"(zero) : "memory", "vcc")
            RANK16(0);
            tau = (u32)__builtin_amdgcn_readlane((int)v, (int)__builtin_ctzll(__ballot(rank == 7)));
            u32 pos;
            asm volatile("s_nop 1\n\tv_mov_b32 %[n], 0\n\tv_cmp_le_u32 %[a], %[t], v36\n\tv_cmp_le_u32 %[b], %[t], v37\n\tv_cmp_le_u32 %[c], %[t], v38\n\tv_cmp_le_u32 %[d], %[t], v39\n\t"
                         "v_addc_co_u32 %[n], vcc, 0, %[n], %[a]\n\tv_addc_co_u32 %[n], vcc, 0, %[n], %[b]\n\tv_addc_co_u32 %[n], vcc, 0, %[n], %[c]\n\tv_addc_co_u32 %[n], vcc, 0, %[n], %[d]\n\t"
                         "v_lshlrev_b32 %[n], 2, %[n]\n\tv_cmpx_ne_u32 0, %[n]\n\tds_add_rtn_u32 %[p], %[z], %[n] offset:64\n\ts_waitcnt lgkmcnt(0)\n\t"
                         "s_mov_b64 exec, %[a]\n\tds_write_b32 %[p], v36 offset:256\n\tv_add_u32 %[p], 4, %[p]\n\t"
                         "s_mov_b64 exec, %[b]\n\tds_write_b32 %[p], v37 offset:256\n\tv_add_u32 %[p], 4, %[p]\n\t"
                         "s_mov_b64 exec, %[c]\n\tds_write_b32 %[p], v38 offset:256\n\tv_add_u32 %[p], 4, %[p]\n\t"
                         "s_mov_b64 exec, %[d]\n\tds_write_b32 %[p], v39 offset:256\n\t"
                         "s_mov_b64 exec, -1\n\ts_waitcnt lgkmcnt(0)\n\ts_barrier"
                         : [n] "=&v"(cnt), [p] "=&v"(pos), [a] "=&s"(m0_), [b] "=&s"(m1_), [c] "=&s"(m2_), [d] "=&s"(m3_) : [t] "s"(tau), [z] "v"(zero), "{v[36:39]}"(qa) : "memory", "vcc");
            if (wave == 0) {
                RANK16(256);
                const int nc = __builtin_amdgcn_readfirstlane((int)nv) >> 2;
                if (__builtin_expect(nc > 16, 0)) {
                    v = (u32)__shfl((int)top8_d(smem, lane, nc), (lane >> 2) & 7);
                    rank = lane >> 2;
                }
                const u64 m8 = __ballot(rank < 8);
                asm volatile("s_and_b64 exec, %[m], %[k2]\n\tglobal_store_dwordx2 %[so], %[d], %[ws] sc1\n\ts_mov_b64 exec, %[m]\n\ts_mov_b32 m0, 0x8000\n\ts_nop 0\n\t"
                             "global_load_lds_dword %[xo], %[x]\n\tglobal_load_lds_dword %[xo], %[x] offset:512\n\ts_and_b64 exec, %[m], %[k1]\n\tglobal_load_lds_dword %[xo], %[x] offset:1024\n\ts_mov_b64 exec, -1"
                             :: [m] "s"(m8), [k2] "s"(0x3333333333333333ull), [k1] "s"(0x1111111111111111ull), [so] "v"(so + rank * 8), [d] "v"((v2u){v, tag}), [ws] "s"(p.ws),
                                [xo] "v"(xo + (~v & 2047u) * 1152u), [x] "s"(p.x) : "memory", "scc");
                asm volatile("s_mov_b64 exec, 1\n\tglobal_store_dwordx2 %0, %1, %2 sc1\n\ts_mov_b64 exec, -1" :: "v"(OFF_CI + (416 + xcd) * 8), "v"((v2u){tag, tag}), "s"(p.ws) : "memory");
            }
        }
        STAMP(3);
        asm volatile("s_waitcnt vmcnt(0)" :: "v"(qb), "v"(qc), "v"(qd), "v"(qe) : "memory");
        if (xcd == 0) {
            const u32 cio = OFF_CI + (tid < 212 ? tid : 0) * 16;
            for (u32 spins = 0;; ++spins) {
                REREAD();
                const v4u q2 = __builtin_amdgcn_raw_buffer_load_b128(WSR(), cio, 0, 16);
                if (__all((q2.y == tag) & (q2.w == tag)) || spins > SPIN_CAP) break;
                __builtin_amdgcn_s_sleep(16);
            }
            BAR();
            if (tid == 0) __hip_atomic_store((GAS u32*)p.ws, tag, RLX_AGENT);
        }
        ROLE_END();
    }
    else
        {
            ROLE_BEGIN(3);
            const int pw = wave - 4;
            u32 tagv;
            asm volatile("global_load_dword %0, %1, %2 sc1" : "=v"(tagv) : "v"(0u), "s"(p.ws) : "memory");
            FFN_FILL();
            v2u sel;
            const u32 so = OFF_SEL + xcd * 256 + (2 * pw + (lane & 1)) * 8, l16 = lane * 16;
#define POLL_S "global_load_dwordx2 v[10:11], %[so], %[ws] sc1\n\t"
            asm volatile("s_waitcnt vmcnt(18)" : "+v"(tagv) :: "memory");
            const u32 tag = __builtin_amdgcn_readfirstlane(tagv) + 1u;
            *(LAS v4u*)(smem + FL_Z + min(l16, 256u)) = (v4u){lane >= 16 ? tag : 0u, 0u, 0u, 0u};
            FFN_ADDR();
            u32 one = 1u;
            const char* dsrc = (const char*)(p.wd + (size_t)(64 * w + 32 * (pw >> 1)) * D);
            const u32 ddst = (pw >> 1) * 32 * WDP + (pw & 1) * 1024;
            u32 dof[19];
#pragma unroll
            for (int i = 0; i < 19; ++i) {
                const u32 id = min(128u * i + 64u * (pw & 1) + lane, 2367u), r0 = id / 74u;
                dof[i] = r0 * 1152u + min(id - 74u * r0, 71u) * 16u;
                asm volatile("" : "+v"(dof[i]));
            }
#define LD_WD(i) asm volatile("s_mov_b32 m0, %2\n\ts_nop 0\n\tglobal_load_lds_dwordx4 %0, %1" :: "v"(dof[i]), "s"(dsrc), "s"(ddst + (i) * 2048) : "memory")
            asm volatile("s_waitcnt vmcnt(12) lgkmcnt(0)\n\ts_barrier\n\ts_waitcnt vmcnt(6)\n\ts_barrier\n\ts_waitcnt vmcnt(%[pk])\n\t" POLL_S "s_waitcnt vmcnt(1)\n\ts_barrier" : "=&{v[10:11]}"(sel) : [so] "v"(so), [ws] "s"(p.ws), [pk] "n"(PK) : "memory");
#pragma unroll
            for (int i = 0; i < K1; ++i) LD_WD(i);
            u32 c_;
            asm volatile("s_mov_b32 %[c], 0x80000\n\ts_waitcnt vmcnt(%[k])\n1:\n\tv_cmp_ne_u32 vcc, %[tag], v11\n\ts_cmp_eq_u64 vcc, 0\n\ts_cbranch_scc1 2f\n\t" POLL_S "s_waitcnt vmcnt(0)\n\ts_sub_u32 %[c], %[c], 1\n\ts_cbranch_scc0 1b\n2:"
                         : "+{v[10:11]}"(sel), [c] "=&s"(c_) : [so] "v"(so), [ws] "s"(p.ws), [tag] "s"(tag), [k] "n"(K1) : "memory", "scc", "vcc");
            STAMP(3);
#pragma unroll
            for (int k = 0; k < 2; ++k) {
                const u32 t = ~(u32)__builtin_amdgcn_readlane((int)sel.x, k) & 2047u;
                const char* xr = (const char*)p.x + t * (D * 2);
                asm volatile("s_mov_b32 m0, %2\n\ts_nop 0\n\tglobal_load_lds_dwordx4 %0, %1\n\ts_mov_b64 exec, 0xff\n\tglobal_load_lds_dwordx4 %0, %1 offset:1024\n\ts_mov_b64 exec, -1"
                             :: "v"(l16), "s"(xr), "s"(FL_XS + (2 * pw + k) * XSP) : "memory");
            }
#pragma unroll
            for (int i = K1; i < K1 + K2; ++i) LD_WD(i);
            asm volatile("s_waitcnt vmcnt(%0)\n\ts_barrier" :: "n"(K2) : "memory");
            STAMP(10);
#pragma unroll
            for (int i = K1 + K2; i < 18; ++i) LD_WD(i);
            if (!(pw & 1)) LD_WD(18);
            asm volatile("s_mov_b64 exec, 1\n\tglobal_store_dwordx2 %0, %1, %2 sc1\n\ts_mov_b64 exec, -1\n\ts_waitcnt vmcnt(1)" :: "v"(OFF_CI + (128 + 4 * w + pw) * 8), "v"((v2u){tag, tag}), "s"(p.ws) : "memory");
            LDS_ADD1(ctl, one, 4);
            LSPIN(ctl, 4, 4);
            STAMP(13);
            s16x4 a[3][8];
            FFN_WDFRAG(0, 8 + pw); FFN_WDFRAG(1, 12 + pw); FFN_WDFRAG(2, 16 + (pw & 1));
            asm volatile("s_barrier" ::: "memory");
            FFN_HFRAG();
            v4f ya[3];
            FFN_DOWN(0, 8 + pw); FFN_DOWN(1, 12 + pw);
            if (pw < 2) { FFN_DOWN(2, 16 + pw); FFN_PUB(2, 16 + pw); }
            FFN_PUB(0, 8 + pw); FFN_PUB(1, 12 + pw);
            STAMP(9);
            ROLE_END();
        }
    } else
    {
        ROLE_BEGIN(5);
        if (XT) {
            const u32 id = ((slot - 28) + 4 * wave) * 64 + lane, dst = (u32)(uintptr_t)(smem + wave * 256), lo = (id & 1) ? 1024u : xcd * 128u;
            if (XT & 2) asm volatile("s_mov_b32 m0, %2\n\ts_nop 0\n\tglobal_load_lds_dword %0, %1" :: "v"((((id >> 3) & 255) * 4 + 2 + ((id >> 1) & 1)) * 1152u + lo), "s"(p.x), "s"(dst) : "memory");
            asm volatile("s_mov_b32 m0, %2\n\ts_nop 0\n\tglobal_load_lds_dword %0, %1" :: "v"((1024 + (id >> 1)) * 1152u + lo), "s"(p.x), "s"(dst) : "memory");
        }
        if (FDELAY) __builtin_amdgcn_s_sleep(FDELAY);
        _Pragma("unroll 1") for (int e = (slot - 28) + 4 * wave; touch(p, xcd, e, lane, (u32)(uintptr_t)(smem + wave * 256)); e += 32) {}
        STAMP(11);
        asm volatile("s_waitcnt vmcnt(0)" ::: "memory");
        STAMP(6);
        ROLE_END();
    }
    } else
        {
            ROLE_BEGIN(2);
            FFN_FILL();
            FFN_ADDR();
            LAS char* gp = smem + q * 2048 + ((lane >> 2) & 3) * 128 + ((2 * wave + ((lane >> 1) & 1)) ^ (((lane >> 2) & 2) | ((q & 1) << 2))) * 16 + (lane & 1) * 8;
            LAS char* xp = smem + FL_XS + (n & 7) * XSP + q * 16;
            const int hc = 16 * wave + n;
            LAS char* hw_ = smem + FL_H + (4 * (q & 1) + 2 * (q >> 1)) * 144 + ((hc & 35) | ((hc & 12) << 1) | ((hc & 16) >> 2)) * 2;
            asm volatile("" : "+v"(gp), "+v"(xp), "+v"(hw_));
            bf16x8 gf[18], uf[18];
#pragma unroll
            for (int sl = 0; sl < 3; ++sl) {
                if (sl == 0) asm volatile("s_waitcnt vmcnt(12)\n\ts_barrier" ::: "memory");
                if (sl == 1) asm volatile("s_waitcnt vmcnt(6)\n\ts_barrier" ::: "memory");
                if (sl == 2) asm volatile("s_waitcnt vmcnt(0) lgkmcnt(0)\n\ts_barrier" ::: "memory");
                if (sl == 0) STAMP(5);
                if (sl == 2) STAMP(6);
                LAS char* g_ = gp + sl * 49152;
                asm volatile("" : "+v"(g_));
#pragma unroll
                for (int i = 0; i < 6; ++i) {
                    gf[6 * sl + i] = __builtin_shufflevector(__builtin_amdgcn_ds_read_tr16_b64_v4i16((LAS s16x4*)(g_ + i * 8192)), __builtin_amdgcn_ds_read_tr16_b64_v4i16((LAS s16x4*)(g_ + i * 8192 + 512)), 0, 1, 2, 3, 4, 5, 6, 7);
                    uf[6 * sl + i] = __builtin_shufflevector(__builtin_amdgcn_ds_read_tr16_b64_v4i16((LAS s16x4*)(g_ + i * 8192 + 1024)), __builtin_amdgcn_ds_read_tr16_b64_v4i16((LAS s16x4*)(g_ + i * 8192 + 1536)), 0, 1, 2, 3, 4, 5, 6, 7);
                }
#pragma unroll
                for (int i = 0; i < 6; ++i) asm volatile("" : "+v"(gf[6 * sl + i]), "+v"(uf[6 * sl + i]));
            }
            u32 tag = *(LAS u32*)(smem + FL_TAG);
            asm volatile("s_barrier" : "+v"(tag) :: "memory");
            STAMP(1);
            bf16x8 xf[18];
            v4f ag = {0.f, 0.f, 0.f, 0.f}, au = ag;
#pragma unroll
            for (int s = 0; s < 4; ++s) xf[s] = *(LAS bf16x8*)(xp + s * 64);
            SBAR();
#pragma unroll
            for (int s = 0; s < 18; ++s) {
                ag = MF(xf[s], gf[s], ag);
                if (s + 4 < 18) xf[s + 4] = *(LAS bf16x8*)(xp + (s + 4) * 64);
                au = MF(xf[s], uf[s], au);
                SBAR();
            }
            STAMP(4);
            {
                const u32 ab = cvtpk(q < 2 ? ag[0] : ag[2], q < 2 ? ag[1] : ag[3]), ub = cvtpk(q < 2 ? au[0] : au[2], q < 2 ? au[1] : au[3]);
                const u32 sl = cvtpk(bflo(ab) * sigmoidf_(bflo(ab)), bfhi(ab) * sigmoidf_(bfhi(ab)));
                const u32 hh = cvtpk(bflo(sl) * bflo(ub), bfhi(sl) * bfhi(ub));
                *(LAS u16*)(hw_) = (u16)hh; *(LAS u16*)(hw_ + 144) = (u16)(hh >> 16);
            }
            STAMP(8);
            LSPIN(ctl, 4, 4);
            s16x4 a[2][8];
            FFN_WDFRAG(0, 2 * wave); FFN_WDFRAG(1, 2 * wave + 1);
            asm volatile("s_barrier" ::: "memory");
            STAMP(13);
            FFN_HFRAG();
            v4f ya[2];
            FFN_DOWN(0, 2 * wave); FFN_DOWN(1, 2 * wave + 1);
            FFN_PUB(0, 2 * wave); FFN_PUB(1, 2 * wave + 1);
            STAMP(9);
            ROLE_END();
        }
    } else {
    if (wave >= 4)
    {
        ROLE_BEGIN(1);
        const int hw = wave - 4, ht = tid - 256;
        const v4u z4 = {0, 0, 0, 0};
        const int sub = lane & 15;
        v4u xr[5];
        u32 tagv;
        const int rrow = 16 * (cw & 127) + 4 * hw;
        {
            const u32 ro = (u32)((rrow + (lane >> 4)) * D + sub * 8) * 2, ro4 = ro - (sub & 8) * 16;
#pragma unroll
            for (int j = 0; j < 4; ++j) asm volatile("global_load_dwordx4 %0, %1, %2 offset:%3" : "=v"(xr[j]) : "v"(ro), "s"(p.x), "n"(j * 256));
            asm volatile("global_load_dwordx4 %0, %1, %2 offset:1024" : "=v"(xr[4]) : "v"(ro4), "s"(p.x));
            SBAR();
            const u32 dst = (u32)(uintptr_t)(smem + LDS_RW) + hw * 3072, l16 = lane * 16;
            asm volatile("s_mov_b32 m0, %5\n\tglobal_load_dword %0, %1, %2 sc1\n\tglobal_load_lds_dwordx4 %3, %4" : "=&v"(tagv) : "v"(0u), "s"(p.ws), "v"(l16), "s"(p.wr), "s"(dst) : "memory");
            glds16(lane < 8 ? p.wr + 512 + lane * 8 : p.gw + (lane - 8) * 8, dst + 1024, false);
            asm volatile("s_mov_b32 m0, %2\n\ts_nop 0\n\tglobal_load_lds_dwordx4 %0, %1" :: "v"(896 + sub * 16), "s"(p.gw), "s"(dst + 2048) : "memory");
        }
        __builtin_amdgcn_s_barrier();
        STAMP(1);
        if (XPF) {
            const int id = min((slot * 4 + hw) * 64 + lane, 4095);
            u32 keep;
            asm volatile("s_mov_b32 %0, m0\n\ts_mov_b32 m0, %3\n\ts_nop 0\n\tglobal_load_lds_dword %1, %2\n\ts_mov_b32 m0, %0" : "=&s"(keep) : "v"((u32)((id >> 1) * 1152 + ((id & 1) ? 1024 : xcd * 128))), "s"(p.x), "s"((u32)(uintptr_t)(smem + LDS_DUMMY)) : "memory");
        }
        v2u uu[8];
        const u32 uo0 = (u32)((256 * hw + 4 * lane) * D + chbase) * 2, uo1 = uo0 + 1024 * D * 2;
#define LD_U(i) asm volatile("global_load_dwordx2 %0, %1, %2 offset:%3" : "=v"(uu[i]) : "v"((i) < 4 ? uo0 : uo1), "s"(p.x), "n"(((i) & 3) * D * 2))
#pragma unroll
        for (int i = 0; i < NB1; ++i) LD_U(i);
        *(LAS v4u*)(smem + LDS_TAP + ht * 16) = z4; *(LAS v4u*)(smem + LDS_TAP + 4096 + ht * 16) = z4;
        *(LAS v4u*)(smem + LDS_TAP + 8192 + ht * 16 + (ht >> 7) * (LDS_U - LDS_TAP - 8192 + ((ht >> 5) & 3) * 4608 + (ht & 31) * 16 - ht * 16)) = z4;
        u32 ctl = (u32)(uintptr_t)(smem + LDS_CTL), one = 1u, cty = ctl + 64 * hw + (lane & 3) * 16;
        const u32 late = (u32)((127 - cw) >> 31), pa_ = OFF_RG + hline(p.ws, cw & 127) + hw * 32, pk_ = pa_ ^ ((pa_ ^ (OFF_CI + ((cw - 128) * 4 + hw) * 16)) & late);
        u32 po = pk_ + (lane >> 1) + (sub != 0) * (OFF_G + hline(p.ws, ((lane - 1) & 3) * 64 + ((cw & 127) >> 1)) + ((4 * cw + hw) & 7) * 16 - pk_ - (lane >> 1));
        const u64 pmask = 1ull | ((u64)(~late & 1u) << 32) | (~late & ((1u << (1 + REPG)) - 1));
        u32 nrow = 0xffffu - (rrow + (lane >> 4));
        u32 go = OFF_G + hline(p.ws, (cw % REPG) * 64 + 8 * hw + (lane >> 3)) + (lane & 7) * 16, go2 = OFF_G + hline(p.ws, (cw % REPG) * 64 + 32 + 8 * hw + (lane >> 3)) + (lane & 7) * 16;
        LAS char* up = smem + LDS_U + (256 + 256 * hw + 4 * lane) * 2;
        LAS char* yp = smem + CH_Y + hw * 1152 + (lane + (lane >> 3)) * 16;
        LAS char* rw = smem + LDS_RW + hw * 3072 + sub * 16;
        asm volatile("" : "+v"(ctl), "+v"(one), "+v"(cty), "+v"(po), "+v"(go), "+v"(go2), "+v"(up), "+v"(yp), "+v"(rw), "+v"(nrow));
        u32 wt[3];
#pragma unroll
        for (int k = WPF; k < 3; ++k) {
            wt[k] = wd_line(xcd, (36 * k + slot * 2 + (hw & 1)) * 48 + min(lane, 47));
            asm volatile("" : "+v"(wt[k]));
        }
        BAR();
        __builtin_amdgcn_s_setprio(3);
        u32 tag;
        {
            asm volatile("s_waitcnt vmcnt(%6)" : "+v"(xr[0]), "+v"(xr[1]), "+v"(xr[2]), "+v"(xr[3]), "+v"(xr[4]), "+v"(tagv) : "n"(NB1 + XPF) : "memory");
            STAMP(2);
            tag = __builtin_amdgcn_readfirstlane(tagv) + 1u;
            if (sub >= 8) xr[4] = z4;
            float r = 0.f, s = 0.f;
#pragma unroll
            for (int j = 0; j < 5; ++j) {
                const v4u a = *(LAS v4u*)(rw + j * 256), g = *(LAS v4u*)(rw + 1152 + j * 256);
#pragma unroll
                for (int e = 0; e < 4; ++e) {
                    const u32 xe = xr[j][e], ae = a[e], ge = g[e];
                    r = __builtin_amdgcn_fdot2_f32_bf16(__builtin_bit_cast(bf16x2_t, xe), __builtin_bit_cast(bf16x2_t, ae), r, false);
                    s = __builtin_amdgcn_fdot2_f32_bf16(__builtin_bit_cast(bf16x2_t, xe), __builtin_bit_cast(bf16x2_t, ge), s, false);
                }
            }
            r = dpp_add<0x128, 0xf>(r); s = dpp_add<0x128, 0xf>(s); r = dpp_add<0x124, 0xf>(r); s = dpp_add<0x124, 0xf>(s);
            r = dpp_add<0x122, 0xf>(r); s = dpp_add<0x122, 0xf>(s); r = dpp_add<0x121, 0xf>(r); s = dpp_add<0x121, 0xf>(s);
            const u32 pay = make_payload(r, sigmoidf_(rbf(s))), kk = (pay & 0xffff0000u) | nrow;
            const u32 p0 = (u32)__builtin_amdgcn_readlane((int)pay, 0), p1 = (u32)__builtin_amdgcn_readlane((int)pay, 16), p2 = (u32)__builtin_amdgcn_readlane((int)pay, 32), p3 = (u32)__builtin_amdgcn_readlane((int)pay, 48);
            const auto sw = __builtin_amdgcn_permlane16_swap(kk, kk, false, false);
            const u32 g01 = (p0 & 0xffffu) | (p1 << 16), g23 = (p2 & 0xffffu) | (p3 << 16);
            const v4u pv = {sub ? g01 : sw[0], tag, sub ? g23 : sw[1], tag};
            asm volatile("s_mov_b64 exec, %3\n\tglobal_store_dwordx4 %0, %1, %2 sc1\n\ts_mov_b64 exec, -1\n\ts_nop 0" :: "v"(po), "v"(pv), "s"(p.ws), "s"(pmask) : "memory");
        }
        STAMP(3);
        __builtin_amdgcn_s_setprio(HPRIO);
        *(LAS u32*)(smem + LDS_CTL + C_TAG) = tag;
        LDS_ADD1(ctl, one, C_PUB);
        LWAIT(ctl, C_PUB, 4);
#pragma unroll
        for (int i = NB1; i < 4; ++i) LD_U(i);
        LDS_ADD1(ctl, one, C_PUB);
        LWAIT(ctl, C_PUB, 8);
        v4u gq[2];
#define POLL_G "global_load_dwordx4 v[100:103], %[go], %[ws] sc1\n\tglobal_load_dwordx4 v[104:107], %[go2], %[ws] sc1\n\t"
#define LD_U4 "global_load_dwordx2 %[u4], %[uo], %[x]\n\tglobal_load_dwordx2 %[u5], %[uo], %[x] offset:1152\n\tglobal_load_dwordx2 %[u6], %[uo], %[x] offset:2304\n\tglobal_load_dwordx2 %[u7], %[uo], %[x] offset:3456\n\t"
        asm volatile("" : "=&{v[100:103]}"(gq[0]), "=&{v[104:107]}"(gq[1]), "=&v"(uu[4]), "=&v"(uu[5]), "=&v"(uu[6]), "=&v"(uu[7]));
        asm volatile("s_cmp_ge_u32 %[hw], 2\n\ts_cbranch_scc1 1f\n\t" LD_U4 POLL_G "s_waitcnt vmcnt(6)\n\ts_branch 2f\n1:\n\ts_waitcnt vmcnt(0)\n2:"
                     : "+{v[100:103]}"(gq[0]), "+{v[104:107]}"(gq[1]), [u4] "+v"(uu[4]), [u5] "+v"(uu[5]), [u6] "+v"(uu[6]), [u7] "+v"(uu[7]), "+v"(uu[0]), "+v"(uu[1]), "+v"(uu[2]), "+v"(uu[3])
                     : [go] "v"(go), [go2] "v"(go2), [ws] "s"(p.ws), [uo] "v"(uo1), [x] "s"(p.x), [hw] "s"(hw) : "memory", "scc");
        STAMP(4);
        u32 ct2 = ctl + (hw & 2) * 2;
        auto stage = [&](int g) __attribute__((always_inline)) {
            const v2u* u4 = uu + 4 * g;
            LAS char* d = up + g * 2048;
            *(LAS v2u*)(d) = (v2u){__builtin_amdgcn_perm(u4[1].x, u4[0].x, 0x05040100u), __builtin_amdgcn_perm(u4[3].x, u4[2].x, 0x05040100u)};
            *(LAS v2u*)(d + 4608) = (v2u){__builtin_amdgcn_perm(u4[1].x, u4[0].x, 0x07060302u), __builtin_amdgcn_perm(u4[3].x, u4[2].x, 0x07060302u)};
            *(LAS v2u*)(d + 2 * 4608) = (v2u){__builtin_amdgcn_perm(u4[1].y, u4[0].y, 0x05040100u), __builtin_amdgcn_perm(u4[3].y, u4[2].y, 0x05040100u)};
            *(LAS v2u*)(d + 3 * 4608) = (v2u){__builtin_amdgcn_perm(u4[1].y, u4[0].y, 0x07060302u), __builtin_amdgcn_perm(u4[3].y, u4[2].y, 0x07060302u)};
            if (g) LDS_ADD1(ct2, one, C_ST2); else LDS_ADD1(ctl, one, C_STG);
        };
        stage(0);
        STAMP(5);
        asm volatile("s_cmp_lt_u32 %[hw], 2\n\ts_cbranch_scc1 1f\n\t" POLL_G LD_U4 "\n1:"
                     : "+{v[100:103]}"(gq[0]), "+{v[104:107]}"(gq[1]), [u4] "+v"(uu[4]), [u5] "+v"(uu[5]), [u6] "+v"(uu[6]), [u7] "+v"(uu[7])
                     : [go] "v"(go), [go2] "v"(go2), [ws] "s"(p.ws), [uo] "v"(uo1), [x] "s"(p.x), [hw] "s"(hw) : "memory", "scc");
        if (hw < 2) {
            asm volatile("s_waitcnt vmcnt(2)" : "+v"(uu[4]), "+v"(uu[5]), "+v"(uu[6]), "+v"(uu[7]));
            stage(1);
            STAMP(6);
        }
        {
            u32 c_;
            asm volatile("s_mov_b32 %[c], 0x80000\n\ts_waitcnt vmcnt(4)\n\ts_cmp_ge_u32 %[hw], 2\n\ts_cbranch_scc1 1f\n\ts_waitcnt vmcnt(0)\n1:\n\tv_cmpx_eq_u32 %[tag], v101\n\tv_cmpx_eq_u32 %[tag], v103\n\tv_cmpx_eq_u32 %[tag], v105\n\tv_cmpx_eq_u32 %[tag], v107\n\t"
                         "s_cmp_eq_u64 exec, -1\n\ts_mov_b64 exec, -1\n\ts_cbranch_scc1 2f\n\ts_sleep 4\n\t" POLL_G "s_waitcnt vmcnt(0)\n\ts_sub_u32 %[c], %[c], 1\n\ts_cbranch_scc0 1b\n2:\n\ts_nop 4"
                         : "+{v[100:103]}"(gq[0]), "+{v[104:107]}"(gq[1]), [c] "=&s"(c_) : [go] "v"(go), [go2] "v"(go2), [ws] "s"(p.ws), [tag] "s"(tag), [hw] "s"(hw) : "memory", "scc", "vcc");
        }
        STAMP(7);
        *(LAS v2u*)(smem + LDS_G + hw * 3072 + lane * 8) = (v2u){gq[0].x, gq[0].z}; *(LAS v2u*)(smem + LDS_G + hw * 3072 + 512 + lane * 8) = (v2u){gq[1].x, gq[1].z};
        if (hw < 2) {
#pragma unroll
            for (int k = WPF; k < 3; ++k) asm volatile("s_mov_b32 m0, %2\n\ts_nop 0\n\tglobal_load_lds_dword %0, %1" :: "v"(wt[k]), "s"(p.wd), "n"(LDS_DUMMY) : "memory");
        }
#pragma unroll
        for (int g = 0; g < 2; ++g) {
            if (g == 1 && hw >= 2) {
                asm volatile("s_waitcnt vmcnt(4)" : "+v"(uu[4]), "+v"(uu[5]), "+v"(uu[6]), "+v"(uu[7]));
                stage(1);
                STAMP(6);
            }
            const v2u* u4 = uu + 4 * g;
            const float gr[4] = {bflo(gq[g].x), bfhi(gq[g].x), bflo(gq[g].z), bfhi(gq[g].z)};
            float o[4][4];
#pragma unroll
            for (int r = 0; r < 4; ++r) {
                const float xv[4] = {bflo(u4[r].x), bfhi(u4[r].x), bflo(u4[r].y), bfhi(u4[r].y)};
#pragma unroll
                for (int c = 0; c < 4; ++c) asm("v_fma_f32 %0, -%1, %2, %2" : "=v"(o[r][c]) : "v"(gr[r]), "v"(xv[c]));
                asm volatile("" : "+v"(o[r][0]), "+v"(o[r][1]), "+v"(o[r][2]), "+v"(o[r][3]));
            }
            {
                u32 t_, c_;
                asm volatile("s_mov_b32 %1, 0x400000\n1:\n\tds_read_b32 %0, %2 offset:%3\n\ts_waitcnt lgkmcnt(0)\n\tv_cmp_eq_u32 vcc, 0, %0\n\ts_cmp_eq_u64 vcc, 0\n\ts_cbranch_scc1 2f\n\ts_sleep %4\n\ts_sub_u32 %1, %1, 1\n\ts_cbranch_scc0 1b\n2:"
                             : "=&v"(t_), "=&s"(c_) : "v"(cty), "n"(C_Y + 256 * g), "n"(YSLEEP) : "memory", "scc", "vcc");
            }
            if (g == 0) STAMP(11);
            v4f y[4];
#pragma unroll
            for (int c = 0; c < 4; ++c) y[c] = *(LAS v4f*)(yp + c * KCH + g * 4608);
            SBAR();
#define ST_O(i, v) asm volatile("global_store_dwordx2 %0, %1, %2 offset:%3" :: "v"(g ? uo1 : uo0), "v"(v), "s"(p.out), "n"((i) * D * 2) : "memory")
#pragma unroll
            for (int r = 0; r < 4; ++r) {
#pragma unroll
                for (int c = 0; c < 4; ++c) { const float yv = y[c][r]; asm("v_fmac_f32 %0, %1, %2" : "+v"(o[r][c]) : "v"(gr[r]), "v"(yv)); }
                ST_O(r, ((v2u){cvtpk(o[r][0], o[r][1]), cvtpk(o[r][2], o[r][3])}));
            }
            STAMP(8 + g);
        }
        asm volatile("s_waitcnt vmcnt(0)" ::: "memory");
        LDS_ADD1(ctl, one, C_ACK);
        STAMP(10);
        ROLE_END();
    }
    else
    {
        ROLE_BEGIN(0);
        const int ch = wave;
        const int dch = chbase + ch, pi = dch * NST + lane * 2;
        const u32 are = __builtin_nontemporal_load((const u32*)(p.Are + pi)), aim = __builtin_nontemporal_load((const u32*)(p.Aim + pi)), bre = __builtin_nontemporal_load((const u32*)(p.Bre + pi));
        const u32 bim = __builtin_nontemporal_load((const u32*)(p.Bim + pi)), cre = __builtin_nontemporal_load((const u32*)(p.Cre + pi)), cim = __builtin_nontemporal_load((const u32*)(p.Cim + pi));
        const u16 ldt = p.logdt[dch], dsk = p.dskip[dch];
        SBAR();
        __builtin_amdgcn_s_barrier();
        SBAR();
        LAS char* kb = smem + ch * KCH;
        const int r32 = lane & 31, h = lane >> 5, q = lane >> 4, n = lane & 15;
        const int ai = 4 * (r32 >> 3) + (r32 & 3), hh = (r32 >> 2) & 1;
        const u64 tsel = ai < 8 ? (hh ? KROW_1LO : KROW_0LO) : (hh ? KROW_1HI : KROW_0HI);
        const int o = (int)((tsel >> (8 * (ai & 7))) & 0xff);
        LAS char* ap = kb + o * STG_ROW + h * 16;
        LAS char* bp = kb + r32 * STG_ROW + h * 16;
        u32 sg[14];
#pragma unroll
        for (int k = 0; k < 14; ++k) { sg[k] = (u32)(uintptr_t)(kb + lane * 8 + k * 4 * STG_ROW); asm volatile("" : "+v"(sg[k])); }
        bf16x8 b5 = band(((0x1fu << 6) << n) >> (8 * q), 0x3f803f80u), b3 = band(((0x7u << 11) << n) >> (8 * q), 0x3f803f80u);
        bf16x8 b9 = band(((0x1ffu << 4) << n) >> (8 * q), 0x3f803f80u), b7 = band(((0x7fu << 5) << n) >> (8 * q), 0x3f803f80u);
        bf16x8 pat = band(((0x3u << 12) << n) >> (8 * q), 0xffffffffu);
        LAS char* k0 = kb + 32 + lane * 16;
        LAS char* kt = kb + (lane & 3) * KCOPY + (lane < 16 ? 32 + 1024 + (lane >> 2) * 16 : ((lane >> 2) & 1) * 16);
        LAS char* a0 = kb + ((lane >> 2) & 3) * KCOPY + (28 - 8 * q + 4 * (lane & 3)) * 2;
        LAS char* bb = smem + LDS_U + ch * 4608 + (16 * n + 8 * q - 16) * 2;
        LAS char* yb = kb + CH_Y + (4 * n + q + (n >> 1)) * 16;
        LAS char* tp = smem + LDS_TAP + ch * 2304 + ((n & 1) ? 13 + n - 4 * q : 96 + 14 + n - 4 * q) * 4;
        LAS char* ta = smem + LDS_TAP + ch * 2304 + (h ? 0 : 768) + 64 + 2 * r32;
        LAS char* tb = smem + LDS_TAP + ch * 2304 + 1600 + 64 * h + 2 * r32;
        u32 ctl = (u32)(uintptr_t)(smem + LDS_CTL), ctc = ctl + C_Y + ch * 16 + (lane & 15), one = 1u, so = OFF_SEL + (8 + xcd) * 256 + (lane & 7) * 8;
        u32 m01 = lane < 2 ? ~0u : 0u, m2 = lane == 2 ? ~0u : 0u, m3 = lane == 3 ? ~0u : 0u, m0_ = lane == 0 ? ~0u : 0u;
        asm volatile("" : "+v"(b5), "+v"(b3), "+v"(b9), "+v"(b7), "+v"(pat), "+v"(ap), "+v"(bp), "+v"(m01), "+v"(m2), "+v"(m3), "+v"(m0_));
        asm volatile("" : "+v"(k0), "+v"(kt), "+v"(a0), "+v"(bb), "+v"(yb), "+v"(tp), "+v"(ta), "+v"(tb), "+v"(ctl), "+v"(ctc), "+v"(one), "+v"(so));
        __builtin_amdgcn_s_barrier();
        SBAR();
        const float dt = rbf(expf(bf2f(ldt)));
        STAMP(1);
        v16f acc;
        u32 stg;
        float kscale;
        {
            const v2f a2 = (v2f){bflo(are), bfhi(are)} * dt * 1.44269504088896341f;
            const v2f brev = (v2f){bflo(aim), bfhi(aim)} * dt * 0.15915494309189535f;
            const cf2 Bc = {{dt * bflo(bre), dt * bflo(bim)}, {dt * bfhi(bre), dt * bfhi(bim)}};
            const cf2 Cc = {{bflo(cre), bflo(cim)}, {bfhi(cre), bfhi(cim)}};
            cf2 W = cmul2(Cc, Bc);
            u32 mx = max(max(__float_as_uint(W.a.x) & 0x7f800000u, __float_as_uint(W.a.y) & 0x7f800000u), max(__float_as_uint(W.b.x) & 0x7f800000u, __float_as_uint(W.b.y) & 0x7f800000u));
            mx = dpp_umax<0x111, 0xf>(mx); mx = dpp_umax<0x112, 0xf>(mx); mx = dpp_umax<0x114, 0xf>(mx); mx = dpp_umax<0x118, 0xf>(mx);
            mx = dpp_umax<0x142, 0xa>(mx); mx = dpp_umax<0x143, 0xc>(mx);
            mx = min(max((u32)__builtin_amdgcn_readlane((int)mx, 63), 0x00800000u), 0x7e800000u);
            kscale = __uint_as_float(mx);
            const float inv = __uint_as_float(0x7f000000u - mx);
            W.a *= inv; W.b *= inv;
            const cf2 E1 = cexp2(a2, brev, 1.f), E8 = cexp2(a2, brev, 8.f), E256 = cexp2(a2, brev, 256.f);
            asm volatile("s_nop 0\n\t" KGEN_ASM :: "{v[48:49]}"(W.a), "{v[50:51]}"(W.b), "{v[52:53]}"(E1.a), "{v[54:55]}"(E1.b), "{v[56:57]}"(E8.a), "{v[58:59]}"(E8.b), "{v[60:61]}"(E256.a), "{v[62:63]}"(E256.b),
                         [b0] "v"(sg[0]), [b1] "v"(sg[1]), [b2] "v"(sg[2]), [b3] "v"(sg[3]), [b4] "v"(sg[4]), [b5] "v"(sg[5]), [b6] "v"(sg[6]), [b7] "v"(sg[7]), [b8] "v"(sg[8]), [b9] "v"(sg[9]),
                         [b10] "v"(sg[10]), [b11] "v"(sg[11]), [b12] "v"(sg[12]), [b13] "v"(sg[13]) : "memory", KGEN_CLOBBER);
            STAMP(2);
            asm volatile(KMFMA_ASM : "=&{v[48:63]}"(acc), [stg] "=&v"(stg) : [ap] "v"(ap), [bp] "v"(bp), [ctl] "v"(ctl) : "memory", KMFMA_CLOBBER);
            acc *= kscale;
        }
        STAMP(3);
        acc[0] += __uint_as_float(((u32)dsk << 16) & m0_);
        u32 t78 = cvtpk(acc[7], acc[8]), t9a = cvtpk(acc[9], acc[10]);
        {
            const auto s0 = __builtin_amdgcn_permlane32_swap(cvtpk(acc[0], acc[1]), cvtpk(acc[4], acc[4]), false, false);
            const auto s1 = __builtin_amdgcn_permlane32_swap(cvtpk(acc[2], acc[3]), cvtpk(acc[5], acc[6]), false, false);
            const v4u ww = {s0[0], s1[0], s0[1], s1[1]};
            const u32 pz = (u32)__builtin_amdgcn_update_dpp(0, (int)ww.z, 0x138, 0xf, 0xf, true), pw = (u32)__builtin_amdgcn_update_dpp(0, (int)ww.w, 0x138, 0xf, 0xf, true);
            const v4u c1 = {__builtin_amdgcn_alignbit(ww.x, pw, 16), __builtin_amdgcn_alignbit(ww.y, ww.x, 16), __builtin_amdgcn_alignbit(ww.z, ww.y, 16), __builtin_amdgcn_alignbit(ww.w, ww.z, 16)};
            *(LAS v4u*)(k0) = ww;
            *(LAS v4u*)(k0 + KCOPY) = c1;
            *(LAS v4u*)(k0 + 2 * KCOPY) = (v4u){pw, ww.x, ww.y, ww.z};
            *(LAS v4u*)(k0 + 3 * KCOPY) = (v4u){__builtin_amdgcn_alignbit(pw, pz, 16), c1.x, c1.y, c1.z};
            const u32 w0 = (u32)__builtin_amdgcn_readlane((int)ww.z, 63), w1 = (u32)__builtin_amdgcn_readlane((int)ww.w, 63);
            const u32 w3 = w1 >> 16, w2 = w3 * 0x10001u, w10 = (w0 >> 16) | (w1 << 16);
            *(LAS v4u*)kt = (v4u){(w2 & m01) | (w1 & m2) | (w10 & m3), (w3 & m0_) | (w2 & ((m01 ^ m0_) | m2 | m3)), (w3 & m2) | (w2 & m3), 0u};
        }
        asm volatile("" ::: "memory");
        pat = __builtin_bit_cast(bf16x8, __builtin_bit_cast(v4u, pat) & (((u32)__builtin_amdgcn_readlane((int)t78, 63) >> 16) * 0x10001u));
#define POLL_SEL "global_load_dwordx2 v[240:241], %[so], %[ws] sc1\n\t"
        v2u sq;
        const v4f zero4 = {0.f, 0.f, 0.f, 0.f};
        if (MPRIO) __builtin_amdgcn_s_setprio(MPRIO);
        STAMP(4);
        {
            u32 s_, c_;
            asm volatile(CONV_ASM : "=&{v[240:241]}"(sq), [s] "=&s"(s_), [c] "=&s"(c_)
                         : "{v[4:7]}"(b5), "{v[8:11]}"(b3), "{v[12:15]}"(b9), "{v[16:19]}"(b7), "{v[20:23]}"(pat), [a0] "v"(a0), [bb] "v"(bb), [yb] "v"(yb), [tp] "v"(tp), [ta] "v"(ta), [tb] "v"(tb),
                           [ctl] "v"(ctl), [ctc] "v"(ctc), [one] "v"(one), [so] "v"(so), [t78] "v"(t78), [t9a] "v"(t9a), [stg] "v"(stg), [ws] "s"(p.ws) : "memory", "scc", CONV_CLOBBER);
        }
        STAMP(9);
        asm volatile("s_cmp_eq_u32 %0, 0\n\ts_cbranch_scc1 1f\n\ts_endpgm\n1:" :: "s"(wave) : "memory", "scc");
        {
            const u32 tag = __builtin_amdgcn_readfirstlane(*(LAS u32*)(smem + LDS_CTL + C_TAG));
            u32 c_;
            u32 pb[9];
#pragma unroll
            for (int k = 0; k < 9; ++k) { pb[k] = OFF_PART + cw * PP + hline(p.ws, 8 * k + (lane >> 3)) + (lane & 7) * 16; asm volatile("" : "+v"(pb[k])); }
            {
                u32 h_;
                asm volatile("s_mov_b32 %[c], 64\n1:\n\ts_load_dword %[h], %[a], 0x4 glc\n\ts_waitcnt lgkmcnt(0)\n\ts_cmp_eq_u32 %[h], %[tag]\n\ts_cbranch_scc1 2f\n\ts_sub_u32 %[c], %[c], 1\n\ts_cbranch_scc0 1b\n2:"
                             : [h] "=&s"(h_), [c] "=&s"(c_) : [a] "s"(p.ws + (OFF_PART + cw * PP + hline(p.ws, cw >> 1))), [tag] "s"(tag) : "memory", "scc");
            }
            STAMP(10);
            typedef __attribute__((ext_vector_type(32))) u32 v32u;
            v32u ra, rb; v4u rc, rd;
            asm volatile("global_load_dwordx4 v[64:67], %[pb0], %[ws] sc1\n\tglobal_load_dwordx4 v[68:71], %[pb1], %[ws] sc1\n\tglobal_load_dwordx4 v[72:75], %[pb2], %[ws] sc1\n\tglobal_load_dwordx4 v[76:79], %[pb3], %[ws] sc1\n\tglobal_load_dwordx4 v[80:83], %[pb4], %[ws] sc1\n\tglobal_load_dwordx4 v[84:87], %[pb5], %[ws] sc1\n\tglobal_load_dwordx4 v[88:91], %[pb6], %[ws] sc1\n\tglobal_load_dwordx4 v[92:95], %[pb7], %[ws] sc1\n\tglobal_load_dwordx4 v[96:99], %[pb8], %[ws] sc1\n\t" : "=&{v[64:95]}"(ra), "=&{v[96:99]}"(rc) : [pb0] "v"(pb[0]), [pb1] "v"(pb[1]), [pb2] "v"(pb[2]), [pb3] "v"(pb[3]), [pb4] "v"(pb[4]), [pb5] "v"(pb[5]), [pb6] "v"(pb[6]), [pb7] "v"(pb[7]), [pb8] "v"(pb[8]), [ws] "s"(p.ws) : "memory");
            asm volatile("s_mov_b32 %[c], 0x80000\n\ts_waitcnt vmcnt(9)\n1:\n\tv_cmpx_eq_u32 %[tag], v241\n\t"
                         "s_cmp_eq_u64 exec, -1\n\ts_mov_b64 exec, -1\n\ts_cbranch_scc1 2f\n\t" POLL_SEL "s_waitcnt vmcnt(0)\n\ts_sub_u32 %[c], %[c], 1\n\ts_cbranch_scc0 1b\n2:\n\ts_nop 4"
                         : "+{v[240:241]}"(sq), [c] "=&s"(c_) : [so] "v"(so), [ws] "s"(p.ws), [tag] "s"(tag) : "memory", "scc", "vcc");
            const int t = (int)(0xffffu - (sq.x & 0xffffu)) & 2047;
            const float rs = rbf(sigmoidf_(key_to_r(sq.x)));
            LWAIT(ctl, C_ACK, 4);
            const float g = bf2f(*(LAS u16*)(smem + LDS_G + ((t >> 8) & 3) * 3072 + (t >> 10) * 512 + (t & 255) * 2));
            const int uq = (t & 255) >> 2;
            float xv[4], av[4];
#pragma unroll
            for (int c = 0; c < 4; ++c) {
                xv[c] = bf2f(*(LAS u16*)(smem + LDS_U + c * 4608 + (256 + t) * 2));
                av[c] = g * *(LAS float*)(smem + c * KCH + CH_Y + (t >> 8) * 1152 + (uq + (uq >> 3)) * 16 + (t & 3) * 4);
            }
            const u32 e1 = n < 4 ? ((n & 1) ? 0x3f800000u : 0x00003f80u) : 0u;
            bf16x8 sel4 = __builtin_bit_cast(bf16x8, (n & 2) ? (v4u){0u, 0u, e1, e1} : (v4u){e1, e1, 0u, 0u});
            u32 oo = (u32)(t * D + chbase) * 2;
            float omg = 1.f - g;
            asm volatile("" : "+v"(sel4), "+v"(oo), "+v"(omg), "+v"(xv[0]), "+v"(xv[1]), "+v"(xv[2]), "+v"(xv[3]), "+v"(av[0]), "+v"(av[1]), "+v"(av[2]), "+v"(av[3]));
            v4f s0, s1;
            asm volatile("s_mov_b32 %[c], 0x40000\n\tglobal_load_dwordx4 v[100:103], %[pb0], %[ws] sc1\n\tglobal_load_dwordx4 v[104:107], %[pb1], %[ws] sc1\n\tglobal_load_dwordx4 v[108:111], %[pb2], %[ws] sc1\n\tglobal_load_dwordx4 v[112:115], %[pb3], %[ws] sc1\n\tglobal_load_dwordx4 v[116:119], %[pb4], %[ws] sc1\n\tglobal_load_dwordx4 v[120:123], %[pb5], %[ws] sc1\n\tglobal_load_dwordx4 v[124:127], %[pb6], %[ws] sc1\n\tglobal_load_dwordx4 v[128:131], %[pb7], %[ws] sc1\n\tglobal_load_dwordx4 v[132:135], %[pb8], %[ws] sc1\n\t"
                         "1:\n\ts_waitcnt vmcnt(9)\n\tv_cmpx_eq_u32 %[tag], v65\n\tv_cmpx_eq_u32 %[tag], v67\n\tv_cmpx_eq_u32 %[tag], v69\n\tv_cmpx_eq_u32 %[tag], v71\n\tv_cmpx_eq_u32 %[tag], v73\n\tv_cmpx_eq_u32 %[tag], v75\n\tv_cmpx_eq_u32 %[tag], v77\n\tv_cmpx_eq_u32 %[tag], v79\n\tv_cmpx_eq_u32 %[tag], v81\n\tv_cmpx_eq_u32 %[tag], v83\n\tv_cmpx_eq_u32 %[tag], v85\n\tv_cmpx_eq_u32 %[tag], v87\n\tv_cmpx_eq_u32 %[tag], v89\n\tv_cmpx_eq_u32 %[tag], v91\n\tv_cmpx_eq_u32 %[tag], v93\n\tv_cmpx_eq_u32 %[tag], v95\n\tv_cmpx_eq_u32 %[tag], v97\n\tv_cmpx_eq_u32 %[tag], v99\n\ts_cmp_eq_u64 exec, -1\n\ts_mov_b64 exec, -1\n\ts_cbranch_scc1 3f\n\tglobal_load_dwordx4 v[64:67], %[pb0], %[ws] sc1\n\tglobal_load_dwordx4 v[68:71], %[pb1], %[ws] sc1\n\tglobal_load_dwordx4 v[72:75], %[pb2], %[ws] sc1\n\tglobal_load_dwordx4 v[76:79], %[pb3], %[ws] sc1\n\tglobal_load_dwordx4 v[80:83], %[pb4], %[ws] sc1\n\tglobal_load_dwordx4 v[84:87], %[pb5], %[ws] sc1\n\tglobal_load_dwordx4 v[88:91], %[pb6], %[ws] sc1\n\tglobal_load_dwordx4 v[92:95], %[pb7], %[ws] sc1\n\tglobal_load_dwordx4 v[96:99], %[pb8], %[ws] sc1\n\t"
                         "s_waitcnt vmcnt(9)\n\tv_cmpx_eq_u32 %[tag], v101\n\tv_cmpx_eq_u32 %[tag], v103\n\tv_cmpx_eq_u32 %[tag], v105\n\tv_cmpx_eq_u32 %[tag], v107\n\tv_cmpx_eq_u32 %[tag], v109\n\tv_cmpx_eq_u32 %[tag], v111\n\tv_cmpx_eq_u32 %[tag], v113\n\tv_cmpx_eq_u32 %[tag], v115\n\tv_cmpx_eq_u32 %[tag], v117\n\tv_cmpx_eq_u32 %[tag], v119\n\tv_cmpx_eq_u32 %[tag], v121\n\tv_cmpx_eq_u32 %[tag], v123\n\tv_cmpx_eq_u32 %[tag], v125\n\tv_cmpx_eq_u32 %[tag], v127\n\tv_cmpx_eq_u32 %[tag], v129\n\tv_cmpx_eq_u32 %[tag], v131\n\tv_cmpx_eq_u32 %[tag], v133\n\tv_cmpx_eq_u32 %[tag], v135\n\ts_cmp_eq_u64 exec, -1\n\ts_mov_b64 exec, -1\n\ts_cbranch_scc1 2f\n\tglobal_load_dwordx4 v[100:103], %[pb0], %[ws] sc1\n\tglobal_load_dwordx4 v[104:107], %[pb1], %[ws] sc1\n\tglobal_load_dwordx4 v[108:111], %[pb2], %[ws] sc1\n\tglobal_load_dwordx4 v[112:115], %[pb3], %[ws] sc1\n\tglobal_load_dwordx4 v[116:119], %[pb4], %[ws] sc1\n\tglobal_load_dwordx4 v[120:123], %[pb5], %[ws] sc1\n\tglobal_load_dwordx4 v[124:127], %[pb6], %[ws] sc1\n\tglobal_load_dwordx4 v[128:131], %[pb7], %[ws] sc1\n\tglobal_load_dwordx4 v[132:135], %[pb8], %[ws] sc1\n\t"
                         "s_sub_u32 %[c], %[c], 1\n\ts_cbranch_scc0 1b\n\ts_waitcnt vmcnt(0)\n"
                         "2:\n\tv_mov_b32 v101, v104\n\tv_mov_b32 v103, v106\n\tv_mov_b32 v109, v112\n\tv_mov_b32 v111, v114\n\tv_mov_b32 v117, v120\n\tv_mov_b32 v119, v122\n\tv_mov_b32 v125, v128\n\tv_mov_b32 v127, v130\n\tv_mov_b32 v133, 0\n\tv_mov_b32 v135, 0\n\tv_mfma_f32_16x16x32_bf16 v[136:139], %[sel], v[100:103], 0\n\tv_mfma_f32_16x16x32_bf16 v[140:143], %[sel], v[108:111], 0\n\tv_mfma_f32_16x16x32_bf16 v[136:139], %[sel], v[116:119], v[136:139]\n\tv_mfma_f32_16x16x32_bf16 v[140:143], %[sel], v[124:127], v[140:143]\n\tv_mfma_f32_16x16x32_bf16 v[136:139], %[sel], v[132:135], v[136:139]\n\ts_branch 4f\n"
                         "3:\n\tv_mov_b32 v65, v68\n\tv_mov_b32 v67, v70\n\tv_mov_b32 v73, v76\n\tv_mov_b32 v75, v78\n\tv_mov_b32 v81, v84\n\tv_mov_b32 v83, v86\n\tv_mov_b32 v89, v92\n\tv_mov_b32 v91, v94\n\tv_mov_b32 v97, 0\n\tv_mov_b32 v99, 0\n\tv_mfma_f32_16x16x32_bf16 v[136:139], %[sel], v[64:67], 0\n\tv_mfma_f32_16x16x32_bf16 v[140:143], %[sel], v[72:75], 0\n\tv_mfma_f32_16x16x32_bf16 v[136:139], %[sel], v[80:83], v[136:139]\n\tv_mfma_f32_16x16x32_bf16 v[140:143], %[sel], v[88:91], v[140:143]\n\tv_mfma_f32_16x16x32_bf16 v[136:139], %[sel], v[96:99], v[136:139]\n\t\n4:\n\ts_nop 7"
                         : "+{v[64:95]}"(ra), "+{v[96:99]}"(rc), "=&{v[100:131]}"(rb), "=&{v[132:135]}"(rd), "=&{v[136:139]}"(s0), "=&{v[140:143]}"(s1), [c] "=&s"(c_)
                         : [pb0] "v"(pb[0]), [pb1] "v"(pb[1]), [pb2] "v"(pb[2]), [pb3] "v"(pb[3]), [pb4] "v"(pb[4]), [pb5] "v"(pb[5]), [pb6] "v"(pb[6]), [pb7] "v"(pb[7]), [pb8] "v"(pb[8]), [ws] "s"(p.ws), [tag] "s"(tag), [sel] "v"(sel4) : "memory", "scc", "vcc");
            STAMP(11);
            float o[4];
#pragma unroll
            for (int c = 0; c < 4; ++c) o[c] = fmaf(omg, xv[c] + rbf(rbf(dpp_add<0x128, 0xf>(s0[c] + s1[c])) * rs), av[c]);
            asm volatile("s_mov_b64 exec, 0xff\n\tglobal_store_dwordx2 %0, %1, %2\n\ts_mov_b64 exec, -1" :: "v"(oo), "v"((v2u){cvtpk(o[0], o[1]), cvtpk(o[2], o[3])}), "s"(p.out) : "memory");
            STAMP(12);
            asm volatile("s_waitcnt vmcnt(0)" :: "v"(ra), "v"(rb), "v"(rc), "v"(rd) : "memory");
        }
        ROLE_END();
    }
    }
}

extern "C" void kernel_launch(void* const* d_in, const int* in_sizes, int n_in,
                              void* d_out, int out_size, void* d_ws, size_t ws_size,
                              hipStream_t stream) {
    Params p;
    p.x = (const u16*)d_in[0]; p.Are = (const u16*)d_in[1]; p.Aim = (const u16*)d_in[2]; p.Bre = (const u16*)d_in[3];
    p.Bim = (const u16*)d_in[4]; p.Cre = (const u16*)d_in[5]; p.Cim = (const u16*)d_in[6]; p.logdt = (const u16*)d_in[7];
    p.dskip = (const u16*)d_in[8]; p.wr = (const u16*)d_in[10]; p.wg = (const u16*)d_in[11]; p.wu = (const u16*)d_in[12];
    p.wd = (const u16*)d_in[13]; p.gw = (const u16*)d_in[14]; p.out = (u16*)d_out; p.ws = (char*)d_ws;
    (void)hipFuncSetAttribute((const void*)fused_kernel, hipFuncAttributeMaxDynamicSharedMemorySize, LDS_ALLOC);
    fused_kernel<<<256, 512, LDS_ALLOC, stream>>>(p);
}
```

```cpp
#include <hip/hip_runtime.h>
#include <stdint.h>

typedef unsigned short u16;
typedef unsigned int u32;
typedef unsigned long long u64;
typedef __attribute__((ext_vector_type(4))) u32 v4u;
typedef __attribute__((ext_vector_type(2))) u32 v2u;
typedef __attribute__((ext_vector_type(2))) float v2f;
typedef __attribute__((ext_vector_type(4))) float v4f;
typedef __attribute__((ext_vector_type(16))) float v16f;
typedef __attribute__((ext_vector_type(8))) short bf16x8;
typedef __attribute__((ext_vector_type(4))) short s16x4;
typedef __attribute__((ext_vector_type(2))) __bf16 bf16x2_t;
typedef __attribute__((ext_vector_type(2))) _Float16 f16x2_t;
typedef __attribute__((ext_vector_type(8))) _Float16 f16x8;
#define LAS __attribute__((address_space(3)))
#define GAS __attribute__((address_space(1)))
#define RLX_AGENT __ATOMIC_RELAXED, __HIP_MEMORY_SCOPE_AGENT
#define SBAR() __builtin_amdgcn_sched_barrier(0)
#define BAR() asm volatile("s_waitcnt lgkmcnt(0)\n\ts_barrier" ::: "memory")
#define WSR() __builtin_amdgcn_make_buffer_rsrc(p.ws, 0, 0x7fffffff, 0x00020000)
#define REREAD() asm volatile("" ::: "memory")

#define WNT " nt"
#define CODE_KB 25
#define NB1 2
#define REPG 2
#define PFG0 480
#define FDELAY 0
#define PFR0 16
#define SELPF 0
#define SELWAIT 50
#define MPRIO 0
#define YSLEEP 1
#define HPRIO 0
#define K2 4
#define TOUCH 0
#define XT 0
#define WPF 1
#define K1 6
#define PK 2
#define UDELAY 0
#define WT 0
#define XPF 0

constexpr int L = 2048, D = 576, NST = 128, F = 4608;
constexpr int NFFN = 72;
constexpr u32 OFF_RG = 4096;
constexpr u32 OFF_G = OFF_RG + 20480;
constexpr u32 OFF_SEL = OFF_G + REPG * 10240;
constexpr u32 OFF_CI = OFF_SEL + 16 * 256;
constexpr u32 OFF_PART = OFF_CI + 4096;
constexpr u32 PP = 11 * 1024;
constexpr u32 SPIN_CAP = 1u << 19;

#define STAMP(i)
#define STAMP0_FIX()
#define STAMP_END()
#define STAMP_FLUSH()

struct Params {
    const u16 *x, *Are, *Aim, *Bre, *Bim, *Cre, *Cim, *logdt, *dskip, *wr, *wg, *wu, *wd, *gw;
    u16* out;
    char* ws;
};

__device__ __forceinline__ float bflo(u32 w) { return __uint_as_float(w << 16); }
__device__ __forceinline__ float bfhi(u32 w) { return __uint_as_float(w & 0xffff0000u); }
__device__ __forceinline__ float bf2f(u16 h) { return __uint_as_float((u32)h << 16); }
__device__ __forceinline__ u32 cvtpk(float lo, float hi) { v2f v = {lo, hi}; bf16x2_t r = __builtin_convertvector(v, bf16x2_t); return __builtin_bit_cast(u32, r); }
__device__ __forceinline__ u32 cvth(v2f v) { return __builtin_bit_cast(u32, __builtin_convertvector(v, f16x2_t)); }
__device__ __forceinline__ float rbf(float f) { return bflo(cvtpk(f, f)); }
__device__ __forceinline__ float sigmoidf_(float s) { return __builtin_amdgcn_rcpf(1.f + __expf(-s)); }

template <int CTRL, int RM> __device__ __forceinline__ float dpp_add(float v) {
    return v + __builtin_bit_cast(float, __builtin_amdgcn_update_dpp(0, __builtin_bit_cast(int, v), CTRL, RM, 0xf, true));
}
__device__ __forceinline__ float wave_sum63(float v) {
    v = dpp_add<0x111, 0xf>(v); v = dpp_add<0x112, 0xf>(v); v = dpp_add<0x114, 0xf>(v); v = dpp_add<0x118, 0xf>(v);
    v = dpp_add<0x142, 0xa>(v); v = dpp_add<0x143, 0xc>(v);
    return v;
}
template <int CTRL, int RM> __device__ __forceinline__ u32 dpp_umax(u32 v) {
    u32 o = (u32)__builtin_amdgcn_update_dpp(0, (int)v, CTRL, RM, 0xf, true);
    return v > o ? v : o;
}

typedef v2f cf;
__device__ __forceinline__ cf cmul(cf a, cf b) { cf t = a.yy * (cf){-b.y, b.x}; return __builtin_elementwise_fma(a.xx, b, t); }
__device__ __forceinline__ cf cexp_(float a2, float brev, float rho) {
    float m = __builtin_amdgcn_exp2f(a2 * rho);
    float ph = __builtin_amdgcn_fractf(brev * rho);
    cf c; c.x = m * __builtin_amdgcn_cosf(ph); c.y = m * __builtin_amdgcn_sinf(ph); return c;
}

struct cf2 { cf a, b; };
__device__ __forceinline__ cf2 cmul2(cf2 x, cf2 y) { return {cmul(x.a, y.a), cmul(x.b, y.b)}; }
__device__ __forceinline__ cf2 cexp2(v2f a2, v2f brev, float rho) { return {cexp_(a2.x, brev.x, rho), cexp_(a2.y, brev.y, rho)}; }

__device__ __forceinline__ u32 make_payload(float r, float g) {
    const u32 w = cvtpk(g, r);
    return w ^ (((u32)((int)w >> 31) & 0x7fff0000u) | 0x80000000u);
}
__device__ __forceinline__ u32 make_key(u32 pay, u32 t) { return (pay & 0xffff0000u) | (0xffffu - t); }
__device__ __forceinline__ float key_to_r(u32 k) {
    u32 hi = k & 0xffff0000u;
    return __uint_as_float((hi & 0x80000000u) ? (hi ^ 0x80000000u) : (~hi & 0xffff0000u));
}

constexpr int STG_ROW = 528;
constexpr int NA = 21;
constexpr int KCH = (NA + 32) * STG_ROW;
constexpr int KCOPY = 1344;
constexpr int CH_Y = 4 * KCOPY;
constexpr int LDS_U = 4 * KCH;
constexpr int LDS_TAP = LDS_U + 4 * 4608;
constexpr int LDS_CTL = LDS_TAP + 4 * 2304;
constexpr int C_TAG = 0, C_STG = 4, C_PUB = 8, C_ACK = 12, C_ST2 = 20, C_Y = 64;
constexpr int LDS_RW = LDS_CTL + 1024;
constexpr int LDS_G = LDS_RW;
constexpr int LDS_DUMMY = LDS_RW + 4 * 3072;
constexpr int LDS_TOTAL = LDS_DUMMY + 1024;
static_assert(CH_Y + 8 * 1152 <= KCH && LDS_TOTAL <= 160 * 1024, "lds");
constexpr int WDP = 1184;
constexpr int XSP = 1168;
constexpr int FL_XS = 144 * 1024;
constexpr int FL_H = FL_XS + 8 * XSP;
constexpr int FL_Z = FL_H + 8 * 144;
constexpr int FL_TAG = FL_Z + 256;
constexpr int LDS_ALLOC = FL_TAG + 64 > LDS_TOTAL ? FL_TAG + 64 : LDS_TOTAL;
static_assert(LDS_ALLOC <= 160 * 1024, "lds");

__device__ __forceinline__ int sum_rows(int v) {
    auto a = __builtin_amdgcn_permlane16_swap((u32)v, (u32)v, false, false);
    v = (int)(a[0] + a[1]);
    auto b = __builtin_amdgcn_permlane32_swap((u32)v, (u32)v, false, false);
    return (int)(b[0] + b[1]);
}
__device__ __forceinline__ float sum_rows_f(float v) {
    auto a = __builtin_amdgcn_permlane16_swap(__float_as_uint(v), __float_as_uint(v), false, false);
    v = __uint_as_float(a[0]) + __uint_as_float(a[1]);
    auto b = __builtin_amdgcn_permlane32_swap(__float_as_uint(v), __float_as_uint(v), false, false);
    return __uint_as_float(b[0]) + __uint_as_float(b[1]);
}
__device__ __forceinline__ u32 top8_d(LAS char* t8, int lane, int n) {
    LAS u32* cand = (LAS u32*)(t8 + 256);
    u32 res = 0;
    _Pragma("unroll 1") for (int i0 = 0; i0 < n; i0 += 64) {
        const int i = i0 + lane;
        const u32 k = i < n ? cand[i] : 0u;
        int rk = 0;
        _Pragma("unroll 1") for (int j0 = 0; j0 < n; j0 += 16) {
            v4u c[4];
#pragma unroll
            for (int e = 0; e < 4; ++e) c[e] = *(LAS v4u*)(cand + j0 + 4 * e);
#pragma unroll
            for (int e = 0; e < 16; ++e) rk += (c[e >> 2][e & 3] > k) & (j0 + e < n);
        }
        const bool hit = i < n && rk < 8;
        const u32 got = (u32)__builtin_amdgcn_ds_permute((hit ? rk : 63) * 4, (int)(hit ? k : 0u));
        if (lane < 8) res |= got;
    }
    return res;
}

__device__ __forceinline__ u32 lds_peek(LAS char* word) { return __hip_atomic_load((LAS u32*)word, __ATOMIC_RELAXED, __HIP_MEMORY_SCOPE_WORKGROUP); }
#define LWAIT_BODY "1:\n\tds_read_b32 %0, %3 offset:%4\n\ts_waitcnt lgkmcnt(0)\n\tv_readfirstlane_b32 %1, %0\n\ts_cmp_ge_u32 %1, %5\n\ts_cbranch_scc1 2f\n\ts_sleep 1\n\ts_sub_u32 %2, %2, 1\n\ts_cbranch_scc0 1b\n2:"
#define LSPIN(addr, off, want) do { u32 t_, s_, c_; asm volatile("s_mov_b32 %2, 0x800000\n1:\n\tds_read_b32 %0, %3 offset:%4\n\ts_waitcnt lgkmcnt(0)\n\tv_readfirstlane_b32 %1, %0\n\ts_cmp_ge_u32 %1, %5\n\ts_cbranch_scc1 2f\n\ts_sub_u32 %2, %2, 1\n\ts_cbranch_scc0 1b\n2:" : "=&v"(t_), "=&s"(s_), "=&s"(c_) : "v"(addr), "n"(off), "n"(want) : "memory", "scc"); } while (0)
#define LWAIT(addr, off, want) do { u32 t_, s_, c_; asm volatile("s_mov_b32 %2, 0x400000\n" LWAIT_BODY : "=&v"(t_), "=&s"(s_), "=&s"(c_) : "v"(addr), "n"(off), "n"(want) : "memory", "scc"); } while (0)
#define LWAIT_PEEK(peek, addr, off, want) do { u32 t_, s_, c_; asm volatile("s_nop 0\n\tv_readfirstlane_b32 %1, %6\n\ts_mov_b32 %2, 0x400000\n\ts_cmp_ge_u32 %1, %5\n\ts_cbranch_scc1 2f\n" LWAIT_BODY : "=&v"(t_), "=&s"(s_), "=&s"(c_) : "v"(addr), "n"(off), "n"(want), "v"(peek) : "memory", "scc"); } while (0)
#define LDS_ADD1(addr, one, off) asm volatile("s_mov_b64 exec, 1\n\tds_add_u32 %0, %1 offset:%2\n\ts_mov_b64 exec, -1" :: "v"(addr), "v"(one), "n"(off) : "memory")
#define LDS_SETB(addr, one, off) asm volatile("s_mov_b64 exec, 1\n\tds_write_b8 %0, %1 offset:%2\n\ts_mov_b64 exec, -1" :: "v"(addr), "v"(one), "n"(off) : "memory")
__device__ __forceinline__ void glds16(const void* g, u32 lds_dst, bool nt) {
    u32 keep;
    if (nt) asm volatile("s_mov_b32 %0, m0\n\ts_mov_b32 m0, %2\n\ts_nop 0\n\tglobal_load_lds_dwordx4 %1, off" WNT "\n\ts_mov_b32 m0, %0" : "=&s"(keep) : "v"(g), "s"(lds_dst) : "memory");
    else asm volatile("s_mov_b32 %0, m0\n\ts_mov_b32 m0, %2\n\ts_nop 0\n\tglobal_load_lds_dwordx4 %1, off\n\ts_mov_b32 m0, %0" : "=&s"(keep) : "v"(g), "s"(lds_dst) : "memory");
}
__device__ __forceinline__ bf16x8 band(u32 bits, u32 val) {
    v4u r;
#pragma unroll
    for (int j = 0; j < 4; ++j) r[j] = __builtin_amdgcn_perm((u32)__builtin_amdgcn_sbfe((int)bits, 2 * j + 1, 1), (u32)__builtin_amdgcn_sbfe((int)bits, 2 * j, 1), 0x05040100u) & val;
    return __builtin_bit_cast(bf16x8, r);
}

__device__ __forceinline__ u32 hline(const char* ws, u32 i) { const u32 q = i / 7u, r = i - 7u * q; return (8u * q + r + (r >= ((3u - (u32)((uintptr_t)ws >> 7)) & 7u))) * 128u; }
__device__ __forceinline__ u32 wd_line(u32 xcd, u32 i) {
    const u32 k = i / 9u, r = k / 9u;
    return ((xcd * 9u + k - 9u * r) * 64u + (r >> 1) + 32u * (r & 1)) * 1152u + (i - 9u * k) * 128u;
}
__device__ __forceinline__ bool touch(const Params& p, int xcd, int e, int lane, u32 dst) {
    const int wa = xcd * 9 + ((3 - (int)(((uintptr_t)p.wg >> 7) & 7) - xcd * 9) & 7), two = wa == xcd * 9;
    const int g0 = two ? 576 : PFG0, na = (g0 * 2) << two, nb = (576 - g0) * 18, nc = WPF * 1728;
    if (e * 64 >= na + nb + nc) return false;
    const u32 i = min(e * 64 + lane, na + nb + nc - 1);
    const u32 j = i - na, k = j / 9u;
    const bool first = i < (u32)na, last = i >= (u32)(na + nb);
    const u32 seg = first ? i >> two : k + 2 * g0, col = first ? (wa + 8 * (i & two)) * 128u : xcd * 1152u + (j - 9u * k) * 128u;
    const char* a = (const char*)(last ? p.wd : ((seg & 1) ? p.wu : p.wg)) + (last ? wd_line(xcd, j - nb) : (seg >> 1) * (F * 2) + col);
    asm volatile("s_mov_b32 m0, %1\n\ts_nop 0\n\tglobal_load_lds_dword %0, off" :: "v"(a), "s"(dst) : "memory");
    return true;
}

#define KGEN_ASM ".p2align 3\n\tv_mov_b32 v64, 0x3c00\n\tv_mov_b32 v65, 0x3c00\n\tv_pk_mul_f32 v[66:67], v[56:57], v[56:57] op_sel:[1,1] op_sel_hi:[1,0] neg_lo:[0,1]\n\tv_pk_mul_f32 v[68:69], v[58:59], v[58:59] op_sel:[1,1] op_sel_hi:[1,0] neg_lo:[0,1]\n\tv_pk_mul_f32 v[70:71], v[52:53], v[52:53] op_sel:[1,1] op_sel_hi:[1,0] neg_lo:[0,1]\n\tv_pk_mul_f32 v[72:73], v[54:55], v[54:55] op_sel:[1,1] op_sel_hi:[1,0] neg_lo:[0,1]\n\tv_pk_fma_f32 v[66:67], v[56:57], v[56:57], v[66:67] op_sel:[0,0,0] op_sel_hi:[0,1,1]\n\tv_pk_fma_f32 v[68:69], v[58:59], v[58:59], v[68:69] op_sel:[0,0,0] op_sel_hi:[0,1,1]\n\tv_pk_fma_f32 v[70:71], v[52:53], v[52:53], v[70:71] op_sel:[0,0,0] op_sel_hi:[0,1,1]\n\tv_pk_fma_f32 v[72:73], v[54:55], v[54:55], v[72:73] op_sel:[0,0,0] op_sel_hi:[0,1,1]\n\tv_cvt_pk_f16_f32 v74, v56, -v57\n\tv_cvt_pk_f16_f32 v75, v58, -v59\n\tv_cvt_pk_f16_f32 v76, v48, v49\n\tv_cvt_pk_f16_f32 v77, v50, v51\n\tv_pk_mul_f32 v[78:79], v[48:49], v[60:61] op_sel:[1,1] op_sel_hi:[1,0] neg_lo:[0,1]\n\tv_pk_mul_f32 v[80:81], v[50:51], v[62:63] op_sel:[1,1] op_sel_hi:[1,0] neg_lo:[0,1]\n\tv_pk_mul_f32 v[82:83], v[48:49], v[52:53] op_sel:[1,1] op_sel_hi:[1,0] neg_lo:[0,1]\n\tv_pk_mul_f32 v[84:85], v[50:51], v[54:55] op_sel:[1,1] op_sel_hi:[1,0] neg_lo:[0,1]\n\tds_write2_b64 %[b0], v[64:65], v[74:75] offset0:0 offset1:66\n\tv_pk_fma_f32 v[78:79], v[48:49], v[60:61], v[78:79] op_sel:[0,0,0] op_sel_hi:[0,1,1]\n\tv_pk_fma_f32 v[80:81], v[50:51], v[62:63], v[80:81] op_sel:[0,0,0] op_sel_hi:[0,1,1]\n\tv_pk_fma_f32 v[82:83], v[48:49], v[52:53], v[82:83] op_sel:[0,0,0] op_sel_hi:[0,1,1]\n\tv_pk_fma_f32 v[84:85], v[50:51], v[54:55], v[84:85] op_sel:[0,0,0] op_sel_hi:[0,1,1]\n\tv_cvt_pk_f16_f32 v86, v66, -v67\n\tv_cvt_pk_f16_f32 v87, v68, -v69\n\tv_pk_mul_f32 v[88:89], v[66:67], v[66:67] op_sel:[1,1] op_sel_hi:[1,0] neg_lo:[0,1]\n\tv_pk_mul_f32 v[90:91], v[68:69], v[68:69] op_sel:[1,1] op_sel_hi:[1,0] neg_lo:[0,1]\n\tv_pk_mul_f32 v[92:93], v[66:67], v[56:57] op_sel:[1,1] op_sel_hi:[1,0] neg_lo:[0,1]\n\tv_pk_mul_f32 v[94:95], v[68:69], v[58:59] op_sel:[1,1] op_sel_hi:[1,0] neg_lo:[0,1]\n\tv_pk_fma_f32 v[88:89], v[66:67], v[66:67], v[88:89] op_sel:[0,0,0] op_sel_hi:[0,1,1]\n\tv_pk_fma_f32 v[90:91], v[68:69], v[68:69], v[90:91] op_sel:[0,0,0] op_sel_hi:[0,1,1]\n\tv_pk_fma_f32 v[92:93], v[66:67], v[56:57], v[92:93] op_sel:[0,0,0] op_sel_hi:[0,1,1]\n\tv_pk_fma_f32 v[94:95], v[68:69], v[58:59], v[94:95] op_sel:[0,0,0] op_sel_hi:[0,1,1]\n\tv_cvt_pk_f16_f32 v64, v78, v79\n\tv_cvt_pk_f16_f32 v65, v80, v81\n\tv_cvt_pk_f16_f32 v96, v82, v83\n\tv_cvt_pk_f16_f32 v97, v84, v85\n\tv_pk_mul_f32 v[98:99], v[70:71], v[70:71] op_sel:[1,1] op_sel_hi:[1,0] neg_lo:[0,1]\n\tv_pk_mul_f32 v[100:101], v[72:73], v[72:73] op_sel:[1,1] op_sel_hi:[1,0] neg_lo:[0,1]\n\tv_pk_mul_f32 v[102:103], v[78:79], v[56:57] op_sel:[1,1] op_sel_hi:[1,0] neg_lo:[0,1]\n\tv_pk_mul_f32 v[104:105], v[80:81], v[58:59] op_sel:[1,1] op_sel_hi:[1,0] neg_lo:[0,1]\n\tds_write2_b64 %[b8], v[76:77], v[96:97] offset0:0 offset1:66\n\tv_pk_fma_f32 v[98:99], v[70:71], v[70:71], v[98:99] op_sel:[0,0,0] op_sel_hi:[0,1,1]\n\tv_pk_fma_f32 v[100:101], v[72:73], v[72:73], v[100:101] op_sel:[0,0,0] op_sel_hi:[0,1,1]\n\tv_pk_fma_f32 v[102:103], v[78:79], v[56:57], v[102:103] op_sel:[0,0,0] op_sel_hi:[0,1,1]\n\tv_pk_fma_f32 v[104:105], v[80:81], v[58:59], v[104:105] op_sel:[0,0,0] op_sel_hi:[0,1,1]\n\tv_cvt_pk_f16_f32 v106, v88, -v89\n\tv_cvt_pk_f16_f32 v107, v90, -v91\n\tv_cvt_pk_f16_f32 v108, v92, -v93\n\tv_cvt_pk_f16_f32 v109, v94, -v95\n\tv_pk_mul_f32 v[110:111], v[78:79], v[70:71] op_sel:[1,1] op_sel_hi:[1,0] neg_lo:[0,1]\n\tv_pk_mul_f32 v[112:113], v[80:81], v[72:73] op_sel:[1,1] op_sel_hi:[1,0] neg_lo:[0,1]\n\tv_pk_mul_f32 v[114:115], v[48:49], v[70:71] op_sel:[1,1] op_sel_hi:[1,0] neg_lo:[0,1]\n\tv_pk_mul_f32 v[116:117], v[50:51], v[72:73] op_sel:[1,1] op_sel_hi:[1,0] neg_lo:[0,1]\n\tds_write2_b64 %[b0], v[86:87], v[108:109] offset0:132 offset1:198\n\tv_pk_fma_f32 v[110:111], v[78:79], v[70:71], v[110:111] op_sel:[0,0,0] op_sel_hi:[0,1,1]\n\tv_pk_fma_f32 v[112:113], v[80:81], v[72:73], v[112:113] op_sel:[0,0,0] op_sel_hi:[0,1,1]\n\tv_pk_fma_f32 v[114:115], v[48:49], v[70:71], v[114:115] op_sel:[0,0,0] op_sel_hi:[0,1,1]\n\tv_pk_fma_f32 v[116:117], v[50:51], v[72:73], v[116:117] op_sel:[0,0,0] op_sel_hi:[0,1,1]\n\tv_cvt_pk_f16_f32 v76, v102, v103\n\tv_cvt_pk_f16_f32 v77, v104, v105\n\tv_pk_mul_f32 v[96:97], v[82:83], v[70:71] op_sel:[1,1] op_sel_hi:[1,0] neg_lo:[0,1]\n\tv_pk_mul_f32 v[118:119], v[84:85], v[72:73] op_sel:[1,1] op_sel_hi:[1,0] neg_lo:[0,1]\n\tv_pk_mul_f32 v[120:121], v[88:89], v[88:89] op_sel:[1,1] op_sel_hi:[1,0] neg_lo:[0,1]\n\tv_pk_mul_f32 v[122:123], v[90:91], v[90:91] op_sel:[1,1] op_sel_hi:[1,0] neg_lo:[0,1]\n\tv_pk_fma_f32 v[96:97], v[82:83], v[70:71], v[96:97] op_sel:[0,0,0] op_sel_hi:[0,1,1]\n\tv_pk_fma_f32 v[118:119], v[84:85], v[72:73], v[118:119] op_sel:[0,0,0] op_sel_hi:[0,1,1]\n\tv_pk_fma_f32 v[120:121], v[88:89], v[88:89], v[120:121] op_sel:[0,0,0] op_sel_hi:[0,1,1]\n\tv_pk_fma_f32 v[122:123], v[90:91], v[90:91], v[122:123] op_sel:[0,0,0] op_sel_hi:[0,1,1]\n\tv_cvt_pk_f16_f32 v124, v110, v111\n\tv_cvt_pk_f16_f32 v125, v112, v113\n\tv_cvt_pk_f16_f32 v126, v114, v115\n\tv_cvt_pk_f16_f32 v127, v116, v117\n\tv_pk_mul_f32 v[70:71], v[88:89], v[56:57] op_sel:[1,1] op_sel_hi:[1,0] neg_lo:[0,1]\n\tv_pk_mul_f32 v[72:73], v[90:91], v[58:59] op_sel:[1,1] op_sel_hi:[1,0] neg_lo:[0,1]\n\tv_pk_mul_f32 v[128:129], v[102:103], v[60:61] op_sel:[1,1] op_sel_hi:[1,0] neg_lo:[0,1]\n\tv_pk_mul_f32 v[130:131], v[104:105], v[62:63] op_sel:[1,1] op_sel_hi:[1,0] neg_lo:[0,1]\n\tds_write2_b64 %[b11], v[64:65], v[124:125] offset0:0 offset1:66\n\tv_pk_fma_f32 v[70:71], v[88:89], v[56:57], v[70:71] op_sel:[0,0,0] op_sel_hi:[0,1,1]\n\tv_pk_fma_f32 v[72:73], v[90:91], v[58:59], v[72:73] op_sel:[0,0,0] op_sel_hi:[0,1,1]\n\tv_pk_fma_f32 v[128:129], v[102:103], v[60:61], v[128:129] op_sel:[0,0,0] op_sel_hi:[0,1,1]\n\tv_pk_fma_f32 v[130:131], v[104:105], v[62:63], v[130:131] op_sel:[0,0,0] op_sel_hi:[0,1,1]\n\tv_cvt_pk_f16_f32 v132, v96, v97\n\tv_cvt_pk_f16_f32 v133, v118, v119\n\tv_cvt_pk_f16_f32 v134, v120, -v121\n\tv_cvt_pk_f16_f32 v135, v122, -v123\n\tv_pk_mul_f32 v[102:103], v[78:79], v[98:99] op_sel:[1,1] op_sel_hi:[1,0] neg_lo:[0,1]\n\tv_pk_mul_f32 v[104:105], v[80:81], v[100:101] op_sel:[1,1] op_sel_hi:[1,0] neg_lo:[0,1]\n\tv_pk_mul_f32 v[136:137], v[88:89], v[66:67] op_sel:[1,1] op_sel_hi:[1,0] neg_lo:[0,1]\n\tv_pk_mul_f32 v[138:139], v[90:91], v[68:69] op_sel:[1,1] op_sel_hi:[1,0] neg_lo:[0,1]\n\tds_write2_b64 %[b8], v[126:127], v[132:133] offset0:132 offset1:198\n\tv_pk_fma_f32 v[102:103], v[78:79], v[98:99], v[102:103] op_sel:[0,0,0] op_sel_hi:[0,1,1]\n\tv_pk_fma_f32 v[104:105], v[80:81], v[100:101], v[104:105] op_sel:[0,0,0] op_sel_hi:[0,1,1]\n\tv_pk_fma_f32 v[136:137], v[88:89], v[66:67], v[136:137] op_sel:[0,0,0] op_sel_hi:[0,1,1]\n\tv_pk_fma_f32 v[138:139], v[90:91], v[68:69], v[138:139] op_sel:[0,0,0] op_sel_hi:[0,1,1]\n\tv_cvt_pk_f16_f32 v64, v70, -v71\n\tv_cvt_pk_f16_f32 v65, v72, -v73\n\tv_cvt_pk_f16_f32 v124, v128, v129\n\tv_cvt_pk_f16_f32 v125, v130, v131\n\tv_pk_mul_f32 v[78:79], v[88:89], v[92:93] op_sel:[1,1] op_sel_hi:[1,0] neg_lo:[0,1]\n\tv_pk_mul_f32 v[80:81], v[90:91], v[94:95] op_sel:[1,1] op_sel_hi:[1,0] neg_lo:[0,1]\n\tv_pk_mul_f32 v[140:141], v[120:121], v[120:121] op_sel:[1,1] op_sel_hi:[1,0] neg_lo:[0,1]\n\tv_pk_mul_f32 v[142:143], v[122:123], v[122:123] op_sel:[1,1] op_sel_hi:[1,0] neg_lo:[0,1]\n\tds_write2_b64 %[b1], v[106:107], v[64:65] offset0:0 offset1:66\n\tv_pk_fma_f32 v[78:79], v[88:89], v[92:93], v[78:79] op_sel:[0,0,0] op_sel_hi:[0,1,1]\n\tv_pk_fma_f32 v[80:81], v[90:91], v[94:95], v[80:81] op_sel:[0,0,0] op_sel_hi:[0,1,1]\n\tv_pk_fma_f32 v[140:141], v[120:121], v[120:121], v[140:141] op_sel:[0,0,0] op_sel_hi:[0,1,1]\n\tv_pk_fma_f32 v[142:143], v[122:123], v[122:123], v[142:143] op_sel:[0,0,0] op_sel_hi:[0,1,1]\n\tv_cvt_pk_f16_f32 v126, v102, v103\n\tv_cvt_pk_f16_f32 v127, v104, v105\n\tv_cvt_pk_f16_f32 v132, v136, -v137\n\tv_cvt_pk_f16_f32 v133, v138, -v139\n\tv_pk_mul_f32 v[144:145], v[120:121], v[56:57] op_sel:[1,1] op_sel_hi:[1,0] neg_lo:[0,1]\n\tv_pk_mul_f32 v[146:147], v[122:123], v[58:59] op_sel:[1,1] op_sel_hi:[1,0] neg_lo:[0,1]\n\tv_pk_mul_f32 v[148:149], v[128:129], v[60:61] op_sel:[1,1] op_sel_hi:[1,0] neg_lo:[0,1]\n\tv_pk_mul_f32 v[150:151], v[130:131], v[62:63] op_sel:[1,1] op_sel_hi:[1,0] neg_lo:[0,1]\n\tds_write2_b64 %[b9], v[76:77], v[124:125] offset0:0 offset1:66\n\tv_pk_fma_f32 v[144:145], v[120:121], v[56:57], v[144:145] op_sel:[0,0,0] op_sel_hi:[0,1,1]\n\tv_pk_fma_f32 v[146:147], v[122:123], v[58:59], v[146:147] op_sel:[0,0,0] op_sel_hi:[0,1,1]\n\tv_pk_fma_f32 v[148:149], v[128:129], v[60:61], v[148:149] op_sel:[0,0,0] op_sel_hi:[0,1,1]\n\tv_pk_fma_f32 v[150:151], v[130:131], v[62:63], v[150:151] op_sel:[0,0,0] op_sel_hi:[0,1,1]\n\tv_cvt_pk_f16_f32 v152, v78, -v79\n\tv_cvt_pk_f16_f32 v153, v80, -v81\n\tv_cvt_pk_f16_f32 v154, v140, -v141\n\tv_cvt_pk_f16_f32 v155, v142, -v143\n\tv_pk_mul_f32 v[128:129], v[102:103], v[60:61] op_sel:[1,1] op_sel_hi:[1,0] neg_lo:[0,1]\n\tv_pk_mul_f32 v[130:131], v[104:105], v[62:63] op_sel:[1,1] op_sel_hi:[1,0] neg_lo:[0,1]\n\tv_pk_mul_f32 v[140:141], v[110:111], v[98:99] op_sel:[1,1] op_sel_hi:[1,0] neg_lo:[0,1]\n\tv_pk_mul_f32 v[142:143], v[112:113], v[100:101] op_sel:[1,1] op_sel_hi:[1,0] neg_lo:[0,1]\n\tds_write2_b64 %[b1], v[132:133], v[152:153] offset0:132 offset1:198\n\tv_pk_fma_f32 v[128:129], v[102:103], v[60:61], v[128:129] op_sel:[0,0,0] op_sel_hi:[0,1,1]\n\tv_pk_fma_f32 v[130:131], v[104:105], v[62:63], v[130:131] op_sel:[0,0,0] op_sel_hi:[0,1,1]\n\tv_pk_fma_f32 v[140:141], v[110:111], v[98:99], v[140:141] op_sel:[0,0,0] op_sel_hi:[0,1,1]\n\tv_pk_fma_f32 v[142:143], v[112:113], v[100:101], v[142:143] op_sel:[0,0,0] op_sel_hi:[0,1,1]\n\tv_cvt_pk_f16_f32 v76, v144, -v145\n\tv_cvt_pk_f16_f32 v77, v146, -v147\n\tv_cvt_pk_f16_f32 v124, v148, v149\n\tv_cvt_pk_f16_f32 v125, v150, v151\n\tv_pk_mul_f32 v[102:103], v[120:121], v[66:67] op_sel:[1,1] op_sel_hi:[1,0] neg_lo:[0,1]\n\tv_pk_mul_f32 v[104:105], v[122:123], v[68:69] op_sel:[1,1] op_sel_hi:[1,0] neg_lo:[0,1]\n\tv_pk_mul_f32 v[110:111], v[120:121], v[92:93] op_sel:[1,1] op_sel_hi:[1,0] neg_lo:[0,1]\n\tv_pk_mul_f32 v[112:113], v[122:123], v[94:95] op_sel:[1,1] op_sel_hi:[1,0] neg_lo:[0,1]\n\tds_write2_b64 %[b2], v[134:135], v[76:77] offset0:0 offset1:66\n\tv_pk_fma_f32 v[102:103], v[120:121], v[66:67], v[102:103] op_sel:[0,0,0] op_sel_hi:[0,1,1]\n\tv_pk_fma_f32 v[104:105], v[122:123], v[68:69], v[104:105] op_sel:[0,0,0] op_sel_hi:[0,1,1]\n\tv_pk_fma_f32 v[110:111], v[120:121], v[92:93], v[110:111] op_sel:[0,0,0] op_sel_hi:[0,1,1]\n\tv_pk_fma_f32 v[112:113], v[122:123], v[94:95], v[112:113] op_sel:[0,0,0] op_sel_hi:[0,1,1]\n\tv_cvt_pk_f16_f32 v144, v128, v129\n\tv_cvt_pk_f16_f32 v145, v130, v131\n\tv_cvt_pk_f16_f32 v146, v140, v141\n\tv_cvt_pk_f16_f32 v147, v142, v143\n\tv_pk_mul_f32 v[66:67], v[148:149], v[60:61] op_sel:[1,1] op_sel_hi:[1,0] neg_lo:[0,1]\n\tv_pk_mul_f32 v[68:69], v[150:151], v[62:63] op_sel:[1,1] op_sel_hi:[1,0] neg_lo:[0,1]\n\tv_pk_mul_f32 v[92:93], v[128:129], v[60:61] op_sel:[1,1] op_sel_hi:[1,0] neg_lo:[0,1]\n\tv_pk_mul_f32 v[94:95], v[130:131], v[62:63] op_sel:[1,1] op_sel_hi:[1,0] neg_lo:[0,1]\n\tds_write2_b64 %[b9], v[124:125], v[146:147] offset0:132 offset1:198\n\tv_pk_fma_f32 v[66:67], v[148:149], v[60:61], v[66:67] op_sel:[0,0,0] op_sel_hi:[0,1,1]\n\tv_pk_fma_f32 v[68:69], v[150:151], v[62:63], v[68:69] op_sel:[0,0,0] op_sel_hi:[0,1,1]\n\tv_pk_fma_f32 v[92:93], v[128:129], v[60:61], v[92:93] op_sel:[0,0,0] op_sel_hi:[0,1,1]\n\tv_pk_fma_f32 v[94:95], v[130:131], v[62:63], v[94:95] op_sel:[0,0,0] op_sel_hi:[0,1,1]\n\tv_cvt_pk_f16_f32 v140, v102, -v103\n\tv_cvt_pk_f16_f32 v141, v104, -v105\n\tv_cvt_pk_f16_f32 v142, v110, -v111\n\tv_cvt_pk_f16_f32 v143, v112, -v113\n\tv_pk_mul_f32 v[102:103], v[120:121], v[88:89] op_sel:[1,1] op_sel_hi:[1,0] neg_lo:[0,1]\n\tv_pk_mul_f32 v[104:105], v[122:123], v[90:91] op_sel:[1,1] op_sel_hi:[1,0] neg_lo:[0,1]\n\tv_pk_mul_f32 v[110:111], v[120:121], v[70:71] op_sel:[1,1] op_sel_hi:[1,0] neg_lo:[0,1]\n\tv_pk_mul_f32 v[112:113], v[122:123], v[72:73] op_sel:[1,1] op_sel_hi:[1,0] neg_lo:[0,1]\n\tds_write2_b64 %[b2], v[140:141], v[142:143] offset0:132 offset1:198\n\tv_pk_fma_f32 v[102:103], v[120:121], v[88:89], v[102:103] op_sel:[0,0,0] op_sel_hi:[0,1,1]\n\tv_pk_fma_f32 v[104:105], v[122:123], v[90:91], v[104:105] op_sel:[0,0,0] op_sel_hi:[0,1,1]\n\tv_pk_fma_f32 v[110:111], v[120:121], v[70:71], v[110:111] op_sel:[0,0,0] op_sel_hi:[0,1,1]\n\tv_pk_fma_f32 v[112:113], v[122:123], v[72:73], v[112:113] op_sel:[0,0,0] op_sel_hi:[0,1,1]\n\tv_cvt_pk_f16_f32 v124, v66, v67\n\tv_cvt_pk_f16_f32 v125, v68, v69\n\tv_cvt_pk_f16_f32 v128, v92, v93\n\tv_cvt_pk_f16_f32 v129, v94, v95\n\tv_pk_mul_f32 v[70:71], v[48:49], v[98:99] op_sel:[1,1] op_sel_hi:[1,0] neg_lo:[0,1]\n\tv_pk_mul_f32 v[72:73], v[50:51], v[100:101] op_sel:[1,1] op_sel_hi:[1,0] neg_lo:[0,1]\n\tv_pk_mul_f32 v[88:89], v[82:83], v[98:99] op_sel:[1,1] op_sel_hi:[1,0] neg_lo:[0,1]\n\tv_pk_mul_f32 v[90:91], v[84:85], v[100:101] op_sel:[1,1] op_sel_hi:[1,0] neg_lo:[0,1]\n\tds_write2_b64 %[b10], v[126:127], v[144:145] offset0:132 offset1:198\n\tv_pk_fma_f32 v[70:71], v[48:49], v[98:99], v[70:71] op_sel:[0,0,0] op_sel_hi:[0,1,1]\n\tv_pk_fma_f32 v[72:73], v[50:51], v[100:101], v[72:73] op_sel:[0,0,0] op_sel_hi:[0,1,1]\n\tv_pk_fma_f32 v[88:89], v[82:83], v[98:99], v[88:89] op_sel:[0,0,0] op_sel_hi:[0,1,1]\n\tv_pk_fma_f32 v[90:91], v[84:85], v[100:101], v[90:91] op_sel:[0,0,0] op_sel_hi:[0,1,1]\n\tv_cvt_pk_f16_f32 v92, v102, -v103\n\tv_cvt_pk_f16_f32 v93, v104, -v105\n\tv_cvt_pk_f16_f32 v94, v110, -v111\n\tv_cvt_pk_f16_f32 v95, v112, -v113\n\tv_pk_mul_f32 v[82:83], v[66:67], v[60:61] op_sel:[1,1] op_sel_hi:[1,0] neg_lo:[0,1]\n\tv_pk_mul_f32 v[84:85], v[68:69], v[62:63] op_sel:[1,1] op_sel_hi:[1,0] neg_lo:[0,1]\n\tv_pk_mul_f32 v[102:103], v[120:121], v[136:137] op_sel:[1,1] op_sel_hi:[1,0] neg_lo:[0,1]\n\tv_pk_mul_f32 v[104:105], v[122:123], v[138:139] op_sel:[1,1] op_sel_hi:[1,0] neg_lo:[0,1]\n\tds_write2_b64 %[b3], v[92:93], v[94:95] offset0:0 offset1:66\n\tv_pk_fma_f32 v[82:83], v[66:67], v[60:61], v[82:83] op_sel:[0,0,0] op_sel_hi:[0,1,1]\n\tv_pk_fma_f32 v[84:85], v[68:69], v[62:63], v[84:85] op_sel:[0,0,0] op_sel_hi:[0,1,1]\n\tv_pk_fma_f32 v[102:103], v[120:121], v[136:137], v[102:103] op_sel:[0,0,0] op_sel_hi:[0,1,1]\n\tv_pk_fma_f32 v[104:105], v[122:123], v[138:139], v[104:105] op_sel:[0,0,0] op_sel_hi:[0,1,1]\n\tv_cvt_pk_f16_f32 v110, v70, v71\n\tv_cvt_pk_f16_f32 v111, v72, v73\n\tv_cvt_pk_f16_f32 v112, v88, v89\n\tv_cvt_pk_f16_f32 v113, v90, v91\n\tv_pk_mul_f32 v[66:67], v[120:121], v[78:79] op_sel:[1,1] op_sel_hi:[1,0] neg_lo:[0,1]\n\tv_pk_mul_f32 v[68:69], v[122:123], v[80:81] op_sel:[1,1] op_sel_hi:[1,0] neg_lo:[0,1]\n\tv_pk_mul_f32 v[70:71], v[114:115], v[98:99] op_sel:[1,1] op_sel_hi:[1,0] neg_lo:[0,1]\n\tv_pk_mul_f32 v[72:73], v[116:117], v[100:101] op_sel:[1,1] op_sel_hi:[1,0] neg_lo:[0,1]\n\tds_write2_b64 %[b11], v[110:111], v[112:113] offset0:132 offset1:198\n\tv_pk_fma_f32 v[66:67], v[120:121], v[78:79], v[66:67] op_sel:[0,0,0] op_sel_hi:[0,1,1]\n\tv_pk_fma_f32 v[68:69], v[122:123], v[80:81], v[68:69] op_sel:[0,0,0] op_sel_hi:[0,1,1]\n\tv_pk_fma_f32 v[70:71], v[114:115], v[98:99], v[70:71] op_sel:[0,0,0] op_sel_hi:[0,1,1]\n\tv_pk_fma_f32 v[72:73], v[116:117], v[100:101], v[72:73] op_sel:[0,0,0] op_sel_hi:[0,1,1]\n\tv_cvt_pk_f16_f32 v88, v82, v83\n\tv_cvt_pk_f16_f32 v89, v84, v85\n\tv_cvt_pk_f16_f32 v90, v102, -v103\n\tv_cvt_pk_f16_f32 v91, v104, -v105\n\tv_pk_mul_f32 v[78:79], v[96:97], v[98:99] op_sel:[1,1] op_sel_hi:[1,0] neg_lo:[0,1]\n\tv_pk_mul_f32 v[80:81], v[118:119], v[100:101] op_sel:[1,1] op_sel_hi:[1,0] neg_lo:[0,1]\n\tv_pk_mul_f32 v[102:103], v[82:83], v[60:61] op_sel:[1,1] op_sel_hi:[1,0] neg_lo:[0,1]\n\tv_pk_mul_f32 v[104:105], v[84:85], v[62:63] op_sel:[1,1] op_sel_hi:[1,0] neg_lo:[0,1]\n\tds_write2_b64 %[b12], v[128:129], v[124:125] offset0:132 offset1:198\n\tv_pk_fma_f32 v[78:79], v[96:97], v[98:99], v[78:79] op_sel:[0,0,0] op_sel_hi:[0,1,1]\n\tv_pk_fma_f32 v[80:81], v[118:119], v[100:101], v[80:81] op_sel:[0,0,0] op_sel_hi:[0,1,1]\n\tv_pk_fma_f32 v[102:103], v[82:83], v[60:61], v[102:103] op_sel:[0,0,0] op_sel_hi:[0,1,1]\n\tv_pk_fma_f32 v[104:105], v[84:85], v[62:63], v[104:105] op_sel:[0,0,0] op_sel_hi:[0,1,1]\n\tv_cvt_pk_f16_f32 v110, v66, -v67\n\tv_cvt_pk_f16_f32 v111, v68, -v69\n\tv_cvt_pk_f16_f32 v112, v70, v71\n\tv_cvt_pk_f16_f32 v113, v72, v73\n\tv_pk_mul_f16 v66, v154, v74 op_sel:[1,1] op_sel_hi:[1,0] neg_lo:[0,1]\n\tv_pk_mul_f16 v67, v155, v75 op_sel:[1,1] op_sel_hi:[1,0] neg_lo:[0,1]\n\tv_pk_mul_f16 v68, v154, v86 op_sel:[1,1] op_sel_hi:[1,0] neg_lo:[0,1]\n\tv_pk_mul_f16 v69, v155, v87 op_sel:[1,1] op_sel_hi:[1,0] neg_lo:[0,1]\n\tds_write2_b64 %[b3], v[90:91], v[110:111] offset0:132 offset1:198\n\tv_pk_fma_f16 v66, v154, v74, v66 op_sel:[0,0,0] op_sel_hi:[0,1,1]\n\tv_pk_fma_f16 v67, v155, v75, v67 op_sel:[0,0,0] op_sel_hi:[0,1,1]\n\tv_pk_fma_f16 v68, v154, v86, v68 op_sel:[0,0,0] op_sel_hi:[0,1,1]\n\tv_pk_fma_f16 v69, v155, v87, v69 op_sel:[0,0,0] op_sel_hi:[0,1,1]\n\tv_cvt_pk_f16_f32 v70, v78, v79\n\tv_cvt_pk_f16_f32 v71, v80, v81\n\tv_cvt_pk_f16_f32 v72, v102, v103\n\tv_cvt_pk_f16_f32 v73, v104, v105\n\tv_pk_mul_f16 v78, v154, v108 op_sel:[1,1] op_sel_hi:[1,0] neg_lo:[0,1]\n\tv_pk_mul_f16 v79, v155, v109 op_sel:[1,1] op_sel_hi:[1,0] neg_lo:[0,1]\n\tv_pk_mul_f32 v[80:81], v[102:103], v[60:61] op_sel:[1,1] op_sel_hi:[1,0] neg_lo:[0,1]\n\tv_pk_mul_f32 v[82:83], v[104:105], v[62:63] op_sel:[1,1] op_sel_hi:[1,0] neg_lo:[0,1]\n\tds_write2_b64 %[b4], v[154:155], v[66:67] offset0:0 offset1:66\n\tv_pk_fma_f16 v78, v154, v108, v78 op_sel:[0,0,0] op_sel_hi:[0,1,1]\n\tv_pk_fma_f16 v79, v155, v109, v79 op_sel:[0,0,0] op_sel_hi:[0,1,1]\n\tv_pk_fma_f32 v[80:81], v[102:103], v[60:61], v[80:81] op_sel:[0,0,0] op_sel_hi:[0,1,1]\n\tv_pk_fma_f32 v[82:83], v[104:105], v[62:63], v[82:83] op_sel:[0,0,0] op_sel_hi:[0,1,1]\n\tv_pk_mul_f16 v74, v154, v106 op_sel:[1,1] op_sel_hi:[1,0] neg_lo:[0,1]\n\tv_pk_mul_f16 v75, v155, v107 op_sel:[1,1] op_sel_hi:[1,0] neg_lo:[0,1]\n\tv_pk_mul_f16 v84, v154, v64 op_sel:[1,1] op_sel_hi:[1,0] neg_lo:[0,1]\n\tv_pk_mul_f16 v85, v155, v65 op_sel:[1,1] op_sel_hi:[1,0] neg_lo:[0,1]\n\tds_write2_b64 %[b4], v[68:69], v[78:79] offset0:132 offset1:198\n\tv_pk_fma_f16 v74, v154, v106, v74 op_sel:[0,0,0] op_sel_hi:[0,1,1]\n\tv_pk_fma_f16 v75, v155, v107, v75 op_sel:[0,0,0] op_sel_hi:[0,1,1]\n\tv_pk_fma_f16 v84, v154, v64, v84 op_sel:[0,0,0] op_sel_hi:[0,1,1]\n\tv_pk_fma_f16 v85, v155, v65, v85 op_sel:[0,0,0] op_sel_hi:[0,1,1]\n\tv_cvt_pk_f16_f32 v66, v80, v81\n\tv_cvt_pk_f16_f32 v67, v82, v83\n\tv_pk_mul_f16 v80, v154, v132 op_sel:[1,1] op_sel_hi:[1,0] neg_lo:[0,1]\n\tv_pk_mul_f16 v81, v155, v133 op_sel:[1,1] op_sel_hi:[1,0] neg_lo:[0,1]\n\tv_pk_mul_f16 v82, v154, v152 op_sel:[1,1] op_sel_hi:[1,0] neg_lo:[0,1]\n\tv_pk_mul_f16 v83, v155, v153 op_sel:[1,1] op_sel_hi:[1,0] neg_lo:[0,1]\n\tds_write2_b64 %[b5], v[74:75], v[84:85] offset0:0 offset1:66\n\tv_pk_fma_f16 v80, v154, v132, v80 op_sel:[0,0,0] op_sel_hi:[0,1,1]\n\tv_pk_fma_f16 v81, v155, v133, v81 op_sel:[0,0,0] op_sel_hi:[0,1,1]\n\tv_pk_fma_f16 v82, v154, v152, v82 op_sel:[0,0,0] op_sel_hi:[0,1,1]\n\tv_pk_fma_f16 v83, v155, v153, v83 op_sel:[0,0,0] op_sel_hi:[0,1,1]\n\tv_pk_mul_f16 v64, v154, v134 op_sel:[1,1] op_sel_hi:[1,0] neg_lo:[0,1]\n\tv_pk_mul_f16 v65, v155, v135 op_sel:[1,1] op_sel_hi:[1,0] neg_lo:[0,1]\n\tv_pk_mul_f16 v68, v154, v76 op_sel:[1,1] op_sel_hi:[1,0] neg_lo:[0,1]\n\tv_pk_mul_f16 v69, v155, v77 op_sel:[1,1] op_sel_hi:[1,0] neg_lo:[0,1]\n\tds_write2_b64 %[b5], v[80:81], v[82:83] offset0:132 offset1:198\n\tv_pk_fma_f16 v64, v154, v134, v64 op_sel:[0,0,0] op_sel_hi:[0,1,1]\n\tv_pk_fma_f16 v65, v155, v135, v65 op_sel:[0,0,0] op_sel_hi:[0,1,1]\n\tv_pk_fma_f16 v68, v154, v76, v68 op_sel:[0,0,0] op_sel_hi:[0,1,1]\n\tv_pk_fma_f16 v69, v155, v77, v69 op_sel:[0,0,0] op_sel_hi:[0,1,1]\n\tv_pk_mul_f16 v74, v154, v140 op_sel:[1,1] op_sel_hi:[1,0] neg_lo:[0,1]\n\tv_pk_mul_f16 v75, v155, v141 op_sel:[1,1] op_sel_hi:[1,0] neg_lo:[0,1]\n\tv_pk_mul_f16 v78, v154, v142 op_sel:[1,1] op_sel_hi:[1,0] neg_lo:[0,1]\n\tv_pk_mul_f16 v79, v155, v143 op_sel:[1,1] op_sel_hi:[1,0] neg_lo:[0,1]\n\tds_write2_b64 %[b6], v[64:65], v[68:69] offset0:0 offset1:66\n\tv_pk_fma_f16 v74, v154, v140, v74 op_sel:[0,0,0] op_sel_hi:[0,1,1]\n\tv_pk_fma_f16 v75, v155, v141, v75 op_sel:[0,0,0] op_sel_hi:[0,1,1]\n\tv_pk_fma_f16 v78, v154, v142, v78 op_sel:[0,0,0] op_sel_hi:[0,1,1]\n\tv_pk_fma_f16 v79, v155, v143, v79 op_sel:[0,0,0] op_sel_hi:[0,1,1]\n\tv_pk_mul_f16 v76, v154, v92 op_sel:[1,1] op_sel_hi:[1,0] neg_lo:[0,1]\n\tv_pk_mul_f16 v77, v155, v93 op_sel:[1,1] op_sel_hi:[1,0] neg_lo:[0,1]\n\tv_pk_mul_f16 v80, v154, v94 op_sel:[1,1] op_sel_hi:[1,0] neg_lo:[0,1]\n\tv_pk_mul_f16 v81, v155, v95 op_sel:[1,1] op_sel_hi:[1,0] neg_lo:[0,1]\n\tds_write2_b64 %[b6], v[74:75], v[78:79] offset0:132 offset1:198\n\tv_pk_fma_f16 v76, v154, v92, v76 op_sel:[0,0,0] op_sel_hi:[0,1,1]\n\tv_pk_fma_f16 v77, v155, v93, v77 op_sel:[0,0,0] op_sel_hi:[0,1,1]\n\tv_pk_fma_f16 v80, v154, v94, v80 op_sel:[0,0,0] op_sel_hi:[0,1,1]\n\tv_pk_fma_f16 v81, v155, v95, v81 op_sel:[0,0,0] op_sel_hi:[0,1,1]\n\tv_pk_mul_f16 v64, v154, v90 op_sel:[1,1] op_sel_hi:[1,0] neg_lo:[0,1]\n\tv_pk_mul_f16 v65, v155, v91 op_sel:[1,1] op_sel_hi:[1,0] neg_lo:[0,1]\n\tv_pk_mul_f16 v68, v154, v110 op_sel:[1,1] op_sel_hi:[1,0] neg_lo:[0,1]\n\tv_pk_mul_f16 v69, v155, v111 op_sel:[1,1] op_sel_hi:[1,0] neg_lo:[0,1]\n\tds_write2_b64 %[b7], v[76:77], v[80:81] offset0:0 offset1:66\n\tv_pk_fma_f16 v64, v154, v90, v64 op_sel:[0,0,0] op_sel_hi:[0,1,1]\n\tv_pk_fma_f16 v65, v155, v91, v65 op_sel:[0,0,0] op_sel_hi:[0,1,1]\n\tv_pk_fma_f16 v68, v154, v110, v68 op_sel:[0,0,0] op_sel_hi:[0,1,1]\n\tv_pk_fma_f16 v69, v155, v111, v69 op_sel:[0,0,0] op_sel_hi:[0,1,1]\n\ts_nop 0\n\tds_write2_b64 %[b7], v[64:65], v[68:69] offset0:132 offset1:198\n\tds_write2_b64 %[b10], v[88:89], v[66:67] offset0:0 offset1:66\n\tds_write2_b64 %[b12], v[112:113], v[70:71] offset0:0 offset1:66\n\tds_write_b64 %[b13], v[72:73] offset:0"
#define KGEN_CLOBBER "v64", "v65", "v66", "v67", "v68", "v69", "v70", "v71", "v72", "v73", "v74", "v75", "v76", "v77", "v78", "v79", "v80", "v81", "v82", "v83", "v84", "v85", "v86", "v87", "v88", "v89", "v90", "v91", "v92", "v93", "v94", "v95", "v96", "v97", "v98", "v99", "v100", "v101", "v102", "v103", "v104", "v105", "v106", "v107", "v108", "v109", "v110", "v111", "v112", "v113", "v114", "v115", "v116", "v117", "v118", "v119", "v120", "v121", "v122", "v123", "v124", "v125", "v126", "v127", "v128", "v129", "v130", "v131", "v132", "v133", "v134", "v135", "v136", "v137", "v138", "v139", "v140", "v141", "v142", "v143", "v144", "v145", "v146", "v147", "v148", "v149", "v150", "v151", "v152", "v153", "v154", "v155"
#define KROW_0LO 0x2b2a2d2c23222120ull
#define KROW_0HI 0x202020202b343332ull
#define KROW_1LO 0x2524272a31302f2eull
#define KROW_1HI 0x2b2b2b2b20292826ull
#define KMFMA_ASM "ds_read_b128 v[64:67], %[ap] offset:0\n\tds_read_b128 v[68:71], %[bp] offset:0\n\tds_read_b128 v[72:75], %[ap] offset:32\n\tds_read_b128 v[76:79], %[bp] offset:32\n\tds_read_b128 v[80:83], %[ap] offset:64\n\tds_read_b128 v[84:87], %[bp] offset:64\n\tds_read_b128 v[88:91], %[ap] offset:96\n\tds_read_b128 v[92:95], %[bp] offset:96\n\ts_waitcnt lgkmcnt(6)\n\tv_mfma_f32_32x32x16_f16 v[48:63], v[64:67], v[68:71], 0\n\tds_read_b128 v[96:99], %[ap] offset:128\n\tds_read_b128 v[100:103], %[bp] offset:128\n\ts_waitcnt lgkmcnt(6)\n\tv_mfma_f32_32x32x16_f16 v[48:63], v[72:75], v[76:79], v[48:63]\n\tds_read_b128 v[104:107], %[ap] offset:160\n\tds_read_b128 v[108:111], %[bp] offset:160\n\ts_waitcnt lgkmcnt(6)\n\tv_mfma_f32_32x32x16_f16 v[48:63], v[80:83], v[84:87], v[48:63]\n\tds_read_b128 v[64:67], %[ap] offset:192\n\tds_read_b128 v[68:71], %[bp] offset:192\n\ts_waitcnt lgkmcnt(6)\n\tv_mfma_f32_32x32x16_f16 v[48:63], v[88:91], v[92:95], v[48:63]\n\tds_read_b128 v[72:75], %[ap] offset:224\n\tds_read_b128 v[76:79], %[bp] offset:224\n\ts_waitcnt lgkmcnt(6)\n\tv_mfma_f32_32x32x16_f16 v[48:63], v[96:99], v[100:103], v[48:63]\n\tds_read_b128 v[80:83], %[ap] offset:256\n\tds_read_b128 v[84:87], %[bp] offset:256\n\ts_waitcnt lgkmcnt(6)\n\tv_mfma_f32_32x32x16_f16 v[48:63], v[104:107], v[108:111], v[48:63]\n\tds_read_b128 v[88:91], %[ap] offset:288\n\tds_read_b128 v[92:95], %[bp] offset:288\n\ts_waitcnt lgkmcnt(6)\n\tv_mfma_f32_32x32x16_f16 v[48:63], v[64:67], v[68:71], v[48:63]\n\tds_read_b128 v[96:99], %[ap] offset:320\n\tds_read_b128 v[100:103], %[bp] offset:320\n\ts_waitcnt lgkmcnt(6)\n\tv_mfma_f32_32x32x16_f16 v[48:63], v[72:75], v[76:79], v[48:63]\n\tds_read_b128 v[104:107], %[ap] offset:352\n\tds_read_b128 v[108:111], %[bp] offset:352\n\ts_waitcnt lgkmcnt(6)\n\tv_mfma_f32_32x32x16_f16 v[48:63], v[80:83], v[84:87], v[48:63]\n\tds_read_b128 v[64:67], %[ap] offset:384\n\tds_read_b128 v[68:71], %[bp] offset:384\n\ts_waitcnt lgkmcnt(6)\n\tv_mfma_f32_32x32x16_f16 v[48:63], v[88:91], v[92:95], v[48:63]\n\tds_read_b128 v[72:75], %[ap] offset:416\n\tds_read_b128 v[76:79], %[bp] offset:416\n\ts_waitcnt lgkmcnt(6)\n\tv_mfma_f32_32x32x16_f16 v[48:63], v[96:99], v[100:103], v[48:63]\n\tds_read_b128 v[80:83], %[ap] offset:448\n\tds_read_b128 v[84:87], %[bp] offset:448\n\ts_waitcnt lgkmcnt(6)\n\tv_mfma_f32_32x32x16_f16 v[48:63], v[104:107], v[108:111], v[48:63]\n\tds_read_b128 v[88:91], %[ap] offset:480\n\tds_read_b128 v[92:95], %[bp] offset:480\n\ts_waitcnt lgkmcnt(6)\n\tv_mfma_f32_32x32x16_f16 v[48:63], v[64:67], v[68:71], v[48:63]\n\tds_read_b32 %[stg], %[ctl] offset:4\n\ts_waitcnt lgkmcnt(4)\n\tv_mfma_f32_32x32x16_f16 v[48:63], v[72:75], v[76:79], v[48:63]\n\ts_waitcnt lgkmcnt(2)\n\tv_mfma_f32_32x32x16_f16 v[48:63], v[80:83], v[84:87], v[48:63]\n\ts_waitcnt lgkmcnt(0)\n\tv_mfma_f32_32x32x16_f16 v[48:63], v[88:91], v[92:95], v[48:63]\n\ts_nop 7\n\ts_nop 3\n\ts_waitcnt lgkmcnt(0)"
#define KMFMA_CLOBBER "v64", "v65", "v66", "v67", "v68", "v69", "v70", "v71", "v72", "v73", "v74", "v75", "v76", "v77", "v78", "v79", "v80", "v81", "v82", "v83", "v84", "v85", "v86", "v87", "v88", "v89", "v90", "v91", "v92", "v93", "v94", "v95", "v96", "v97", "v98", "v99", "v100", "v101", "v102", "v103", "v104", "v105", "v106", "v107", "v108", "v109", "v110", "v111"
#define CONV_ASM ".p2align 3\n\tds_read_b64_tr_b16 v[52:53], %[a0] offset:456\n\tds_read_b64_tr_b16 v[54:55], %[a0] offset:448\n\tds_read_b64_tr_b16 v[84:85], %[a0] offset:968\n\tds_read_b64_tr_b16 v[86:87], %[a0] offset:960\n\tds_read_b64_tr_b16 v[48:49], %[a0] offset:392\n\tds_read_b64_tr_b16 v[50:51], %[a0] offset:384\n\tds_read_b64_tr_b16 v[80:81], %[a0] offset:904\n\tds_read_b64_tr_b16 v[82:83], %[a0] offset:896\n\tds_read_b64_tr_b16 v[44:45], %[a0] offset:328\n\tds_read_b64_tr_b16 v[46:47], %[a0] offset:320\n\tds_read_b64_tr_b16 v[76:77], %[a0] offset:840\n\tds_read_b64_tr_b16 v[78:79], %[a0] offset:832\n\tv_readfirstlane_b32 %[s], %[stg]\n\ts_mov_b32 %[c], 0x400000\n\ts_cmp_ge_u32 %[s], 4\n\ts_cbranch_scc1 2f\n\t1:\n\tds_read_b32 v216, %[ctl] offset:4\n\ts_waitcnt lgkmcnt(0)\n\tv_readfirstlane_b32 %[s], v216\n\ts_cmp_ge_u32 %[s], 4\n\ts_cbranch_scc1 2f\n\ts_sleep 1\n\ts_sub_u32 %[c], %[c], 1\n\ts_cbranch_scc0 1b\n\t.p2align 3\n\t2:\n\tds_read_b128 v[164:167], %[bb] offset:64\n\tds_read_b128 v[168:171], %[bb] offset:128\n\tds_read_b128 v[172:175], %[bb] offset:192\n\tds_read_b64_tr_b16 v[40:41], %[a0] offset:264\n\tds_read_b64_tr_b16 v[42:43], %[a0] offset:256\n\tds_read_b128 v[176:179], %[bb] offset:256\n\ts_waitcnt lgkmcnt(5)\n\ts_nop 0\n\tv_mfma_f32_16x16x32_bf16 v[132:135], v[52:55], v[164:167], 0\n\tds_read_b64_tr_b16 v[72:73], %[a0] offset:776\n\tds_read_b64_tr_b16 v[74:75], %[a0] offset:768\n\tds_read_b64_tr_b16 v[36:37], %[a0] offset:200\n\tv_mfma_f32_16x16x32_bf16 v[136:139], v[84:87], v[164:167], 0\n\tds_read_b64_tr_b16 v[38:39], %[a0] offset:192\n\ts_waitcnt lgkmcnt(8)\n\ts_nop 0\n\tv_mfma_f32_16x16x32_bf16 v[132:135], v[48:51], v[168:171], v[132:135]\n\tds_read_b128 v[180:183], %[bb] offset:320\n\tds_read_b64_tr_b16 v[68:69], %[a0] offset:712\n\tds_read_b64_tr_b16 v[70:71], %[a0] offset:704\n\tv_mfma_f32_16x16x32_bf16 v[136:139], v[80:83], v[168:171], v[136:139]\n\tds_read_b64_tr_b16 v[32:33], %[a0] offset:136\n\ts_waitcnt lgkmcnt(8)\n\ts_nop 0\n\tv_mfma_f32_16x16x32_bf16 v[132:135], v[44:47], v[172:175], v[132:135]\n\tds_read_b64_tr_b16 v[34:35], %[a0] offset:128\n\tds_read_b128 v[184:187], %[bb] offset:384\n\tds_read_b64_tr_b16 v[64:65], %[a0] offset:648\n\tv_mfma_f32_16x16x32_bf16 v[136:139], v[76:79], v[172:175], v[136:139]\n\tds_read_b64_tr_b16 v[66:67], %[a0] offset:640\n\tds_read_b64_tr_b16 v[28:29], %[a0] offset:72\n\tds_read_b64_tr_b16 v[30:31], %[a0] offset:64\n\tv_mfma_f32_16x16x32_bf16 v[132:135], v[40:43], v[176:179], v[132:135]\n\tds_read_b128 v[164:167], %[bb] offset:448\n\ts_waitcnt lgkmcnt(8)\n\ts_nop 0\n\tv_mfma_f32_16x16x32_bf16 v[136:139], v[72:75], v[176:179], v[136:139]\n\tds_read_b64_tr_b16 v[60:61], %[a0] offset:584\n\tds_read_b64_tr_b16 v[62:63], %[a0] offset:576\n\tds_read_b64_tr_b16 v[24:25], %[a0] offset:8\n\tv_mfma_f32_16x16x32_bf16 v[132:135], v[36:39], v[180:183], v[132:135]\n\tds_read_b64_tr_b16 v[26:27], %[a0] offset:0\n\tds_read_b128 v[168:171], %[bb] offset:512\n\tds_read_b64_tr_b16 v[56:57], %[a0] offset:520\n\tv_mfma_f32_16x16x32_bf16 v[136:139], v[68:71], v[180:183], v[136:139]\n\tds_read_b64_tr_b16 v[58:59], %[a0] offset:512\n\ts_waitcnt lgkmcnt(8)\n\ts_nop 0\n\tv_mfma_f32_16x16x32_bf16 v[132:135], v[32:35], v[184:187], v[132:135]\n\tds_read_b64_tr_b16 v[88:89], %[a0] offset:1032\n\tds_read_b64_tr_b16 v[90:91], %[a0] offset:1024\n\tds_read_b128 v[188:191], %[bb] offset:496\n\tv_mfma_f32_16x16x32_bf16 v[136:139], v[64:67], v[184:187], v[136:139]\n\tds_read_b128 v[172:175], %[bb] offset:576\n\ts_waitcnt lgkmcnt(8)\n\ts_nop 0\n\tv_mfma_f32_16x16x32_bf16 v[132:135], v[28:31], v[164:167], v[132:135]\n\tds_read_b128 v[176:179], %[bb] offset:640\n\tv_mfma_f32_16x16x32_bf16 v[136:139], v[60:63], v[164:167], v[136:139]\n\ts_waitcnt lgkmcnt(5)\n\ts_nop 0\n\tv_mfma_f32_16x16x32_bf16 v[132:135], v[24:27], v[168:171], v[132:135]\n\tds_write_b16 %[ta], %[t78]\n\tds_read_b128 v[180:183], %[bb] offset:704\n\tv_mfma_f32_16x16x32_bf16 v[136:139], v[56:59], v[168:171], v[136:139]\n\ts_waitcnt lgkmcnt(3)\n\ts_nop 0\n\tv_mfma_f32_16x16x32_bf16 v[140:143], v[88:91], v[168:171], 0\n\tds_write_b16_d16_hi %[ta], %[t78] offset:64\n\tds_read_b128 v[184:187], %[bb] offset:768\n\tv_mfma_f32_16x16x32_bf16 v[204:207], v[188:191], v[4:7], 0\n\tds_write_b128 %[yb], v[132:135] offset:0\n\tv_mfma_f32_16x16x32_bf16 v[218:221], v[168:171], v[4:7], 0\n\tds_write_b16 %[ta], %[t78] offset:388\n\tds_read_b128 v[164:167], %[bb] offset:832\n\tv_mfma_f32_16x16x32_bf16 v[208:211], v[188:191], v[8:11], 0\n\tds_write_b8 %[ctc], %[one] offset:0\n\tds_write_b16_d16_hi %[ta], %[t78] offset:452\n\tv_mfma_f32_16x16x32_bf16 v[222:225], v[168:171], v[8:11], 0\n\tds_write_b16 %[tb], %[t9a]\n\tv_mfma_f32_16x16x32_bf16 v[212:215], v[188:191], v[12:15], 0\n\tds_write_b16_d16_hi %[tb], %[t9a] offset:128\n\tv_cvt_pk_bf16_f32 v192, v221, v207\n\tv_mfma_f32_16x16x32_bf16 v[226:229], v[168:171], v[16:19], 0\n\tds_write_b16 %[tb], %[t9a] offset:388\n\tv_cvt_pk_bf16_f32 v193, v220, v206\n\tv_mfma_f32_16x16x32_bf16 v[148:151], v[20:23], v[168:171], 0\n\tds_write_b16_d16_hi %[tb], %[t9a] offset:516\n\tds_read_b128 v[168:171], %[bb] offset:896\n\tv_mfma_f32_16x16x32_bf16 v[136:139], v[52:55], v[172:175], v[136:139]\n\tds_read_b64 v[92:93], %[tp] offset:0\n\tds_read_b64 v[94:95], %[tp] offset:8\n\tds_read_b64 v[96:97], %[tp] offset:64\n\tv_mfma_f32_16x16x32_bf16 v[140:143], v[84:87], v[172:175], v[140:143]\n\tds_read_b128 v[172:175], %[bb] offset:960\n\ts_waitcnt lgkmcnt(7)\n\ts_nop 0\n\tv_mfma_f32_16x16x32_bf16 v[136:139], v[48:51], v[176:179], v[136:139]\n\tds_read_b64 v[98:99], %[tp] offset:72\n\tds_read_b64 v[100:101], %[tp] offset:128\n\tds_read_b64 v[102:103], %[tp] offset:136\n\tv_mfma_f32_16x16x32_bf16 v[140:143], v[80:83], v[176:179], v[140:143]\n\tds_read_b128 v[176:179], %[bb] offset:1024\n\tds_read_b64 v[104:105], %[tp] offset:768\n\tds_read_b64 v[106:107], %[tp] offset:776\n\tv_mfma_f32_16x16x32_bf16 v[136:139], v[44:47], v[180:183], v[136:139]\n\tds_read_b64 v[108:109], %[tp] offset:832\n\tds_read_b64 v[110:111], %[tp] offset:840\n\tds_read_b64 v[112:113], %[tp] offset:896\n\tv_mfma_f32_16x16x32_bf16 v[140:143], v[76:79], v[180:183], v[140:143]\n\tds_read_b64 v[114:115], %[tp] offset:904\n\tds_read_b64 v[116:117], %[tp] offset:1536\n\tds_read_b64 v[118:119], %[tp] offset:1544\n\tv_mfma_f32_16x16x32_bf16 v[136:139], v[40:43], v[184:187], v[136:139]\n\tds_read_b64 v[120:121], %[tp] offset:1600\n\tds_read_b64 v[122:123], %[tp] offset:1608\n\tv_cvt_pk_bf16_f32 v194, v219, v205\n\tv_mfma_f32_16x16x32_bf16 v[140:143], v[72:75], v[184:187], v[140:143]\n\tv_cvt_pk_bf16_f32 v195, v218, v204\n\tds_read_b64 v[124:125], %[tp] offset:1664\n\tds_read_b64 v[126:127], %[tp] offset:1672\n\tv_mfma_f32_16x16x32_bf16 v[136:139], v[36:39], v[164:167], v[136:139]\n\tds_read_b64 v[128:129], %[tp] offset:1728\n\tds_read_b64 v[130:131], %[tp] offset:1736\n\tv_cvt_pk_bf16_f32 v196, v225, v211\n\tv_mfma_f32_16x16x32_bf16 v[140:143], v[68:71], v[164:167], v[140:143]\n\tv_cvt_pk_bf16_f32 v197, v224, v210\n\ts_waitcnt lgkmcnt(6)\n\ts_nop 0\n\tv_mfma_f32_16x16x32_bf16 v[136:139], v[32:35], v[168:171], v[136:139]\n\tv_cvt_pk_bf16_f32 v198, v223, v209\n\tv_mfma_f32_16x16x32_bf16 v[140:143], v[64:67], v[168:171], v[140:143]\n\tv_cvt_pk_bf16_f32 v199, v222, v208\n\tv_mfma_f32_16x16x32_bf16 v[136:139], v[28:31], v[172:175], v[136:139]\n\tv_cvt_pk_bf16_f32 v200, v229, v215\n\tv_mfma_f32_16x16x32_bf16 v[140:143], v[60:63], v[172:175], v[140:143]\n\tds_read_b128 v[188:191], %[bb] offset:1008\n\tv_cvt_pk_bf16_f32 v201, v228, v214\n\tv_mfma_f32_16x16x32_bf16 v[136:139], v[24:27], v[176:179], v[136:139]\n\tv_cvt_pk_bf16_f32 v202, v227, v213\n\tv_mfma_f32_16x16x32_bf16 v[140:143], v[56:59], v[176:179], v[140:143]\n\tv_cvt_pk_bf16_f32 v203, v226, v212\n\tv_mfma_f32_16x16x32_bf16 v[144:147], v[88:91], v[176:179], 0\n\tv_mfma_f32_16x16x32_bf16 v[140:143], v[192:195], v[92:95], v[140:143]\n\tv_mfma_f32_16x16x32_bf16 v[144:147], v[192:195], v[96:99], v[144:147]\n\tv_mfma_f32_16x16x32_bf16 v[148:151], v[192:195], v[100:103], v[148:151]\n\tv_mfma_f32_16x16x32_bf16 v[140:143], v[196:199], v[104:107], v[140:143]\n\tds_write_b128 %[yb], v[136:139] offset:1152\n\tds_read_b128 v[180:183], %[bb] offset:1088\n\tv_mfma_f32_16x16x32_bf16 v[144:147], v[196:199], v[108:111], v[144:147]\n\tv_mfma_f32_16x16x32_bf16 v[148:151], v[196:199], v[112:115], v[148:151]\n\tds_write_b8 %[ctc], %[one] offset:64\n\tds_read_b128 v[184:187], %[bb] offset:1152\n\tv_mfma_f32_16x16x32_bf16 v[148:151], v[200:203], v[116:119], v[148:151]\n\ts_waitcnt lgkmcnt(4)\n\ts_nop 0\n\tv_mfma_f32_16x16x32_bf16 v[152:155], v[200:203], v[120:123], 0\n\tds_read_b128 v[164:167], %[bb] offset:1216\n\tv_mfma_f32_16x16x32_bf16 v[156:159], v[200:203], v[124:127], 0\n\tv_mfma_f32_16x16x32_bf16 v[160:163], v[200:203], v[128:131], 0\n\tds_read_b128 v[168:171], %[bb] offset:1280\n\tv_mfma_f32_16x16x32_bf16 v[204:207], v[188:191], v[4:7], 0\n\tv_mfma_f32_16x16x32_bf16 v[218:221], v[176:179], v[4:7], 0\n\tds_read_b128 v[172:175], %[bb] offset:1344\n\tv_mfma_f32_16x16x32_bf16 v[208:211], v[188:191], v[8:11], 0\n\tv_mfma_f32_16x16x32_bf16 v[222:225], v[176:179], v[8:11], 0\n\tv_mfma_f32_16x16x32_bf16 v[212:215], v[188:191], v[12:15], 0\n\tv_mfma_f32_16x16x32_bf16 v[226:229], v[176:179], v[16:19], 0\n\tv_mfma_f32_16x16x32_bf16 v[152:155], v[20:23], v[176:179], v[152:155]\n\ts_waitcnt lgkmcnt(0)\n\ts_nop 0\n\tv_mfma_f32_16x16x32_bf16 v[140:143], v[52:55], v[180:183], v[140:143]\n\tds_read_b128 v[176:179], %[bb] offset:1408\n\tv_cvt_pk_bf16_f32 v192, v221, v207\n\tv_mfma_f32_16x16x32_bf16 v[144:147], v[84:87], v[180:183], v[144:147]\n\tds_read_b128 v[180:183], %[bb] offset:1472\n\tv_cvt_pk_bf16_f32 v193, v220, v206\n\tv_mfma_f32_16x16x32_bf16 v[140:143], v[48:51], v[184:187], v[140:143]\n\tv_cvt_pk_bf16_f32 v194, v219, v205\n\tv_mfma_f32_16x16x32_bf16 v[144:147], v[80:83], v[184:187], v[144:147]\n\tds_read_b128 v[184:187], %[bb] offset:1536\n\tv_cvt_pk_bf16_f32 v195, v218, v204\n\tv_mfma_f32_16x16x32_bf16 v[140:143], v[44:47], v[164:167], v[140:143]\n\tv_cvt_pk_bf16_f32 v196, v225, v211\n\tv_mfma_f32_16x16x32_bf16 v[144:147], v[76:79], v[164:167], v[144:147]\n\tv_cvt_pk_bf16_f32 v197, v224, v210\n\tv_mfma_f32_16x16x32_bf16 v[140:143], v[40:43], v[168:171], v[140:143]\n\tv_cvt_pk_bf16_f32 v198, v223, v209\n\tv_mfma_f32_16x16x32_bf16 v[144:147], v[72:75], v[168:171], v[144:147]\n\tv_cvt_pk_bf16_f32 v199, v222, v208\n\tv_mfma_f32_16x16x32_bf16 v[140:143], v[36:39], v[172:175], v[140:143]\n\tv_cvt_pk_bf16_f32 v200, v229, v215\n\tv_mfma_f32_16x16x32_bf16 v[144:147], v[68:71], v[172:175], v[144:147]\n\ts_waitcnt lgkmcnt(0)\n\ts_nop 0\n\tv_mfma_f32_16x16x32_bf16 v[140:143], v[32:35], v[176:179], v[140:143]\n\tv_cvt_pk_bf16_f32 v201, v228, v214\n\tv_mfma_f32_16x16x32_bf16 v[144:147], v[64:67], v[176:179], v[144:147]\n\tv_cvt_pk_bf16_f32 v202, v227, v213\n\tv_mfma_f32_16x16x32_bf16 v[140:143], v[28:31], v[180:183], v[140:143]\n\tds_read_b128 v[188:191], %[bb] offset:1520\n\tv_cvt_pk_bf16_f32 v203, v226, v212\n\tv_mfma_f32_16x16x32_bf16 v[144:147], v[60:63], v[180:183], v[144:147]\n\tv_mfma_f32_16x16x32_bf16 v[140:143], v[24:27], v[184:187], v[140:143]\n\tv_mfma_f32_16x16x32_bf16 v[144:147], v[56:59], v[184:187], v[144:147]\n\tv_mfma_f32_16x16x32_bf16 v[148:151], v[88:91], v[184:187], v[148:151]\n\tv_mfma_f32_16x16x32_bf16 v[144:147], v[192:195], v[92:95], v[144:147]\n\tv_mfma_f32_16x16x32_bf16 v[148:151], v[192:195], v[96:99], v[148:151]\n\tv_mfma_f32_16x16x32_bf16 v[152:155], v[192:195], v[100:103], v[152:155]\n\tds_read_b128 v[164:167], %[bb] offset:1600\n\tv_mfma_f32_16x16x32_bf16 v[144:147], v[196:199], v[104:107], v[144:147]\n\tv_mfma_f32_16x16x32_bf16 v[148:151], v[196:199], v[108:111], v[148:151]\n\tds_write_b128 %[yb], v[140:143] offset:2304\n\tv_mfma_f32_16x16x32_bf16 v[152:155], v[196:199], v[112:115], v[152:155]\n\tds_read_b128 v[168:171], %[bb] offset:1664\n\tv_mfma_f32_16x16x32_bf16 v[152:155], v[200:203], v[116:119], v[152:155]\n\tds_write_b8 %[ctc], %[one] offset:128\n\tds_read_b128 v[172:175], %[bb] offset:1728\n\tv_mfma_f32_16x16x32_bf16 v[156:159], v[200:203], v[120:123], v[156:159]\n\tv_mfma_f32_16x16x32_bf16 v[160:163], v[200:203], v[124:127], v[160:163]\n\ts_waitcnt lgkmcnt(3)\n\ts_nop 0\n\tv_mfma_f32_16x16x32_bf16 v[204:207], v[188:191], v[4:7], 0\n\tds_read_b128 v[176:179], %[bb] offset:1792\n\tv_mfma_f32_16x16x32_bf16 v[218:221], v[184:187], v[4:7], 0\n\tds_read_b128 v[180:183], %[bb] offset:1856\n\tv_mfma_f32_16x16x32_bf16 v[208:211], v[188:191], v[8:11], 0\n\tv_mfma_f32_16x16x32_bf16 v[222:225], v[184:187], v[8:11], 0\n\tv_mfma_f32_16x16x32_bf16 v[212:215], v[188:191], v[12:15], 0\n\tv_mfma_f32_16x16x32_bf16 v[226:229], v[184:187], v[16:19], 0\n\tv_mfma_f32_16x16x32_bf16 v[156:159], v[20:23], v[184:187], v[156:159]\n\tds_read_b128 v[184:187], %[bb] offset:1920\n\tv_mfma_f32_16x16x32_bf16 v[144:147], v[52:55], v[164:167], v[144:147]\n\tv_cvt_pk_bf16_f32 v192, v221, v207\n\tv_mfma_f32_16x16x32_bf16 v[148:151], v[84:87], v[164:167], v[148:151]\n\ts_waitcnt lgkmcnt(1)\n\ts_nop 0\n\tv_mfma_f32_16x16x32_bf16 v[144:147], v[48:51], v[168:171], v[144:147]\n\tds_read_b128 v[164:167], %[bb] offset:1984\n\tv_cvt_pk_bf16_f32 v193, v220, v206\n\tv_mfma_f32_16x16x32_bf16 v[148:151], v[80:83], v[168:171], v[148:151]\n\tds_read_b128 v[168:171], %[bb] offset:2048\n\tv_cvt_pk_bf16_f32 v194, v219, v205\n\tv_mfma_f32_16x16x32_bf16 v[144:147], v[44:47], v[172:175], v[144:147]\n\tds_read_b128 v[188:191], %[bb] offset:2032\n\tv_cvt_pk_bf16_f32 v195, v218, v204\n\tv_mfma_f32_16x16x32_bf16 v[148:151], v[76:79], v[172:175], v[148:151]\n\tv_cvt_pk_bf16_f32 v196, v225, v211\n\tv_mfma_f32_16x16x32_bf16 v[144:147], v[40:43], v[176:179], v[144:147]\n\tv_cvt_pk_bf16_f32 v197, v224, v210\n\tv_mfma_f32_16x16x32_bf16 v[148:151], v[72:75], v[176:179], v[148:151]\n\tv_cvt_pk_bf16_f32 v198, v223, v209\n\tv_mfma_f32_16x16x32_bf16 v[144:147], v[36:39], v[180:183], v[144:147]\n\tv_cvt_pk_bf16_f32 v199, v222, v208\n\tv_mfma_f32_16x16x32_bf16 v[148:151], v[68:71], v[180:183], v[148:151]\n\ts_waitcnt lgkmcnt(0)\n\ts_nop 0\n\tv_mfma_f32_16x16x32_bf16 v[144:147], v[32:35], v[184:187], v[144:147]\n\tv_cvt_pk_bf16_f32 v200, v229, v215\n\tv_mfma_f32_16x16x32_bf16 v[148:151], v[64:67], v[184:187], v[148:151]\n\tv_cvt_pk_bf16_f32 v201, v228, v214\n\tv_mfma_f32_16x16x32_bf16 v[144:147], v[28:31], v[164:167], v[144:147]\n\tv_cvt_pk_bf16_f32 v202, v227, v213\n\tv_mfma_f32_16x16x32_bf16 v[148:151], v[60:63], v[164:167], v[148:151]\n\tv_cvt_pk_bf16_f32 v203, v226, v212\n\tv_mfma_f32_16x16x32_bf16 v[144:147], v[24:27], v[168:171], v[144:147]\n\tv_mfma_f32_16x16x32_bf16 v[148:151], v[56:59], v[168:171], v[148:151]\n\tv_mfma_f32_16x16x32_bf16 v[152:155], v[88:91], v[168:171], v[152:155]\n\tv_mfma_f32_16x16x32_bf16 v[204:207], v[188:191], v[4:7], 0\n\tv_mfma_f32_16x16x32_bf16 v[218:221], v[168:171], v[4:7], 0\n\tv_mfma_f32_16x16x32_bf16 v[208:211], v[188:191], v[8:11], 0\n\tv_mfma_f32_16x16x32_bf16 v[222:225], v[168:171], v[8:11], 0\n\tv_mfma_f32_16x16x32_bf16 v[212:215], v[188:191], v[12:15], 0\n\tv_mfma_f32_16x16x32_bf16 v[226:229], v[168:171], v[16:19], 0\n\tds_write_b128 %[yb], v[144:147] offset:3456\n\tv_mfma_f32_16x16x32_bf16 v[160:163], v[20:23], v[168:171], v[160:163]\n\tds_read_b32 v216, %[ctl] offset:20\n\tv_mfma_f32_16x16x32_bf16 v[148:151], v[192:195], v[92:95], v[148:151]\n\tds_write_b8 %[ctc], %[one] offset:192\n\tv_mfma_f32_16x16x32_bf16 v[152:155], v[192:195], v[96:99], v[152:155]\n\tv_mfma_f32_16x16x32_bf16 v[156:159], v[192:195], v[100:103], v[156:159]\n\tv_mfma_f32_16x16x32_bf16 v[148:151], v[196:199], v[104:107], v[148:151]\n\tv_cvt_pk_bf16_f32 v192, v221, v207\n\tv_mfma_f32_16x16x32_bf16 v[152:155], v[196:199], v[108:111], v[152:155]\n\tv_cvt_pk_bf16_f32 v193, v220, v206\n\tv_mfma_f32_16x16x32_bf16 v[156:159], v[196:199], v[112:115], v[156:159]\n\tv_cvt_pk_bf16_f32 v194, v219, v205\n\tv_mfma_f32_16x16x32_bf16 v[156:159], v[200:203], v[116:119], v[156:159]\n\tv_cvt_pk_bf16_f32 v195, v218, v204\n\tv_mfma_f32_16x16x32_bf16 v[160:163], v[200:203], v[120:123], v[160:163]\n\ts_waitcnt lgkmcnt(0)\n\ts_nop 0\n\tv_readfirstlane_b32 %[s], v216\n\ts_mov_b32 %[c], 0x400000\n\ts_cmp_ge_u32 %[s], 2\n\ts_cbranch_scc1 2f\n\t1:\n\tds_read_b32 v216, %[ctl] offset:20\n\ts_waitcnt lgkmcnt(0)\n\tv_readfirstlane_b32 %[s], v216\n\ts_cmp_ge_u32 %[s], 2\n\ts_cbranch_scc1 2f\n\ts_sleep 1\n\ts_sub_u32 %[c], %[c], 1\n\ts_cbranch_scc0 1b\n\t.p2align 3\n\t2:\n\tv_mfma_f32_16x16x32_bf16 v[152:155], v[192:195], v[92:95], v[152:155]\n\tds_read_b128 v[172:175], %[bb] offset:2112\n\tv_cvt_pk_bf16_f32 v196, v225, v211\n\tv_mfma_f32_16x16x32_bf16 v[156:159], v[192:195], v[96:99], v[156:159]\n\tds_read_b128 v[176:179], %[bb] offset:2176\n\tv_cvt_pk_bf16_f32 v197, v224, v210\n\tv_mfma_f32_16x16x32_bf16 v[160:163], v[192:195], v[100:103], v[160:163]\n\tv_cvt_pk_bf16_f32 v198, v223, v209\n\tds_read_b128 v[180:183], %[bb] offset:2240\n\tv_cvt_pk_bf16_f32 v199, v222, v208\n\ts_nop 1\n\ts_nop 0\n\tv_mfma_f32_16x16x32_bf16 v[152:155], v[196:199], v[104:107], v[152:155]\n\tds_read_b128 v[184:187], %[bb] offset:2304\n\tv_cvt_pk_bf16_f32 v200, v229, v215\n\tv_mfma_f32_16x16x32_bf16 v[156:159], v[196:199], v[108:111], v[156:159]\n\tv_cvt_pk_bf16_f32 v201, v228, v214\n\tds_read_b128 v[164:167], %[bb] offset:2368\n\tv_mfma_f32_16x16x32_bf16 v[160:163], v[196:199], v[112:115], v[160:163]\n\tv_cvt_pk_bf16_f32 v202, v227, v213\n\tds_read_b128 v[168:171], %[bb] offset:2432\n\tv_cvt_pk_bf16_f32 v203, v226, v212\n\ts_nop 1\n\ts_nop 0\n\tv_mfma_f32_16x16x32_bf16 v[160:163], v[200:203], v[116:119], v[160:163]\n\ts_waitcnt lgkmcnt(3)\n\ts_nop 0\n\tv_mfma_f32_16x16x32_bf16 v[148:151], v[52:55], v[172:175], v[148:151]\n\tv_mfma_f32_16x16x32_bf16 v[152:155], v[84:87], v[172:175], v[152:155]\n\tds_read_b128 v[172:175], %[bb] offset:2496\n\tv_mfma_f32_16x16x32_bf16 v[148:151], v[48:51], v[176:179], v[148:151]\n\tv_mfma_f32_16x16x32_bf16 v[152:155], v[80:83], v[176:179], v[152:155]\n\tds_read_b128 v[176:179], %[bb] offset:2560\n\tv_mfma_f32_16x16x32_bf16 v[148:151], v[44:47], v[180:183], v[148:151]\n\tds_read_b128 v[188:191], %[bb] offset:2544\n\tv_mfma_f32_16x16x32_bf16 v[152:155], v[76:79], v[180:183], v[152:155]\n\ts_waitcnt lgkmcnt(2)\n\ts_nop 0\n\tv_mfma_f32_16x16x32_bf16 v[148:151], v[40:43], v[184:187], v[148:151]\n\tv_mfma_f32_16x16x32_bf16 v[152:155], v[72:75], v[184:187], v[152:155]\n\tds_read_b128 v[180:183], %[bb] offset:2624\n\tv_mfma_f32_16x16x32_bf16 v[148:151], v[36:39], v[164:167], v[148:151]\n\tv_mfma_f32_16x16x32_bf16 v[152:155], v[68:71], v[164:167], v[152:155]\n\tds_read_b128 v[184:187], %[bb] offset:2688\n\tv_mfma_f32_16x16x32_bf16 v[148:151], v[32:35], v[168:171], v[148:151]\n\tv_mfma_f32_16x16x32_bf16 v[152:155], v[64:67], v[168:171], v[152:155]\n\tds_read_b128 v[164:167], %[bb] offset:2752\n\tv_mfma_f32_16x16x32_bf16 v[148:151], v[28:31], v[172:175], v[148:151]\n\tv_mfma_f32_16x16x32_bf16 v[152:155], v[60:63], v[172:175], v[152:155]\n\ts_waitcnt lgkmcnt(1)\n\ts_nop 0\n\tv_mfma_f32_16x16x32_bf16 v[148:151], v[24:27], v[176:179], v[148:151]\n\tds_read_b128 v[168:171], %[bb] offset:2816\n\tv_mfma_f32_16x16x32_bf16 v[152:155], v[56:59], v[176:179], v[152:155]\n\tds_read_b128 v[172:175], %[bb] offset:2880\n\tv_mfma_f32_16x16x32_bf16 v[156:159], v[88:91], v[176:179], v[156:159]\n\tv_mfma_f32_16x16x32_bf16 v[204:207], v[188:191], v[4:7], 0\n\tv_mfma_f32_16x16x32_bf16 v[218:221], v[176:179], v[4:7], 0\n\tv_mfma_f32_16x16x32_bf16 v[208:211], v[188:191], v[8:11], 0\n\tv_mfma_f32_16x16x32_bf16 v[222:225], v[176:179], v[8:11], 0\n\tds_write_b128 %[yb], v[148:151] offset:4608\n\tv_mfma_f32_16x16x32_bf16 v[152:155], v[52:55], v[180:183], v[152:155]\n\tds_read_b128 v[176:179], %[bb] offset:2944\n\tv_mfma_f32_16x16x32_bf16 v[156:159], v[84:87], v[180:183], v[156:159]\n\tds_write_b8 %[ctc], %[one] offset:256\n\tds_read_b128 v[180:183], %[bb] offset:3008\n\tv_mfma_f32_16x16x32_bf16 v[152:155], v[48:51], v[184:187], v[152:155]\n\tv_cvt_pk_bf16_f32 v192, v221, v207\n\tv_mfma_f32_16x16x32_bf16 v[156:159], v[80:83], v[184:187], v[156:159]\n\ts_waitcnt lgkmcnt(3)\n\ts_nop 0\n\tv_mfma_f32_16x16x32_bf16 v[152:155], v[44:47], v[164:167], v[152:155]\n\tds_read_b128 v[184:187], %[bb] offset:3072\n\tv_cvt_pk_bf16_f32 v193, v220, v206\n\tv_mfma_f32_16x16x32_bf16 v[156:159], v[76:79], v[164:167], v[156:159]\n\tv_cvt_pk_bf16_f32 v194, v219, v205\n\tv_mfma_f32_16x16x32_bf16 v[152:155], v[40:43], v[168:171], v[152:155]\n\tv_cvt_pk_bf16_f32 v195, v218, v204\n\tv_mfma_f32_16x16x32_bf16 v[156:159], v[72:75], v[168:171], v[156:159]\n\tds_read_b128 v[188:191], %[bb] offset:3056\n\tv_cvt_pk_bf16_f32 v196, v225, v211\n\tv_mfma_f32_16x16x32_bf16 v[152:155], v[36:39], v[172:175], v[152:155]\n\tv_cvt_pk_bf16_f32 v197, v224, v210\n\tv_mfma_f32_16x16x32_bf16 v[156:159], v[68:71], v[172:175], v[156:159]\n\ts_waitcnt lgkmcnt(1)\n\ts_nop 0\n\tv_mfma_f32_16x16x32_bf16 v[152:155], v[32:35], v[176:179], v[152:155]\n\tds_read_b32 v216, %[ctl] offset:24\n\tv_cvt_pk_bf16_f32 v198, v223, v209\n\tv_mfma_f32_16x16x32_bf16 v[156:159], v[64:67], v[176:179], v[156:159]\n\tv_cvt_pk_bf16_f32 v199, v222, v208\n\tv_mfma_f32_16x16x32_bf16 v[152:155], v[28:31], v[180:183], v[152:155]\n\tv_mfma_f32_16x16x32_bf16 v[156:159], v[60:63], v[180:183], v[156:159]\n\tv_mfma_f32_16x16x32_bf16 v[152:155], v[24:27], v[184:187], v[152:155]\n\tv_mfma_f32_16x16x32_bf16 v[156:159], v[56:59], v[184:187], v[156:159]\n\tv_mfma_f32_16x16x32_bf16 v[160:163], v[88:91], v[184:187], v[160:163]\n\tv_mfma_f32_16x16x32_bf16 v[156:159], v[192:195], v[92:95], v[156:159]\n\ts_waitcnt lgkmcnt(0)\n\ts_nop 0\n\tv_readfirstlane_b32 %[s], v216\n\ts_mov_b32 %[c], 0x400000\n\ts_cmp_ge_u32 %[s], 2\n\ts_cbranch_scc1 2f\n\t1:\n\tds_read_b32 v216, %[ctl] offset:24\n\ts_waitcnt lgkmcnt(0)\n\tv_readfirstlane_b32 %[s], v216\n\ts_cmp_ge_u32 %[s], 2\n\ts_cbranch_scc1 2f\n\ts_sleep 1\n\ts_sub_u32 %[c], %[c], 1\n\ts_cbranch_scc0 1b\n\t.p2align 3\n\t2:\n\tv_mfma_f32_16x16x32_bf16 v[160:163], v[192:195], v[96:99], v[160:163]\n\tds_write_b128 %[yb], v[152:155] offset:5760\n\tv_mfma_f32_16x16x32_bf16 v[156:159], v[196:199], v[104:107], v[156:159]\n\tds_read_b128 v[164:167], %[bb] offset:3136\n\tv_mfma_f32_16x16x32_bf16 v[160:163], v[196:199], v[108:111], v[160:163]\n\tds_write_b8 %[ctc], %[one] offset:320\n\tds_read_b128 v[168:171], %[bb] offset:3200\n\tv_mfma_f32_16x16x32_bf16 v[204:207], v[188:191], v[4:7], 0\n\tds_read_b128 v[172:175], %[bb] offset:3264\n\tv_mfma_f32_16x16x32_bf16 v[218:221], v[184:187], v[4:7], 0\n\tds_read_b128 v[176:179], %[bb] offset:3328\n\tv_mfma_f32_16x16x32_bf16 v[208:211], v[188:191], v[8:11], 0\n\tds_read_b128 v[180:183], %[bb] offset:3392\n\tv_mfma_f32_16x16x32_bf16 v[222:225], v[184:187], v[8:11], 0\n\ts_waitcnt lgkmcnt(3)\n\ts_nop 0\n\tv_mfma_f32_16x16x32_bf16 v[156:159], v[52:55], v[164:167], v[156:159]\n\tds_read_b128 v[184:187], %[bb] offset:3456\n\tv_cvt_pk_bf16_f32 v192, v221, v207\n\tv_mfma_f32_16x16x32_bf16 v[160:163], v[84:87], v[164:167], v[160:163]\n\tds_read_b128 v[164:167], %[bb] offset:3520\n\tv_cvt_pk_bf16_f32 v193, v220, v206\n\tv_mfma_f32_16x16x32_bf16 v[156:159], v[48:51], v[168:171], v[156:159]\n\tv_cvt_pk_bf16_f32 v194, v219, v205\n\tv_mfma_f32_16x16x32_bf16 v[160:163], v[80:83], v[168:171], v[160:163]\n\ts_waitcnt lgkmcnt(2)\n\ts_nop 0\n\tv_mfma_f32_16x16x32_bf16 v[156:159], v[44:47], v[172:175], v[156:159]\n\tds_read_b128 v[168:171], %[bb] offset:3584\n\tv_cvt_pk_bf16_f32 v195, v218, v204\n\tv_mfma_f32_16x16x32_bf16 v[160:163], v[76:79], v[172:175], v[160:163]\n\tds_read_b128 v[172:175], %[bb] offset:3648\n\tv_cvt_pk_bf16_f32 v196, v225, v211\n\tv_mfma_f32_16x16x32_bf16 v[156:159], v[40:43], v[176:179], v[156:159]\n\tglobal_load_dwordx2 v[240:241], %[so], %[ws] sc1\n\tv_cvt_pk_bf16_f32 v197, v224, v210\n\tv_mfma_f32_16x16x32_bf16 v[160:163], v[72:75], v[176:179], v[160:163]\n\tds_read_b128 v[176:179], %[bb] offset:3712\n\tv_cvt_pk_bf16_f32 v198, v223, v209\n\tv_mfma_f32_16x16x32_bf16 v[156:159], v[36:39], v[180:183], v[156:159]\n\tv_cvt_pk_bf16_f32 v199, v222, v208\n\tv_mfma_f32_16x16x32_bf16 v[160:163], v[68:71], v[180:183], v[160:163]\n\ts_waitcnt lgkmcnt(1)\n\ts_nop 0\n\tv_mfma_f32_16x16x32_bf16 v[156:159], v[32:35], v[184:187], v[156:159]\n\tds_read_b128 v[180:183], %[bb] offset:3776\n\tv_mfma_f32_16x16x32_bf16 v[160:163], v[64:67], v[184:187], v[160:163]\n\tds_read_b128 v[184:187], %[bb] offset:3840\n\tv_mfma_f32_16x16x32_bf16 v[156:159], v[28:31], v[164:167], v[156:159]\n\tv_mfma_f32_16x16x32_bf16 v[160:163], v[60:63], v[164:167], v[160:163]\n\tds_read_b128 v[164:167], %[bb] offset:3904\n\tv_mfma_f32_16x16x32_bf16 v[156:159], v[24:27], v[168:171], v[156:159]\n\tv_mfma_f32_16x16x32_bf16 v[160:163], v[56:59], v[168:171], v[160:163]\n\tds_read_b128 v[168:171], %[bb] offset:3968\n\tv_mfma_f32_16x16x32_bf16 v[160:163], v[192:195], v[92:95], v[160:163]\n\tv_mfma_f32_16x16x32_bf16 v[160:163], v[196:199], v[104:107], v[160:163]\n\tv_mfma_f32_16x16x32_bf16 v[160:163], v[52:55], v[172:175], v[160:163]\n\ts_waitcnt lgkmcnt(1)\n\ts_nop 0\n\tv_mfma_f32_16x16x32_bf16 v[160:163], v[48:51], v[176:179], v[160:163]\n\tds_write_b128 %[yb], v[156:159] offset:6912\n\tv_mfma_f32_16x16x32_bf16 v[160:163], v[44:47], v[180:183], v[160:163]\n\tds_read_b128 v[172:175], %[bb] offset:4032\n\tv_mfma_f32_16x16x32_bf16 v[160:163], v[40:43], v[184:187], v[160:163]\n\tds_write_b8 %[ctc], %[one] offset:384\n\tds_read_b128 v[176:179], %[bb] offset:4096\n\tv_mfma_f32_16x16x32_bf16 v[160:163], v[36:39], v[164:167], v[160:163]\n\ts_waitcnt lgkmcnt(4)\n\ts_nop 0\n\tv_mfma_f32_16x16x32_bf16 v[160:163], v[32:35], v[168:171], v[160:163]\n\ts_waitcnt lgkmcnt(2)\n\ts_nop 0\n\tv_mfma_f32_16x16x32_bf16 v[160:163], v[28:31], v[172:175], v[160:163]\n\ts_waitcnt lgkmcnt(0)\n\ts_nop 0\n\tv_mfma_f32_16x16x32_bf16 v[160:163], v[24:27], v[176:179], v[160:163]\n\ts_nop 7\n\tds_write_b128 %[yb], v[160:163] offset:8064\n\tds_write_b8 %[ctc], %[one] offset:448"
#define CONV_CLOBBER "v24", "v25", "v26", "v27", "v28", "v29", "v30", "v31", "v32", "v33", "v34", "v35", "v36", "v37", "v38", "v39", "v40", "v41", "v42", "v43", "v44", "v45", "v46", "v47", "v48", "v49", "v50", "v51", "v52", "v53", "v54", "v55", "v56", "v57", "v58", "v59", "v60", "v61", "v62", "v63", "v64", "v65", "v66", "v67", "v68", "v69", "v70", "v71", "v72", "v73", "v74", "v75", "v76", "v77", "v78", "v79", "v80", "v81", "v82", "v83", "v84", "v85", "v86", "v87", "v88", "v89", "v90", "v91", "v92", "v93", "v94", "v95", "v96", "v97", "v98", "v99", "v100", "v101", "v102", "v103", "v104", "v105", "v106", "v107", "v108", "v109", "v110", "v111", "v112", "v113", "v114", "v115", "v116", "v117", "v118", "v119", "v120", "v121", "v122", "v123", "v124", "v125", "v126", "v127", "v128", "v129", "v130", "v131", "v132", "v133", "v134", "v135", "v136", "v137", "v138", "v139", "v140", "v141", "v142", "v143", "v144", "v145", "v146", "v147", "v148", "v149", "v150", "v151", "v152", "v153", "v154", "v155", "v156", "v157", "v158", "v159", "v160", "v161", "v162", "v163", "v164", "v165", "v166", "v167", "v168", "v169", "v170", "v171", "v172", "v173", "v174", "v175", "v176", "v177", "v178", "v179", "v180", "v181", "v182", "v183", "v184", "v185", "v186", "v187", "v188", "v189", "v190", "v191", "v192", "v193", "v194", "v195", "v196", "v197", "v198", "v199", "v200", "v201", "v202", "v203", "v204", "v205", "v206", "v207", "v208", "v209", "v210", "v211", "v212", "v213", "v214", "v215", "v216", "v217", "v218", "v219", "v220", "v221", "v222", "v223", "v224", "v225", "v226", "v227", "v228", "v229"

typedef __attribute__((address_space(4))) const Params* KArg;
#define ROLE_BEGIN(N) int b, tid; \
    asm volatile("fk_role" #N ":\n\tv_mov_b32 %0, v0" : "=&v"(tid) : "{s2}"(b0), "{v0}"(tid0) : "memory"); \
    asm volatile("s_mov_b32 %0, s2" : "=&s"(b) : "{s2}"(b0)); \
    const int lane = tid & 63, wave = __builtin_amdgcn_readfirstlane(tid >> 6), xcd = b & 7, slot = b >> 3; \
    const int cw = slot < 16 ? xcd * 16 + slot : 112 + xcd * 2 + slot, chbase = cw * 4, w = xcd * 9 + (slot - 18); (void)chbase; (void)w; (void)lane
#define ROLE_ARGS() Params p; SBAR(); \
    asm volatile("s_waitcnt lgkmcnt(0)" : "={s[56:57]}"(p.x), "={s[58:59]}"(p.Are), "={s[60:61]}"(p.Aim), "={s[62:63]}"(p.Bre), "={s[64:65]}"(p.Bim), "={s[66:67]}"(p.Cre), "={s[68:69]}"(p.Cim), "={s[70:71]}"(p.logdt), \
                 "={s[72:73]}"(p.dskip), "={s[74:75]}"(p.wr), "={s[76:77]}"(p.wg), "={s[78:79]}"(p.wu), "={s[80:81]}"(p.wd), "={s[82:83]}"(p.gw), "={s[84:85]}"(p.out), "={s[86:87]}"(p.ws)); STAMP0_FIX()
#define ROLE_END() do { STAMP_END(); STAMP_FLUSH(); __builtin_amdgcn_s_waitcnt(0x0070); asm volatile("s_endpgm" ::: "memory"); } while (0)
#define MF(A_, B_, C_) __builtin_amdgcn_mfma_f32_16x16x32_bf16(A_, B_, C_, 0, 0, 0)
#define GRP(mask, n) __builtin_amdgcn_sched_group_barrier(mask, n, 0)

#define FFN_PRE() u32 gof = w * 128 + ((wave >> 1) * 8 + (lane >> 3)) * (F * 2) + ((lane & 7) ^ (((lane >> 3) & 2) | ((wave & 2) << 1))) * 16; asm volatile("" : "+v"(gof)); ROLE_ARGS()
#define FFN_FILL() \
    if (FDELAY) __builtin_amdgcn_s_sleep(FDELAY); \
    { const char* gsrc = (const char*)((wave & 1) ? p.wu : p.wg); \
      _Pragma("unroll") for (int i = 0; i < 18; ++i) { \
          asm volatile("s_mov_b32 m0, %2\n\ts_nop 0\n\tglobal_load_lds_dwordx4 %0, %1" WNT :: "v"(gof), "s"(gsrc + i * (32 * F * 2)), "s"((8 * i + wave) * 1024) : "memory"); \
          if (i == 5 || i == 11) __builtin_amdgcn_s_barrier(); } } \
    STAMP(11)
#define FFN_WDFRAG(k, pr) do { LAS char* t_ = ta + (pr) * 64; \
    _Pragma("unroll") for (int e = 0; e < 8; ++e) a[k][e] = __builtin_amdgcn_ds_read_tr16_b64_v4i16((LAS s16x4*)(t_ + (e >> 2) * 32 + (e & 3) * 16 * WDP)); } while (0)
#define FFN_DOWN(k, pr) do { v4f y_ = MF(__builtin_shufflevector(a[k][0], a[k][1], 0, 1, 2, 3, 4, 5, 6, 7), hf[0], ((v4f){0.f, 0.f, 0.f, 0.f})); \
    y_ = MF(__builtin_shufflevector(a[k][4], a[k][5], 0, 1, 2, 3, 4, 5, 6, 7), hf[1], y_); \
    y_ = MF(__builtin_shufflevector(a[k][2], a[k][3], 0, 1, 2, 3, 4, 5, 6, 7), hf[2], y_); \
    ya[k] = MF(__builtin_shufflevector(a[k][6], a[k][7], 0, 1, 2, 3, 4, 5, 6, 7), hf[3], y_); } while (0)
#define FFN_ADDR() const int n = lane & 15, q = lane >> 4; \
    LAS char* ta = smem + (lane >> 2) * WDP + (lane & 3) * 8; \
    LAS char* hrow = smem + FL_H + (n & 7) * 144 + q * 16; \
    LAS char* ha = n < 8 ? hrow : smem + FL_Z + q * 16; \
    LAS char* hb = n < 8 ? smem + FL_Z + q * 16 : hrow; \
    u32 obase = OFF_PART + (4 * (n >> 3) + q) * PP + hline(p.ws, w) + (n & 7) * 16, ctl = (u32)(uintptr_t)(smem + FL_TAG); \
    asm volatile("" : "+v"(ta), "+v"(ha), "+v"(hb), "+v"(obase), "+v"(ctl))
#define FFN_HFRAG() const bf16x8 hf[4] = {*(LAS bf16x8*)(ha), *(LAS bf16x8*)(hb), *(LAS bf16x8*)(ha + 64), *(LAS bf16x8*)(hb + 64)}
#define FFN_PUB(k, pr) __builtin_amdgcn_raw_buffer_store_b128((v4u){cvtpk(ya[k][0], ya[k][1]), tag, cvtpk(ya[k][2], ya[k][3]), tag}, WSR(), obase + (pr) * (8 * PP), 0, 16)

__global__ __launch_bounds__(512, 2) void fused_kernel(Params p_unused) {
    extern __shared__ __attribute__((aligned(16))) char smem_[];
    LAS char* smem = (LAS char*)smem_;
    const KArg kp0 = (KArg)__builtin_amdgcn_kernarg_segment_ptr();
    const int tid0 = threadIdx.x, b0 = blockIdx.x;
    asm volatile("s_load_dwordx16 s[56:71], s[0:1], 0x0\n\ts_load_dwordx16 s[72:87], s[0:1], 0x40\n\tv_readfirstlane_b32 s92, v0\n\t"
                 "s_cmp_ge_u32 s2, 32\n\ts_cbranch_scc1 1f\n\t"
                 "s_getpc_b64 s[90:91]\n\ts_and_b32 s90, s90, 0xffffff00\n\ts_and_b32 s93, s2, -8\n\ts_lshr_b32 s89, s92, 6\n\ts_add_u32 s93, s93, s89\n\ts_min_u32 s93, s93, %3\n\ts_lshl_b32 s93, s93, 10\n\t"
                 "v_and_b32 v1, 63, v0\n\tv_lshlrev_b32 v1, 4, v1\n\tv_add_u32 v1, s93, v1\n\ts_mov_b32 m0, %4\n\ts_nop 0\n\tglobal_load_lds_dwordx4 v1, s[90:91]\n"
                 "1:\n\ts_cmp_lt_u32 s2, 144\n\ts_cbranch_scc0 2f\n\ts_cmp_lt_u32 s92, 256\n\ts_cbranch_scc1 fk_role0\n\ts_branch fk_role1\n"
                 "2:\n\ts_cmp_lt_u32 s2, 216\n\ts_cbranch_scc0 3f\n\ts_cmp_lt_u32 s92, 256\n\ts_cbranch_scc1 fk_role2\n\ts_branch fk_role3\n"
                 "3:\n\ts_cmp_lt_u32 s2, 224\n\ts_cbranch_scc1 fk_role4\n\ts_branch fk_role5"
                 :: "{s[0:1]}"(kp0), "{s2}"(b0), "{v0}"(tid0), "n"(CODE_KB - 1), "n"(LDS_DUMMY) : "memory", "scc", "s89", "s90", "s91", "s92", "s93", "v1");
    const int slot = b0 >> 3, wave = __builtin_amdgcn_readfirstlane(tid0 >> 6);
    if (slot >= 18) {
    if (slot >= 27 || wave >= 4) {
    if (slot < 28) {
    if (slot == 27)
    {
        ROLE_BEGIN(4);
        ROLE_ARGS();
        u32 tagv;
        asm volatile("v_mov_b32 %0, 0\n\tglobal_load_dword %0, %0, %1 sc1\n\ts_waitcnt vmcnt(0)" : "=&v"(tagv) : "s"(p.ws) : "memory");
        const u32 tag = __builtin_amdgcn_readfirstlane(tagv) + 1u;
        if (tid < 128) ((LAS u32*)smem)[tid] = 0;
        u32 ga = (u32)(uintptr_t)(smem + (lane & 15) * 4), gv = (u32)(uintptr_t)(smem + (lane >> 2) * 4), gq_ = (u32)(uintptr_t)(smem + (lane & 3) * 16), zero = (u32)(uintptr_t)smem;
        u32 ro = OFF_RG + hline(p.ws, tid >> 3) + (tid & 7) * 16, ro2 = OFF_RG + hline(p.ws, 64 + (tid >> 3)) + (tid & 7) * 16, so = OFF_SEL + ((lane & 1) * 8 + xcd) * 256, xo = (lane & 3) * 128;
        asm volatile("" : "+v"(ga), "+v"(gv), "+v"(gq_), "+v"(zero), "+v"(ro), "+v"(ro2), "+v"(so), "+v"(xo));
        BAR();
        __builtin_amdgcn_s_sleep(SELWAIT);
        v4u qa, qb;
        u32 c_;
#define POLL_A "global_load_dwordx4 v[20:23], %[ro], %[ws] sc1\n\tglobal_load_dwordx4 v[24:27], %[r2], %[ws] sc1\n\t"
#define POLL_B "global_load_dwordx4 v[28:31], %[ro], %[ws] sc1\n\tglobal_load_dwordx4 v[32:35], %[r2], %[ws] sc1\n\t"
        asm volatile("s_mov_b32 %[c], 0x40000\n\t" POLL_A "s_sleep 8\n\t" POLL_B
                     "1:\n\ts_waitcnt vmcnt(2)\n\tv_cmpx_eq_u32 %[tag], v21\n\tv_cmpx_eq_u32 %[tag], v23\n\tv_cmpx_eq_u32 %[tag], v25\n\tv_cmpx_eq_u32 %[tag], v27\n\t"
                     "s_cmp_eq_u64 exec, -1\n\ts_mov_b64 exec, -1\n\ts_cbranch_scc1 3f\n\t" POLL_A
                     "s_waitcnt vmcnt(2)\n\tv_cmpx_eq_u32 %[tag], v29\n\tv_cmpx_eq_u32 %[tag], v31\n\tv_cmpx_eq_u32 %[tag], v33\n\tv_cmpx_eq_u32 %[tag], v35\n\t"
                     "s_cmp_eq_u64 exec, -1\n\ts_mov_b64 exec, -1\n\ts_cbranch_scc1 2f\n\t" POLL_B
                     "s_sub_u32 %[c], %[c], 1\n\ts_cbranch_scc0 1b\n\ts_waitcnt vmcnt(0)\n"
                     "2:\n\tv_mov_b32 v36, v28\n\tv_mov_b32 v37, v30\n\tv_mov_b32 v38, v32\n\tv_mov_b32 v39, v34\n\ts_branch 4f\n"
                     "3:\n\tv_mov_b32 v36, v20\n\tv_mov_b32 v37, v22\n\tv_mov_b32 v38, v24\n\tv_mov_b32 v39, v26\n"
                     "4:"
                     : "=&{v[36:39]}"(qa), [c] "=&s"(c_) : [ro] "v"(ro), [r2] "v"(ro2), [ws] "s"(p.ws), [tag] "s"(tag)
                     : "memory", "scc", "vcc", "v20", "v21", "v22", "v23", "v24", "v25", "v26", "v27", "v28", "v29", "v30", "v31", "v32", "v33", "v34", "v35");
        asm volatile("" : "=&{v[20:23]}"(qb));
        v4u qc, qd, qe;
        asm volatile("" : "=&{v[24:27]}"(qc), "=&{v[28:31]}"(qd), "=&{v[32:35]}"(qe));
        STAMP(2);
        u32 rank, tau, cnt;
        u64 m0_, m1_, m2_, m3_;
        {
            u32 v, nv; v4u q4;
            asm volatile("ds_max_u32 %0, %1\n\ts_waitcnt lgkmcnt(0)\n\ts_barrier" :: "v"(ga), "v"(max(max(qa.x, qa.y), max(qa.z, qa.w))) : "memory");
#define RANK16(off) asm volatile("ds_read_b32 %[v], %[gv] offset:" #off "\n\tds_read_b128 v[40:43], %[gq] offset:" #off "\n\tds_read_b32 %[n], %[z] offset:64\n\tv_mov_b32 %[r], 0\n\ts_waitcnt lgkmcnt(0)\n\t" \
                         "v_cmp_gt_u32 %[a], v40, %[v]\n\tv_cmp_gt_u32 %[b], v41, %[v]\n\tv_cmp_gt_u32 %[c], v42, %[v]\n\tv_cmp_gt_u32 %[d], v43, %[v]\n\t" \
                         "v_addc_co_u32 %[r], vcc, 0, %[r], %[a]\n\tv_addc_co_u32 %[r], vcc, 0, %[r], %[b]\n\tv_addc_co_u32 %[r], vcc, 0, %[r], %[c]\n\tv_addc_co_u32 %[r], vcc, 0, %[r], %[d]\n\ts_nop 1\n\t" \
                         "v_add_u32_dpp %[r], %[r], %[r] quad_perm:[1,0,3,2] row_mask:0xf bank_mask:0xf\n\ts_nop 1\n\tv_add_u32_dpp %[r], %[r], %[r] quad_perm:[2,3,0,1] row_mask:0xf bank_mask:0xf\n\ts_nop 0" \
                         : [v] "=&v"(v), [r] "=&v"(rank), [n] "=&v"(nv), "=&{v[40:43]}"(q4), [a] "=&s"(m0_), [b] "=&s"(m1_), [c] "=&s"(m2_), [d] "=&s"(m3_) : [gv] "v"(gv), [gq] "v"(gq_), [z] "v"(zero) : "memory", "vcc")
            RANK16(0);
            tau = (u32)__builtin_amdgcn_readlane((int)v, (int)__builtin_ctzll(__ballot(rank == 7)));
            u32 pos;
            asm volatile("s_nop 1\n\tv_mov_b32 %[n], 0\n\tv_cmp_le_u32 %[a], %[t], v36\n\tv_cmp_le_u32 %[b], %[t], v37\n\tv_cmp_le_u32 %[c], %[t], v38\n\tv_cmp_le_u32 %[d], %[t], v39\n\t"
                         "v_addc_co_u32 %[n], vcc, 0, %[n], %[a]\n\tv_addc_co_u32 %[n], vcc, 0, %[n], %[b]\n\tv_addc_co_u32 %[n], vcc, 0, %[n], %[c]\n\tv_addc_co_u32 %[n], vcc, 0, %[n], %[d]\n\t"
                         "v_lshlrev_b32 %[n], 2, %[n]\n\tv_cmpx_ne_u32 0, %[n]\n\tds_add_rtn_u32 %[p], %[z], %[n] offset:64\n\ts_waitcnt lgkmcnt(0)\n\t"
                         "s_mov_b64 exec, %[a]\n\tds_write_b32 %[p], v36 offset:256\n\tv_add_u32 %[p], 4, %[p]\n\t"
                         "s_mov_b64 exec, %[b]\n\tds_write_b32 %[p], v37 offset:256\n\tv_add_u32 %[p], 4, %[p]\n\t"
                         "s_mov_b64 exec, %[c]\n\tds_write_b32 %[p], v38 offset:256\n\tv_add_u32 %[p], 4, %[p]\n\t"
                         "s_mov_b64 exec, %[d]\n\tds_write_b32 %[p], v39 offset:256\n\t"
                         "s_mov_b64 exec, -1\n\ts_waitcnt lgkmcnt(0)\n\ts_barrier"
                         : [n] "=&v"(cnt), [p] "=&v"(pos), [a] "=&s"(m0_), [b] "=&s"(m1_), [c] "=&s"(m2_), [d] "=&s"(m3_) : [t] "s"(tau), [z] "v"(zero), "{v[36:39]}"(qa) : "memory", "vcc");
            if (wave == 0) {
                RANK16(256);
                const int nc = __builtin_amdgcn_readfirstlane((int)nv) >> 2;
                if (__builtin_expect(nc > 16, 0)) {
                    v = (u32)__shfl((int)top8_d(smem, lane, nc), (lane >> 2) & 7);
                    rank = lane >> 2;
                }
                const u64 m8 = __ballot(rank < 8);
                asm volatile("s_and_b64 exec, %[m], %[k2]\n\tglobal_store_dwordx2 %[so], %[d], %[ws] sc1\n\ts_mov_b64 exec, %[m]\n\ts_mov_b32 m0, 0x8000\n\ts_nop 0\n\t"
                             "global_load_lds_dword %[xo], %[x]\n\tglobal_load_lds_dword %[xo], %[x] offset:512\n\ts_and_b64 exec, %[m], %[k1]\n\tglobal_load_lds_dword %[xo], %[x] offset:1024\n\ts_mov_b64 exec, -1"
                             :: [m] "s"(m8), [k2] "s"(0x3333333333333333ull), [k1] "s"(0x1111111111111111ull), [so] "v"(so + rank * 8), [d] "v"((v2u){v, tag}), [ws] "s"(p.ws),
                                [xo] "v"(xo + (~v & 2047u) * 1152u), [x] "s"(p.x) : "memory", "scc");
                asm volatile("s_mov_b64 exec, 1\n\tglobal_store_dwordx2 %0, %1, %2 sc1\n\ts_mov_b64 exec, -1" :: "v"(OFF_CI + (416 + xcd) * 8), "v"((v2u){tag, tag}), "s"(p.ws) : "memory");
            }
        }
        STAMP(3);
        asm volatile("s_waitcnt vmcnt(0)" :: "v"(qb), "v"(qc), "v"(qd), "v"(qe) : "memory");
        if (xcd == 0) {
            const u32 cio = OFF_CI + (tid < 212 ? tid : 0) * 16;
            for (u32 spins = 0;; ++spins) {
                REREAD();
                const v4u q2 = __builtin_amdgcn_raw_buffer_load_b128(WSR(), cio, 0, 16);
                if (__all((q2.y == tag) & (q2.w == tag)) || spins > SPIN_CAP) break;
                __builtin_amdgcn_s_sleep(16);
            }
            BAR();
            if (tid == 0) __hip_atomic_store((GAS u32*)p.ws, tag, RLX_AGENT);
        }
        ROLE_END();
    }
    else
        {
            ROLE_BEGIN(3);
            FFN_PRE();
            const int pw = wave - 4;
            u32 tagv;
            asm volatile("v_mov_b32 %0, 0\n\tglobal_load_dword %0, %0, %1 sc1" : "=&v"(tagv) : "s"(p.ws) : "memory");
            FFN_FILL();
            v2u sel;
            const u32 so = OFF_SEL + xcd * 256 + (2 * pw + (lane & 1)) * 8, l16 = lane * 16;
#define POLL_S "global_load_dwordx2 v[10:11], %[so], %[ws] sc1\n\t"
            asm volatile("s_waitcnt vmcnt(18)" : "+v"(tagv) :: "memory");
            const u32 tag = __builtin_amdgcn_readfirstlane(tagv) + 1u;
            *(LAS v4u*)(smem + FL_Z + min(l16, 256u)) = (v4u){lane >= 16 ? tag : 0u, 0u, 0u, 0u};
            FFN_ADDR();
            u32 one = 1u;
            const char* dsrc = (const char*)(p.wd + (size_t)(64 * w + 32 * (pw >> 1)) * D);
            const u32 ddst = (pw >> 1) * 32 * WDP + (pw & 1) * 1024;
            u32 dof[19];
#pragma unroll
            for (int i = 0; i < 19; ++i) {
                const u32 id = min(128u * i + 64u * (pw & 1) + lane, 2367u), r0 = id / 74u;
                dof[i] = r0 * 1152u + min(id - 74u * r0, 71u) * 16u;
                asm volatile("" : "+v"(dof[i]));
            }
#define LD_WD(i) asm volatile("s_mov_b32 m0, %2\n\ts_nop 0\n\tglobal_load_lds_dwordx4 %0, %1" :: "v"(dof[i]), "s"(dsrc), "s"(ddst + (i) * 2048) : "memory")
            asm volatile("s_waitcnt vmcnt(12) lgkmcnt(0)\n\ts_barrier\n\ts_waitcnt vmcnt(6)\n\ts_barrier\n\ts_waitcnt vmcnt(%[pk])\n\t" POLL_S "s_waitcnt vmcnt(1)\n\ts_barrier" : "=&{v[10:11]}"(sel) : [so] "v"(so), [ws] "s"(p.ws), [pk] "n"(PK) : "memory");
            u32 c_, early, mw = ddst;
            asm volatile("s_mov_b32 %[e], 0\n\ts_waitcnt vmcnt(0)\n\tv_cmp_ne_u32 vcc, %[tag], v11\n\ts_cmp_eq_u64 vcc, 0\n\ts_cbranch_scc1 2f\n\t"
                         "s_mov_b32 %[e], 1\n\ts_mov_b32 %[c], 0x80000\n\ts_mov_b32 m0, %[m]\n\ts_add_u32 %[m], %[m], 0x800\n\tglobal_load_lds_dwordx4 %[d0], %[src]\n\ts_mov_b32 m0, %[m]\n\ts_add_u32 %[m], %[m], 0x800\n\tglobal_load_lds_dwordx4 %[d1], %[src]\n\ts_mov_b32 m0, %[m]\n\ts_add_u32 %[m], %[m], 0x800\n\tglobal_load_lds_dwordx4 %[d2], %[src]\n\ts_mov_b32 m0, %[m]\n\ts_add_u32 %[m], %[m], 0x800\n\tglobal_load_lds_dwordx4 %[d3], %[src]\n\ts_mov_b32 m0, %[m]\n\ts_add_u32 %[m], %[m], 0x800\n\tglobal_load_lds_dwordx4 %[d4], %[src]\n\ts_mov_b32 m0, %[m]\n\ts_add_u32 %[m], %[m], 0x800\n\tglobal_load_lds_dwordx4 %[d5], %[src]\n\t"
                         "1:\n\t" POLL_S "s_waitcnt vmcnt(0)\n\tv_cmp_ne_u32 vcc, %[tag], v11\n\ts_cmp_eq_u64 vcc, 0\n\ts_cbranch_scc1 2f\n\ts_sub_u32 %[c], %[c], 1\n\ts_cbranch_scc0 1b\n2:"
                         : "+{v[10:11]}"(sel), [c] "=&s"(c_), [e] "=&s"(early), [m] "+s"(mw) : [so] "v"(so), [ws] "s"(p.ws), [tag] "s"(tag), [src] "s"(dsrc), [d0] "v"(dof[0]), [d1] "v"(dof[1]), [d2] "v"(dof[2]), [d3] "v"(dof[3]), [d4] "v"(dof[4]), [d5] "v"(dof[5]) : "memory", "scc", "vcc");
            STAMP(3);
#pragma unroll
            for (int k = 0; k < 2; ++k) {
                const u32 t = ~(u32)__builtin_amdgcn_readlane((int)sel.x, k) & 2047u;
                const char* xr = (const char*)p.x + t * (D * 2);
                asm volatile("s_mov_b32 m0, %2\n\ts_nop 0\n\tglobal_load_lds_dwordx4 %0, %1\n\ts_mov_b64 exec, 0xff\n\tglobal_load_lds_dwordx4 %0, %1 offset:1024\n\ts_mov_b64 exec, -1"
                             :: "v"(l16), "s"(xr), "s"(FL_XS + (2 * pw + k) * XSP) : "memory");
            }
            asm volatile("s_cmp_eq_u32 %[e], 0\n\ts_cbranch_scc0 1f\n\ts_mov_b32 m0, %[m]\n\ts_add_u32 %[m], %[m], 0x800\n\tglobal_load_lds_dwordx4 %[d0], %[src]\n\ts_mov_b32 m0, %[m]\n\ts_add_u32 %[m], %[m], 0x800\n\tglobal_load_lds_dwordx4 %[d1], %[src]\n\ts_mov_b32 m0, %[m]\n\ts_add_u32 %[m], %[m], 0x800\n\tglobal_load_lds_dwordx4 %[d2], %[src]\n\ts_mov_b32 m0, %[m]\n\ts_add_u32 %[m], %[m], 0x800\n\tglobal_load_lds_dwordx4 %[d3], %[src]\n\ts_mov_b32 m0, %[m]\n\ts_add_u32 %[m], %[m], 0x800\n\tglobal_load_lds_dwordx4 %[d4], %[src]\n\ts_mov_b32 m0, %[m]\n\ts_add_u32 %[m], %[m], 0x800\n\tglobal_load_lds_dwordx4 %[d5], %[src]\n\t\ts_waitcnt vmcnt(6)\n\ts_barrier\n\ts_mov_b32 m0, %[m]\n\ts_add_u32 %[m], %[m], 0x800\n\tglobal_load_lds_dwordx4 %[d6], %[src]\n\ts_mov_b32 m0, %[m]\n\ts_add_u32 %[m], %[m], 0x800\n\tglobal_load_lds_dwordx4 %[d7], %[src]\n\ts_mov_b32 m0, %[m]\n\ts_add_u32 %[m], %[m], 0x800\n\tglobal_load_lds_dwordx4 %[d8], %[src]\n\ts_mov_b32 m0, %[m]\n\ts_add_u32 %[m], %[m], 0x800\n\tglobal_load_lds_dwordx4 %[d9], %[src]\n\ts_mov_b32 m0, %[m]\n\ts_add_u32 %[m], %[m], 0x800\n\tglobal_load_lds_dwordx4 %[d10], %[src]\n\ts_mov_b32 m0, %[m]\n\ts_add_u32 %[m], %[m], 0x800\n\tglobal_load_lds_dwordx4 %[d11], %[src]\n\ts_branch 2f\n1:\n\ts_mov_b32 m0, %[m]\n\ts_add_u32 %[m], %[m], 0x800\n\tglobal_load_lds_dwordx4 %[d6], %[src]\n\ts_mov_b32 m0, %[m]\n\ts_add_u32 %[m], %[m], 0x800\n\tglobal_load_lds_dwordx4 %[d7], %[src]\n\ts_mov_b32 m0, %[m]\n\ts_add_u32 %[m], %[m], 0x800\n\tglobal_load_lds_dwordx4 %[d8], %[src]\n\ts_mov_b32 m0, %[m]\n\ts_add_u32 %[m], %[m], 0x800\n\tglobal_load_lds_dwordx4 %[d9], %[src]\n\ts_mov_b32 m0, %[m]\n\ts_add_u32 %[m], %[m], 0x800\n\tglobal_load_lds_dwordx4 %[d10], %[src]\n\ts_mov_b32 m0, %[m]\n\ts_add_u32 %[m], %[m], 0x800\n\tglobal_load_lds_dwordx4 %[d11], %[src]\n\ts_waitcnt vmcnt(6)\n\ts_barrier\n2:"
                         : [m] "+s"(mw) : [e] "s"(early), [src] "s"(dsrc), [d0] "v"(dof[0]), [d1] "v"(dof[1]), [d2] "v"(dof[2]), [d3] "v"(dof[3]), [d4] "v"(dof[4]), [d5] "v"(dof[5]), [d6] "v"(dof[6]), [d7] "v"(dof[7]), [d8] "v"(dof[8]), [d9] "v"(dof[9]), [d10] "v"(dof[10]), [d11] "v"(dof[11]) : "memory", "scc");
            STAMP(10);
#pragma unroll
            for (int i = 12; i < 18; ++i) LD_WD(i);
            if (!(pw & 1)) LD_WD(18);
            asm volatile("s_mov_b64 exec, 1\n\tglobal_store_dwordx2 %0, %1, %2 sc1\n\ts_mov_b64 exec, -1\n\ts_waitcnt vmcnt(1)" :: "v"(OFF_CI + (128 + 4 * w + pw) * 8), "v"((v2u){tag, tag}), "s"(p.ws) : "memory");
            LDS_ADD1(ctl, one, 4);
            LSPIN(ctl, 4, 4);
            STAMP(13);
            s16x4 a[3][8];
            FFN_WDFRAG(0, 8 + pw); FFN_WDFRAG(1, 12 + pw); FFN_WDFRAG(2, 16 + (pw & 1));
            asm volatile("s_barrier" ::: "memory");
            FFN_HFRAG();
            v4f ya[3];
            FFN_DOWN(0, 8 + pw); FFN_DOWN(1, 12 + pw);
            if (pw < 2) { FFN_DOWN(2, 16 + pw); FFN_PUB(2, 16 + pw); }
            FFN_PUB(0, 8 + pw); FFN_PUB(1, 12 + pw);
            STAMP(9);
            ROLE_END();
        }
    } else
    {
        ROLE_BEGIN(5);
        ROLE_ARGS();
        if (XT) {
            const u32 id = ((slot - 28) + 4 * wave) * 64 + lane, dst = (u32)(uintptr_t)(smem + wave * 256), lo = (id & 1) ? 1024u : xcd * 128u;
            if (XT & 2) asm volatile("s_mov_b32 m0, %2\n\ts_nop 0\n\tglobal_load_lds_dword %0, %1" :: "v"((((id >> 3) & 255) * 4 + 2 + ((id >> 1) & 1)) * 1152u + lo), "s"(p.x), "s"(dst) : "memory");
            asm volatile("s_mov_b32 m0, %2\n\ts_nop 0\n\tglobal_load_lds_dword %0, %1" :: "v"((1024 + (id >> 1)) * 1152u + lo), "s"(p.x), "s"(dst) : "memory");
        }
        if (FDELAY) __builtin_amdgcn_s_sleep(FDELAY);
        _Pragma("unroll 1") for (int e = (slot - 28) + 4 * wave; touch(p, xcd, e, lane, (u32)(uintptr_t)(smem + wave * 256)); e += 32) {}
        STAMP(11);
        asm volatile("s_waitcnt vmcnt(0)" ::: "memory");
        STAMP(6);
        ROLE_END();
    }
    } else
        {
            ROLE_BEGIN(2);
            FFN_PRE();
            FFN_FILL();
            FFN_ADDR();
            LAS char* gp = smem + q * 2048 + ((lane >> 2) & 3) * 128 + ((2 * wave + ((lane >> 1) & 1)) ^ (((lane >> 2) & 2) | ((q & 1) << 2))) * 16 + (lane & 1) * 8;
            LAS char* xp = smem + FL_XS + (n & 7) * XSP + q * 16;
            const int hc = 16 * wave + n;
            LAS char* hw_ = smem + FL_H + (4 * (q & 1) + 2 * (q >> 1)) * 144 + ((hc & 35) | ((hc & 12) << 1) | ((hc & 16) >> 2)) * 2;
            asm volatile("" : "+v"(gp), "+v"(xp), "+v"(hw_));
            bf16x8 gf[18], uf[18];
#pragma unroll
            for (int sl = 0; sl < 3; ++sl) {
                if (sl == 0) asm volatile("s_waitcnt vmcnt(12)\n\ts_barrier" ::: "memory");
                if (sl == 1) asm volatile("s_waitcnt vmcnt(6)\n\ts_barrier" ::: "memory");
                if (sl == 2) asm volatile("s_waitcnt vmcnt(0) lgkmcnt(0)\n\ts_barrier" ::: "memory");
                if (sl == 0) STAMP(5);
                if (sl == 2) STAMP(6);
                LAS char* g_ = gp + sl * 49152;
                asm volatile("" : "+v"(g_));
#pragma unroll
                for (int i = 0; i < 6; ++i) {
                    gf[6 * sl + i] = __builtin_shufflevector(__builtin_amdgcn_ds_read_tr16_b64_v4i16((LAS s16x4*)(g_ + i * 8192)), __builtin_amdgcn_ds_read_tr16_b64_v4i16((LAS s16x4*)(g_ + i * 8192 + 512)), 0, 1, 2, 3, 4, 5, 6, 7);
                    uf[6 * sl + i] = __builtin_shufflevector(__builtin_amdgcn_ds_read_tr16_b64_v4i16((LAS s16x4*)(g_ + i * 8192 + 1024)), __builtin_amdgcn_ds_read_tr16_b64_v4i16((LAS s16x4*)(g_ + i * 8192 + 1536)), 0, 1, 2, 3, 4, 5, 6, 7);
                }
#pragma unroll
                for (int i = 0; i < 6; ++i) asm volatile("" : "+v"(gf[6 * sl + i]), "+v"(uf[6 * sl + i]));
            }
            u32 tag = *(LAS u32*)(smem + FL_TAG);
            asm volatile("s_barrier" : "+v"(tag) :: "memory");
            STAMP(1);
            bf16x8 xf[18];
            v4f ag = {0.f, 0.f, 0.f, 0.f}, au = ag;
#pragma unroll
            for (int s = 0; s < 4; ++s) xf[s] = *(LAS bf16x8*)(xp + s * 64);
            SBAR();
#pragma unroll
            for (int s = 0; s < 18; ++s) {
                ag = MF(xf[s], gf[s], ag);
                if (s + 4 < 18) xf[s + 4] = *(LAS bf16x8*)(xp + (s + 4) * 64);
                au = MF(xf[s], uf[s], au);
                SBAR();
            }
            STAMP(4);
            {
                const u32 ab = cvtpk(q < 2 ? ag[0] : ag[2], q < 2 ? ag[1] : ag[3]), ub = cvtpk(q < 2 ? au[0] : au[2], q < 2 ? au[1] : au[3]);
                const u32 sl = cvtpk(bflo(ab) * sigmoidf_(bflo(ab)), bfhi(ab) * sigmoidf_(bfhi(ab)));
                const u32 hh = cvtpk(bflo(sl) * bflo(ub), bfhi(sl) * bfhi(ub));
                *(LAS u16*)(hw_) = (u16)hh; *(LAS u16*)(hw_ + 144) = (u16)(hh >> 16);
            }
            STAMP(8);
            LSPIN(ctl, 4, 4);
            s16x4 a[2][8];
            FFN_WDFRAG(0, 2 * wave); FFN_WDFRAG(1, 2 * wave + 1);
            asm volatile("s_barrier" ::: "memory");
            STAMP(13);
            FFN_HFRAG();
            v4f ya[2];
            FFN_DOWN(0, 2 * wave); FFN_DOWN(1, 2 * wave + 1);
            FFN_PUB(0, 2 * wave); FFN_PUB(1, 2 * wave + 1);
            STAMP(9);
            ROLE_END();
        }
    } else {
    if (wave >= 4)
    {
        ROLE_BEGIN(1);
        const int hw = wave - 4, ht = tid - 256;
        const v4u z4 = {0, 0, 0, 0};
        const int sub = lane & 15;
        v4u xr[5];
        u32 tagv;
        const int rrow = 16 * (cw & 127) + 4 * hw;
        u32 dst = (u32)(uintptr_t)(smem + LDS_RW) + hw * 3072, l16 = lane * 16;
        u32 ro = (u32)(((rrow + (lane >> 4)) & ~((127 - cw) >> 31)) * D + sub * 8) * 2, ro4 = ro - (sub & 8) * 16;
        u32 uo0 = (u32)((256 * hw + 4 * lane) * D + chbase) * 2;
        asm volatile("" : "+s"(dst), "+v"(l16), "+v"(ro), "+v"(ro4), "+v"(uo0));
        ROLE_ARGS();
        {
            asm volatile("s_mov_b32 m0, %5\n\tv_mov_b32 %0, 0\n\tglobal_load_dword %0, %0, %2 sc1\n\tglobal_load_lds_dwordx4 %3, %4\n\ts_mov_b64 exec, 0xff\n\tglobal_load_lds_dwordx4 %3, %4 offset:1024\n\t"
                         "s_add_u32 m0, m0, 1280\n\ts_mov_b64 exec, -1\n\tglobal_load_lds_dwordx4 %3, %6\n\ts_mov_b64 exec, 0xff\n\tglobal_load_lds_dwordx4 %3, %6 offset:1024\n\ts_mov_b64 exec, -1"
                         : "=&v"(tagv) : "n"(0), "s"(p.ws), "v"(l16), "s"(p.wr), "s"(dst), "s"(p.gw) : "memory", "scc");
#pragma unroll
            for (int j = 0; j < 4; ++j) asm volatile("global_load_dwordx4 %0, %1, %2 offset:%3" : "=v"(xr[j]) : "v"(ro), "s"(p.x), "n"(j * 256));
            asm volatile("global_load_dwordx4 %0, %1, %2 offset:1024" : "=v"(xr[4]) : "v"(ro4), "s"(p.x));
        }
        __builtin_amdgcn_s_barrier();
        STAMP(1);
        if (XPF) {
            const int id = min((slot * 4 + hw) * 64 + lane, 4095);
            u32 keep;
            asm volatile("s_mov_b32 %0, m0\n\ts_mov_b32 m0, %3\n\ts_nop 0\n\tglobal_load_lds_dword %1, %2\n\ts_mov_b32 m0, %0" : "=&s"(keep) : "v"((u32)((id >> 1) * 1152 + ((id & 1) ? 1024 : xcd * 128))), "s"(p.x), "s"((u32)(uintptr_t)(smem + LDS_DUMMY)) : "memory");
        }
        v2u uu[8];
        const u32 uo1 = uo0 + 1024 * D * 2;
#define LD_U(i) asm volatile("global_load_dwordx2 %0, %1, %2 offset:%3" : "=v"(uu[i]) : "v"((i) < 4 ? uo0 : uo1), "s"(p.x), "n"(((i) & 3) * D * 2))
#pragma unroll
        for (int i = 0; i < NB1; ++i) LD_U(i);
        *(LAS v4u*)(smem + LDS_TAP + ht * 16) = z4; *(LAS v4u*)(smem + LDS_TAP + 4096 + ht * 16) = z4;
        *(LAS v4u*)(smem + LDS_TAP + 8192 + ht * 16 + (ht >> 7) * (LDS_U - LDS_TAP - 8192 + ((ht >> 5) & 3) * 4608 + (ht & 31) * 16 - ht * 16)) = z4;
        u32 ctl = (u32)(uintptr_t)(smem + LDS_CTL), one = 1u, cty = ctl + 64 * hw + (lane & 3) * 16;
        const u32 late = (u32)((127 - cw) >> 31), pa_ = OFF_RG + hline(p.ws, cw & 127) + hw * 32, pk_ = pa_ ^ ((pa_ ^ (OFF_CI + ((cw - 128) * 4 + hw) * 16)) & late);
        u32 po = pk_ + (lane >> 1) + (sub != 0) * (OFF_G + hline(p.ws, ((lane - 1) & 3) * 64 + ((cw & 127) >> 1)) + ((4 * cw + hw) & 7) * 16 - pk_ - (lane >> 1));
        const u64 pmask = 1ull | ((u64)(~late & 1u) << 32) | (~late & ((1u << (1 + REPG)) - 1));
        u32 nrow = 0xffffu - (rrow + (lane >> 4));
        u32 go = OFF_G + hline(p.ws, (cw % REPG) * 64 + 8 * hw + (lane >> 3)) + (lane & 7) * 16, go2 = OFF_G + hline(p.ws, (cw % REPG) * 64 + 32 + 8 * hw + (lane >> 3)) + (lane & 7) * 16;
        LAS char* up = smem + LDS_U + (256 + 256 * hw + 4 * lane) * 2;
        LAS char* yp = smem + CH_Y + hw * 1152 + (lane + (lane >> 3)) * 16;
        LAS char* rw = smem + LDS_RW + hw * 3072 + sub * 16;
        LAS char* rw4 = rw - (sub & 8) * 16;
        asm volatile("" : "+v"(ctl), "+v"(one), "+v"(cty), "+v"(po), "+v"(go), "+v"(go2), "+v"(up), "+v"(yp), "+v"(rw), "+v"(rw4), "+v"(nrow));
        u32 wt[3];
#pragma unroll
        for (int k = WPF; k < 3; ++k) {
            wt[k] = wd_line(xcd, (36 * k + slot * 2 + (hw & 1)) * 48 + min(lane, 47));
            asm volatile("" : "+v"(wt[k]));
        }
        BAR();
        __builtin_amdgcn_s_setprio(3);
        u32 tag;
        {
            asm volatile("s_waitcnt vmcnt(%1)" : "+v"(tagv) : "n"(NB1 + XPF + 5) : "memory");
            tag = __builtin_amdgcn_readfirstlane(tagv) + 1u;
            v4u wa[5], wg_[5];
#pragma unroll
            for (int j = 0; j < 5; ++j) { wa[j] = *(LAS v4u*)((j < 4 ? rw : rw4) + j * 256); wg_[j] = *(LAS v4u*)((j < 4 ? rw : rw4) + 1280 + j * 256); }
#pragma unroll
            for (int j = 0; j < 5; ++j) asm volatile("" : "+v"(wa[j]), "+v"(wg_[j]));
            asm volatile("s_waitcnt vmcnt(%5)" : "+v"(xr[0]), "+v"(xr[1]), "+v"(xr[2]), "+v"(xr[3]), "+v"(xr[4]) : "n"(NB1 + XPF) : "memory");
            STAMP(2);
            if (sub >= 8) xr[4] = z4;
            float r = 0.f, s = 0.f;
#pragma unroll
            for (int j = 0; j < 5; ++j) {
                const v4u a = wa[j], g = wg_[j];
#pragma unroll
                for (int e = 0; e < 4; ++e) {
                    const u32 xe = xr[j][e], ae = a[e], ge = g[e];
                    r = __builtin_amdgcn_fdot2_f32_bf16(__builtin_bit_cast(bf16x2_t, xe), __builtin_bit_cast(bf16x2_t, ae), r, false);
                    s = __builtin_amdgcn_fdot2_f32_bf16(__builtin_bit_cast(bf16x2_t, xe), __builtin_bit_cast(bf16x2_t, ge), s, false);
                }
            }
            r = dpp_add<0x128, 0xf>(r); s = dpp_add<0x128, 0xf>(s); r = dpp_add<0x124, 0xf>(r); s = dpp_add<0x124, 0xf>(s);
            r = dpp_add<0x122, 0xf>(r); s = dpp_add<0x122, 0xf>(s); r = dpp_add<0x121, 0xf>(r); s = dpp_add<0x121, 0xf>(s);
            const u32 pay = make_payload(r, sigmoidf_(rbf(s))), kk = (pay & 0xffff0000u) | nrow;
            const u32 p0 = (u32)__builtin_amdgcn_readlane((int)pay, 0), p1 = (u32)__builtin_amdgcn_readlane((int)pay, 16), p2 = (u32)__builtin_amdgcn_readlane((int)pay, 32), p3 = (u32)__builtin_amdgcn_readlane((int)pay, 48);
            const auto sw = __builtin_amdgcn_permlane16_swap(kk, kk, false, false);
            const u32 g01 = (p0 & 0xffffu) | (p1 << 16), g23 = (p2 & 0xffffu) | (p3 << 16);
            const v4u pv = {sub ? g01 : sw[0], tag, sub ? g23 : sw[1], tag};
            asm volatile("s_mov_b64 exec, %3\n\tglobal_store_dwordx4 %0, %1, %2 sc1\n\ts_mov_b64 exec, -1\n\ts_nop 0" :: "v"(po), "v"(pv), "s"(p.ws), "s"(pmask) : "memory");
        }
        STAMP(3);
        __builtin_amdgcn_s_setprio(HPRIO);
        *(LAS u32*)(smem + LDS_CTL + C_TAG) = tag;
        LDS_ADD1(ctl, one, C_PUB);
        LWAIT(ctl, C_PUB, 4);
#pragma unroll
        for (int i = NB1; i < 4; ++i) LD_U(i);
        LDS_ADD1(ctl, one, C_PUB);
        LWAIT(ctl, C_PUB, 8);
        v4u gq[2];
#define POLL_G "global_load_dwordx4 v[100:103], %[go], %[ws] sc1\n\tglobal_load_dwordx4 v[104:107], %[go2], %[ws] sc1\n\t"
#define LD_U4 "global_load_dwordx2 %[u4], %[uo], %[x]\n\tglobal_load_dwordx2 %[u5], %[uo], %[x] offset:1152\n\tglobal_load_dwordx2 %[u6], %[uo], %[x] offset:2304\n\tglobal_load_dwordx2 %[u7], %[uo], %[x] offset:3456\n\t"
        asm volatile("" : "=&{v[100:103]}"(gq[0]), "=&{v[104:107]}"(gq[1]), "=&v"(uu[4]), "=&v"(uu[5]), "=&v"(uu[6]), "=&v"(uu[7]));
        asm volatile("s_cmp_ge_u32 %[hw], 2\n\ts_cbranch_scc1 1f\n\ts_sleep %[ud]\n\t" LD_U4 POLL_G "s_waitcnt vmcnt(6)\n\ts_branch 2f\n1:\n\ts_waitcnt vmcnt(0)\n2:"
                     : "+{v[100:103]}"(gq[0]), "+{v[104:107]}"(gq[1]), [u4] "+v"(uu[4]), [u5] "+v"(uu[5]), [u6] "+v"(uu[6]), [u7] "+v"(uu[7]), "+v"(uu[0]), "+v"(uu[1]), "+v"(uu[2]), "+v"(uu[3])
                     : [ud] "n"(UDELAY), [go] "v"(go), [go2] "v"(go2), [ws] "s"(p.ws), [uo] "v"(uo1), [x] "s"(p.x), [hw] "s"(hw) : "memory", "scc");
        STAMP(4);
        u32 ct2 = ctl + (hw & 2) * 2;
        auto stage = [&](int g) __attribute__((always_inline)) {
            const v2u* u4 = uu + 4 * g;
            LAS char* d = up + g * 2048;
            *(LAS v2u*)(d) = (v2u){__builtin_amdgcn_perm(u4[1].x, u4[0].x, 0x05040100u), __builtin_amdgcn_perm(u4[3].x, u4[2].x, 0x05040100u)};
            *(LAS v2u*)(d + 4608) = (v2u){__builtin_amdgcn_perm(u4[1].x, u4[0].x, 0x07060302u), __builtin_amdgcn_perm(u4[3].x, u4[2].x, 0x07060302u)};
            *(LAS v2u*)(d + 2 * 4608) = (v2u){__builtin_amdgcn_perm(u4[1].y, u4[0].y, 0x05040100u), __builtin_amdgcn_perm(u4[3].y, u4[2].y, 0x05040100u)};
            *(LAS v2u*)(d + 3 * 4608) = (v2u){__builtin_amdgcn_perm(u4[1].y, u4[0].y, 0x07060302u), __builtin_amdgcn_perm(u4[3].y, u4[2].y, 0x07060302u)};
            if (g) LDS_ADD1(ct2, one, C_ST2); else LDS_ADD1(ctl, one, C_STG);
        };
        stage(0);
        STAMP(5);
        asm volatile("s_cmp_lt_u32 %[hw], 2\n\ts_cbranch_scc1 1f\n\t" POLL_G LD_U4 "\n1:"
                     : "+{v[100:103]}"(gq[0]), "+{v[104:107]}"(gq[1]), [u4] "+v"(uu[4]), [u5] "+v"(uu[5]), [u6] "+v"(uu[6]), [u7] "+v"(uu[7])
                     : [go] "v"(go), [go2] "v"(go2), [ws] "s"(p.ws), [uo] "v"(uo1), [x] "s"(p.x), [hw] "s"(hw) : "memory", "scc");
        if (hw < 2) {
            asm volatile("s_waitcnt vmcnt(2)" : "+v"(uu[4]), "+v"(uu[5]), "+v"(uu[6]), "+v"(uu[7]));
            stage(1);
            STAMP(6);
        }
        {
            u32 c_;
            asm volatile("s_mov_b32 %[c], 0x80000\n\ts_waitcnt vmcnt(4)\n\ts_cmp_ge_u32 %[hw], 2\n\ts_cbranch_scc1 1f\n\ts_waitcnt vmcnt(0)\n1:\n\tv_cmpx_eq_u32 %[tag], v101\n\tv_cmpx_eq_u32 %[tag], v103\n\tv_cmpx_eq_u32 %[tag], v105\n\tv_cmpx_eq_u32 %[tag], v107\n\t"
                         "s_cmp_eq_u64 exec, -1\n\ts_mov_b64 exec, -1\n\ts_cbranch_scc1 2f\n\ts_sleep 4\n\t" POLL_G "s_waitcnt vmcnt(0)\n\ts_sub_u32 %[c], %[c], 1\n\ts_cbranch_scc0 1b\n2:\n\ts_nop 4"
                         : "+{v[100:103]}"(gq[0]), "+{v[104:107]}"(gq[1]), [c] "=&s"(c_) : [go] "v"(go), [go2] "v"(go2), [ws] "s"(p.ws), [tag] "s"(tag), [hw] "s"(hw) : "memory", "scc", "vcc");
        }
        STAMP(7);
        *(LAS v2u*)(smem + LDS_G + hw * 3072 + lane * 8) = (v2u){gq[0].x, gq[0].z}; *(LAS v2u*)(smem + LDS_G + hw * 3072 + 512 + lane * 8) = (v2u){gq[1].x, gq[1].z};
        if (hw < 2) {
#pragma unroll
            for (int k = WPF; k < 3; ++k) asm volatile("s_mov_b32 m0, %2\n\ts_nop 0\n\tglobal_load_lds_dword %0, %1" :: "v"(wt[k]), "s"(p.wd), "n"(LDS_DUMMY) : "memory");
        }
#pragma unroll
        for (int g = 0; g < 2; ++g) {
            if (g == 1 && hw >= 2) {
                asm volatile("s_waitcnt vmcnt(4)" : "+v"(uu[4]), "+v"(uu[5]), "+v"(uu[6]), "+v"(uu[7]));
                stage(1);
                STAMP(6);
            }
            const v2u* u4 = uu + 4 * g;
            const float gr[4] = {bflo(gq[g].x), bfhi(gq[g].x), bflo(gq[g].z), bfhi(gq[g].z)};
            float o[4][4];
#pragma unroll
            for (int r = 0; r < 4; ++r) {
                const float xv[4] = {bflo(u4[r].x), bfhi(u4[r].x), bflo(u4[r].y), bfhi(u4[r].y)};
#pragma unroll
                for (int c = 0; c < 4; ++c) asm("v_fma_f32 %0, -%1, %2, %2" : "=v"(o[r][c]) : "v"(gr[r]), "v"(xv[c]));
                asm volatile("" : "+v"(o[r][0]), "+v"(o[r][1]), "+v"(o[r][2]), "+v"(o[r][3]));
            }
            {
                u32 t_, c_;
                asm volatile("s_mov_b32 %1, 0x400000\n1:\n\tds_read_b32 %0, %2 offset:%3\n\ts_waitcnt lgkmcnt(0)\n\tv_cmp_eq_u32 vcc, 0, %0\n\ts_cmp_eq_u64 vcc, 0\n\ts_cbranch_scc1 2f\n\ts_sleep %4\n\ts_sub_u32 %1, %1, 1\n\ts_cbranch_scc0 1b\n2:"
                             : "=&v"(t_), "=&s"(c_) : "v"(cty), "n"(C_Y + 256 * g), "n"(YSLEEP) : "memory", "scc", "vcc");
            }
            if (g == 0) STAMP(11);
            v4f y[4];
#pragma unroll
            for (int c = 0; c < 4; ++c) y[c] = *(LAS v4f*)(yp + c * KCH + g * 4608);
            SBAR();
#define ST_O(i, v) asm volatile("global_store_dwordx2 %0, %1, %2 offset:%3" :: "v"(g ? uo1 : uo0), "v"(v), "s"(p.out), "n"((i) * D * 2) : "memory")
#pragma unroll
            for (int r = 0; r < 4; ++r) {
#pragma unroll
                for (int c = 0; c < 4; ++c) { const float yv = y[c][r]; asm("v_fmac_f32 %0, %1, %2" : "+v"(o[r][c]) : "v"(gr[r]), "v"(yv)); }
                ST_O(r, ((v2u){cvtpk(o[r][0], o[r][1]), cvtpk(o[r][2], o[r][3])}));
            }
            STAMP(8 + g);
        }
        asm volatile("s_waitcnt vmcnt(0)" ::: "memory");
        LDS_ADD1(ctl, one, C_ACK);
        STAMP(10);
        ROLE_END();
    }
    else
    {
        ROLE_BEGIN(0);
        const int ch = wave;
        u32 pi = ((chbase + ch) * NST + lane * 2) * 2, dl = (chbase + ch) * 2;
        asm volatile("" : "+v"(pi), "+v"(dl));
        ROLE_ARGS();
#define GLD(T, ptr, idx) __builtin_nontemporal_load((const GAS T*)((const GAS char*)(ptr) + (idx)))
        const u32 are = GLD(u32, p.Are, pi), aim = GLD(u32, p.Aim, pi), bre = GLD(u32, p.Bre, pi), bim = GLD(u32, p.Bim, pi), cre = GLD(u32, p.Cre, pi), cim = GLD(u32, p.Cim, pi);
        const u16 ldt = *(const GAS u16*)((const GAS char*)p.logdt + dl), dsk = *(const GAS u16*)((const GAS char*)p.dskip + dl);
        SBAR();
        __builtin_amdgcn_s_barrier();
        SBAR();
        LAS char* kb = smem + ch * KCH;
        const int r32 = lane & 31, h = lane >> 5, q = lane >> 4, n = lane & 15;
        const int ai = 4 * (r32 >> 3) + (r32 & 3), hh = (r32 >> 2) & 1;
        const u64 tsel = ai < 8 ? (hh ? KROW_1LO : KROW_0LO) : (hh ? KROW_1HI : KROW_0HI);
        const int o = (int)((tsel >> (8 * (ai & 7))) & 0xff);
        LAS char* ap = kb + o * STG_ROW + h * 16;
        LAS char* bp = kb + r32 * STG_ROW + h * 16;
        u32 sg[14];
#pragma unroll
        for (int k = 0; k < 14; ++k) { sg[k] = (u32)(uintptr_t)(kb + lane * 8 + k * 4 * STG_ROW); asm volatile("" : "+v"(sg[k])); }
        bf16x8 b5 = band(((0x1fu << 6) << n) >> (8 * q), 0x3f803f80u), b3 = band(((0x7u << 11) << n) >> (8 * q), 0x3f803f80u);
        bf16x8 b9 = band(((0x1ffu << 4) << n) >> (8 * q), 0x3f803f80u), b7 = band(((0x7fu << 5) << n) >> (8 * q), 0x3f803f80u);
        bf16x8 pat = band(((0x3u << 12) << n) >> (8 * q), 0xffffffffu);
        LAS char* k0 = kb + 32 + lane * 16;
        LAS char* kt = kb + (lane & 3) * KCOPY + (lane < 16 ? 32 + 1024 + (lane >> 2) * 16 : ((lane >> 2) & 1) * 16);
        LAS char* a0 = kb + ((lane >> 2) & 3) * KCOPY + (28 - 8 * q + 4 * (lane & 3)) * 2;
        LAS char* bb = smem + LDS_U + ch * 4608 + (16 * n + 8 * q - 16) * 2;
        LAS char* yb = kb + CH_Y + (4 * n + q + (n >> 1)) * 16;
        LAS char* tp = smem + LDS_TAP + ch * 2304 + ((n & 1) ? 13 + n - 4 * q : 96 + 14 + n - 4 * q) * 4;
        LAS char* ta = smem + LDS_TAP + ch * 2304 + (h ? 0 : 768) + 64 + 2 * r32;
        LAS char* tb = smem + LDS_TAP + ch * 2304 + 1600 + 64 * h + 2 * r32;
        u32 ctl = (u32)(uintptr_t)(smem + LDS_CTL), ctc = ctl + C_Y + ch * 16 + (lane & 15), one = 1u, so = OFF_SEL + (8 + xcd) * 256 + (lane & 7) * 8;
        u32 m01 = lane < 2 ? ~0u : 0u, m2 = lane == 2 ? ~0u : 0u, m3 = lane == 3 ? ~0u : 0u, m0_ = lane == 0 ? ~0u : 0u;
        asm volatile("" : "+v"(b5), "+v"(b3), "+v"(b9), "+v"(b7), "+v"(pat), "+v"(ap), "+v"(bp), "+v"(m01), "+v"(m2), "+v"(m3), "+v"(m0_));
        asm volatile("" : "+v"(k0), "+v"(kt), "+v"(a0), "+v"(bb), "+v"(yb), "+v"(tp), "+v"(ta), "+v"(tb), "+v"(ctl), "+v"(ctc), "+v"(one), "+v"(so));
        const char* hint = p.ws + (OFF_PART + cw * PP + hline(p.ws, cw >> 1));
        asm volatile("" : "+s"(hint));
        __builtin_amdgcn_s_barrier();
        SBAR();
        const float dt = rbf(expf(bf2f(ldt)));
        STAMP(1);
        v16f acc;
        u32 stg;
        float kscale;
        {
            const v2f a2 = (v2f){bflo(are), bfhi(are)} * dt * 1.44269504088896341f;
            const v2f brev = (v2f){bflo(aim), bfhi(aim)} * dt * 0.15915494309189535f;
            const cf2 Bc = {{dt * bflo(bre), dt * bflo(bim)}, {dt * bfhi(bre), dt * bfhi(bim)}};
            const cf2 Cc = {{bflo(cre), bflo(cim)}, {bfhi(cre), bfhi(cim)}};
            cf2 W = cmul2(Cc, Bc);
            u32 mx = max(max(__float_as_uint(W.a.x) & 0x7f800000u, __float_as_uint(W.a.y) & 0x7f800000u), max(__float_as_uint(W.b.x) & 0x7f800000u, __float_as_uint(W.b.y) & 0x7f800000u));
            mx = dpp_umax<0x111, 0xf>(mx); mx = dpp_umax<0x112, 0xf>(mx); mx = dpp_umax<0x114, 0xf>(mx); mx = dpp_umax<0x118, 0xf>(mx);
            mx = dpp_umax<0x142, 0xa>(mx); mx = dpp_umax<0x143, 0xc>(mx);
            mx = min(max((u32)__builtin_amdgcn_readlane((int)mx, 63), 0x00800000u), 0x7e800000u);
            kscale = __uint_as_float(mx);
            const float inv = __uint_as_float(0x7f000000u - mx);
            W.a *= inv; W.b *= inv;
            const cf2 E1 = cexp2(a2, brev, 1.f), E8 = cexp2(a2, brev, 8.f), E256 = cexp2(a2, brev, 256.f);
            asm volatile("s_nop 0\n\t" KGEN_ASM :: "{v[48:49]}"(W.a), "{v[50:51]}"(W.b), "{v[52:53]}"(E1.a), "{v[54:55]}"(E1.b), "{v[56:57]}"(E8.a), "{v[58:59]}"(E8.b), "{v[60:61]}"(E256.a), "{v[62:63]}"(E256.b),
                         [b0] "v"(sg[0]), [b1] "v"(sg[1]), [b2] "v"(sg[2]), [b3] "v"(sg[3]), [b4] "v"(sg[4]), [b5] "v"(sg[5]), [b6] "v"(sg[6]), [b7] "v"(sg[7]), [b8] "v"(sg[8]), [b9] "v"(sg[9]),
                         [b10] "v"(sg[10]), [b11] "v"(sg[11]), [b12] "v"(sg[12]), [b13] "v"(sg[13]) : "memory", KGEN_CLOBBER);
            STAMP(2);
            asm volatile(KMFMA_ASM : "=&{v[48:63]}"(acc), [stg] "=&v"(stg) : [ap] "v"(ap), [bp] "v"(bp), [ctl] "v"(ctl) : "memory", KMFMA_CLOBBER);
            acc *= kscale;
        }
        STAMP(3);
        acc[0] += __uint_as_float(((u32)dsk << 16) & m0_);
        u32 t78 = cvtpk(acc[7], acc[8]), t9a = cvtpk(acc[9], acc[10]);
        {
            const auto s0 = __builtin_amdgcn_permlane32_swap(cvtpk(acc[0], acc[1]), cvtpk(acc[4], acc[4]), false, false);
            const auto s1 = __builtin_amdgcn_permlane32_swap(cvtpk(acc[2], acc[3]), cvtpk(acc[5], acc[6]), false, false);
            const v4u ww = {s0[0], s1[0], s0[1], s1[1]};
            const u32 pz = (u32)__builtin_amdgcn_update_dpp(0, (int)ww.z, 0x138, 0xf, 0xf, true), pw = (u32)__builtin_amdgcn_update_dpp(0, (int)ww.w, 0x138, 0xf, 0xf, true);
            const v4u c1 = {__builtin_amdgcn_alignbit(ww.x, pw, 16), __builtin_amdgcn_alignbit(ww.y, ww.x, 16), __builtin_amdgcn_alignbit(ww.z, ww.y, 16), __builtin_amdgcn_alignbit(ww.w, ww.z, 16)};
            *(LAS v4u*)(k0) = ww;
            *(LAS v4u*)(k0 + KCOPY) = c1;
            *(LAS v4u*)(k0 + 2 * KCOPY) = (v4u){pw, ww.x, ww.y, ww.z};
            *(LAS v4u*)(k0 + 3 * KCOPY) = (v4u){__builtin_amdgcn_alignbit(pw, pz, 16), c1.x, c1.y, c1.z};
            const u32 w0 = (u32)__builtin_amdgcn_readlane((int)ww.z, 63), w1 = (u32)__builtin_amdgcn_readlane((int)ww.w, 63);
            const u32 w3 = w1 >> 16, w2 = w3 * 0x10001u, w10 = (w0 >> 16) | (w1 << 16);
            *(LAS v4u*)kt = (v4u){(w2 & m01) | (w1 & m2) | (w10 & m3), (w3 & m0_) | (w2 & ((m01 ^ m0_) | m2 | m3)), (w3 & m2) | (w2 & m3), 0u};
        }
        asm volatile("" ::: "memory");
        pat = __builtin_bit_cast(bf16x8, __builtin_bit_cast(v4u, pat) & (((u32)__builtin_amdgcn_readlane((int)t78, 63) >> 16) * 0x10001u));
#define POLL_SEL "global_load_dwordx2 v[240:241], %[so], %[ws] sc1\n\t"
        v2u sq;
        const v4f zero4 = {0.f, 0.f, 0.f, 0.f};
        if (MPRIO) __builtin_amdgcn_s_setprio(MPRIO);
        STAMP(4);
        {
            u32 s_, c_;
            asm volatile(CONV_ASM : "=&{v[240:241]}"(sq), [s] "=&s"(s_), [c] "=&s"(c_)
                         : "{v[4:7]}"(b5), "{v[8:11]}"(b3), "{v[12:15]}"(b9), "{v[16:19]}"(b7), "{v[20:23]}"(pat), [a0] "v"(a0), [bb] "v"(bb), [yb] "v"(yb), [tp] "v"(tp), [ta] "v"(ta), [tb] "v"(tb),
                           [ctl] "v"(ctl), [ctc] "v"(ctc), [one] "v"(one), [so] "v"(so), [t78] "v"(t78), [t9a] "v"(t9a), [stg] "v"(stg), [ws] "s"(p.ws) : "memory", "scc", CONV_CLOBBER);
        }
        STAMP(9);
        asm volatile("s_cmp_eq_u32 %0, 0\n\ts_cbranch_scc1 1f\n\ts_endpgm\n1:" :: "s"(wave) : "memory", "scc");
        {
            u32 h_;
            asm volatile("s_load_dword %0, %1, 0x4 glc" : "=&s"(h_) : "s"(hint) : "memory");
            const u32 tag = __builtin_amdgcn_readfirstlane(*(LAS u32*)(smem + LDS_CTL + C_TAG));
            u32 c_;
            u32 pb[9];
#pragma unroll
            for (int k = 0; k < 9; ++k) { pb[k] = OFF_PART + cw * PP + hline(p.ws, 8 * k + (lane >> 3)) + (lane & 7) * 16; asm volatile("" : "+v"(pb[k])); }
            {
                asm volatile("s_mov_b32 %[c], 64\n1:\n\ts_waitcnt lgkmcnt(0)\n\ts_cmp_eq_u32 %[h], %[tag]\n\ts_cbranch_scc1 2f\n\ts_load_dword %[h], %[a], 0x4 glc\n\ts_sub_u32 %[c], %[c], 1\n\ts_cbranch_scc0 1b\n\ts_waitcnt lgkmcnt(0)\n2:"
                             : [h] "+s"(h_), [c] "=&s"(c_) : [a] "s"(hint), [tag] "s"(tag) : "memory", "scc");
            }
            STAMP(10);
            typedef __attribute__((ext_vector_type(32))) u32 v32u;
            v32u ra, rb; v4u rc, rd;
            asm volatile("global_load_dwordx4 v[64:67], %[pb0], %[ws] sc1\n\tglobal_load_dwordx4 v[68:71], %[pb1], %[ws] sc1\n\tglobal_load_dwordx4 v[72:75], %[pb2], %[ws] sc1\n\tglobal_load_dwordx4 v[76:79], %[pb3], %[ws] sc1\n\tglobal_load_dwordx4 v[80:83], %[pb4], %[ws] sc1\n\tglobal_load_dwordx4 v[84:87], %[pb5], %[ws] sc1\n\tglobal_load_dwordx4 v[88:91], %[pb6], %[ws] sc1\n\tglobal_load_dwordx4 v[92:95], %[pb7], %[ws] sc1\n\tglobal_load_dwordx4 v[96:99], %[pb8], %[ws] sc1\n\t" : "=&{v[64:95]}"(ra), "=&{v[96:99]}"(rc) : [pb0] "v"(pb[0]), [pb1] "v"(pb[1]), [pb2] "v"(pb[2]), [pb3] "v"(pb[3]), [pb4] "v"(pb[4]), [pb5] "v"(pb[5]), [pb6] "v"(pb[6]), [pb7] "v"(pb[7]), [pb8] "v"(pb[8]), [ws] "s"(p.ws) : "memory");
            asm volatile("s_mov_b32 %[c], 0x80000\n\ts_waitcnt vmcnt(9)\n1:\n\tv_cmpx_eq_u32 %[tag], v241\n\t"
                         "s_cmp_eq_u64 exec, -1\n\ts_mov_b64 exec, -1\n\ts_cbranch_scc1 2f\n\t" POLL_SEL "s_waitcnt vmcnt(0)\n\ts_sub_u32 %[c], %[c], 1\n\ts_cbranch_scc0 1b\n2:\n\ts_nop 4"
                         : "+{v[240:241]}"(sq), [c] "=&s"(c_) : [so] "v"(so), [ws] "s"(p.ws), [tag] "s"(tag) : "memory", "scc", "vcc");
            const int t = (int)(0xffffu - (sq.x & 0xffffu)) & 2047;
            const float rs = rbf(sigmoidf_(key_to_r(sq.x)));
            LWAIT(ctl, C_ACK, 4);
            const float g = bf2f(*(LAS u16*)(smem + LDS_G + ((t >> 8) & 3) * 3072 + (t >> 10) * 512 + (t & 255) * 2));
            const int uq = (t & 255) >> 2;
            float xv[4], av[4];
#pragma unroll
            for (int c = 0; c < 4; ++c) {
                xv[c] = bf2f(*(LAS u16*)(smem + LDS_U + c * 4608 + (256 + t) * 2));
                av[c] = g * *(LAS float*)(smem + c * KCH + CH_Y + (t >> 8) * 1152 + (uq + (uq >> 3)) * 16 + (t & 3) * 4);
            }
            const u32 e1 = n < 4 ? ((n & 1) ? 0x3f800000u : 0x00003f80u) : 0u;
            bf16x8 sel4 = __builtin_bit_cast(bf16x8, (n & 2) ? (v4u){0u, 0u, e1, e1} : (v4u){e1, e1, 0u, 0u});
            u32 oo = (u32)(t * D + chbase) * 2;
            float omg = 1.f - g;
            asm volatile("" : "+v"(sel4), "+v"(oo), "+v"(omg), "+v"(xv[0]), "+v"(xv[1]), "+v"(xv[2]), "+v"(xv[3]), "+v"(av[0]), "+v"(av[1]), "+v"(av[2]), "+v"(av[3]));
            v4f s0, s1;
            asm volatile("s_mov_b32 %[c], 0x40000\n\tglobal_load_dwordx4 v[100:103], %[pb0], %[ws] sc1\n\tglobal_load_dwordx4 v[104:107], %[pb1], %[ws] sc1\n\tglobal_load_dwordx4 v[108:111], %[pb2], %[ws] sc1\n\tglobal_load_dwordx4 v[112:115], %[pb3], %[ws] sc1\n\tglobal_load_dwordx4 v[116:119], %[pb4], %[ws] sc1\n\tglobal_load_dwordx4 v[120:123], %[pb5], %[ws] sc1\n\tglobal_load_dwordx4 v[124:127], %[pb6], %[ws] sc1\n\tglobal_load_dwordx4 v[128:131], %[pb7], %[ws] sc1\n\tglobal_load_dwordx4 v[132:135], %[pb8], %[ws] sc1\n\t"
                         "1:\n\ts_waitcnt vmcnt(9)\n\tv_cmpx_eq_u32 %[tag], v65\n\tv_cmpx_eq_u32 %[tag], v67\n\tv_cmpx_eq_u32 %[tag], v69\n\tv_cmpx_eq_u32 %[tag], v71\n\tv_cmpx_eq_u32 %[tag], v73\n\tv_cmpx_eq_u32 %[tag], v75\n\tv_cmpx_eq_u32 %[tag], v77\n\tv_cmpx_eq_u32 %[tag], v79\n\tv_cmpx_eq_u32 %[tag], v81\n\tv_cmpx_eq_u32 %[tag], v83\n\tv_cmpx_eq_u32 %[tag], v85\n\tv_cmpx_eq_u32 %[tag], v87\n\tv_cmpx_eq_u32 %[tag], v89\n\tv_cmpx_eq_u32 %[tag], v91\n\tv_cmpx_eq_u32 %[tag], v93\n\tv_cmpx_eq_u32 %[tag], v95\n\tv_cmpx_eq_u32 %[tag], v97\n\tv_cmpx_eq_u32 %[tag], v99\n\ts_cmp_eq_u64 exec, -1\n\ts_mov_b64 exec, -1\n\ts_cbranch_scc1 3f\n\tglobal_load_dwordx4 v[64:67], %[pb0], %[ws] sc1\n\tglobal_load_dwordx4 v[68:71], %[pb1], %[ws] sc1\n\tglobal_load_dwordx4 v[72:75], %[pb2], %[ws] sc1\n\tglobal_load_dwordx4 v[76:79], %[pb3], %[ws] sc1\n\tglobal_load_dwordx4 v[80:83], %[pb4], %[ws] sc1\n\tglobal_load_dwordx4 v[84:87], %[pb5], %[ws] sc1\n\tglobal_load_dwordx4 v[88:91], %[pb6], %[ws] sc1\n\tglobal_load_dwordx4 v[92:95], %[pb7], %[ws] sc1\n\tglobal_load_dwordx4 v[96:99], %[pb8], %[ws] sc1\n\t"
                         "s_waitcnt vmcnt(9)\n\tv_cmpx_eq_u32 %[tag], v101\n\tv_cmpx_eq_u32 %[tag], v103\n\tv_cmpx_eq_u32 %[tag], v105\n\tv_cmpx_eq_u32 %[tag], v107\n\tv_cmpx_eq_u32 %[tag], v109\n\tv_cmpx_eq_u32 %[tag], v111\n\tv_cmpx_eq_u32 %[tag], v113\n\tv_cmpx_eq_u32 %[tag], v115\n\tv_cmpx_eq_u32 %[tag], v117\n\tv_cmpx_eq_u32 %[tag], v119\n\tv_cmpx_eq_u32 %[tag], v121\n\tv_cmpx_eq_u32 %[tag], v123\n\tv_cmpx_eq_u32 %[tag], v125\n\tv_cmpx_eq_u32 %[tag], v127\n\tv_cmpx_eq_u32 %[tag], v129\n\tv_cmpx_eq_u32 %[tag], v131\n\tv_cmpx_eq_u32 %[tag], v133\n\tv_cmpx_eq_u32 %[tag], v135\n\ts_cmp_eq_u64 exec, -1\n\ts_mov_b64 exec, -1\n\ts_cbranch_scc1 2f\n\tglobal_load_dwordx4 v[100:103], %[pb0], %[ws] sc1\n\tglobal_load_dwordx4 v[104:107], %[pb1], %[ws] sc1\n\tglobal_load_dwordx4 v[108:111], %[pb2], %[ws] sc1\n\tglobal_load_dwordx4 v[112:115], %[pb3], %[ws] sc1\n\tglobal_load_dwordx4 v[116:119], %[pb4], %[ws] sc1\n\tglobal_load_dwordx4 v[120:123], %[pb5], %[ws] sc1\n\tglobal_load_dwordx4 v[124:127], %[pb6], %[ws] sc1\n\tglobal_load_dwordx4 v[128:131], %[pb7], %[ws] sc1\n\tglobal_load_dwordx4 v[132:135], %[pb8], %[ws] sc1\n\t"
                         "s_sub_u32 %[c], %[c], 1\n\ts_cbranch_scc0 1b\n\ts_waitcnt vmcnt(0)\n"
                         "2:\n\tv_mov_b32 v101, v104\n\tv_mov_b32 v103, v106\n\tv_mov_b32 v109, v112\n\tv_mov_b32 v111, v114\n\tv_mov_b32 v117, v120\n\tv_mov_b32 v119, v122\n\tv_mov_b32 v125, v128\n\tv_mov_b32 v127, v130\n\tv_mov_b32 v133, 0\n\tv_mov_b32 v135, 0\n\tv_mfma_f32_16x16x32_bf16 v[136:139], %[sel], v[100:103], 0\n\tv_mfma_f32_16x16x32_bf16 v[140:143], %[sel], v[108:111], 0\n\tv_mfma_f32_16x16x32_bf16 v[136:139], %[sel], v[116:119], v[136:139]\n\tv_mfma_f32_16x16x32_bf16 v[140:143], %[sel], v[124:127], v[140:143]\n\tv_mfma_f32_16x16x32_bf16 v[136:139], %[sel], v[132:135], v[136:139]\n\ts_branch 4f\n"
                         "3:\n\tv_mov_b32 v65, v68\n\tv_mov_b32 v67, v70\n\tv_mov_b32 v73, v76\n\tv_mov_b32 v75, v78\n\tv_mov_b32 v81, v84\n\tv_mov_b32 v83, v86\n\tv_mov_b32 v89, v92\n\tv_mov_b32 v91, v94\n\tv_mov_b32 v97, 0\n\tv_mov_b32 v99, 0\n\tv_mfma_f32_16x16x32_bf16 v[136:139], %[sel], v[64:67], 0\n\tv_mfma_f32_16x16x32_bf16 v[140:143], %[sel], v[72:75], 0\n\tv_mfma_f32_16x16x32_bf16 v[136:139], %[sel], v[80:83], v[136:139]\n\tv_mfma_f32_16x16x32_bf16 v[140:143], %[sel], v[88:91], v[140:143]\n\tv_mfma_f32_16x16x32_bf16 v[136:139], %[sel], v[96:99], v[136:139]\n\t\n4:\n\ts_nop 7"
                         : "+{v[64:95]}"(ra), "+{v[96:99]}"(rc), "=&{v[100:131]}"(rb), "=&{v[132:135]}"(rd), "=&{v[136:139]}"(s0), "=&{v[140:143]}"(s1), [c] "=&s"(c_)
                         : [pb0] "v"(pb[0]), [pb1] "v"(pb[1]), [pb2] "v"(pb[2]), [pb3] "v"(pb[3]), [pb4] "v"(pb[4]), [pb5] "v"(pb[5]), [pb6] "v"(pb[6]), [pb7] "v"(pb[7]), [pb8] "v"(pb[8]), [ws] "s"(p.ws), [tag] "s"(tag), [sel] "v"(sel4) : "memory", "scc", "vcc");
            STAMP(11);
            float o[4];
#pragma unroll
            for (int c = 0; c < 4; ++c) o[c] = fmaf(omg, xv[c] + rbf(rbf(dpp_add<0x128, 0xf>(s0[c] + s1[c])) * rs), av[c]);
            asm volatile("s_mov_b64 exec, 0xff\n\tglobal_store_dwordx2 %0, %1, %2\n\ts_mov_b64 exec, -1" :: "v"(oo), "v"((v2u){cvtpk(o[0], o[1]), cvtpk(o[2], o[3])}), "s"(p.out) : "memory");
            STAMP(12);
            asm volatile("s_waitcnt vmcnt(0)" :: "v"(ra), "v"(rb), "v"(rc), "v"(rd) : "memory");
        }
        ROLE_END();
    }
    }
}

extern "C" void kernel_launch(void* const* d_in, const int* in_sizes, int n_in,
                              void* d_out, int out_size, void* d_ws, size_t ws_size,
                              hipStream_t stream) {
    Params p;
    p.x = (const u16*)d_in[0]; p.Are = (const u16*)d_in[1]; p.Aim = (const u16*)d_in[2]; p.Bre = (const u16*)d_in[3];
    p.Bim = (const u16*)d_in[4]; p.Cre = (const u16*)d_in[5]; p.Cim = (const u16*)d_in[6]; p.logdt = (const u16*)d_in[7];
    p.dskip = (const u16*)d_in[8]; p.wr = (const u16*)d_in[10]; p.wg = (const u16*)d_in[11]; p.wu = (const u16*)d_in[12];
    p.wd = (const u16*)d_in[13]; p.gw = (const u16*)d_in[14]; p.out = (u16*)d_out; p.ws = (char*)d_ws;
    (void)hipFuncSetAttribute((const void*)fused_kernel, hipFuncAttributeMaxDynamicSharedMemorySize, LDS_ALLOC);
    fused_kernel<<<256, 512, LDS_ALLOC, stream>>>(p);
}
```

```cpp
#include <hip/hip_runtime.h>
#include <stdint.h>

typedef unsigned short u16;
typedef unsigned int u32;
typedef unsigned long long u64;
typedef __attribute__((ext_vector_type(4))) u32 v4u;
typedef __attribute__((ext_vector_type(2))) u32 v2u;
typedef __attribute__((ext_vector_type(2))) float v2f;
typedef __attribute__((ext_vector_type(4))) float v4f;
typedef __attribute__((ext_vector_type(16))) float v16f;
typedef __attribute__((ext_vector_type(8))) short bf16x8;
typedef __attribute__((ext_vector_type(4))) short s16x4;
typedef __attribute__((ext_vector_type(2))) __bf16 bf16x2_t;
typedef __attribute__((ext_vector_type(2))) _Float16 f16x2_t;
typedef __attribute__((ext_vector_type(8))) _Float16 f16x8;
#define LAS __attribute__((address_space(3)))
#define GAS __attribute__((address_space(1)))
#define RLX_AGENT __ATOMIC_RELAXED, __HIP_MEMORY_SCOPE_AGENT
#define SBAR() __builtin_amdgcn_sched_barrier(0)
#define BAR() asm volatile("s_waitcnt lgkmcnt(0)\n\ts_barrier" ::: "memory")
#define WSR() __builtin_amdgcn_make_buffer_rsrc(p.ws, 0, 0x7fffffff, 0x00020000)
#define REREAD() asm volatile("" ::: "memory")

#define WNT " nt"
#define CODE_KB 25
#define NB1 2
#define REPG 2
#define PFG0 528
#define FDELAY 0
#define PFR0 16
#define SELPF 0
#define SELWAIT 50
#define MPRIO 0
#define YSLEEP 1
#define HPRIO 0
#define K2 4
#define TOUCH 0
#define XT 0
#define WPF 2
#define K1 6
#define PK 2
#define UDELAY 0
#define HSLEEP 0
#define BSLEEP 0
#define WT 0
#define XPF 0

constexpr int L = 2048, D = 576, NST = 128, F = 4608;
constexpr int NFFN = 72;
constexpr u32 OFF_RG = 4096;
constexpr u32 OFF_G = OFF_RG + 20480;
constexpr u32 OFF_SEL = OFF_G + REPG * 10240;
constexpr u32 OFF_CI = OFF_SEL + 16 * 256;
constexpr u32 OFF_PART = OFF_CI + 4096;
constexpr u32 PP = 11 * 1024;
constexpr u32 SPIN_CAP = 1u << 19;

#define STAMP(i)
#define STAMP0_FIX()
#define STAMP_END()
#define STAMP_FLUSH()

struct Params {
    const u16 *x, *Are, *Aim, *Bre, *Bim, *Cre, *Cim, *logdt, *dskip, *wr, *wg, *wu, *wd, *gw;
    u16* out;
    char* ws;
};

__device__ __forceinline__ float bflo(u32 w) { return __uint_as_float(w << 16); }
__device__ __forceinline__ float bfhi(u32 w) { return __uint_as_float(w & 0xffff0000u); }
__device__ __forceinline__ float bf2f(u16 h) { return __uint_as_float((u32)h << 16); }
__device__ __forceinline__ u32 cvtpk(float lo, float hi) { v2f v = {lo, hi}; bf16x2_t r = __builtin_convertvector(v, bf16x2_t); return __builtin_bit_cast(u32, r); }
__device__ __forceinline__ u32 cvth(v2f v) { return __builtin_bit_cast(u32, __builtin_convertvector(v, f16x2_t)); }
__device__ __forceinline__ float rbf(float f) { return bflo(cvtpk(f, f)); }
__device__ __forceinline__ float sigmoidf_(float s) { return __builtin_amdgcn_rcpf(1.f + __expf(-s)); }

template <int CTRL, int RM> __device__ __forceinline__ float dpp_add(float v) {
    return v + __builtin_bit_cast(float, __builtin_amdgcn_update_dpp(0, __builtin_bit_cast(int, v), CTRL, RM, 0xf, true));
}
__device__ __forceinline__ float wave_sum63(float v) {
    v = dpp_add<0x111, 0xf>(v); v = dpp_add<0x112, 0xf>(v); v = dpp_add<0x114, 0xf>(v); v = dpp_add<0x118, 0xf>(v);
    v = dpp_add<0x142, 0xa>(v); v = dpp_add<0x143, 0xc>(v);
    return v;
}
template <int CTRL, int RM> __device__ __forceinline__ u32 dpp_umax(u32 v) {
    u32 o = (u32)__builtin_amdgcn_update_dpp(0, (int)v, CTRL, RM, 0xf, true);
    return v > o ? v : o;
}

typedef v2f cf;
__device__ __forceinline__ cf cmul(cf a, cf b) { cf t = a.yy * (cf){-b.y, b.x}; return __builtin_elementwise_fma(a.xx, b, t); }
__device__ __forceinline__ cf cexp_(float a2, float brev, float rho) {
    float m = __builtin_amdgcn_exp2f(a2 * rho);
    float ph = __builtin_amdgcn_fractf(brev * rho);
    cf c; c.x = m * __builtin_amdgcn_cosf(ph); c.y = m * __builtin_amdgcn_sinf(ph); return c;
}

struct cf2 { cf a, b; };
__device__ __forceinline__ cf2 cmul2(cf2 x, cf2 y) { return {cmul(x.a, y.a), cmul(x.b, y.b)}; }
__device__ __forceinline__ cf2 cexp2(v2f a2, v2f brev, float rho) { return {cexp_(a2.x, brev.x, rho), cexp_(a2.y, brev.y, rho)}; }

__device__ __forceinline__ u32 make_payload(float r, float g) {
    const u32 w = cvtpk(g, r);
    return w ^ (((u32)((int)w >> 31) & 0x7fff0000u) | 0x80000000u);
}
__device__ __forceinline__ u32 make_key(u32 pay, u32 t) { return (pay & 0xffff0000u) | (0xffffu - t); }
__device__ __forceinline__ float key_to_r(u32 k) {
    u32 hi = k & 0xffff0000u;
    return __uint_as_float((hi & 0x80000000u) ? (hi ^ 0x80000000u) : (~hi & 0xffff0000u));
}

constexpr int STG_ROW = 528;
constexpr int NA = 21;
constexpr int KCH = (NA + 32) * STG_ROW;
constexpr int KCOPY = 1344;
constexpr int CH_Y = 4 * KCOPY;
constexpr int LDS_U = 4 * KCH;
constexpr int LDS_TAP = LDS_U + 4 * 4608;
constexpr int LDS_CTL = LDS_TAP + 4 * 2304;
constexpr int C_TAG = 0, C_STG = 4, C_PUB = 8, C_ACK = 12, C_ST2 = 20, C_Y = 64;
constexpr int LDS_RW = LDS_CTL + 1024;
constexpr int LDS_G = LDS_RW;
constexpr int LDS_DUMMY = LDS_RW + 4 * 3072;
constexpr int LDS_TOTAL = LDS_DUMMY + 1024;
static_assert(CH_Y + 8 * 1152 <= KCH && LDS_TOTAL <= 160 * 1024, "lds");
constexpr int WDP = 1184;
constexpr int XSP = 1168;
constexpr int FL_XS = 144 * 1024;
constexpr int FL_H = FL_XS + 8 * XSP;
constexpr int FL_Z = FL_H + 8 * 144;
constexpr int FL_TAG = FL_Z + 256;
constexpr int LDS_ALLOC = FL_TAG + 64 > LDS_TOTAL ? FL_TAG + 64 : LDS_TOTAL;
static_assert(LDS_ALLOC <= 160 * 1024, "lds");

__device__ __forceinline__ int sum_rows(int v) {
    auto a = __builtin_amdgcn_permlane16_swap((u32)v, (u32)v, false, false);
    v = (int)(a[0] + a[1]);
    auto b = __builtin_amdgcn_permlane32_swap((u32)v, (u32)v, false, false);
    return (int)(b[0] + b[1]);
}
__device__ __forceinline__ float sum_rows_f(float v) {
    auto a = __builtin_amdgcn_permlane16_swap(__float_as_uint(v), __float_as_uint(v), false, false);
    v = __uint_as_float(a[0]) + __uint_as_float(a[1]);
    auto b = __builtin_amdgcn_permlane32_swap(__float_as_uint(v), __float_as_uint(v), false, false);
    return __uint_as_float(b[0]) + __uint_as_float(b[1]);
}
__device__ __forceinline__ u32 top8_d(LAS char* t8, int lane, int n) {
    LAS u32* cand = (LAS u32*)(t8 + 256);
    u32 res = 0;
    _Pragma("unroll 1") for (int i0 = 0; i0 < n; i0 += 64) {
        const int i = i0 + lane;
        const u32 k = i < n ? cand[i] : 0u;
        int rk = 0;
        _Pragma("unroll 1") for (int j0 = 0; j0 < n; j0 += 16) {
            v4u c[4];
#pragma unroll
            for (int e = 0; e < 4; ++e) c[e] = *(LAS v4u*)(cand + j0 + 4 * e);
#pragma unroll
            for (int e = 0; e < 16; ++e) rk += (c[e >> 2][e & 3] > k) & (j0 + e < n);
        }
        const bool hit = i < n && rk < 8;
        const u32 got = (u32)__builtin_amdgcn_ds_permute((hit ? rk : 63) * 4, (int)(hit ? k : 0u));
        if (lane < 8) res |= got;
    }
    return res;
}

__device__ __forceinline__ u32 lds_peek(LAS char* word) { return __hip_atomic_load((LAS u32*)word, __ATOMIC_RELAXED, __HIP_MEMORY_SCOPE_WORKGROUP); }
#define LWAIT_BODY "1:\n\tds_read_b32 %0, %3 offset:%4\n\ts_waitcnt lgkmcnt(0)\n\tv_readfirstlane_b32 %1, %0\n\ts_cmp_ge_u32 %1, %5\n\ts_cbranch_scc1 2f\n\ts_sleep 1\n\ts_sub_u32 %2, %2, 1\n\ts_cbranch_scc0 1b\n2:"
#define LSPIN(addr, off, want) do { u32 t_, s_, c_; asm volatile("s_mov_b32 %2, 0x800000\n1:\n\tds_read_b32 %0, %3 offset:%4\n\ts_waitcnt lgkmcnt(0)\n\tv_readfirstlane_b32 %1, %0\n\ts_cmp_ge_u32 %1, %5\n\ts_cbranch_scc1 2f\n\ts_sub_u32 %2, %2, 1\n\ts_cbranch_scc0 1b\n2:" : "=&v"(t_), "=&s"(s_), "=&s"(c_) : "v"(addr), "n"(off), "n"(want) : "memory", "scc"); } while (0)
#define LWAIT(addr, off, want) do { u32 t_, s_, c_; asm volatile("s_mov_b32 %2, 0x400000\n" LWAIT_BODY : "=&v"(t_), "=&s"(s_), "=&s"(c_) : "v"(addr), "n"(off), "n"(want) : "memory", "scc"); } while (0)
#define LWAIT_PEEK(peek, addr, off, want) do { u32 t_, s_, c_; asm volatile("s_nop 0\n\tv_readfirstlane_b32 %1, %6\n\ts_mov_b32 %2, 0x400000\n\ts_cmp_ge_u32 %1, %5\n\ts_cbranch_scc1 2f\n" LWAIT_BODY : "=&v"(t_), "=&s"(s_), "=&s"(c_) : "v"(addr), "n"(off), "n"(want), "v"(peek) : "memory", "scc"); } while (0)
#define LDS_ADD1(addr, one, off) asm volatile("s_mov_b64 exec, 1\n\tds_add_u32 %0, %1 offset:%2\n\ts_mov_b64 exec, -1" :: "v"(addr), "v"(one), "n"(off) : "memory")
#define LDS_SETB(addr, one, off) asm volatile("s_mov_b64 exec, 1\n\tds_write_b8 %0, %1 offset:%2\n\ts_mov_b64 exec, -1" :: "v"(addr), "v"(one), "n"(off) : "memory")
__device__ __forceinline__ void glds16(const void* g, u32 lds_dst, bool nt) {
    u32 keep;
    if (nt) asm volatile("s_mov_b32 %0, m0\n\ts_mov_b32 m0, %2\n\ts_nop 0\n\tglobal_load_lds_dwordx4 %1, off" WNT "\n\ts_mov_b32 m0, %0" : "=&s"(keep) : "v"(g), "s"(lds_dst) : "memory");
    else asm volatile("s_mov_b32 %0, m0\n\ts_mov_b32 m0, %2\n\ts_nop 0\n\tglobal_load_lds_dwordx4 %1, off\n\ts_mov_b32 m0, %0" : "=&s"(keep) : "v"(g), "s"(lds_dst) : "memory");
}
__device__ __forceinline__ bf16x8 band(u32 bits, u32 val) {
    v4u r;
#pragma unroll
    for (int j = 0; j < 4; ++j) r[j] = __builtin_amdgcn_perm((u32)__builtin_amdgcn_sbfe((int)bits, 2 * j + 1, 1), (u32)__builtin_amdgcn_sbfe((int)bits, 2 * j, 1), 0x05040100u) & val;
    return __builtin_bit_cast(bf16x8, r);
}

__device__ __forceinline__ u32 hline(const char* ws, u32 i) { const u32 q = i / 7u, r = i - 7u * q; return (8u * q + r + (r >= ((3u - (u32)((uintptr_t)ws >> 7)) & 7u))) * 128u; }
__device__ __forceinline__ u32 wd_line(u32 xcd, u32 i) {
    const u32 k = i / 9u, r = k / 9u;
    return ((xcd * 9u + k - 9u * r) * 64u + (r >> 1) + 32u * (r & 1)) * 1152u + (i - 9u * k) * 128u;
}
__device__ __forceinline__ bool touch(const Params& p, int xcd, int e, int lane, u32 dst) {
    const int wa = xcd * 9 + ((3 - (int)(((uintptr_t)p.wg >> 7) & 7) - xcd * 9) & 7), two = wa == xcd * 9;
    const int g0 = two ? 576 : PFG0, na = (g0 * 2) << two, nb = (576 - g0) * 18, nc = WPF * 1728;
    if (e * 64 >= na + nb + nc) return false;
    const u32 i = min(e * 64 + lane, na + nb + nc - 1);
    const u32 j = i - na, k = j / 9u;
    const bool first = i < (u32)na, last = i >= (u32)(na + nb);
    const u32 seg = first ? i >> two : k + 2 * g0, col = first ? (wa + 8 * (i & two)) * 128u : xcd * 1152u + (j - 9u * k) * 128u;
    const char* a = (const char*)(last ? p.wd : ((seg & 1) ? p.wu : p.wg)) + (last ? wd_line(xcd, j - nb) : (seg >> 1) * (F * 2) + col);
    asm volatile("s_mov_b32 m0, %1\n\ts_nop 0\n\tglobal_load_lds_dword %0, off" :: "v"(a), "s"(dst) : "memory");
    return true;
}

#define KGEN_ASM ".p2align 3\n\tv_mov_b32 v64, 0x3c00\n\tv_mov_b32 v65, 0x3c00\n\tv_pk_mul_f32 v[66:67], v[56:57], v[56:57] op_sel:[1,1] op_sel_hi:[1,0] neg_lo:[0,1]\n\tv_pk_mul_f32 v[68:69], v[58:59], v[58:59] op_sel:[1,1] op_sel_hi:[1,0] neg_lo:[0,1]\n\tv_pk_mul_f32 v[70:71], v[52:53], v[52:53] op_sel:[1,1] op_sel_hi:[1,0] neg_lo:[0,1]\n\tv_pk_mul_f32 v[72:73], v[54:55], v[54:55] op_sel:[1,1] op_sel_hi:[1,0] neg_lo:[0,1]\n\tv_pk_fma_f32 v[66:67], v[56:57], v[56:57], v[66:67] op_sel:[0,0,0] op_sel_hi:[0,1,1]\n\tv_pk_fma_f32 v[68:69], v[58:59], v[58:59], v[68:69] op_sel:[0,0,0] op_sel_hi:[0,1,1]\n\tv_pk_fma_f32 v[70:71], v[52:53], v[52:53], v[70:71] op_sel:[0,0,0] op_sel_hi:[0,1,1]\n\tv_pk_fma_f32 v[72:73], v[54:55], v[54:55], v[72:73] op_sel:[0,0,0] op_sel_hi:[0,1,1]\n\tv_cvt_pk_f16_f32 v74, v56, -v57\n\tv_cvt_pk_f16_f32 v75, v58, -v59\n\tv_cvt_pk_f16_f32 v76, v48, v49\n\tv_cvt_pk_f16_f32 v77, v50, v51\n\tv_pk_mul_f32 v[78:79], v[48:49], v[60:61] op_sel:[1,1] op_sel_hi:[1,0] neg_lo:[0,1]\n\tv_pk_mul_f32 v[80:81], v[50:51], v[62:63] op_sel:[1,1] op_sel_hi:[1,0] neg_lo:[0,1]\n\tv_pk_mul_f32 v[82:83], v[48:49], v[52:53] op_sel:[1,1] op_sel_hi:[1,0] neg_lo:[0,1]\n\tv_pk_mul_f32 v[84:85], v[50:51], v[54:55] op_sel:[1,1] op_sel_hi:[1,0] neg_lo:[0,1]\n\tds_write2_b64 %[b0], v[64:65], v[74:75] offset0:0 offset1:66\n\tv_pk_fma_f32 v[78:79], v[48:49], v[60:61], v[78:79] op_sel:[0,0,0] op_sel_hi:[0,1,1]\n\tv_pk_fma_f32 v[80:81], v[50:51], v[62:63], v[80:81] op_sel:[0,0,0] op_sel_hi:[0,1,1]\n\tv_pk_fma_f32 v[82:83], v[48:49], v[52:53], v[82:83] op_sel:[0,0,0] op_sel_hi:[0,1,1]\n\tv_pk_fma_f32 v[84:85], v[50:51], v[54:55], v[84:85] op_sel:[0,0,0] op_sel_hi:[0,1,1]\n\tv_cvt_pk_f16_f32 v86, v66, -v67\n\tv_cvt_pk_f16_f32 v87, v68, -v69\n\tv_pk_mul_f32 v[88:89], v[66:67], v[66:67] op_sel:[1,1] op_sel_hi:[1,0] neg_lo:[0,1]\n\tv_pk_mul_f32 v[90:91], v[68:69], v[68:69] op_sel:[1,1] op_sel_hi:[1,0] neg_lo:[0,1]\n\tv_pk_mul_f32 v[92:93], v[66:67], v[56:57] op_sel:[1,1] op_sel_hi:[1,0] neg_lo:[0,1]\n\tv_pk_mul_f32 v[94:95], v[68:69], v[58:59] op_sel:[1,1] op_sel_hi:[1,0] neg_lo:[0,1]\n\tv_pk_fma_f32 v[88:89], v[66:67], v[66:67], v[88:89] op_sel:[0,0,0] op_sel_hi:[0,1,1]\n\tv_pk_fma_f32 v[90:91], v[68:69], v[68:69], v[90:91] op_sel:[0,0,0] op_sel_hi:[0,1,1]\n\tv_pk_fma_f32 v[92:93], v[66:67], v[56:57], v[92:93] op_sel:[0,0,0] op_sel_hi:[0,1,1]\n\tv_pk_fma_f32 v[94:95], v[68:69], v[58:59], v[94:95] op_sel:[0,0,0] op_sel_hi:[0,1,1]\n\tv_cvt_pk_f16_f32 v64, v78, v79\n\tv_cvt_pk_f16_f32 v65, v80, v81\n\tv_cvt_pk_f16_f32 v96, v82, v83\n\tv_cvt_pk_f16_f32 v97, v84, v85\n\tv_pk_mul_f32 v[98:99], v[70:71], v[70:71] op_sel:[1,1] op_sel_hi:[1,0] neg_lo:[0,1]\n\tv_pk_mul_f32 v[100:101], v[72:73], v[72:73] op_sel:[1,1] op_sel_hi:[1,0] neg_lo:[0,1]\n\tv_pk_mul_f32 v[102:103], v[78:79], v[56:57] op_sel:[1,1] op_sel_hi:[1,0] neg_lo:[0,1]\n\tv_pk_mul_f32 v[104:105], v[80:81], v[58:59] op_sel:[1,1] op_sel_hi:[1,0] neg_lo:[0,1]\n\tds_write2_b64 %[b8], v[76:77], v[96:97] offset0:0 offset1:66\n\tv_pk_fma_f32 v[98:99], v[70:71], v[70:71], v[98:99] op_sel:[0,0,0] op_sel_hi:[0,1,1]\n\tv_pk_fma_f32 v[100:101], v[72:73], v[72:73], v[100:101] op_sel:[0,0,0] op_sel_hi:[0,1,1]\n\tv_pk_fma_f32 v[102:103], v[78:79], v[56:57], v[102:103] op_sel:[0,0,0] op_sel_hi:[0,1,1]\n\tv_pk_fma_f32 v[104:105], v[80:81], v[58:59], v[104:105] op_sel:[0,0,0] op_sel_hi:[0,1,1]\n\tv_cvt_pk_f16_f32 v106, v88, -v89\n\tv_cvt_pk_f16_f32 v107, v90, -v91\n\tv_cvt_pk_f16_f32 v108, v92, -v93\n\tv_cvt_pk_f16_f32 v109, v94, -v95\n\tv_pk_mul_f32 v[110:111], v[78:79], v[70:71] op_sel:[1,1] op_sel_hi:[1,0] neg_lo:[0,1]\n\tv_pk_mul_f32 v[112:113], v[80:81], v[72:73] op_sel:[1,1] op_sel_hi:[1,0] neg_lo:[0,1]\n\tv_pk_mul_f32 v[114:115], v[48:49], v[70:71] op_sel:[1,1] op_sel_hi:[1,0] neg_lo:[0,1]\n\tv_pk_mul_f32 v[116:117], v[50:51], v[72:73] op_sel:[1,1] op_sel_hi:[1,0] neg_lo:[0,1]\n\tds_write2_b64 %[b0], v[86:87], v[108:109] offset0:132 offset1:198\n\tv_pk_fma_f32 v[110:111], v[78:79], v[70:71], v[110:111] op_sel:[0,0,0] op_sel_hi:[0,1,1]\n\tv_pk_fma_f32 v[112:113], v[80:81], v[72:73], v[112:113] op_sel:[0,0,0] op_sel_hi:[0,1,1]\n\tv_pk_fma_f32 v[114:115], v[48:49], v[70:71], v[114:115] op_sel:[0,0,0] op_sel_hi:[0,1,1]\n\tv_pk_fma_f32 v[116:117], v[50:51], v[72:73], v[116:117] op_sel:[0,0,0] op_sel_hi:[0,1,1]\n\tv_cvt_pk_f16_f32 v76, v102, v103\n\tv_cvt_pk_f16_f32 v77, v104, v105\n\tv_pk_mul_f32 v[96:97], v[82:83], v[70:71] op_sel:[1,1] op_sel_hi:[1,0] neg_lo:[0,1]\n\tv_pk_mul_f32 v[118:119], v[84:85], v[72:73] op_sel:[1,1] op_sel_hi:[1,0] neg_lo:[0,1]\n\tv_pk_mul_f32 v[120:121], v[88:89], v[88:89] op_sel:[1,1] op_sel_hi:[1,0] neg_lo:[0,1]\n\tv_pk_mul_f32 v[122:123], v[90:91], v[90:91] op_sel:[1,1] op_sel_hi:[1,0] neg_lo:[0,1]\n\tv_pk_fma_f32 v[96:97], v[82:83], v[70:71], v[96:97] op_sel:[0,0,0] op_sel_hi:[0,1,1]\n\tv_pk_fma_f32 v[118:119], v[84:85], v[72:73], v[118:119] op_sel:[0,0,0] op_sel_hi:[0,1,1]\n\tv_pk_fma_f32 v[120:121], v[88:89], v[88:89], v[120:121] op_sel:[0,0,0] op_sel_hi:[0,1,1]\n\tv_pk_fma_f32 v[122:123], v[90:91], v[90:91], v[122:123] op_sel:[0,0,0] op_sel_hi:[0,1,1]\n\tv_cvt_pk_f16_f32 v124, v110, v111\n\tv_cvt_pk_f16_f32 v125, v112, v113\n\tv_cvt_pk_f16_f32 v126, v114, v115\n\tv_cvt_pk_f16_f32 v127, v116, v117\n\tv_pk_mul_f32 v[70:71], v[88:89], v[56:57] op_sel:[1,1] op_sel_hi:[1,0] neg_lo:[0,1]\n\tv_pk_mul_f32 v[72:73], v[90:91], v[58:59] op_sel:[1,1] op_sel_hi:[1,0] neg_lo:[0,1]\n\tv_pk_mul_f32 v[128:129], v[102:103], v[60:61] op_sel:[1,1] op_sel_hi:[1,0] neg_lo:[0,1]\n\tv_pk_mul_f32 v[130:131], v[104:105], v[62:63] op_sel:[1,1] op_sel_hi:[1,0] neg_lo:[0,1]\n\tds_write2_b64 %[b11], v[64:65], v[124:125] offset0:0 offset1:66\n\tv_pk_fma_f32 v[70:71], v[88:89], v[56:57], v[70:71] op_sel:[0,0,0] op_sel_hi:[0,1,1]\n\tv_pk_fma_f32 v[72:73], v[90:91], v[58:59], v[72:73] op_sel:[0,0,0] op_sel_hi:[0,1,1]\n\tv_pk_fma_f32 v[128:129], v[102:103], v[60:61], v[128:129] op_sel:[0,0,0] op_sel_hi:[0,1,1]\n\tv_pk_fma_f32 v[130:131], v[104:105], v[62:63], v[130:131] op_sel:[0,0,0] op_sel_hi:[0,1,1]\n\tv_cvt_pk_f16_f32 v132, v96, v97\n\tv_cvt_pk_f16_f32 v133, v118, v119\n\tv_cvt_pk_f16_f32 v134, v120, -v121\n\tv_cvt_pk_f16_f32 v135, v122, -v123\n\tv_pk_mul_f32 v[102:103], v[78:79], v[98:99] op_sel:[1,1] op_sel_hi:[1,0] neg_lo:[0,1]\n\tv_pk_mul_f32 v[104:105], v[80:81], v[100:101] op_sel:[1,1] op_sel_hi:[1,0] neg_lo:[0,1]\n\tv_pk_mul_f32 v[136:137], v[88:89], v[66:67] op_sel:[1,1] op_sel_hi:[1,0] neg_lo:[0,1]\n\tv_pk_mul_f32 v[138:139], v[90:91], v[68:69] op_sel:[1,1] op_sel_hi:[1,0] neg_lo:[0,1]\n\tds_write2_b64 %[b8], v[126:127], v[132:133] offset0:132 offset1:198\n\tv_pk_fma_f32 v[102:103], v[78:79], v[98:99], v[102:103] op_sel:[0,0,0] op_sel_hi:[0,1,1]\n\tv_pk_fma_f32 v[104:105], v[80:81], v[100:101], v[104:105] op_sel:[0,0,0] op_sel_hi:[0,1,1]\n\tv_pk_fma_f32 v[136:137], v[88:89], v[66:67], v[136:137] op_sel:[0,0,0] op_sel_hi:[0,1,1]\n\tv_pk_fma_f32 v[138:139], v[90:91], v[68:69], v[138:139] op_sel:[0,0,0] op_sel_hi:[0,1,1]\n\tv_cvt_pk_f16_f32 v64, v70, -v71\n\tv_cvt_pk_f16_f32 v65, v72, -v73\n\tv_cvt_pk_f16_f32 v124, v128, v129\n\tv_cvt_pk_f16_f32 v125, v130, v131\n\tv_pk_mul_f32 v[78:79], v[88:89], v[92:93] op_sel:[1,1] op_sel_hi:[1,0] neg_lo:[0,1]\n\tv_pk_mul_f32 v[80:81], v[90:91], v[94:95] op_sel:[1,1] op_sel_hi:[1,0] neg_lo:[0,1]\n\tv_pk_mul_f32 v[140:141], v[120:121], v[120:121] op_sel:[1,1] op_sel_hi:[1,0] neg_lo:[0,1]\n\tv_pk_mul_f32 v[142:143], v[122:123], v[122:123] op_sel:[1,1] op_sel_hi:[1,0] neg_lo:[0,1]\n\tds_write2_b64 %[b1], v[106:107], v[64:65] offset0:0 offset1:66\n\tv_pk_fma_f32 v[78:79], v[88:89], v[92:93], v[78:79] op_sel:[0,0,0] op_sel_hi:[0,1,1]\n\tv_pk_fma_f32 v[80:81], v[90:91], v[94:95], v[80:81] op_sel:[0,0,0] op_sel_hi:[0,1,1]\n\tv_pk_fma_f32 v[140:141], v[120:121], v[120:121], v[140:141] op_sel:[0,0,0] op_sel_hi:[0,1,1]\n\tv_pk_fma_f32 v[142:143], v[122:123], v[122:123], v[142:143] op_sel:[0,0,0] op_sel_hi:[0,1,1]\n\tv_cvt_pk_f16_f32 v126, v102, v103\n\tv_cvt_pk_f16_f32 v127, v104, v105\n\tv_cvt_pk_f16_f32 v132, v136, -v137\n\tv_cvt_pk_f16_f32 v133, v138, -v139\n\tv_pk_mul_f32 v[144:145], v[120:121], v[56:57] op_sel:[1,1] op_sel_hi:[1,0] neg_lo:[0,1]\n\tv_pk_mul_f32 v[146:147], v[122:123], v[58:59] op_sel:[1,1] op_sel_hi:[1,0] neg_lo:[0,1]\n\tv_pk_mul_f32 v[148:149], v[128:129], v[60:61] op_sel:[1,1] op_sel_hi:[1,0] neg_lo:[0,1]\n\tv_pk_mul_f32 v[150:151], v[130:131], v[62:63] op_sel:[1,1] op_sel_hi:[1,0] neg_lo:[0,1]\n\tds_write2_b64 %[b9], v[76:77], v[124:125] offset0:0 offset1:66\n\tv_pk_fma_f32 v[144:145], v[120:121], v[56:57], v[144:145] op_sel:[0,0,0] op_sel_hi:[0,1,1]\n\tv_pk_fma_f32 v[146:147], v[122:123], v[58:59], v[146:147] op_sel:[0,0,0] op_sel_hi:[0,1,1]\n\tv_pk_fma_f32 v[148:149], v[128:129], v[60:61], v[148:149] op_sel:[0,0,0] op_sel_hi:[0,1,1]\n\tv_pk_fma_f32 v[150:151], v[130:131], v[62:63], v[150:151] op_sel:[0,0,0] op_sel_hi:[0,1,1]\n\tv_cvt_pk_f16_f32 v152, v78, -v79\n\tv_cvt_pk_f16_f32 v153, v80, -v81\n\tv_cvt_pk_f16_f32 v154, v140, -v141\n\tv_cvt_pk_f16_f32 v155, v142, -v143\n\tv_pk_mul_f32 v[128:129], v[102:103], v[60:61] op_sel:[1,1] op_sel_hi:[1,0] neg_lo:[0,1]\n\tv_pk_mul_f32 v[130:131], v[104:105], v[62:63] op_sel:[1,1] op_sel_hi:[1,0] neg_lo:[0,1]\n\tv_pk_mul_f32 v[140:141], v[110:111], v[98:99] op_sel:[1,1] op_sel_hi:[1,0] neg_lo:[0,1]\n\tv_pk_mul_f32 v[142:143], v[112:113], v[100:101] op_sel:[1,1] op_sel_hi:[1,0] neg_lo:[0,1]\n\tds_write2_b64 %[b1], v[132:133], v[152:153] offset0:132 offset1:198\n\tv_pk_fma_f32 v[128:129], v[102:103], v[60:61], v[128:129] op_sel:[0,0,0] op_sel_hi:[0,1,1]\n\tv_pk_fma_f32 v[130:131], v[104:105], v[62:63], v[130:131] op_sel:[0,0,0] op_sel_hi:[0,1,1]\n\tv_pk_fma_f32 v[140:141], v[110:111], v[98:99], v[140:141] op_sel:[0,0,0] op_sel_hi:[0,1,1]\n\tv_pk_fma_f32 v[142:143], v[112:113], v[100:101], v[142:143] op_sel:[0,0,0] op_sel_hi:[0,1,1]\n\tv_cvt_pk_f16_f32 v76, v144, -v145\n\tv_cvt_pk_f16_f32 v77, v146, -v147\n\tv_cvt_pk_f16_f32 v124, v148, v149\n\tv_cvt_pk_f16_f32 v125, v150, v151\n\tv_pk_mul_f32 v[102:103], v[120:121], v[66:67] op_sel:[1,1] op_sel_hi:[1,0] neg_lo:[0,1]\n\tv_pk_mul_f32 v[104:105], v[122:123], v[68:69] op_sel:[1,1] op_sel_hi:[1,0] neg_lo:[0,1]\n\tv_pk_mul_f32 v[110:111], v[120:121], v[92:93] op_sel:[1,1] op_sel_hi:[1,0] neg_lo:[0,1]\n\tv_pk_mul_f32 v[112:113], v[122:123], v[94:95] op_sel:[1,1] op_sel_hi:[1,0] neg_lo:[0,1]\n\tds_write2_b64 %[b2], v[134:135], v[76:77] offset0:0 offset1:66\n\tv_pk_fma_f32 v[102:103], v[120:121], v[66:67], v[102:103] op_sel:[0,0,0] op_sel_hi:[0,1,1]\n\tv_pk_fma_f32 v[104:105], v[122:123], v[68:69], v[104:105] op_sel:[0,0,0] op_sel_hi:[0,1,1]\n\tv_pk_fma_f32 v[110:111], v[120:121], v[92:93], v[110:111] op_sel:[0,0,0] op_sel_hi:[0,1,1]\n\tv_pk_fma_f32 v[112:113], v[122:123], v[94:95], v[112:113] op_sel:[0,0,0] op_sel_hi:[0,1,1]\n\tv_cvt_pk_f16_f32 v144, v128, v129\n\tv_cvt_pk_f16_f32 v145, v130, v131\n\tv_cvt_pk_f16_f32 v146, v140, v141\n\tv_cvt_pk_f16_f32 v147, v142, v143\n\tv_pk_mul_f32 v[66:67], v[148:149], v[60:61] op_sel:[1,1] op_sel_hi:[1,0] neg_lo:[0,1]\n\tv_pk_mul_f32 v[68:69], v[150:151], v[62:63] op_sel:[1,1] op_sel_hi:[1,0] neg_lo:[0,1]\n\tv_pk_mul_f32 v[92:93], v[128:129], v[60:61] op_sel:[1,1] op_sel_hi:[1,0] neg_lo:[0,1]\n\tv_pk_mul_f32 v[94:95], v[130:131], v[62:63] op_sel:[1,1] op_sel_hi:[1,0] neg_lo:[0,1]\n\tds_write2_b64 %[b9], v[124:125], v[146:147] offset0:132 offset1:198\n\tv_pk_fma_f32 v[66:67], v[148:149], v[60:61], v[66:67] op_sel:[0,0,0] op_sel_hi:[0,1,1]\n\tv_pk_fma_f32 v[68:69], v[150:151], v[62:63], v[68:69] op_sel:[0,0,0] op_sel_hi:[0,1,1]\n\tv_pk_fma_f32 v[92:93], v[128:129], v[60:61], v[92:93] op_sel:[0,0,0] op_sel_hi:[0,1,1]\n\tv_pk_fma_f32 v[94:95], v[130:131], v[62:63], v[94:95] op_sel:[0,0,0] op_sel_hi:[0,1,1]\n\tv_cvt_pk_f16_f32 v140, v102, -v103\n\tv_cvt_pk_f16_f32 v141, v104, -v105\n\tv_cvt_pk_f16_f32 v142, v110, -v111\n\tv_cvt_pk_f16_f32 v143, v112, -v113\n\tv_pk_mul_f32 v[102:103], v[120:121], v[88:89] op_sel:[1,1] op_sel_hi:[1,0] neg_lo:[0,1]\n\tv_pk_mul_f32 v[104:105], v[122:123], v[90:91] op_sel:[1,1] op_sel_hi:[1,0] neg_lo:[0,1]\n\tv_pk_mul_f32 v[110:111], v[120:121], v[70:71] op_sel:[1,1] op_sel_hi:[1,0] neg_lo:[0,1]\n\tv_pk_mul_f32 v[112:113], v[122:123], v[72:73] op_sel:[1,1] op_sel_hi:[1,0] neg_lo:[0,1]\n\tds_write2_b64 %[b2], v[140:141], v[142:143] offset0:132 offset1:198\n\tv_pk_fma_f32 v[102:103], v[120:121], v[88:89], v[102:103] op_sel:[0,0,0] op_sel_hi:[0,1,1]\n\tv_pk_fma_f32 v[104:105], v[122:123], v[90:91], v[104:105] op_sel:[0,0,0] op_sel_hi:[0,1,1]\n\tv_pk_fma_f32 v[110:111], v[120:121], v[70:71], v[110:111] op_sel:[0,0,0] op_sel_hi:[0,1,1]\n\tv_pk_fma_f32 v[112:113], v[122:123], v[72:73], v[112:113] op_sel:[0,0,0] op_sel_hi:[0,1,1]\n\tv_cvt_pk_f16_f32 v124, v66, v67\n\tv_cvt_pk_f16_f32 v125, v68, v69\n\tv_cvt_pk_f16_f32 v128, v92, v93\n\tv_cvt_pk_f16_f32 v129, v94, v95\n\tv_pk_mul_f32 v[70:71], v[48:49], v[98:99] op_sel:[1,1] op_sel_hi:[1,0] neg_lo:[0,1]\n\tv_pk_mul_f32 v[72:73], v[50:51], v[100:101] op_sel:[1,1] op_sel_hi:[1,0] neg_lo:[0,1]\n\tv_pk_mul_f32 v[88:89], v[82:83], v[98:99] op_sel:[1,1] op_sel_hi:[1,0] neg_lo:[0,1]\n\tv_pk_mul_f32 v[90:91], v[84:85], v[100:101] op_sel:[1,1] op_sel_hi:[1,0] neg_lo:[0,1]\n\tds_write2_b64 %[b10], v[126:127], v[144:145] offset0:132 offset1:198\n\tv_pk_fma_f32 v[70:71], v[48:49], v[98:99], v[70:71] op_sel:[0,0,0] op_sel_hi:[0,1,1]\n\tv_pk_fma_f32 v[72:73], v[50:51], v[100:101], v[72:73] op_sel:[0,0,0] op_sel_hi:[0,1,1]\n\tv_pk_fma_f32 v[88:89], v[82:83], v[98:99], v[88:89] op_sel:[0,0,0] op_sel_hi:[0,1,1]\n\tv_pk_fma_f32 v[90:91], v[84:85], v[100:101], v[90:91] op_sel:[0,0,0] op_sel_hi:[0,1,1]\n\tv_cvt_pk_f16_f32 v92, v102, -v103\n\tv_cvt_pk_f16_f32 v93, v104, -v105\n\tv_cvt_pk_f16_f32 v94, v110, -v111\n\tv_cvt_pk_f16_f32 v95, v112, -v113\n\tv_pk_mul_f32 v[82:83], v[66:67], v[60:61] op_sel:[1,1] op_sel_hi:[1,0] neg_lo:[0,1]\n\tv_pk_mul_f32 v[84:85], v[68:69], v[62:63] op_sel:[1,1] op_sel_hi:[1,0] neg_lo:[0,1]\n\tv_pk_mul_f32 v[102:103], v[120:121], v[136:137] op_sel:[1,1] op_sel_hi:[1,0] neg_lo:[0,1]\n\tv_pk_mul_f32 v[104:105], v[122:123], v[138:139] op_sel:[1,1] op_sel_hi:[1,0] neg_lo:[0,1]\n\tds_write2_b64 %[b3], v[92:93], v[94:95] offset0:0 offset1:66\n\tv_pk_fma_f32 v[82:83], v[66:67], v[60:61], v[82:83] op_sel:[0,0,0] op_sel_hi:[0,1,1]\n\tv_pk_fma_f32 v[84:85], v[68:69], v[62:63], v[84:85] op_sel:[0,0,0] op_sel_hi:[0,1,1]\n\tv_pk_fma_f32 v[102:103], v[120:121], v[136:137], v[102:103] op_sel:[0,0,0] op_sel_hi:[0,1,1]\n\tv_pk_fma_f32 v[104:105], v[122:123], v[138:139], v[104:105] op_sel:[0,0,0] op_sel_hi:[0,1,1]\n\tv_cvt_pk_f16_f32 v110, v70, v71\n\tv_cvt_pk_f16_f32 v111, v72, v73\n\tv_cvt_pk_f16_f32 v112, v88, v89\n\tv_cvt_pk_f16_f32 v113, v90, v91\n\tv_pk_mul_f32 v[66:67], v[120:121], v[78:79] op_sel:[1,1] op_sel_hi:[1,0] neg_lo:[0,1]\n\tv_pk_mul_f32 v[68:69], v[122:123], v[80:81] op_sel:[1,1] op_sel_hi:[1,0] neg_lo:[0,1]\n\tv_pk_mul_f32 v[70:71], v[114:115], v[98:99] op_sel:[1,1] op_sel_hi:[1,0] neg_lo:[0,1]\n\tv_pk_mul_f32 v[72:73], v[116:117], v[100:101] op_sel:[1,1] op_sel_hi:[1,0] neg_lo:[0,1]\n\tds_write2_b64 %[b11], v[110:111], v[112:113] offset0:132 offset1:198\n\tv_pk_fma_f32 v[66:67], v[120:121], v[78:79], v[66:67] op_sel:[0,0,0] op_sel_hi:[0,1,1]\n\tv_pk_fma_f32 v[68:69], v[122:123], v[80:81], v[68:69] op_sel:[0,0,0] op_sel_hi:[0,1,1]\n\tv_pk_fma_f32 v[70:71], v[114:115], v[98:99], v[70:71] op_sel:[0,0,0] op_sel_hi:[0,1,1]\n\tv_pk_fma_f32 v[72:73], v[116:117], v[100:101], v[72:73] op_sel:[0,0,0] op_sel_hi:[0,1,1]\n\tv_cvt_pk_f16_f32 v88, v82, v83\n\tv_cvt_pk_f16_f32 v89, v84, v85\n\tv_cvt_pk_f16_f32 v90, v102, -v103\n\tv_cvt_pk_f16_f32 v91, v104, -v105\n\tv_pk_mul_f32 v[78:79], v[96:97], v[98:99] op_sel:[1,1] op_sel_hi:[1,0] neg_lo:[0,1]\n\tv_pk_mul_f32 v[80:81], v[118:119], v[100:101] op_sel:[1,1] op_sel_hi:[1,0] neg_lo:[0,1]\n\tv_pk_mul_f32 v[102:103], v[82:83], v[60:61] op_sel:[1,1] op_sel_hi:[1,0] neg_lo:[0,1]\n\tv_pk_mul_f32 v[104:105], v[84:85], v[62:63] op_sel:[1,1] op_sel_hi:[1,0] neg_lo:[0,1]\n\tds_write2_b64 %[b12], v[128:129], v[124:125] offset0:132 offset1:198\n\tv_pk_fma_f32 v[78:79], v[96:97], v[98:99], v[78:79] op_sel:[0,0,0] op_sel_hi:[0,1,1]\n\tv_pk_fma_f32 v[80:81], v[118:119], v[100:101], v[80:81] op_sel:[0,0,0] op_sel_hi:[0,1,1]\n\tv_pk_fma_f32 v[102:103], v[82:83], v[60:61], v[102:103] op_sel:[0,0,0] op_sel_hi:[0,1,1]\n\tv_pk_fma_f32 v[104:105], v[84:85], v[62:63], v[104:105] op_sel:[0,0,0] op_sel_hi:[0,1,1]\n\tv_cvt_pk_f16_f32 v110, v66, -v67\n\tv_cvt_pk_f16_f32 v111, v68, -v69\n\tv_cvt_pk_f16_f32 v112, v70, v71\n\tv_cvt_pk_f16_f32 v113, v72, v73\n\tv_pk_mul_f16 v66, v154, v74 op_sel:[1,1] op_sel_hi:[1,0] neg_lo:[0,1]\n\tv_pk_mul_f16 v67, v155, v75 op_sel:[1,1] op_sel_hi:[1,0] neg_lo:[0,1]\n\tv_pk_mul_f16 v68, v154, v86 op_sel:[1,1] op_sel_hi:[1,0] neg_lo:[0,1]\n\tv_pk_mul_f16 v69, v155, v87 op_sel:[1,1] op_sel_hi:[1,0] neg_lo:[0,1]\n\tds_write2_b64 %[b3], v[90:91], v[110:111] offset0:132 offset1:198\n\tv_pk_fma_f16 v66, v154, v74, v66 op_sel:[0,0,0] op_sel_hi:[0,1,1]\n\tv_pk_fma_f16 v67, v155, v75, v67 op_sel:[0,0,0] op_sel_hi:[0,1,1]\n\tv_pk_fma_f16 v68, v154, v86, v68 op_sel:[0,0,0] op_sel_hi:[0,1,1]\n\tv_pk_fma_f16 v69, v155, v87, v69 op_sel:[0,0,0] op_sel_hi:[0,1,1]\n\tv_cvt_pk_f16_f32 v70, v78, v79\n\tv_cvt_pk_f16_f32 v71, v80, v81\n\tv_cvt_pk_f16_f32 v72, v102, v103\n\tv_cvt_pk_f16_f32 v73, v104, v105\n\tv_pk_mul_f16 v78, v154, v108 op_sel:[1,1] op_sel_hi:[1,0] neg_lo:[0,1]\n\tv_pk_mul_f16 v79, v155, v109 op_sel:[1,1] op_sel_hi:[1,0] neg_lo:[0,1]\n\tv_pk_mul_f32 v[80:81], v[102:103], v[60:61] op_sel:[1,1] op_sel_hi:[1,0] neg_lo:[0,1]\n\tv_pk_mul_f32 v[82:83], v[104:105], v[62:63] op_sel:[1,1] op_sel_hi:[1,0] neg_lo:[0,1]\n\tds_write2_b64 %[b4], v[154:155], v[66:67] offset0:0 offset1:66\n\tv_pk_fma_f16 v78, v154, v108, v78 op_sel:[0,0,0] op_sel_hi:[0,1,1]\n\tv_pk_fma_f16 v79, v155, v109, v79 op_sel:[0,0,0] op_sel_hi:[0,1,1]\n\tv_pk_fma_f32 v[80:81], v[102:103], v[60:61], v[80:81] op_sel:[0,0,0] op_sel_hi:[0,1,1]\n\tv_pk_fma_f32 v[82:83], v[104:105], v[62:63], v[82:83] op_sel:[0,0,0] op_sel_hi:[0,1,1]\n\tv_pk_mul_f16 v74, v154, v106 op_sel:[1,1] op_sel_hi:[1,0] neg_lo:[0,1]\n\tv_pk_mul_f16 v75, v155, v107 op_sel:[1,1] op_sel_hi:[1,0] neg_lo:[0,1]\n\tv_pk_mul_f16 v84, v154, v64 op_sel:[1,1] op_sel_hi:[1,0] neg_lo:[0,1]\n\tv_pk_mul_f16 v85, v155, v65 op_sel:[1,1] op_sel_hi:[1,0] neg_lo:[0,1]\n\tds_write2_b64 %[b4], v[68:69], v[78:79] offset0:132 offset1:198\n\tv_pk_fma_f16 v74, v154, v106, v74 op_sel:[0,0,0] op_sel_hi:[0,1,1]\n\tv_pk_fma_f16 v75, v155, v107, v75 op_sel:[0,0,0] op_sel_hi:[0,1,1]\n\tv_pk_fma_f16 v84, v154, v64, v84 op_sel:[0,0,0] op_sel_hi:[0,1,1]\n\tv_pk_fma_f16 v85, v155, v65, v85 op_sel:[0,0,0] op_sel_hi:[0,1,1]\n\tv_cvt_pk_f16_f32 v66, v80, v81\n\tv_cvt_pk_f16_f32 v67, v82, v83\n\tv_pk_mul_f16 v80, v154, v132 op_sel:[1,1] op_sel_hi:[1,0] neg_lo:[0,1]\n\tv_pk_mul_f16 v81, v155, v133 op_sel:[1,1] op_sel_hi:[1,0] neg_lo:[0,1]\n\tv_pk_mul_f16 v82, v154, v152 op_sel:[1,1] op_sel_hi:[1,0] neg_lo:[0,1]\n\tv_pk_mul_f16 v83, v155, v153 op_sel:[1,1] op_sel_hi:[1,0] neg_lo:[0,1]\n\tds_write2_b64 %[b5], v[74:75], v[84:85] offset0:0 offset1:66\n\tv_pk_fma_f16 v80, v154, v132, v80 op_sel:[0,0,0] op_sel_hi:[0,1,1]\n\tv_pk_fma_f16 v81, v155, v133, v81 op_sel:[0,0,0] op_sel_hi:[0,1,1]\n\tv_pk_fma_f16 v82, v154, v152, v82 op_sel:[0,0,0] op_sel_hi:[0,1,1]\n\tv_pk_fma_f16 v83, v155, v153, v83 op_sel:[0,0,0] op_sel_hi:[0,1,1]\n\tv_pk_mul_f16 v64, v154, v134 op_sel:[1,1] op_sel_hi:[1,0] neg_lo:[0,1]\n\tv_pk_mul_f16 v65, v155, v135 op_sel:[1,1] op_sel_hi:[1,0] neg_lo:[0,1]\n\tv_pk_mul_f16 v68, v154, v76 op_sel:[1,1] op_sel_hi:[1,0] neg_lo:[0,1]\n\tv_pk_mul_f16 v69, v155, v77 op_sel:[1,1] op_sel_hi:[1,0] neg_lo:[0,1]\n\tds_write2_b64 %[b5], v[80:81], v[82:83] offset0:132 offset1:198\n\tv_pk_fma_f16 v64, v154, v134, v64 op_sel:[0,0,0] op_sel_hi:[0,1,1]\n\tv_pk_fma_f16 v65, v155, v135, v65 op_sel:[0,0,0] op_sel_hi:[0,1,1]\n\tv_pk_fma_f16 v68, v154, v76, v68 op_sel:[0,0,0] op_sel_hi:[0,1,1]\n\tv_pk_fma_f16 v69, v155, v77, v69 op_sel:[0,0,0] op_sel_hi:[0,1,1]\n\tv_pk_mul_f16 v74, v154, v140 op_sel:[1,1] op_sel_hi:[1,0] neg_lo:[0,1]\n\tv_pk_mul_f16 v75, v155, v141 op_sel:[1,1] op_sel_hi:[1,0] neg_lo:[0,1]\n\tv_pk_mul_f16 v78, v154, v142 op_sel:[1,1] op_sel_hi:[1,0] neg_lo:[0,1]\n\tv_pk_mul_f16 v79, v155, v143 op_sel:[1,1] op_sel_hi:[1,0] neg_lo:[0,1]\n\tds_write2_b64 %[b6], v[64:65], v[68:69] offset0:0 offset1:66\n\tv_pk_fma_f16 v74, v154, v140, v74 op_sel:[0,0,0] op_sel_hi:[0,1,1]\n\tv_pk_fma_f16 v75, v155, v141, v75 op_sel:[0,0,0] op_sel_hi:[0,1,1]\n\tv_pk_fma_f16 v78, v154, v142, v78 op_sel:[0,0,0] op_sel_hi:[0,1,1]\n\tv_pk_fma_f16 v79, v155, v143, v79 op_sel:[0,0,0] op_sel_hi:[0,1,1]\n\tv_pk_mul_f16 v76, v154, v92 op_sel:[1,1] op_sel_hi:[1,0] neg_lo:[0,1]\n\tv_pk_mul_f16 v77, v155, v93 op_sel:[1,1] op_sel_hi:[1,0] neg_lo:[0,1]\n\tv_pk_mul_f16 v80, v154, v94 op_sel:[1,1] op_sel_hi:[1,0] neg_lo:[0,1]\n\tv_pk_mul_f16 v81, v155, v95 op_sel:[1,1] op_sel_hi:[1,0] neg_lo:[0,1]\n\tds_write2_b64 %[b6], v[74:75], v[78:79] offset0:132 offset1:198\n\tv_pk_fma_f16 v76, v154, v92, v76 op_sel:[0,0,0] op_sel_hi:[0,1,1]\n\tv_pk_fma_f16 v77, v155, v93, v77 op_sel:[0,0,0] op_sel_hi:[0,1,1]\n\tv_pk_fma_f16 v80, v154, v94, v80 op_sel:[0,0,0] op_sel_hi:[0,1,1]\n\tv_pk_fma_f16 v81, v155, v95, v81 op_sel:[0,0,0] op_sel_hi:[0,1,1]\n\tv_pk_mul_f16 v64, v154, v90 op_sel:[1,1] op_sel_hi:[1,0] neg_lo:[0,1]\n\tv_pk_mul_f16 v65, v155, v91 op_sel:[1,1] op_sel_hi:[1,0] neg_lo:[0,1]\n\tv_pk_mul_f16 v68, v154, v110 op_sel:[1,1] op_sel_hi:[1,0] neg_lo:[0,1]\n\tv_pk_mul_f16 v69, v155, v111 op_sel:[1,1] op_sel_hi:[1,0] neg_lo:[0,1]\n\tds_write2_b64 %[b7], v[76:77], v[80:81] offset0:0 offset1:66\n\tv_pk_fma_f16 v64, v154, v90, v64 op_sel:[0,0,0] op_sel_hi:[0,1,1]\n\tv_pk_fma_f16 v65, v155, v91, v65 op_sel:[0,0,0] op_sel_hi:[0,1,1]\n\tv_pk_fma_f16 v68, v154, v110, v68 op_sel:[0,0,0] op_sel_hi:[0,1,1]\n\tv_pk_fma_f16 v69, v155, v111, v69 op_sel:[0,0,0] op_sel_hi:[0,1,1]\n\ts_nop 0\n\tds_write2_b64 %[b7], v[64:65], v[68:69] offset0:132 offset1:198\n\tds_write2_b64 %[b10], v[88:89], v[66:67] offset0:0 offset1:66\n\tds_write2_b64 %[b12], v[112:113], v[70:71] offset0:0 offset1:66\n\tds_write_b64 %[b13], v[72:73] offset:0"
#define KGEN_CLOBBER "v64", "v65", "v66", "v67", "v68", "v69", "v70", "v71", "v72", "v73", "v74", "v75", "v76", "v77", "v78", "v79", "v80", "v81", "v82", "v83", "v84", "v85", "v86", "v87", "v88", "v89", "v90", "v91", "v92", "v93", "v94", "v95", "v96", "v97", "v98", "v99", "v100", "v101", "v102", "v103", "v104", "v105", "v106", "v107", "v108", "v109", "v110", "v111", "v112", "v113", "v114", "v115", "v116", "v117", "v118", "v119", "v120", "v121", "v122", "v123", "v124", "v125", "v126", "v127", "v128", "v129", "v130", "v131", "v132", "v133", "v134", "v135", "v136", "v137", "v138", "v139", "v140", "v141", "v142", "v143", "v144", "v145", "v146", "v147", "v148", "v149", "v150", "v151", "v152", "v153", "v154", "v155"
#define KROW_0LO 0x2b2a2d2c23222120ull
#define KROW_0HI 0x202020202b343332ull
#define KROW_1LO 0x2524272a31302f2eull
#define KROW_1HI 0x2b2b2b2b20292826ull
#define KMFMA_ASM "ds_read_b128 v[64:67], %[ap] offset:0\n\tds_read_b128 v[68:71], %[bp] offset:0\n\tds_read_b128 v[72:75], %[ap] offset:32\n\tds_read_b128 v[76:79], %[bp] offset:32\n\tds_read_b128 v[80:83], %[ap] offset:64\n\tds_read_b128 v[84:87], %[bp] offset:64\n\tds_read_b128 v[88:91], %[ap] offset:96\n\tds_read_b128 v[92:95], %[bp] offset:96\n\ts_waitcnt lgkmcnt(6)\n\tv_mfma_f32_32x32x16_f16 v[48:63], v[64:67], v[68:71], 0\n\tds_read_b128 v[96:99], %[ap] offset:128\n\tds_read_b128 v[100:103], %[bp] offset:128\n\ts_waitcnt lgkmcnt(6)\n\tv_mfma_f32_32x32x16_f16 v[48:63], v[72:75], v[76:79], v[48:63]\n\tds_read_b128 v[104:107], %[ap] offset:160\n\tds_read_b128 v[108:111], %[bp] offset:160\n\ts_waitcnt lgkmcnt(6)\n\tv_mfma_f32_32x32x16_f16 v[48:63], v[80:83], v[84:87], v[48:63]\n\tds_read_b128 v[64:67], %[ap] offset:192\n\tds_read_b128 v[68:71], %[bp] offset:192\n\ts_waitcnt lgkmcnt(6)\n\tv_mfma_f32_32x32x16_f16 v[48:63], v[88:91], v[92:95], v[48:63]\n\tds_read_b128 v[72:75], %[ap] offset:224\n\tds_read_b128 v[76:79], %[bp] offset:224\n\ts_waitcnt lgkmcnt(6)\n\tv_mfma_f32_32x32x16_f16 v[48:63], v[96:99], v[100:103], v[48:63]\n\tds_read_b128 v[80:83], %[ap] offset:256\n\tds_read_b128 v[84:87], %[bp] offset:256\n\ts_waitcnt lgkmcnt(6)\n\tv_mfma_f32_32x32x16_f16 v[48:63], v[104:107], v[108:111], v[48:63]\n\tds_read_b128 v[88:91], %[ap] offset:288\n\tds_read_b128 v[92:95], %[bp] offset:288\n\ts_waitcnt lgkmcnt(6)\n\tv_mfma_f32_32x32x16_f16 v[48:63], v[64:67], v[68:71], v[48:63]\n\tds_read_b128 v[96:99], %[ap] offset:320\n\tds_read_b128 v[100:103], %[bp] offset:320\n\ts_waitcnt lgkmcnt(6)\n\tv_mfma_f32_32x32x16_f16 v[48:63], v[72:75], v[76:79], v[48:63]\n\tds_read_b128 v[104:107], %[ap] offset:352\n\tds_read_b128 v[108:111], %[bp] offset:352\n\ts_waitcnt lgkmcnt(6)\n\tv_mfma_f32_32x32x16_f16 v[48:63], v[80:83], v[84:87], v[48:63]\n\tds_read_b128 v[64:67], %[ap] offset:384\n\tds_read_b128 v[68:71], %[bp] offset:384\n\ts_waitcnt lgkmcnt(6)\n\tv_mfma_f32_32x32x16_f16 v[48:63], v[88:91], v[92:95], v[48:63]\n\tds_read_b128 v[72:75], %[ap] offset:416\n\tds_read_b128 v[76:79], %[bp] offset:416\n\ts_waitcnt lgkmcnt(6)\n\tv_mfma_f32_32x32x16_f16 v[48:63], v[96:99], v[100:103], v[48:63]\n\tds_read_b128 v[80:83], %[ap] offset:448\n\tds_read_b128 v[84:87], %[bp] offset:448\n\ts_waitcnt lgkmcnt(6)\n\tv_mfma_f32_32x32x16_f16 v[48:63], v[104:107], v[108:111], v[48:63]\n\tds_read_b128 v[88:91], %[ap] offset:480\n\tds_read_b128 v[92:95], %[bp] offset:480\n\ts_waitcnt lgkmcnt(6)\n\tv_mfma_f32_32x32x16_f16 v[48:63], v[64:67], v[68:71], v[48:63]\n\tds_read_b32 %[stg], %[ctl] offset:4\n\ts_waitcnt lgkmcnt(4)\n\tv_mfma_f32_32x32x16_f16 v[48:63], v[72:75], v[76:79], v[48:63]\n\ts_waitcnt lgkmcnt(2)\n\tv_mfma_f32_32x32x16_f16 v[48:63], v[80:83], v[84:87], v[48:63]\n\ts_waitcnt lgkmcnt(0)\n\tv_mfma_f32_32x32x16_f16 v[48:63], v[88:91], v[92:95], v[48:63]\n\ts_nop 7\n\ts_nop 3\n\ts_waitcnt lgkmcnt(0)"
#define KMFMA_CLOBBER "v64", "v65", "v66", "v67", "v68", "v69", "v70", "v71", "v72", "v73", "v74", "v75", "v76", "v77", "v78", "v79", "v80", "v81", "v82", "v83", "v84", "v85", "v86", "v87", "v88", "v89", "v90", "v91", "v92", "v93", "v94", "v95", "v96", "v97", "v98", "v99", "v100", "v101", "v102", "v103", "v104", "v105", "v106", "v107", "v108", "v109", "v110", "v111"
#define CONV_ASM ".p2align 3\n\tds_read_b64_tr_b16 v[52:53], %[a0] offset:456\n\tds_read_b64_tr_b16 v[54:55], %[a0] offset:448\n\tds_read_b64_tr_b16 v[84:85], %[a0] offset:968\n\tds_read_b64_tr_b16 v[86:87], %[a0] offset:960\n\tds_read_b64_tr_b16 v[48:49], %[a0] offset:392\n\tds_read_b64_tr_b16 v[50:51], %[a0] offset:384\n\tds_read_b64_tr_b16 v[80:81], %[a0] offset:904\n\tds_read_b64_tr_b16 v[82:83], %[a0] offset:896\n\tds_read_b64_tr_b16 v[44:45], %[a0] offset:328\n\tds_read_b64_tr_b16 v[46:47], %[a0] offset:320\n\tds_read_b64_tr_b16 v[76:77], %[a0] offset:840\n\tds_read_b64_tr_b16 v[78:79], %[a0] offset:832\n\tv_readfirstlane_b32 %[s], %[stg]\n\ts_mov_b32 %[c], 0x400000\n\ts_cmp_ge_u32 %[s], 4\n\ts_cbranch_scc1 2f\n\t1:\n\tds_read_b32 v216, %[ctl] offset:4\n\ts_waitcnt lgkmcnt(0)\n\tv_readfirstlane_b32 %[s], v216\n\ts_cmp_ge_u32 %[s], 4\n\ts_cbranch_scc1 2f\n\ts_sleep 1\n\ts_sub_u32 %[c], %[c], 1\n\ts_cbranch_scc0 1b\n\t.p2align 3\n\t2:\n\tds_read_b128 v[164:167], %[bb] offset:64\n\tds_read_b128 v[168:171], %[bb] offset:128\n\tds_read_b128 v[172:175], %[bb] offset:192\n\tds_read_b64_tr_b16 v[40:41], %[a0] offset:264\n\tds_read_b64_tr_b16 v[42:43], %[a0] offset:256\n\tds_read_b128 v[176:179], %[bb] offset:256\n\ts_waitcnt lgkmcnt(5)\n\ts_nop 0\n\tv_mfma_f32_16x16x32_bf16 v[132:135], v[52:55], v[164:167], 0\n\tds_read_b64_tr_b16 v[72:73], %[a0] offset:776\n\tds_read_b64_tr_b16 v[74:75], %[a0] offset:768\n\tds_read_b64_tr_b16 v[36:37], %[a0] offset:200\n\tv_mfma_f32_16x16x32_bf16 v[136:139], v[84:87], v[164:167], 0\n\tds_read_b64_tr_b16 v[38:39], %[a0] offset:192\n\ts_waitcnt lgkmcnt(8)\n\ts_nop 0\n\tv_mfma_f32_16x16x32_bf16 v[132:135], v[48:51], v[168:171], v[132:135]\n\tds_read_b128 v[180:183], %[bb] offset:320\n\tds_read_b64_tr_b16 v[68:69], %[a0] offset:712\n\tds_read_b64_tr_b16 v[70:71], %[a0] offset:704\n\tv_mfma_f32_16x16x32_bf16 v[136:139], v[80:83], v[168:171], v[136:139]\n\tds_read_b64_tr_b16 v[32:33], %[a0] offset:136\n\ts_waitcnt lgkmcnt(8)\n\ts_nop 0\n\tv_mfma_f32_16x16x32_bf16 v[132:135], v[44:47], v[172:175], v[132:135]\n\tds_read_b64_tr_b16 v[34:35], %[a0] offset:128\n\tds_read_b128 v[184:187], %[bb] offset:384\n\tds_read_b64_tr_b16 v[64:65], %[a0] offset:648\n\tv_mfma_f32_16x16x32_bf16 v[136:139], v[76:79], v[172:175], v[136:139]\n\tds_read_b64_tr_b16 v[66:67], %[a0] offset:640\n\tds_read_b64_tr_b16 v[28:29], %[a0] offset:72\n\tds_read_b64_tr_b16 v[30:31], %[a0] offset:64\n\tv_mfma_f32_16x16x32_bf16 v[132:135], v[40:43], v[176:179], v[132:135]\n\tds_read_b128 v[164:167], %[bb] offset:448\n\ts_waitcnt lgkmcnt(8)\n\ts_nop 0\n\tv_mfma_f32_16x16x32_bf16 v[136:139], v[72:75], v[176:179], v[136:139]\n\tds_read_b64_tr_b16 v[60:61], %[a0] offset:584\n\tds_read_b64_tr_b16 v[62:63], %[a0] offset:576\n\tds_read_b64_tr_b16 v[24:25], %[a0] offset:8\n\tv_mfma_f32_16x16x32_bf16 v[132:135], v[36:39], v[180:183], v[132:135]\n\tds_read_b64_tr_b16 v[26:27], %[a0] offset:0\n\tds_read_b128 v[168:171], %[bb] offset:512\n\tds_read_b64_tr_b16 v[56:57], %[a0] offset:520\n\tv_mfma_f32_16x16x32_bf16 v[136:139], v[68:71], v[180:183], v[136:139]\n\tds_read_b64_tr_b16 v[58:59], %[a0] offset:512\n\ts_waitcnt lgkmcnt(8)\n\ts_nop 0\n\tv_mfma_f32_16x16x32_bf16 v[132:135], v[32:35], v[184:187], v[132:135]\n\tds_read_b64_tr_b16 v[88:89], %[a0] offset:1032\n\tds_read_b64_tr_b16 v[90:91], %[a0] offset:1024\n\tds_read_b128 v[188:191], %[bb] offset:496\n\tv_mfma_f32_16x16x32_bf16 v[136:139], v[64:67], v[184:187], v[136:139]\n\tds_read_b128 v[172:175], %[bb] offset:576\n\ts_waitcnt lgkmcnt(8)\n\ts_nop 0\n\tv_mfma_f32_16x16x32_bf16 v[132:135], v[28:31], v[164:167], v[132:135]\n\tds_read_b128 v[176:179], %[bb] offset:640\n\tv_mfma_f32_16x16x32_bf16 v[136:139], v[60:63], v[164:167], v[136:139]\n\ts_waitcnt lgkmcnt(5)\n\ts_nop 0\n\tv_mfma_f32_16x16x32_bf16 v[132:135], v[24:27], v[168:171], v[132:135]\n\tds_write_b16 %[ta], %[t78]\n\tds_read_b128 v[180:183], %[bb] offset:704\n\tv_mfma_f32_16x16x32_bf16 v[136:139], v[56:59], v[168:171], v[136:139]\n\ts_waitcnt lgkmcnt(3)\n\ts_nop 0\n\tv_mfma_f32_16x16x32_bf16 v[140:143], v[88:91], v[168:171], 0\n\tds_write_b16_d16_hi %[ta], %[t78] offset:64\n\tds_read_b128 v[184:187], %[bb] offset:768\n\tv_mfma_f32_16x16x32_bf16 v[204:207], v[188:191], v[4:7], 0\n\tds_write_b128 %[yb], v[132:135] offset:0\n\tv_mfma_f32_16x16x32_bf16 v[218:221], v[168:171], v[4:7], 0\n\tds_write_b16 %[ta], %[t78] offset:388\n\tds_read_b128 v[164:167], %[bb] offset:832\n\tv_mfma_f32_16x16x32_bf16 v[208:211], v[188:191], v[8:11], 0\n\tds_write_b8 %[ctc], %[one] offset:0\n\tds_write_b16_d16_hi %[ta], %[t78] offset:452\n\tv_mfma_f32_16x16x32_bf16 v[222:225], v[168:171], v[8:11], 0\n\tds_write_b16 %[tb], %[t9a]\n\tv_mfma_f32_16x16x32_bf16 v[212:215], v[188:191], v[12:15], 0\n\tds_write_b16_d16_hi %[tb], %[t9a] offset:128\n\tv_cvt_pk_bf16_f32 v192, v221, v207\n\tv_mfma_f32_16x16x32_bf16 v[226:229], v[168:171], v[16:19], 0\n\tds_write_b16 %[tb], %[t9a] offset:388\n\tv_cvt_pk_bf16_f32 v193, v220, v206\n\tv_mfma_f32_16x16x32_bf16 v[148:151], v[20:23], v[168:171], 0\n\tds_write_b16_d16_hi %[tb], %[t9a] offset:516\n\tds_read_b128 v[168:171], %[bb] offset:896\n\tv_mfma_f32_16x16x32_bf16 v[136:139], v[52:55], v[172:175], v[136:139]\n\tds_read_b64 v[92:93], %[tp] offset:0\n\tds_read_b64 v[94:95], %[tp] offset:8\n\tds_read_b64 v[96:97], %[tp] offset:64\n\tv_mfma_f32_16x16x32_bf16 v[140:143], v[84:87], v[172:175], v[140:143]\n\tds_read_b128 v[172:175], %[bb] offset:960\n\ts_waitcnt lgkmcnt(7)\n\ts_nop 0\n\tv_mfma_f32_16x16x32_bf16 v[136:139], v[48:51], v[176:179], v[136:139]\n\tds_read_b64 v[98:99], %[tp] offset:72\n\tds_read_b64 v[100:101], %[tp] offset:128\n\tds_read_b64 v[102:103], %[tp] offset:136\n\tv_mfma_f32_16x16x32_bf16 v[140:143], v[80:83], v[176:179], v[140:143]\n\tds_read_b128 v[176:179], %[bb] offset:1024\n\tds_read_b64 v[104:105], %[tp] offset:768\n\tds_read_b64 v[106:107], %[tp] offset:776\n\tv_mfma_f32_16x16x32_bf16 v[136:139], v[44:47], v[180:183], v[136:139]\n\tds_read_b64 v[108:109], %[tp] offset:832\n\tds_read_b64 v[110:111], %[tp] offset:840\n\tds_read_b64 v[112:113], %[tp] offset:896\n\tv_mfma_f32_16x16x32_bf16 v[140:143], v[76:79], v[180:183], v[140:143]\n\tds_read_b64 v[114:115], %[tp] offset:904\n\tds_read_b64 v[116:117], %[tp] offset:1536\n\tds_read_b64 v[118:119], %[tp] offset:1544\n\tv_mfma_f32_16x16x32_bf16 v[136:139], v[40:43], v[184:187], v[136:139]\n\tds_read_b64 v[120:121], %[tp] offset:1600\n\tds_read_b64 v[122:123], %[tp] offset:1608\n\tv_cvt_pk_bf16_f32 v194, v219, v205\n\tv_mfma_f32_16x16x32_bf16 v[140:143], v[72:75], v[184:187], v[140:143]\n\tv_cvt_pk_bf16_f32 v195, v218, v204\n\tds_read_b64 v[124:125], %[tp] offset:1664\n\tds_read_b64 v[126:127], %[tp] offset:1672\n\tv_mfma_f32_16x16x32_bf16 v[136:139], v[36:39], v[164:167], v[136:139]\n\tds_read_b64 v[128:129], %[tp] offset:1728\n\tds_read_b64 v[130:131], %[tp] offset:1736\n\tv_cvt_pk_bf16_f32 v196, v225, v211\n\tv_mfma_f32_16x16x32_bf16 v[140:143], v[68:71], v[164:167], v[140:143]\n\tv_cvt_pk_bf16_f32 v197, v224, v210\n\ts_waitcnt lgkmcnt(6)\n\ts_nop 0\n\tv_mfma_f32_16x16x32_bf16 v[136:139], v[32:35], v[168:171], v[136:139]\n\tv_cvt_pk_bf16_f32 v198, v223, v209\n\tv_mfma_f32_16x16x32_bf16 v[140:143], v[64:67], v[168:171], v[140:143]\n\tv_cvt_pk_bf16_f32 v199, v222, v208\n\tv_mfma_f32_16x16x32_bf16 v[136:139], v[28:31], v[172:175], v[136:139]\n\tv_cvt_pk_bf16_f32 v200, v229, v215\n\tv_mfma_f32_16x16x32_bf16 v[140:143], v[60:63], v[172:175], v[140:143]\n\tds_read_b128 v[188:191], %[bb] offset:1008\n\tv_cvt_pk_bf16_f32 v201, v228, v214\n\tv_mfma_f32_16x16x32_bf16 v[136:139], v[24:27], v[176:179], v[136:139]\n\tv_cvt_pk_bf16_f32 v202, v227, v213\n\tv_mfma_f32_16x16x32_bf16 v[140:143], v[56:59], v[176:179], v[140:143]\n\tv_cvt_pk_bf16_f32 v203, v226, v212\n\tv_mfma_f32_16x16x32_bf16 v[144:147], v[88:91], v[176:179], 0\n\tv_mfma_f32_16x16x32_bf16 v[140:143], v[192:195], v[92:95], v[140:143]\n\tv_mfma_f32_16x16x32_bf16 v[144:147], v[192:195], v[96:99], v[144:147]\n\tv_mfma_f32_16x16x32_bf16 v[148:151], v[192:195], v[100:103], v[148:151]\n\tv_mfma_f32_16x16x32_bf16 v[140:143], v[196:199], v[104:107], v[140:143]\n\tds_write_b128 %[yb], v[136:139] offset:1152\n\tds_read_b128 v[180:183], %[bb] offset:1088\n\tv_mfma_f32_16x16x32_bf16 v[144:147], v[196:199], v[108:111], v[144:147]\n\tv_mfma_f32_16x16x32_bf16 v[148:151], v[196:199], v[112:115], v[148:151]\n\tds_write_b8 %[ctc], %[one] offset:64\n\tds_read_b128 v[184:187], %[bb] offset:1152\n\tv_mfma_f32_16x16x32_bf16 v[148:151], v[200:203], v[116:119], v[148:151]\n\ts_waitcnt lgkmcnt(4)\n\ts_nop 0\n\tv_mfma_f32_16x16x32_bf16 v[152:155], v[200:203], v[120:123], 0\n\tds_read_b128 v[164:167], %[bb] offset:1216\n\tv_mfma_f32_16x16x32_bf16 v[156:159], v[200:203], v[124:127], 0\n\tv_mfma_f32_16x16x32_bf16 v[160:163], v[200:203], v[128:131], 0\n\tds_read_b128 v[168:171], %[bb] offset:1280\n\tv_mfma_f32_16x16x32_bf16 v[204:207], v[188:191], v[4:7], 0\n\tv_mfma_f32_16x16x32_bf16 v[218:221], v[176:179], v[4:7], 0\n\tds_read_b128 v[172:175], %[bb] offset:1344\n\tv_mfma_f32_16x16x32_bf16 v[208:211], v[188:191], v[8:11], 0\n\tv_mfma_f32_16x16x32_bf16 v[222:225], v[176:179], v[8:11], 0\n\tv_mfma_f32_16x16x32_bf16 v[212:215], v[188:191], v[12:15], 0\n\tv_mfma_f32_16x16x32_bf16 v[226:229], v[176:179], v[16:19], 0\n\tv_mfma_f32_16x16x32_bf16 v[152:155], v[20:23], v[176:179], v[152:155]\n\ts_waitcnt lgkmcnt(0)\n\ts_nop 0\n\tv_mfma_f32_16x16x32_bf16 v[140:143], v[52:55], v[180:183], v[140:143]\n\tds_read_b128 v[176:179], %[bb] offset:1408\n\tv_cvt_pk_bf16_f32 v192, v221, v207\n\tv_mfma_f32_16x16x32_bf16 v[144:147], v[84:87], v[180:183], v[144:147]\n\tds_read_b128 v[180:183], %[bb] offset:1472\n\tv_cvt_pk_bf16_f32 v193, v220, v206\n\tv_mfma_f32_16x16x32_bf16 v[140:143], v[48:51], v[184:187], v[140:143]\n\tv_cvt_pk_bf16_f32 v194, v219, v205\n\tv_mfma_f32_16x16x32_bf16 v[144:147], v[80:83], v[184:187], v[144:147]\n\tds_read_b128 v[184:187], %[bb] offset:1536\n\tv_cvt_pk_bf16_f32 v195, v218, v204\n\tv_mfma_f32_16x16x32_bf16 v[140:143], v[44:47], v[164:167], v[140:143]\n\tv_cvt_pk_bf16_f32 v196, v225, v211\n\tv_mfma_f32_16x16x32_bf16 v[144:147], v[76:79], v[164:167], v[144:147]\n\tv_cvt_pk_bf16_f32 v197, v224, v210\n\tv_mfma_f32_16x16x32_bf16 v[140:143], v[40:43], v[168:171], v[140:143]\n\tv_cvt_pk_bf16_f32 v198, v223, v209\n\tv_mfma_f32_16x16x32_bf16 v[144:147], v[72:75], v[168:171], v[144:147]\n\tv_cvt_pk_bf16_f32 v199, v222, v208\n\tv_mfma_f32_16x16x32_bf16 v[140:143], v[36:39], v[172:175], v[140:143]\n\tv_cvt_pk_bf16_f32 v200, v229, v215\n\tv_mfma_f32_16x16x32_bf16 v[144:147], v[68:71], v[172:175], v[144:147]\n\ts_waitcnt lgkmcnt(0)\n\ts_nop 0\n\tv_mfma_f32_16x16x32_bf16 v[140:143], v[32:35], v[176:179], v[140:143]\n\tv_cvt_pk_bf16_f32 v201, v228, v214\n\tv_mfma_f32_16x16x32_bf16 v[144:147], v[64:67], v[176:179], v[144:147]\n\tv_cvt_pk_bf16_f32 v202, v227, v213\n\tv_mfma_f32_16x16x32_bf16 v[140:143], v[28:31], v[180:183], v[140:143]\n\tds_read_b128 v[188:191], %[bb] offset:1520\n\tv_cvt_pk_bf16_f32 v203, v226, v212\n\tv_mfma_f32_16x16x32_bf16 v[144:147], v[60:63], v[180:183], v[144:147]\n\tv_mfma_f32_16x16x32_bf16 v[140:143], v[24:27], v[184:187], v[140:143]\n\tv_mfma_f32_16x16x32_bf16 v[144:147], v[56:59], v[184:187], v[144:147]\n\tv_mfma_f32_16x16x32_bf16 v[148:151], v[88:91], v[184:187], v[148:151]\n\tv_mfma_f32_16x16x32_bf16 v[144:147], v[192:195], v[92:95], v[144:147]\n\tv_mfma_f32_16x16x32_bf16 v[148:151], v[192:195], v[96:99], v[148:151]\n\tv_mfma_f32_16x16x32_bf16 v[152:155], v[192:195], v[100:103], v[152:155]\n\tds_read_b128 v[164:167], %[bb] offset:1600\n\tv_mfma_f32_16x16x32_bf16 v[144:147], v[196:199], v[104:107], v[144:147]\n\tv_mfma_f32_16x16x32_bf16 v[148:151], v[196:199], v[108:111], v[148:151]\n\tds_write_b128 %[yb], v[140:143] offset:2304\n\tv_mfma_f32_16x16x32_bf16 v[152:155], v[196:199], v[112:115], v[152:155]\n\tds_read_b128 v[168:171], %[bb] offset:1664\n\tv_mfma_f32_16x16x32_bf16 v[152:155], v[200:203], v[116:119], v[152:155]\n\tds_write_b8 %[ctc], %[one] offset:128\n\tds_read_b128 v[172:175], %[bb] offset:1728\n\tv_mfma_f32_16x16x32_bf16 v[156:159], v[200:203], v[120:123], v[156:159]\n\tv_mfma_f32_16x16x32_bf16 v[160:163], v[200:203], v[124:127], v[160:163]\n\ts_waitcnt lgkmcnt(3)\n\ts_nop 0\n\tv_mfma_f32_16x16x32_bf16 v[204:207], v[188:191], v[4:7], 0\n\tds_read_b128 v[176:179], %[bb] offset:1792\n\tv_mfma_f32_16x16x32_bf16 v[218:221], v[184:187], v[4:7], 0\n\tds_read_b128 v[180:183], %[bb] offset:1856\n\tv_mfma_f32_16x16x32_bf16 v[208:211], v[188:191], v[8:11], 0\n\tv_mfma_f32_16x16x32_bf16 v[222:225], v[184:187], v[8:11], 0\n\tv_mfma_f32_16x16x32_bf16 v[212:215], v[188:191], v[12:15], 0\n\tv_mfma_f32_16x16x32_bf16 v[226:229], v[184:187], v[16:19], 0\n\tv_mfma_f32_16x16x32_bf16 v[156:159], v[20:23], v[184:187], v[156:159]\n\tds_read_b128 v[184:187], %[bb] offset:1920\n\tv_mfma_f32_16x16x32_bf16 v[144:147], v[52:55], v[164:167], v[144:147]\n\tv_cvt_pk_bf16_f32 v192, v221, v207\n\tv_mfma_f32_16x16x32_bf16 v[148:151], v[84:87], v[164:167], v[148:151]\n\ts_waitcnt lgkmcnt(1)\n\ts_nop 0\n\tv_mfma_f32_16x16x32_bf16 v[144:147], v[48:51], v[168:171], v[144:147]\n\tds_read_b128 v[164:167], %[bb] offset:1984\n\tv_cvt_pk_bf16_f32 v193, v220, v206\n\tv_mfma_f32_16x16x32_bf16 v[148:151], v[80:83], v[168:171], v[148:151]\n\tds_read_b128 v[168:171], %[bb] offset:2048\n\tv_cvt_pk_bf16_f32 v194, v219, v205\n\tv_mfma_f32_16x16x32_bf16 v[144:147], v[44:47], v[172:175], v[144:147]\n\tds_read_b128 v[188:191], %[bb] offset:2032\n\tv_cvt_pk_bf16_f32 v195, v218, v204\n\tv_mfma_f32_16x16x32_bf16 v[148:151], v[76:79], v[172:175], v[148:151]\n\tv_cvt_pk_bf16_f32 v196, v225, v211\n\tv_mfma_f32_16x16x32_bf16 v[144:147], v[40:43], v[176:179], v[144:147]\n\tv_cvt_pk_bf16_f32 v197, v224, v210\n\tv_mfma_f32_16x16x32_bf16 v[148:151], v[72:75], v[176:179], v[148:151]\n\tv_cvt_pk_bf16_f32 v198, v223, v209\n\tv_mfma_f32_16x16x32_bf16 v[144:147], v[36:39], v[180:183], v[144:147]\n\tv_cvt_pk_bf16_f32 v199, v222, v208\n\tv_mfma_f32_16x16x32_bf16 v[148:151], v[68:71], v[180:183], v[148:151]\n\ts_waitcnt lgkmcnt(0)\n\ts_nop 0\n\tv_mfma_f32_16x16x32_bf16 v[144:147], v[32:35], v[184:187], v[144:147]\n\tv_cvt_pk_bf16_f32 v200, v229, v215\n\tv_mfma_f32_16x16x32_bf16 v[148:151], v[64:67], v[184:187], v[148:151]\n\tv_cvt_pk_bf16_f32 v201, v228, v214\n\tv_mfma_f32_16x16x32_bf16 v[144:147], v[28:31], v[164:167], v[144:147]\n\tv_cvt_pk_bf16_f32 v202, v227, v213\n\tv_mfma_f32_16x16x32_bf16 v[148:151], v[60:63], v[164:167], v[148:151]\n\tv_cvt_pk_bf16_f32 v203, v226, v212\n\tv_mfma_f32_16x16x32_bf16 v[144:147], v[24:27], v[168:171], v[144:147]\n\tv_mfma_f32_16x16x32_bf16 v[148:151], v[56:59], v[168:171], v[148:151]\n\tv_mfma_f32_16x16x32_bf16 v[152:155], v[88:91], v[168:171], v[152:155]\n\tv_mfma_f32_16x16x32_bf16 v[204:207], v[188:191], v[4:7], 0\n\tv_mfma_f32_16x16x32_bf16 v[218:221], v[168:171], v[4:7], 0\n\tv_mfma_f32_16x16x32_bf16 v[208:211], v[188:191], v[8:11], 0\n\tv_mfma_f32_16x16x32_bf16 v[222:225], v[168:171], v[8:11], 0\n\tv_mfma_f32_16x16x32_bf16 v[212:215], v[188:191], v[12:15], 0\n\tv_mfma_f32_16x16x32_bf16 v[226:229], v[168:171], v[16:19], 0\n\tds_write_b128 %[yb], v[144:147] offset:3456\n\tv_mfma_f32_16x16x32_bf16 v[160:163], v[20:23], v[168:171], v[160:163]\n\tds_read_b32 v216, %[ctl] offset:20\n\tv_mfma_f32_16x16x32_bf16 v[148:151], v[192:195], v[92:95], v[148:151]\n\tds_write_b8 %[ctc], %[one] offset:192\n\tv_mfma_f32_16x16x32_bf16 v[152:155], v[192:195], v[96:99], v[152:155]\n\tv_mfma_f32_16x16x32_bf16 v[156:159], v[192:195], v[100:103], v[156:159]\n\tv_mfma_f32_16x16x32_bf16 v[148:151], v[196:199], v[104:107], v[148:151]\n\tv_cvt_pk_bf16_f32 v192, v221, v207\n\tv_mfma_f32_16x16x32_bf16 v[152:155], v[196:199], v[108:111], v[152:155]\n\tv_cvt_pk_bf16_f32 v193, v220, v206\n\tv_mfma_f32_16x16x32_bf16 v[156:159], v[196:199], v[112:115], v[156:159]\n\tv_cvt_pk_bf16_f32 v194, v219, v205\n\tv_mfma_f32_16x16x32_bf16 v[156:159], v[200:203], v[116:119], v[156:159]\n\tv_cvt_pk_bf16_f32 v195, v218, v204\n\tv_mfma_f32_16x16x32_bf16 v[160:163], v[200:203], v[120:123], v[160:163]\n\ts_waitcnt lgkmcnt(0)\n\ts_nop 0\n\tv_readfirstlane_b32 %[s], v216\n\ts_mov_b32 %[c], 0x400000\n\ts_cmp_ge_u32 %[s], 2\n\ts_cbranch_scc1 2f\n\t1:\n\tds_read_b32 v216, %[ctl] offset:20\n\ts_waitcnt lgkmcnt(0)\n\tv_readfirstlane_b32 %[s], v216\n\ts_cmp_ge_u32 %[s], 2\n\ts_cbranch_scc1 2f\n\ts_sleep 1\n\ts_sub_u32 %[c], %[c], 1\n\ts_cbranch_scc0 1b\n\t.p2align 3\n\t2:\n\tv_mfma_f32_16x16x32_bf16 v[152:155], v[192:195], v[92:95], v[152:155]\n\tds_read_b128 v[172:175], %[bb] offset:2112\n\tv_cvt_pk_bf16_f32 v196, v225, v211\n\tv_mfma_f32_16x16x32_bf16 v[156:159], v[192:195], v[96:99], v[156:159]\n\tds_read_b128 v[176:179], %[bb] offset:2176\n\tv_cvt_pk_bf16_f32 v197, v224, v210\n\tv_mfma_f32_16x16x32_bf16 v[160:163], v[192:195], v[100:103], v[160:163]\n\tv_cvt_pk_bf16_f32 v198, v223, v209\n\tds_read_b128 v[180:183], %[bb] offset:2240\n\tv_cvt_pk_bf16_f32 v199, v222, v208\n\ts_nop 1\n\ts_nop 0\n\tv_mfma_f32_16x16x32_bf16 v[152:155], v[196:199], v[104:107], v[152:155]\n\tds_read_b128 v[184:187], %[bb] offset:2304\n\tv_cvt_pk_bf16_f32 v200, v229, v215\n\tv_mfma_f32_16x16x32_bf16 v[156:159], v[196:199], v[108:111], v[156:159]\n\tv_cvt_pk_bf16_f32 v201, v228, v214\n\tds_read_b128 v[164:167], %[bb] offset:2368\n\tv_mfma_f32_16x16x32_bf16 v[160:163], v[196:199], v[112:115], v[160:163]\n\tv_cvt_pk_bf16_f32 v202, v227, v213\n\tds_read_b128 v[168:171], %[bb] offset:2432\n\tv_cvt_pk_bf16_f32 v203, v226, v212\n\ts_nop 1\n\ts_nop 0\n\tv_mfma_f32_16x16x32_bf16 v[160:163], v[200:203], v[116:119], v[160:163]\n\ts_waitcnt lgkmcnt(3)\n\ts_nop 0\n\tv_mfma_f32_16x16x32_bf16 v[148:151], v[52:55], v[172:175], v[148:151]\n\tv_mfma_f32_16x16x32_bf16 v[152:155], v[84:87], v[172:175], v[152:155]\n\tds_read_b128 v[172:175], %[bb] offset:2496\n\tv_mfma_f32_16x16x32_bf16 v[148:151], v[48:51], v[176:179], v[148:151]\n\tv_mfma_f32_16x16x32_bf16 v[152:155], v[80:83], v[176:179], v[152:155]\n\tds_read_b128 v[176:179], %[bb] offset:2560\n\tv_mfma_f32_16x16x32_bf16 v[148:151], v[44:47], v[180:183], v[148:151]\n\tds_read_b128 v[188:191], %[bb] offset:2544\n\tv_mfma_f32_16x16x32_bf16 v[152:155], v[76:79], v[180:183], v[152:155]\n\ts_waitcnt lgkmcnt(2)\n\ts_nop 0\n\tv_mfma_f32_16x16x32_bf16 v[148:151], v[40:43], v[184:187], v[148:151]\n\tv_mfma_f32_16x16x32_bf16 v[152:155], v[72:75], v[184:187], v[152:155]\n\tds_read_b128 v[180:183], %[bb] offset:2624\n\tv_mfma_f32_16x16x32_bf16 v[148:151], v[36:39], v[164:167], v[148:151]\n\tv_mfma_f32_16x16x32_bf16 v[152:155], v[68:71], v[164:167], v[152:155]\n\tds_read_b128 v[184:187], %[bb] offset:2688\n\tv_mfma_f32_16x16x32_bf16 v[148:151], v[32:35], v[168:171], v[148:151]\n\tv_mfma_f32_16x16x32_bf16 v[152:155], v[64:67], v[168:171], v[152:155]\n\tds_read_b128 v[164:167], %[bb] offset:2752\n\tv_mfma_f32_16x16x32_bf16 v[148:151], v[28:31], v[172:175], v[148:151]\n\tv_mfma_f32_16x16x32_bf16 v[152:155], v[60:63], v[172:175], v[152:155]\n\ts_waitcnt lgkmcnt(1)\n\ts_nop 0\n\tv_mfma_f32_16x16x32_bf16 v[148:151], v[24:27], v[176:179], v[148:151]\n\tds_read_b128 v[168:171], %[bb] offset:2816\n\tv_mfma_f32_16x16x32_bf16 v[152:155], v[56:59], v[176:179], v[152:155]\n\tds_read_b128 v[172:175], %[bb] offset:2880\n\tv_mfma_f32_16x16x32_bf16 v[156:159], v[88:91], v[176:179], v[156:159]\n\tv_mfma_f32_16x16x32_bf16 v[204:207], v[188:191], v[4:7], 0\n\tv_mfma_f32_16x16x32_bf16 v[218:221], v[176:179], v[4:7], 0\n\tv_mfma_f32_16x16x32_bf16 v[208:211], v[188:191], v[8:11], 0\n\tv_mfma_f32_16x16x32_bf16 v[222:225], v[176:179], v[8:11], 0\n\tds_write_b128 %[yb], v[148:151] offset:4608\n\tv_mfma_f32_16x16x32_bf16 v[152:155], v[52:55], v[180:183], v[152:155]\n\tds_read_b128 v[176:179], %[bb] offset:2944\n\tv_mfma_f32_16x16x32_bf16 v[156:159], v[84:87], v[180:183], v[156:159]\n\tds_write_b8 %[ctc], %[one] offset:256\n\tds_read_b128 v[180:183], %[bb] offset:3008\n\tv_mfma_f32_16x16x32_bf16 v[152:155], v[48:51], v[184:187], v[152:155]\n\tv_cvt_pk_bf16_f32 v192, v221, v207\n\tv_mfma_f32_16x16x32_bf16 v[156:159], v[80:83], v[184:187], v[156:159]\n\ts_waitcnt lgkmcnt(3)\n\ts_nop 0\n\tv_mfma_f32_16x16x32_bf16 v[152:155], v[44:47], v[164:167], v[152:155]\n\tds_read_b128 v[184:187], %[bb] offset:3072\n\tv_cvt_pk_bf16_f32 v193, v220, v206\n\tv_mfma_f32_16x16x32_bf16 v[156:159], v[76:79], v[164:167], v[156:159]\n\tv_cvt_pk_bf16_f32 v194, v219, v205\n\tv_mfma_f32_16x16x32_bf16 v[152:155], v[40:43], v[168:171], v[152:155]\n\tv_cvt_pk_bf16_f32 v195, v218, v204\n\tv_mfma_f32_16x16x32_bf16 v[156:159], v[72:75], v[168:171], v[156:159]\n\tds_read_b128 v[188:191], %[bb] offset:3056\n\tv_cvt_pk_bf16_f32 v196, v225, v211\n\tv_mfma_f32_16x16x32_bf16 v[152:155], v[36:39], v[172:175], v[152:155]\n\tv_cvt_pk_bf16_f32 v197, v224, v210\n\tv_mfma_f32_16x16x32_bf16 v[156:159], v[68:71], v[172:175], v[156:159]\n\ts_waitcnt lgkmcnt(1)\n\ts_nop 0\n\tv_mfma_f32_16x16x32_bf16 v[152:155], v[32:35], v[176:179], v[152:155]\n\tds_read_b32 v216, %[ctl] offset:24\n\tv_cvt_pk_bf16_f32 v198, v223, v209\n\tv_mfma_f32_16x16x32_bf16 v[156:159], v[64:67], v[176:179], v[156:159]\n\tv_cvt_pk_bf16_f32 v199, v222, v208\n\tv_mfma_f32_16x16x32_bf16 v[152:155], v[28:31], v[180:183], v[152:155]\n\tv_mfma_f32_16x16x32_bf16 v[156:159], v[60:63], v[180:183], v[156:159]\n\tv_mfma_f32_16x16x32_bf16 v[152:155], v[24:27], v[184:187], v[152:155]\n\tv_mfma_f32_16x16x32_bf16 v[156:159], v[56:59], v[184:187], v[156:159]\n\tv_mfma_f32_16x16x32_bf16 v[160:163], v[88:91], v[184:187], v[160:163]\n\tv_mfma_f32_16x16x32_bf16 v[156:159], v[192:195], v[92:95], v[156:159]\n\ts_waitcnt lgkmcnt(0)\n\ts_nop 0\n\tv_readfirstlane_b32 %[s], v216\n\ts_mov_b32 %[c], 0x400000\n\ts_cmp_ge_u32 %[s], 2\n\ts_cbranch_scc1 2f\n\t1:\n\tds_read_b32 v216, %[ctl] offset:24\n\ts_waitcnt lgkmcnt(0)\n\tv_readfirstlane_b32 %[s], v216\n\ts_cmp_ge_u32 %[s], 2\n\ts_cbranch_scc1 2f\n\ts_sleep 1\n\ts_sub_u32 %[c], %[c], 1\n\ts_cbranch_scc0 1b\n\t.p2align 3\n\t2:\n\tv_mfma_f32_16x16x32_bf16 v[160:163], v[192:195], v[96:99], v[160:163]\n\tds_write_b128 %[yb], v[152:155] offset:5760\n\tv_mfma_f32_16x16x32_bf16 v[156:159], v[196:199], v[104:107], v[156:159]\n\tds_read_b128 v[164:167], %[bb] offset:3136\n\tv_mfma_f32_16x16x32_bf16 v[160:163], v[196:199], v[108:111], v[160:163]\n\tds_write_b8 %[ctc], %[one] offset:320\n\tds_read_b128 v[168:171], %[bb] offset:3200\n\tv_mfma_f32_16x16x32_bf16 v[204:207], v[188:191], v[4:7], 0\n\tds_read_b128 v[172:175], %[bb] offset:3264\n\tv_mfma_f32_16x16x32_bf16 v[218:221], v[184:187], v[4:7], 0\n\tds_read_b128 v[176:179], %[bb] offset:3328\n\tv_mfma_f32_16x16x32_bf16 v[208:211], v[188:191], v[8:11], 0\n\tds_read_b128 v[180:183], %[bb] offset:3392\n\tv_mfma_f32_16x16x32_bf16 v[222:225], v[184:187], v[8:11], 0\n\ts_waitcnt lgkmcnt(3)\n\ts_nop 0\n\tv_mfma_f32_16x16x32_bf16 v[156:159], v[52:55], v[164:167], v[156:159]\n\tds_read_b128 v[184:187], %[bb] offset:3456\n\tv_cvt_pk_bf16_f32 v192, v221, v207\n\tv_mfma_f32_16x16x32_bf16 v[160:163], v[84:87], v[164:167], v[160:163]\n\tds_read_b128 v[164:167], %[bb] offset:3520\n\tv_cvt_pk_bf16_f32 v193, v220, v206\n\tv_mfma_f32_16x16x32_bf16 v[156:159], v[48:51], v[168:171], v[156:159]\n\tv_cvt_pk_bf16_f32 v194, v219, v205\n\tv_mfma_f32_16x16x32_bf16 v[160:163], v[80:83], v[168:171], v[160:163]\n\ts_waitcnt lgkmcnt(2)\n\ts_nop 0\n\tv_mfma_f32_16x16x32_bf16 v[156:159], v[44:47], v[172:175], v[156:159]\n\tds_read_b128 v[168:171], %[bb] offset:3584\n\tv_cvt_pk_bf16_f32 v195, v218, v204\n\tv_mfma_f32_16x16x32_bf16 v[160:163], v[76:79], v[172:175], v[160:163]\n\tds_read_b128 v[172:175], %[bb] offset:3648\n\tv_cvt_pk_bf16_f32 v196, v225, v211\n\tv_mfma_f32_16x16x32_bf16 v[156:159], v[40:43], v[176:179], v[156:159]\n\tglobal_load_dwordx2 v[240:241], %[so], %[ws] sc1\n\tv_cvt_pk_bf16_f32 v197, v224, v210\n\tv_mfma_f32_16x16x32_bf16 v[160:163], v[72:75], v[176:179], v[160:163]\n\tds_read_b128 v[176:179], %[bb] offset:3712\n\tv_cvt_pk_bf16_f32 v198, v223, v209\n\tv_mfma_f32_16x16x32_bf16 v[156:159], v[36:39], v[180:183], v[156:159]\n\tv_cvt_pk_bf16_f32 v199, v222, v208\n\tv_mfma_f32_16x16x32_bf16 v[160:163], v[68:71], v[180:183], v[160:163]\n\ts_waitcnt lgkmcnt(1)\n\ts_nop 0\n\tv_mfma_f32_16x16x32_bf16 v[156:159], v[32:35], v[184:187], v[156:159]\n\tds_read_b128 v[180:183], %[bb] offset:3776\n\tv_mfma_f32_16x16x32_bf16 v[160:163], v[64:67], v[184:187], v[160:163]\n\tds_read_b128 v[184:187], %[bb] offset:3840\n\tv_mfma_f32_16x16x32_bf16 v[156:159], v[28:31], v[164:167], v[156:159]\n\tv_mfma_f32_16x16x32_bf16 v[160:163], v[60:63], v[164:167], v[160:163]\n\tds_read_b128 v[164:167], %[bb] offset:3904\n\tv_mfma_f32_16x16x32_bf16 v[156:159], v[24:27], v[168:171], v[156:159]\n\tv_mfma_f32_16x16x32_bf16 v[160:163], v[56:59], v[168:171], v[160:163]\n\tds_read_b128 v[168:171], %[bb] offset:3968\n\tv_mfma_f32_16x16x32_bf16 v[160:163], v[192:195], v[92:95], v[160:163]\n\tv_mfma_f32_16x16x32_bf16 v[160:163], v[196:199], v[104:107], v[160:163]\n\tv_mfma_f32_16x16x32_bf16 v[160:163], v[52:55], v[172:175], v[160:163]\n\ts_waitcnt lgkmcnt(1)\n\ts_nop 0\n\tv_mfma_f32_16x16x32_bf16 v[160:163], v[48:51], v[176:179], v[160:163]\n\tds_write_b128 %[yb], v[156:159] offset:6912\n\tv_mfma_f32_16x16x32_bf16 v[160:163], v[44:47], v[180:183], v[160:163]\n\tds_read_b128 v[172:175], %[bb] offset:4032\n\tv_mfma_f32_16x16x32_bf16 v[160:163], v[40:43], v[184:187], v[160:163]\n\tds_write_b8 %[ctc], %[one] offset:384\n\tds_read_b128 v[176:179], %[bb] offset:4096\n\tv_mfma_f32_16x16x32_bf16 v[160:163], v[36:39], v[164:167], v[160:163]\n\ts_waitcnt lgkmcnt(4)\n\ts_nop 0\n\tv_mfma_f32_16x16x32_bf16 v[160:163], v[32:35], v[168:171], v[160:163]\n\ts_waitcnt lgkmcnt(2)\n\ts_nop 0\n\tv_mfma_f32_16x16x32_bf16 v[160:163], v[28:31], v[172:175], v[160:163]\n\ts_waitcnt lgkmcnt(0)\n\ts_nop 0\n\tv_mfma_f32_16x16x32_bf16 v[160:163], v[24:27], v[176:179], v[160:163]\n\ts_nop 7\n\tds_write_b128 %[yb], v[160:163] offset:8064\n\tds_write_b8 %[ctc], %[one] offset:448"
#define CONV_CLOBBER "v24", "v25", "v26", "v27", "v28", "v29", "v30", "v31", "v32", "v33", "v34", "v35", "v36", "v37", "v38", "v39", "v40", "v41", "v42", "v43", "v44", "v45", "v46", "v47", "v48", "v49", "v50", "v51", "v52", "v53", "v54", "v55", "v56", "v57", "v58", "v59", "v60", "v61", "v62", "v63", "v64", "v65", "v66", "v67", "v68", "v69", "v70", "v71", "v72", "v73", "v74", "v75", "v76", "v77", "v78", "v79", "v80", "v81", "v82", "v83", "v84", "v85", "v86", "v87", "v88", "v89", "v90", "v91", "v92", "v93", "v94", "v95", "v96", "v97", "v98", "v99", "v100", "v101", "v102", "v103", "v104", "v105", "v106", "v107", "v108", "v109", "v110", "v111", "v112", "v113", "v114", "v115", "v116", "v117", "v118", "v119", "v120", "v121", "v122", "v123", "v124", "v125", "v126", "v127", "v128", "v129", "v130", "v131", "v132", "v133", "v134", "v135", "v136", "v137", "v138", "v139", "v140", "v141", "v142", "v143", "v144", "v145", "v146", "v147", "v148", "v149", "v150", "v151", "v152", "v153", "v154", "v155", "v156", "v157", "v158", "v159", "v160", "v161", "v162", "v163", "v164", "v165", "v166", "v167", "v168", "v169", "v170", "v171", "v172", "v173", "v174", "v175", "v176", "v177", "v178", "v179", "v180", "v181", "v182", "v183", "v184", "v185", "v186", "v187", "v188", "v189", "v190", "v191", "v192", "v193", "v194", "v195", "v196", "v197", "v198", "v199", "v200", "v201", "v202", "v203", "v204", "v205", "v206", "v207", "v208", "v209", "v210", "v211", "v212", "v213", "v214", "v215", "v216", "v217", "v218", "v219", "v220", "v221", "v222", "v223", "v224", "v225", "v226", "v227", "v228", "v229"

typedef __attribute__((address_space(4))) const Params* KArg;
#define ROLE_BEGIN(N) int b, tid; \
    asm volatile("fk_role" #N ":\n\tv_mov_b32 %0, v0" : "=&v"(tid) : "{s2}"(b0), "{v0}"(tid0) : "memory"); \
    asm volatile("s_mov_b32 %0, s2" : "=&s"(b) : "{s2}"(b0)); \
    const int lane = tid & 63, wave = __builtin_amdgcn_readfirstlane(tid >> 6), xcd = b & 7, slot = b >> 3; \
    const int cw = slot < 16 ? xcd * 16 + slot : 112 + xcd * 2 + slot, chbase = cw * 4, w = xcd * 9 + (slot - 18); (void)chbase; (void)w; (void)lane
#define ROLE_ARGS() Params p; SBAR(); \
    asm volatile("s_waitcnt lgkmcnt(0)" : "={s[56:57]}"(p.x), "={s[58:59]}"(p.Are), "={s[60:61]}"(p.Aim), "={s[62:63]}"(p.Bre), "={s[64:65]}"(p.Bim), "={s[66:67]}"(p.Cre), "={s[68:69]}"(p.Cim), "={s[70:71]}"(p.logdt), \
                 "={s[72:73]}"(p.dskip), "={s[74:75]}"(p.wr), "={s[76:77]}"(p.wg), "={s[78:79]}"(p.wu), "={s[80:81]}"(p.wd), "={s[82:83]}"(p.gw), "={s[84:85]}"(p.out), "={s[86:87]}"(p.ws)); STAMP0_FIX()
#define ROLE_END() do { STAMP_END(); STAMP_FLUSH(); __builtin_amdgcn_s_waitcnt(0x0070); asm volatile("s_endpgm" ::: "memory"); } while (0)
#define MF(A_, B_, C_) __builtin_amdgcn_mfma_f32_16x16x32_bf16(A_, B_, C_, 0, 0, 0)
#define GRP(mask, n) __builtin_amdgcn_sched_group_barrier(mask, n, 0)

#define FFN_PRE() u32 gof = w * 128 + ((wave >> 1) * 8 + (lane >> 3)) * (F * 2) + ((lane & 7) ^ (((lane >> 3) & 2) | ((wave & 2) << 1))) * 16; asm volatile("" : "+v"(gof)); ROLE_ARGS()
#define FFN_FILL() \
    if (FDELAY) __builtin_amdgcn_s_sleep(FDELAY); \
    { const char* gsrc = (const char*)((wave & 1) ? p.wu : p.wg); \
      _Pragma("unroll") for (int i = 0; i < 18; ++i) { \
          asm volatile("s_mov_b32 m0, %2\n\ts_nop 0\n\tglobal_load_lds_dwordx4 %0, %1" WNT :: "v"(gof), "s"(gsrc + i * (32 * F * 2)), "s"((8 * i + wave) * 1024) : "memory"); \
          if (i == 5 || i == 11) __builtin_amdgcn_s_barrier(); } } \
    STAMP(11)
#define FFN_WDFRAG(k, pr) do { LAS char* t_ = ta + (pr) * 64; \
    _Pragma("unroll") for (int e = 0; e < 8; ++e) a[k][e] = __builtin_amdgcn_ds_read_tr16_b64_v4i16((LAS s16x4*)(t_ + (e >> 2) * 32 + (e & 3) * 16 * WDP)); } while (0)
#define FFN_DOWN(k, pr) do { v4f y_ = MF(__builtin_shufflevector(a[k][0], a[k][1], 0, 1, 2, 3, 4, 5, 6, 7), hf[0], ((v4f){0.f, 0.f, 0.f, 0.f})); \
    y_ = MF(__builtin_shufflevector(a[k][4], a[k][5], 0, 1, 2, 3, 4, 5, 6, 7), hf[1], y_); \
    y_ = MF(__builtin_shufflevector(a[k][2], a[k][3], 0, 1, 2, 3, 4, 5, 6, 7), hf[2], y_); \
    ya[k] = MF(__builtin_shufflevector(a[k][6], a[k][7], 0, 1, 2, 3, 4, 5, 6, 7), hf[3], y_); } while (0)
#define FFN_ADDR() const int n = lane & 15, q = lane >> 4; \
    LAS char* ta = smem + (lane >> 2) * WDP + (lane & 3) * 8; \
    LAS char* hrow = smem + FL_H + (n & 7) * 144 + q * 16; \
    LAS char* ha = n < 8 ? hrow : smem + FL_Z + q * 16; \
    LAS char* hb = n < 8 ? smem + FL_Z + q * 16 : hrow; \
    u32 obase = OFF_PART + (4 * (n >> 3) + q) * PP + hline(p.ws, w) + (n & 7) * 16, ctl = (u32)(uintptr_t)(smem + FL_TAG); \
    asm volatile("" : "+v"(ta), "+v"(ha), "+v"(hb), "+v"(obase), "+v"(ctl))
#define FFN_HFRAG() const bf16x8 hf[4] = {*(LAS bf16x8*)(ha), *(LAS bf16x8*)(hb), *(LAS bf16x8*)(ha + 64), *(LAS bf16x8*)(hb + 64)}
#define FFN_PUB(k, pr) __builtin_amdgcn_raw_buffer_store_b128((v4u){cvtpk(ya[k][0], ya[k][1]), tag, cvtpk(ya[k][2], ya[k][3]), tag}, WSR(), obase + (pr) * (8 * PP), 0, 16)

__global__ __launch_bounds__(512, 2) void fused_kernel(Params p_unused) {
    extern __shared__ __attribute__((aligned(16))) char smem_[];
    LAS char* smem = (LAS char*)smem_;
    const KArg kp0 = (KArg)__builtin_amdgcn_kernarg_segment_ptr();
    const int tid0 = threadIdx.x, b0 = blockIdx.x;
    asm volatile("s_load_dwordx16 s[56:71], s[0:1], 0x0\n\ts_load_dwordx16 s[72:87], s[0:1], 0x40\n\tv_readfirstlane_b32 s92, v0\n\t"
                 "s_cmp_ge_u32 s2, 32\n\ts_cbranch_scc1 1f\n\t"
                 "s_getpc_b64 s[90:91]\n\ts_and_b32 s90, s90, 0xffffff00\n\ts_and_b32 s93, s2, -8\n\ts_lshr_b32 s89, s92, 6\n\ts_add_u32 s93, s93, s89\n\ts_min_u32 s93, s93, %3\n\ts_lshl_b32 s93, s93, 10\n\t"
                 "v_and_b32 v1, 63, v0\n\tv_lshlrev_b32 v1, 4, v1\n\tv_add_u32 v1, s93, v1\n\ts_mov_b32 m0, %4\n\ts_nop 0\n\tglobal_load_lds_dwordx4 v1, s[90:91]\n"
                 "1:\n\ts_cmp_lt_u32 s2, 144\n\ts_cbranch_scc0 2f\n\ts_cmp_lt_u32 s92, 256\n\ts_cbranch_scc1 fk_role0\n\ts_branch fk_role1\n"
                 "2:\n\ts_cmp_lt_u32 s2, 216\n\ts_cbranch_scc0 3f\n\ts_cmp_lt_u32 s92, 256\n\ts_cbranch_scc1 fk_role2\n\ts_branch fk_role3\n"
                 "3:\n\ts_cmp_lt_u32 s2, 224\n\ts_cbranch_scc1 fk_role4\n\ts_branch fk_role5"
                 :: "{s[0:1]}"(kp0), "{s2}"(b0), "{v0}"(tid0), "n"(CODE_KB - 1), "n"(LDS_DUMMY) : "memory", "scc", "s89", "s90", "s91", "s92", "s93", "v1");
    const int slot = b0 >> 3, wave = __builtin_amdgcn_readfirstlane(tid0 >> 6);
    if (slot >= 18) {
    if (slot >= 27 || wave >= 4) {
    if (slot < 28) {
    if (slot == 27)
    {
        ROLE_BEGIN(4);
        ROLE_ARGS();
        u32 tagv;
        asm volatile("v_mov_b32 %0, 0\n\tglobal_load_dword %0, %0, %1 sc1\n\ts_waitcnt vmcnt(0)" : "=&v"(tagv) : "s"(p.ws) : "memory");
        const u32 tag = __builtin_amdgcn_readfirstlane(tagv) + 1u;
        if (tid < 128) ((LAS u32*)smem)[tid] = 0;
        u32 ga = (u32)(uintptr_t)(smem + (lane & 15) * 4), gv = (u32)(uintptr_t)(smem + (lane >> 2) * 4), gq_ = (u32)(uintptr_t)(smem + (lane & 3) * 16), zero = (u32)(uintptr_t)smem;
        u32 ro = OFF_RG + hline(p.ws, tid >> 3) + (tid & 7) * 16, ro2 = OFF_RG + hline(p.ws, 64 + (tid >> 3)) + (tid & 7) * 16, so = OFF_SEL + ((lane & 1) * 8 + xcd) * 256, xo = (lane & 3) * 128;
        asm volatile("" : "+v"(ga), "+v"(gv), "+v"(gq_), "+v"(zero), "+v"(ro), "+v"(ro2), "+v"(so), "+v"(xo));
        BAR();
        __builtin_amdgcn_s_sleep(SELWAIT);
        v4u qa, qb;
        u32 c_;
#define POLL_A "global_load_dwordx4 v[20:23], %[ro], %[ws] sc1\n\tglobal_load_dwordx4 v[24:27], %[r2], %[ws] sc1\n\t"
#define POLL_B "global_load_dwordx4 v[28:31], %[ro], %[ws] sc1\n\tglobal_load_dwordx4 v[32:35], %[r2], %[ws] sc1\n\t"
        asm volatile("s_mov_b32 %[c], 0x40000\n\t" POLL_A "s_sleep 8\n\t" POLL_B
                     "1:\n\ts_waitcnt vmcnt(2)\n\tv_cmpx_eq_u32 %[tag], v21\n\tv_cmpx_eq_u32 %[tag], v23\n\tv_cmpx_eq_u32 %[tag], v25\n\tv_cmpx_eq_u32 %[tag], v27\n\t"
                     "s_cmp_eq_u64 exec, -1\n\ts_mov_b64 exec, -1\n\ts_cbranch_scc1 3f\n\t" POLL_A
                     "s_waitcnt vmcnt(2)\n\tv_cmpx_eq_u32 %[tag], v29\n\tv_cmpx_eq_u32 %[tag], v31\n\tv_cmpx_eq_u32 %[tag], v33\n\tv_cmpx_eq_u32 %[tag], v35\n\t"
                     "s_cmp_eq_u64 exec, -1\n\ts_mov_b64 exec, -1\n\ts_cbranch_scc1 2f\n\t" POLL_B
                     "s_sub_u32 %[c], %[c], 1\n\ts_cbranch_scc0 1b\n\ts_waitcnt vmcnt(0)\n"
                     "2:\n\tv_mov_b32 v36, v28\n\tv_mov_b32 v37, v30\n\tv_mov_b32 v38, v32\n\tv_mov_b32 v39, v34\n\ts_branch 4f\n"
                     "3:\n\tv_mov_b32 v36, v20\n\tv_mov_b32 v37, v22\n\tv_mov_b32 v38, v24\n\tv_mov_b32 v39, v26\n"
                     "4:"
                     : "=&{v[36:39]}"(qa), [c] "=&s"(c_) : [ro] "v"(ro), [r2] "v"(ro2), [ws] "s"(p.ws), [tag] "s"(tag)
                     : "memory", "scc", "vcc", "v20", "v21", "v22", "v23", "v24", "v25", "v26", "v27", "v28", "v29", "v30", "v31", "v32", "v33", "v34", "v35");
        asm volatile("" : "=&{v[20:23]}"(qb));
        v4u qc, qd, qe;
        asm volatile("" : "=&{v[24:27]}"(qc), "=&{v[28:31]}"(qd), "=&{v[32:35]}"(qe));
        STAMP(2);
        u32 rank, tau, cnt;
        u64 m0_, m1_, m2_, m3_;
        {
            u32 v, nv; v4u q4;
            asm volatile("ds_max_u32 %0, %1\n\ts_waitcnt lgkmcnt(0)\n\ts_barrier" :: "v"(ga), "v"(max(max(qa.x, qa.y), max(qa.z, qa.w))) : "memory");
#define RANK16(off) asm volatile("ds_read_b32 %[v], %[gv] offset:" #off "\n\tds_read_b128 v[40:43], %[gq] offset:" #off "\n\tds_read_b32 %[n], %[z] offset:64\n\tv_mov_b32 %[r], 0\n\ts_waitcnt lgkmcnt(0)\n\t" \
                         "v_cmp_gt_u32 %[a], v40, %[v]\n\tv_cmp_gt_u32 %[b], v41, %[v]\n\tv_cmp_gt_u32 %[c], v42, %[v]\n\tv_cmp_gt_u32 %[d], v43, %[v]\n\t" \
                         "v_addc_co_u32 %[r], vcc, 0, %[r], %[a]\n\tv_addc_co_u32 %[r], vcc, 0, %[r], %[b]\n\tv_addc_co_u32 %[r], vcc, 0, %[r], %[c]\n\tv_addc_co_u32 %[r], vcc, 0, %[r], %[d]\n\ts_nop 1\n\t" \
                         "v_add_u32_dpp %[r], %[r], %[r] quad_perm:[1,0,3,2] row_mask:0xf bank_mask:0xf\n\ts_nop 1\n\tv_add_u32_dpp %[r], %[r], %[r] quad_perm:[2,3,0,1] row_mask:0xf bank_mask:0xf\n\ts_nop 0" \
                         : [v] "=&v"(v), [r] "=&v"(rank), [n] "=&v"(nv), "=&{v[40:43]}"(q4), [a] "=&s"(m0_), [b] "=&s"(m1_), [c] "=&s"(m2_), [d] "=&s"(m3_) : [gv] "v"(gv), [gq] "v"(gq_), [z] "v"(zero) : "memory", "vcc")
            RANK16(0);
            tau = (u32)__builtin_amdgcn_readlane((int)v, (int)__builtin_ctzll(__ballot(rank == 7)));
            u32 pos;
            asm volatile("s_nop 1\n\tv_mov_b32 %[n], 0\n\tv_cmp_le_u32 %[a], %[t], v36\n\tv_cmp_le_u32 %[b], %[t], v37\n\tv_cmp_le_u32 %[c], %[t], v38\n\tv_cmp_le_u32 %[d], %[t], v39\n\t"
                         "v_addc_co_u32 %[n], vcc, 0, %[n], %[a]\n\tv_addc_co_u32 %[n], vcc, 0, %[n], %[b]\n\tv_addc_co_u32 %[n], vcc, 0, %[n], %[c]\n\tv_addc_co_u32 %[n], vcc, 0, %[n], %[d]\n\t"
                         "v_lshlrev_b32 %[n], 2, %[n]\n\tv_cmpx_ne_u32 0, %[n]\n\tds_add_rtn_u32 %[p], %[z], %[n] offset:64\n\ts_waitcnt lgkmcnt(0)\n\t"
                         "s_mov_b64 exec, %[a]\n\tds_write_b32 %[p], v36 offset:256\n\tv_add_u32 %[p], 4, %[p]\n\t"
                         "s_mov_b64 exec, %[b]\n\tds_write_b32 %[p], v37 offset:256\n\tv_add_u32 %[p], 4, %[p]\n\t"
                         "s_mov_b64 exec, %[c]\n\tds_write_b32 %[p], v38 offset:256\n\tv_add_u32 %[p], 4, %[p]\n\t"
                         "s_mov_b64 exec, %[d]\n\tds_write_b32 %[p], v39 offset:256\n\t"
                         "s_mov_b64 exec, -1\n\ts_waitcnt lgkmcnt(0)\n\ts_barrier"
                         : [n] "=&v"(cnt), [p] "=&v"(pos), [a] "=&s"(m0_), [b] "=&s"(m1_), [c] "=&s"(m2_), [d] "=&s"(m3_) : [t] "s"(tau), [z] "v"(zero), "{v[36:39]}"(qa) : "memory", "vcc");
            if (wave == 0) {
                RANK16(256);
                const int nc = __builtin_amdgcn_readfirstlane((int)nv) >> 2;
                if (__builtin_expect(nc > 16, 0)) {
                    v = (u32)__shfl((int)top8_d(smem, lane, nc), (lane >> 2) & 7);
                    rank = lane >> 2;
                }
                const u64 m8 = __ballot(rank < 8);
                asm volatile("s_and_b64 exec, %[m], %[k2]\n\tglobal_store_dwordx2 %[so], %[d], %[ws] sc1\n\ts_mov_b64 exec, %[m]\n\ts_mov_b32 m0, 0x8000\n\ts_nop 0\n\t"
                             "global_load_lds_dword %[xo], %[x]\n\tglobal_load_lds_dword %[xo], %[x] offset:512\n\ts_and_b64 exec, %[m], %[k1]\n\tglobal_load_lds_dword %[xo], %[x] offset:1024\n\ts_mov_b64 exec, -1"
                             :: [m] "s"(m8), [k2] "s"(0x3333333333333333ull), [k1] "s"(0x1111111111111111ull), [so] "v"(so + rank * 8), [d] "v"((v2u){v, tag}), [ws] "s"(p.ws),
                                [xo] "v"(xo + (~v & 2047u) * 1152u), [x] "s"(p.x) : "memory", "scc");
                asm volatile("s_mov_b64 exec, 1\n\tglobal_store_dwordx2 %0, %1, %2 sc1\n\ts_mov_b64 exec, -1" :: "v"(OFF_CI + (416 + xcd) * 8), "v"((v2u){tag, tag}), "s"(p.ws) : "memory");
            }
        }
        STAMP(3);
        asm volatile("s_waitcnt vmcnt(0)" :: "v"(qb), "v"(qc), "v"(qd), "v"(qe) : "memory");
        if (xcd == 0) {
            const u32 cio = OFF_CI + (tid < 212 ? tid : 0) * 16;
            for (u32 spins = 0;; ++spins) {
                REREAD();
                const v4u q2 = __builtin_amdgcn_raw_buffer_load_b128(WSR(), cio, 0, 16);
                if (__all((q2.y == tag) & (q2.w == tag)) || spins > SPIN_CAP) break;
                __builtin_amdgcn_s_sleep(16);
            }
            BAR();
            if (tid == 0) __hip_atomic_store((GAS u32*)p.ws, tag, RLX_AGENT);
        }
        ROLE_END();
    }
    else
        {
            ROLE_BEGIN(3);
            FFN_PRE();
            const int pw = wave - 4;
            u32 tagv;
            asm volatile("v_mov_b32 %0, 0\n\tglobal_load_dword %0, %0, %1 sc1" : "=&v"(tagv) : "s"(p.ws) : "memory");
            FFN_FILL();
            v2u sel;
            const u32 so = OFF_SEL + xcd * 256 + (2 * pw + (lane & 1)) * 8, l16 = lane * 16;
#define POLL_S "global_load_dwordx2 v[10:11], %[so], %[ws] sc1\n\t"
            asm volatile("s_waitcnt vmcnt(18)" : "+v"(tagv) :: "memory");
            const u32 tag = __builtin_amdgcn_readfirstlane(tagv) + 1u;
            *(LAS v4u*)(smem + FL_Z + min(l16, 256u)) = (v4u){lane >= 16 ? tag : 0u, 0u, 0u, 0u};
            FFN_ADDR();
            u32 one = 1u;
            const char* dsrc = (const char*)(p.wd + (size_t)(64 * w + 32 * (pw >> 1)) * D);
            const u32 ddst = (pw >> 1) * 32 * WDP + (pw & 1) * 1024;
            u32 dof[19];
#pragma unroll
            for (int i = 0; i < 19; ++i) {
                const u32 id = min(128u * i + 64u * (pw & 1) + lane, 2367u), r0 = id / 74u;
                dof[i] = r0 * 1152u + min(id - 74u * r0, 71u) * 16u;
                asm volatile("" : "+v"(dof[i]));
            }
#define LD_WD(i) asm volatile("s_mov_b32 m0, %2\n\ts_nop 0\n\tglobal_load_lds_dwordx4 %0, %1" :: "v"(dof[i]), "s"(dsrc), "s"(ddst + (i) * 2048) : "memory")
            asm volatile("s_waitcnt vmcnt(12) lgkmcnt(0)\n\ts_barrier\n\ts_waitcnt vmcnt(6)\n\ts_barrier\n\ts_waitcnt vmcnt(%[pk])\n\t" POLL_S "s_waitcnt vmcnt(1)\n\ts_barrier" : "=&{v[10:11]}"(sel) : [so] "v"(so), [ws] "s"(p.ws), [pk] "n"(PK) : "memory");
            u32 c_, early, mw = ddst;
            asm volatile("s_mov_b32 %[e], 0\n\ts_waitcnt vmcnt(0)\n\tv_cmp_ne_u32 vcc, %[tag], v11\n\ts_cmp_eq_u64 vcc, 0\n\ts_cbranch_scc1 2f\n\t"
                         "s_mov_b32 %[e], 1\n\ts_mov_b32 %[c], 0x80000\n\ts_mov_b32 m0, %[m]\n\ts_add_u32 %[m], %[m], 0x800\n\tglobal_load_lds_dwordx4 %[d0], %[src]\n\ts_mov_b32 m0, %[m]\n\ts_add_u32 %[m], %[m], 0x800\n\tglobal_load_lds_dwordx4 %[d1], %[src]\n\ts_mov_b32 m0, %[m]\n\ts_add_u32 %[m], %[m], 0x800\n\tglobal_load_lds_dwordx4 %[d2], %[src]\n\ts_mov_b32 m0, %[m]\n\ts_add_u32 %[m], %[m], 0x800\n\tglobal_load_lds_dwordx4 %[d3], %[src]\n\ts_mov_b32 m0, %[m]\n\ts_add_u32 %[m], %[m], 0x800\n\tglobal_load_lds_dwordx4 %[d4], %[src]\n\ts_mov_b32 m0, %[m]\n\ts_add_u32 %[m], %[m], 0x800\n\tglobal_load_lds_dwordx4 %[d5], %[src]\n\t"
                         "1:\n\t" POLL_S "s_waitcnt vmcnt(0)\n\tv_cmp_ne_u32 vcc, %[tag], v11\n\ts_cmp_eq_u64 vcc, 0\n\ts_cbranch_scc1 2f\n\ts_sub_u32 %[c], %[c], 1\n\ts_cbranch_scc0 1b\n2:"
                         : "+{v[10:11]}"(sel), [c] "=&s"(c_), [e] "=&s"(early), [m] "+s"(mw) : [so] "v"(so), [ws] "s"(p.ws), [tag] "s"(tag), [src] "s"(dsrc), [d0] "v"(dof[0]), [d1] "v"(dof[1]), [d2] "v"(dof[2]), [d3] "v"(dof[3]), [d4] "v"(dof[4]), [d5] "v"(dof[5]) : "memory", "scc", "vcc");
            STAMP(3);
#pragma unroll
            for (int k = 0; k < 2; ++k) {
                const u32 t = ~(u32)__builtin_amdgcn_readlane((int)sel.x, k) & 2047u;
                const char* xr = (const char*)p.x + t * (D * 2);
                asm volatile("s_mov_b32 m0, %2\n\ts_nop 0\n\tglobal_load_lds_dwordx4 %0, %1\n\ts_mov_b64 exec, 0xff\n\tglobal_load_lds_dwordx4 %0, %1 offset:1024\n\ts_mov_b64 exec, -1"
                             :: "v"(l16), "s"(xr), "s"(FL_XS + (2 * pw + k) * XSP) : "memory");
            }
            asm volatile("s_barrier\n\ts_cmp_eq_u32 %[e], 0\n\ts_cbranch_scc0 1f\n\ts_mov_b32 m0, %[m]\n\ts_add_u32 %[m], %[m], 0x800\n\tglobal_load_lds_dwordx4 %[d0], %[src]\n\ts_mov_b32 m0, %[m]\n\ts_add_u32 %[m], %[m], 0x800\n\tglobal_load_lds_dwordx4 %[d1], %[src]\n\ts_mov_b32 m0, %[m]\n\ts_add_u32 %[m], %[m], 0x800\n\tglobal_load_lds_dwordx4 %[d2], %[src]\n\ts_mov_b32 m0, %[m]\n\ts_add_u32 %[m], %[m], 0x800\n\tglobal_load_lds_dwordx4 %[d3], %[src]\n\ts_mov_b32 m0, %[m]\n\ts_add_u32 %[m], %[m], 0x800\n\tglobal_load_lds_dwordx4 %[d4], %[src]\n\ts_mov_b32 m0, %[m]\n\ts_add_u32 %[m], %[m], 0x800\n\tglobal_load_lds_dwordx4 %[d5], %[src]\n\t\ts_waitcnt vmcnt(6)\n\ts_barrier\n\ts_mov_b32 m0, %[m]\n\ts_add_u32 %[m], %[m], 0x800\n\tglobal_load_lds_dwordx4 %[d6], %[src]\n\ts_mov_b32 m0, %[m]\n\ts_add_u32 %[m], %[m], 0x800\n\tglobal_load_lds_dwordx4 %[d7], %[src]\n\ts_mov_b32 m0, %[m]\n\ts_add_u32 %[m], %[m], 0x800\n\tglobal_load_lds_dwordx4 %[d8], %[src]\n\ts_mov_b32 m0, %[m]\n\ts_add_u32 %[m], %[m], 0x800\n\tglobal_load_lds_dwordx4 %[d9], %[src]\n\ts_mov_b32 m0, %[m]\n\ts_add_u32 %[m], %[m], 0x800\n\tglobal_load_lds_dwordx4 %[d10], %[src]\n\ts_mov_b32 m0, %[m]\n\ts_add_u32 %[m], %[m], 0x800\n\tglobal_load_lds_dwordx4 %[d11], %[src]\n\ts_branch 2f\n1:\n\ts_mov_b32 m0, %[m]\n\ts_add_u32 %[m], %[m], 0x800\n\tglobal_load_lds_dwordx4 %[d6], %[src]\n\ts_mov_b32 m0, %[m]\n\ts_add_u32 %[m], %[m], 0x800\n\tglobal_load_lds_dwordx4 %[d7], %[src]\n\ts_mov_b32 m0, %[m]\n\ts_add_u32 %[m], %[m], 0x800\n\tglobal_load_lds_dwordx4 %[d8], %[src]\n\ts_mov_b32 m0, %[m]\n\ts_add_u32 %[m], %[m], 0x800\n\tglobal_load_lds_dwordx4 %[d9], %[src]\n\ts_mov_b32 m0, %[m]\n\ts_add_u32 %[m], %[m], 0x800\n\tglobal_load_lds_dwordx4 %[d10], %[src]\n\ts_mov_b32 m0, %[m]\n\ts_add_u32 %[m], %[m], 0x800\n\tglobal_load_lds_dwordx4 %[d11], %[src]\n\ts_waitcnt vmcnt(6)\n\ts_barrier\n2:"
                         : [m] "+s"(mw) : [e] "s"(early), [src] "s"(dsrc), [d0] "v"(dof[0]), [d1] "v"(dof[1]), [d2] "v"(dof[2]), [d3] "v"(dof[3]), [d4] "v"(dof[4]), [d5] "v"(dof[5]), [d6] "v"(dof[6]), [d7] "v"(dof[7]), [d8] "v"(dof[8]), [d9] "v"(dof[9]), [d10] "v"(dof[10]), [d11] "v"(dof[11]) : "memory", "scc");
            STAMP(10);
#pragma unroll
            for (int i = 12; i < 18; ++i) LD_WD(i);
            if (!(pw & 1)) LD_WD(18);
            asm volatile("s_mov_b64 exec, 1\n\tglobal_store_dwordx2 %0, %1, %2 sc1\n\ts_mov_b64 exec, -1\n\ts_waitcnt vmcnt(1)" :: "v"(OFF_CI + (128 + 4 * w + pw) * 8), "v"((v2u){tag, tag}), "s"(p.ws) : "memory");
            LDS_ADD1(ctl, one, 4);
            LSPIN(ctl, 4, 4);
            STAMP(13);
            s16x4 a[3][8];
            FFN_WDFRAG(0, 8 + pw); FFN_WDFRAG(1, 12 + pw); FFN_WDFRAG(2, 16 + (pw & 1));
            asm volatile("s_barrier" ::: "memory");
            FFN_HFRAG();
            v4f ya[3];
            FFN_DOWN(0, 8 + pw); FFN_DOWN(1, 12 + pw);
            if (pw < 2) { FFN_DOWN(2, 16 + pw); FFN_PUB(2, 16 + pw); }
            FFN_PUB(0, 8 + pw); FFN_PUB(1, 12 + pw);
            STAMP(9);
            ROLE_END();
        }
    } else
    {
        ROLE_BEGIN(5);
        ROLE_ARGS();
        if (XT) {
            const u32 id = ((slot - 28) + 4 * wave) * 64 + lane, dst = (u32)(uintptr_t)(smem + wave * 256), lo = (id & 1) ? 1024u : xcd * 128u;
            if (XT & 2) asm volatile("s_mov_b32 m0, %2\n\ts_nop 0\n\tglobal_load_lds_dword %0, %1" :: "v"((((id >> 3) & 255) * 4 + 2 + ((id >> 1) & 1)) * 1152u + lo), "s"(p.x), "s"(dst) : "memory");
            asm volatile("s_mov_b32 m0, %2\n\ts_nop 0\n\tglobal_load_lds_dword %0, %1" :: "v"((1024 + (id >> 1)) * 1152u + lo), "s"(p.x), "s"(dst) : "memory");
        }
        if (FDELAY) __builtin_amdgcn_s_sleep(FDELAY);
        _Pragma("unroll 1") for (int e = (slot - 28) + 4 * wave; touch(p, xcd, e, lane, (u32)(uintptr_t)(smem + wave * 256)); e += 32) {}
        STAMP(11);
        asm volatile("s_waitcnt vmcnt(0)" ::: "memory");
        STAMP(6);
        ROLE_END();
    }
    } else
        {
            ROLE_BEGIN(2);
            FFN_PRE();
            FFN_FILL();
            FFN_ADDR();
            LAS char* gp = smem + q * 2048 + ((lane >> 2) & 3) * 128 + ((2 * wave + ((lane >> 1) & 1)) ^ (((lane >> 2) & 2) | ((q & 1) << 2))) * 16 + (lane & 1) * 8;
            LAS char* xp = smem + FL_XS + (n & 7) * XSP + q * 16;
            const int hc = 16 * wave + n;
            LAS char* hw_ = smem + FL_H + (4 * (q & 1) + 2 * (q >> 1)) * 144 + ((hc & 35) | ((hc & 12) << 1) | ((hc & 16) >> 2)) * 2;
            asm volatile("" : "+v"(gp), "+v"(xp), "+v"(hw_));
            bf16x8 gf[18], uf[18];
#pragma unroll
            for (int sl = 0; sl < 3; ++sl) {
                if (sl == 0) asm volatile("s_waitcnt vmcnt(12)\n\ts_barrier" ::: "memory");
                if (sl == 1) asm volatile("s_waitcnt vmcnt(6)\n\ts_barrier" ::: "memory");
                if (sl == 2) asm volatile("s_waitcnt vmcnt(0) lgkmcnt(0)\n\ts_barrier" ::: "memory");
                if (sl == 0) STAMP(5);
                if (sl == 2) STAMP(6);
                LAS char* g_ = gp + sl * 49152;
                asm volatile("" : "+v"(g_));
#pragma unroll
                for (int i = 0; i < 6; ++i) {
                    gf[6 * sl + i] = __builtin_shufflevector(__builtin_amdgcn_ds_read_tr16_b64_v4i16((LAS s16x4*)(g_ + i * 8192)), __builtin_amdgcn_ds_read_tr16_b64_v4i16((LAS s16x4*)(g_ + i * 8192 + 512)), 0, 1, 2, 3, 4, 5, 6, 7);
                    uf[6 * sl + i] = __builtin_shufflevector(__builtin_amdgcn_ds_read_tr16_b64_v4i16((LAS s16x4*)(g_ + i * 8192 + 1024)), __builtin_amdgcn_ds_read_tr16_b64_v4i16((LAS s16x4*)(g_ + i * 8192 + 1536)), 0, 1, 2, 3, 4, 5, 6, 7);
                }
#pragma unroll
                for (int i = 0; i < 6; ++i) asm volatile("" : "+v"(gf[6 * sl + i]), "+v"(uf[6 * sl + i]));
            }
            u32 tag = *(LAS u32*)(smem + FL_TAG);
            asm volatile("s_barrier\n\ts_barrier" : "+v"(tag) :: "memory");
            STAMP(1);
            bf16x8 xf[18];
            v4f ag = {0.f, 0.f, 0.f, 0.f}, au = ag;
#pragma unroll
            for (int s = 0; s < 4; ++s) xf[s] = *(LAS bf16x8*)(xp + s * 64);
            SBAR();
#pragma unroll
            for (int s = 0; s < 18; ++s) {
                ag = MF(xf[s], gf[s], ag);
                if (s + 4 < 18) xf[s + 4] = *(LAS bf16x8*)(xp + (s + 4) * 64);
                au = MF(xf[s], uf[s], au);
                SBAR();
            }
            STAMP(4);
            {
                const u32 ab = cvtpk(q < 2 ? ag[0] : ag[2], q < 2 ? ag[1] : ag[3]), ub = cvtpk(q < 2 ? au[0] : au[2], q < 2 ? au[1] : au[3]);
                const u32 sl = cvtpk(bflo(ab) * sigmoidf_(bflo(ab)), bfhi(ab) * sigmoidf_(bfhi(ab)));
                const u32 hh = cvtpk(bflo(sl) * bflo(ub), bfhi(sl) * bfhi(ub));
                *(LAS u16*)(hw_) = (u16)hh; *(LAS u16*)(hw_ + 144) = (u16)(hh >> 16);
            }
            STAMP(8);
            LSPIN(ctl, 4, 4);
            s16x4 a[2][8];
            FFN_WDFRAG(0, 2 * wave); FFN_WDFRAG(1, 2 * wave + 1);
            asm volatile("s_barrier" ::: "memory");
            STAMP(13);
            FFN_HFRAG();
            v4f ya[2];
            FFN_DOWN(0, 2 * wave); FFN_DOWN(1, 2 * wave + 1);
            FFN_PUB(0, 2 * wave); FFN_PUB(1, 2 * wave + 1);
            STAMP(9);
            ROLE_END();
        }
    } else {
    if (wave >= 4)
    {
        ROLE_BEGIN(1);
        const int hw = wave - 4, ht = tid - 256;
        const v4u z4 = {0, 0, 0, 0};
        const int sub = lane & 15;
        v4u xr[5];
        u32 tagv;
        const int rrow = 16 * (cw & 127) + 4 * hw;
        u32 dst = (u32)(uintptr_t)(smem + LDS_RW) + hw * 3072, l16 = lane * 16;
        u32 ro = (u32)(((rrow + (lane >> 4)) & ~((127 - cw) >> 31)) * D + sub * 8) * 2, ro4 = ro - (sub & 8) * 16;
        u32 uo0 = (u32)((256 * hw + 4 * lane) * D + chbase) * 2;
        asm volatile("" : "+s"(dst), "+v"(l16), "+v"(ro), "+v"(ro4), "+v"(uo0));
        ROLE_ARGS();
        {
            asm volatile("s_mov_b32 m0, %5\n\tv_mov_b32 %0, 0\n\tglobal_load_dword %0, %0, %2 sc1\n\tglobal_load_lds_dwordx4 %3, %4\n\ts_mov_b64 exec, 0xff\n\tglobal_load_lds_dwordx4 %3, %4 offset:1024\n\t"
                         "s_add_u32 m0, m0, 1280\n\ts_mov_b64 exec, -1\n\tglobal_load_lds_dwordx4 %3, %6\n\ts_mov_b64 exec, 0xff\n\tglobal_load_lds_dwordx4 %3, %6 offset:1024\n\ts_mov_b64 exec, -1"
                         : "=&v"(tagv) : "n"(0), "s"(p.ws), "v"(l16), "s"(p.wr), "s"(dst), "s"(p.gw) : "memory", "scc");
#pragma unroll
            for (int j = 0; j < 4; ++j) asm volatile("global_load_dwordx4 %0, %1, %2 offset:%3" : "=v"(xr[j]) : "v"(ro), "s"(p.x), "n"(j * 256));
            asm volatile("global_load_dwordx4 %0, %1, %2 offset:1024" : "=v"(xr[4]) : "v"(ro4), "s"(p.x));
        }
        __builtin_amdgcn_s_barrier();
        STAMP(1);
        if (XPF) {
            const int id = min((slot * 4 + hw) * 64 + lane, 4095);
            u32 keep;
            asm volatile("s_mov_b32 %0, m0\n\ts_mov_b32 m0, %3\n\ts_nop 0\n\tglobal_load_lds_dword %1, %2\n\ts_mov_b32 m0, %0" : "=&s"(keep) : "v"((u32)((id >> 1) * 1152 + ((id & 1) ? 1024 : xcd * 128))), "s"(p.x), "s"((u32)(uintptr_t)(smem + LDS_DUMMY)) : "memory");
        }
        v2u uu[8];
        const u32 uo1 = uo0 + 1024 * D * 2;
#define LD_U(i) asm volatile("global_load_dwordx2 %0, %1, %2 offset:%3" : "=v"(uu[i]) : "v"((i) < 4 ? uo0 : uo1), "s"(p.x), "n"(((i) & 3) * D * 2))
#pragma unroll
        for (int i = 0; i < NB1; ++i) LD_U(i);
        *(LAS v4u*)(smem + LDS_TAP + ht * 16) = z4; *(LAS v4u*)(smem + LDS_TAP + 4096 + ht * 16) = z4;
        *(LAS v4u*)(smem + LDS_TAP + 8192 + ht * 16 + (ht >> 7) * (LDS_U - LDS_TAP - 8192 + ((ht >> 5) & 3) * 4608 + (ht & 31) * 16 - ht * 16)) = z4;
        u32 ctl = (u32)(uintptr_t)(smem + LDS_CTL), one = 1u, cty = ctl + 64 * hw + (lane & 3) * 16;
        const u32 late = (u32)((127 - cw) >> 31), pa_ = OFF_RG + hline(p.ws, cw & 127) + hw * 32, pk_ = pa_ ^ ((pa_ ^ (OFF_CI + ((cw - 128) * 4 + hw) * 16)) & late);
        u32 po = pk_ + (lane >> 1) + (sub != 0) * (OFF_G + hline(p.ws, ((lane - 1) & 3) * 64 + ((cw & 127) >> 1)) + ((4 * cw + hw) & 7) * 16 - pk_ - (lane >> 1));
        const u64 pmask = 1ull | ((u64)(~late & 1u) << 32) | (~late & ((1u << (1 + REPG)) - 1));
        u32 nrow = 0xffffu - (rrow + (lane >> 4));
        u32 go = OFF_G + hline(p.ws, (cw % REPG) * 64 + 8 * hw + (lane >> 3)) + (lane & 7) * 16, go2 = OFF_G + hline(p.ws, (cw % REPG) * 64 + 32 + 8 * hw + (lane >> 3)) + (lane & 7) * 16;
        LAS char* up = smem + LDS_U + (256 + 256 * hw + 4 * lane) * 2;
        LAS char* yp = smem + CH_Y + hw * 1152 + (lane + (lane >> 3)) * 16;
        LAS char* rw = smem + LDS_RW + hw * 3072 + sub * 16;
        LAS char* rw4 = rw - (sub & 8) * 16;
        asm volatile("" : "+v"(ctl), "+v"(one), "+v"(cty), "+v"(po), "+v"(go), "+v"(go2), "+v"(up), "+v"(yp), "+v"(rw), "+v"(rw4), "+v"(nrow));
        u32 wt[3];
#pragma unroll
        for (int k = WPF; k < 3; ++k) {
            wt[k] = wd_line(xcd, (36 * k + slot * 2 + (hw & 1)) * 48 + min(lane, 47));
            asm volatile("" : "+v"(wt[k]));
        }
        BAR();
        __builtin_amdgcn_s_setprio(3);
        u32 tag;
        {
            asm volatile("s_waitcnt vmcnt(%1)" : "+v"(tagv) : "n"(NB1 + XPF + 5) : "memory");
            tag = __builtin_amdgcn_readfirstlane(tagv) + 1u;
            v4u wa[5], wg_[5];
#pragma unroll
            for (int j = 0; j < 5; ++j) { wa[j] = *(LAS v4u*)((j < 4 ? rw : rw4) + j * 256); wg_[j] = *(LAS v4u*)((j < 4 ? rw : rw4) + 1280 + j * 256); }
#pragma unroll
            for (int j = 0; j < 5; ++j) asm volatile("" : "+v"(wa[j]), "+v"(wg_[j]));
            asm volatile("s_waitcnt vmcnt(%5)" : "+v"(xr[0]), "+v"(xr[1]), "+v"(xr[2]), "+v"(xr[3]), "+v"(xr[4]) : "n"(NB1 + XPF) : "memory");
            STAMP(2);
            if (sub >= 8) xr[4] = z4;
            float r = 0.f, s = 0.f;
#pragma unroll
            for (int j = 0; j < 5; ++j) {
                const v4u a = wa[j], g = wg_[j];
#pragma unroll
                for (int e = 0; e < 4; ++e) {
                    const u32 xe = xr[j][e], ae = a[e], ge = g[e];
                    r = __builtin_amdgcn_fdot2_f32_bf16(__builtin_bit_cast(bf16x2_t, xe), __builtin_bit_cast(bf16x2_t, ae), r, false);
                    s = __builtin_amdgcn_fdot2_f32_bf16(__builtin_bit_cast(bf16x2_t, xe), __builtin_bit_cast(bf16x2_t, ge), s, false);
                }
            }
            r = dpp_add<0x128, 0xf>(r); s = dpp_add<0x128, 0xf>(s); r = dpp_add<0x124, 0xf>(r); s = dpp_add<0x124, 0xf>(s);
            r = dpp_add<0x122, 0xf>(r); s = dpp_add<0x122, 0xf>(s); r = dpp_add<0x121, 0xf>(r); s = dpp_add<0x121, 0xf>(s);
            const u32 pay = make_payload(r, sigmoidf_(rbf(s))), kk = (pay & 0xffff0000u) | nrow;
            const u32 p0 = (u32)__builtin_amdgcn_readlane((int)pay, 0), p1 = (u32)__builtin_amdgcn_readlane((int)pay, 16), p2 = (u32)__builtin_amdgcn_readlane((int)pay, 32), p3 = (u32)__builtin_amdgcn_readlane((int)pay, 48);
            const auto sw = __builtin_amdgcn_permlane16_swap(kk, kk, false, false);
            const u32 g01 = (p0 & 0xffffu) | (p1 << 16), g23 = (p2 & 0xffffu) | (p3 << 16);
            const v4u pv = {sub ? g01 : sw[0], tag, sub ? g23 : sw[1], tag};
            asm volatile("s_mov_b64 exec, %3\n\tglobal_store_dwordx4 %0, %1, %2 sc1\n\ts_mov_b64 exec, -1\n\ts_nop 0" :: "v"(po), "v"(pv), "s"(p.ws), "s"(pmask) : "memory");
        }
        STAMP(3);
        __builtin_amdgcn_s_setprio(HPRIO);
        *(LAS u32*)(smem + LDS_CTL + C_TAG) = tag;
        LDS_ADD1(ctl, one, C_PUB);
        LWAIT(ctl, C_PUB, 4);
#pragma unroll
        for (int i = NB1; i < 4; ++i) LD_U(i);
        LDS_ADD1(ctl, one, C_PUB);
        LWAIT(ctl, C_PUB, 8);
        v4u gq[2];
#define POLL_G "global_load_dwordx4 v[100:103], %[go], %[ws] sc1\n\tglobal_load_dwordx4 v[104:107], %[go2], %[ws] sc1\n\t"
#define LD_U4 "global_load_dwordx2 %[u4], %[uo], %[x]\n\tglobal_load_dwordx2 %[u5], %[uo], %[x] offset:1152\n\tglobal_load_dwordx2 %[u6], %[uo], %[x] offset:2304\n\tglobal_load_dwordx2 %[u7], %[uo], %[x] offset:3456\n\t"
        asm volatile("" : "=&{v[100:103]}"(gq[0]), "=&{v[104:107]}"(gq[1]), "=&v"(uu[4]), "=&v"(uu[5]), "=&v"(uu[6]), "=&v"(uu[7]));
        asm volatile("s_cmp_ge_u32 %[hw], 2\n\ts_cbranch_scc1 1f\n\ts_sleep %[ud]\n\t" LD_U4 POLL_G "s_waitcnt vmcnt(6)\n\ts_branch 2f\n1:\n\ts_waitcnt vmcnt(0)\n2:"
                     : "+{v[100:103]}"(gq[0]), "+{v[104:107]}"(gq[1]), [u4] "+v"(uu[4]), [u5] "+v"(uu[5]), [u6] "+v"(uu[6]), [u7] "+v"(uu[7]), "+v"(uu[0]), "+v"(uu[1]), "+v"(uu[2]), "+v"(uu[3])
                     : [ud] "n"(UDELAY), [go] "v"(go), [go2] "v"(go2), [ws] "s"(p.ws), [uo] "v"(uo1), [x] "s"(p.x), [hw] "s"(hw) : "memory", "scc");
        STAMP(4);
        u32 ct2 = ctl + (hw & 2) * 2;
        auto stage = [&](int g) __attribute__((always_inline)) {
            const v2u* u4 = uu + 4 * g;
            LAS char* d = up + g * 2048;
            *(LAS v2u*)(d) = (v2u){__builtin_amdgcn_perm(u4[1].x, u4[0].x, 0x05040100u), __builtin_amdgcn_perm(u4[3].x, u4[2].x, 0x05040100u)};
            *(LAS v2u*)(d + 4608) = (v2u){__builtin_amdgcn_perm(u4[1].x, u4[0].x, 0x07060302u), __builtin_amdgcn_perm(u4[3].x, u4[2].x, 0x07060302u)};
            *(LAS v2u*)(d + 2 * 4608) = (v2u){__builtin_amdgcn_perm(u4[1].y, u4[0].y, 0x05040100u), __builtin_amdgcn_perm(u4[3].y, u4[2].y, 0x05040100u)};
            *(LAS v2u*)(d + 3 * 4608) = (v2u){__builtin_amdgcn_perm(u4[1].y, u4[0].y, 0x07060302u), __builtin_amdgcn_perm(u4[3].y, u4[2].y, 0x07060302u)};
            if (g) LDS_ADD1(ct2, one, C_ST2); else LDS_ADD1(ctl, one, C_STG);
        };
        stage(0);
        STAMP(5);
        asm volatile("s_cmp_lt_u32 %[hw], 2\n\ts_cbranch_scc1 1f\n\t" POLL_G LD_U4 "\n1:"
                     : "+{v[100:103]}"(gq[0]), "+{v[104:107]}"(gq[1]), [u4] "+v"(uu[4]), [u5] "+v"(uu[5]), [u6] "+v"(uu[6]), [u7] "+v"(uu[7])
                     : [go] "v"(go), [go2] "v"(go2), [ws] "s"(p.ws), [uo] "v"(uo1), [x] "s"(p.x), [hw] "s"(hw) : "memory", "scc");
        if (hw < 2) {
            asm volatile("s_waitcnt vmcnt(2)" : "+v"(uu[4]), "+v"(uu[5]), "+v"(uu[6]), "+v"(uu[7]));
            stage(1);
            STAMP(6);
        }
        {
            u32 c_;
            asm volatile("s_mov_b32 %[c], 0x80000\n\ts_waitcnt vmcnt(4)\n\ts_cmp_ge_u32 %[hw], 2\n\ts_cbranch_scc1 1f\n\ts_waitcnt vmcnt(0)\n1:\n\tv_cmpx_eq_u32 %[tag], v101\n\tv_cmpx_eq_u32 %[tag], v103\n\tv_cmpx_eq_u32 %[tag], v105\n\tv_cmpx_eq_u32 %[tag], v107\n\t"
                         "s_cmp_eq_u64 exec, -1\n\ts_mov_b64 exec, -1\n\ts_cbranch_scc1 2f\n\ts_sleep 4\n\t" POLL_G "s_waitcnt vmcnt(0)\n\ts_sub_u32 %[c], %[c], 1\n\ts_cbranch_scc0 1b\n2:\n\ts_nop 4"
                         : "+{v[100:103]}"(gq[0]), "+{v[104:107]}"(gq[1]), [c] "=&s"(c_) : [go] "v"(go), [go2] "v"(go2), [ws] "s"(p.ws), [tag] "s"(tag), [hw] "s"(hw) : "memory", "scc", "vcc");
        }
        STAMP(7);
        *(LAS v2u*)(smem + LDS_G + hw * 3072 + lane * 8) = (v2u){gq[0].x, gq[0].z}; *(LAS v2u*)(smem + LDS_G + hw * 3072 + 512 + lane * 8) = (v2u){gq[1].x, gq[1].z};
        if (hw < 2) {
#pragma unroll
            for (int k = WPF; k < 3; ++k) asm volatile("s_mov_b32 m0, %2\n\ts_nop 0\n\tglobal_load_lds_dword %0, %1" :: "v"(wt[k]), "s"(p.wd), "n"(LDS_DUMMY) : "memory");
        }
#pragma unroll
        for (int g = 0; g < 2; ++g) {
            if (g == 1 && hw >= 2) {
                asm volatile("s_waitcnt vmcnt(4)" : "+v"(uu[4]), "+v"(uu[5]), "+v"(uu[6]), "+v"(uu[7]));
                stage(1);
                STAMP(6);
            }
            const v2u* u4 = uu + 4 * g;
            const float gr[4] = {bflo(gq[g].x), bfhi(gq[g].x), bflo(gq[g].z), bfhi(gq[g].z)};
            float o[4][4];
#pragma unroll
            for (int r = 0; r < 4; ++r) {
                const float xv[4] = {bflo(u4[r].x), bfhi(u4[r].x), bflo(u4[r].y), bfhi(u4[r].y)};
#pragma unroll
                for (int c = 0; c < 4; ++c) asm("v_fma_f32 %0, -%1, %2, %2" : "=v"(o[r][c]) : "v"(gr[r]), "v"(xv[c]));
                asm volatile("" : "+v"(o[r][0]), "+v"(o[r][1]), "+v"(o[r][2]), "+v"(o[r][3]));
            }
            {
                u32 t_, c_;
                asm volatile("s_mov_b32 %1, 0x400000\n1:\n\tds_read_b32 %0, %2 offset:%3\n\ts_waitcnt lgkmcnt(0)\n\tv_cmp_eq_u32 vcc, 0, %0\n\ts_cmp_eq_u64 vcc, 0\n\ts_cbranch_scc1 2f\n\ts_sleep %4\n\ts_sub_u32 %1, %1, 1\n\ts_cbranch_scc0 1b\n2:"
                             : "=&v"(t_), "=&s"(c_) : "v"(cty), "n"(C_Y + 256 * g), "n"(YSLEEP) : "memory", "scc", "vcc");
            }
            if (g == 0) STAMP(11);
            v4f y[4];
#pragma unroll
            for (int c = 0; c < 4; ++c) y[c] = *(LAS v4f*)(yp + c * KCH + g * 4608);
            SBAR();
#define ST_O(i, v) asm volatile("global_store_dwordx2 %0, %1, %2 offset:%3" :: "v"(g ? uo1 : uo0), "v"(v), "s"(p.out), "n"((i) * D * 2) : "memory")
#pragma unroll
            for (int r = 0; r < 4; ++r) {
#pragma unroll
                for (int c = 0; c < 4; ++c) { const float yv = y[c][r]; asm("v_fmac_f32 %0, %1, %2" : "+v"(o[r][c]) : "v"(gr[r]), "v"(yv)); }
                ST_O(r, ((v2u){cvtpk(o[r][0], o[r][1]), cvtpk(o[r][2], o[r][3])}));
            }
            STAMP(8 + g);
        }
        asm volatile("s_waitcnt vmcnt(0)" ::: "memory");
        LDS_ADD1(ctl, one, C_ACK);
        STAMP(10);
        ROLE_END();
    }
    else
    {
        ROLE_BEGIN(0);
        const int ch = wave;
        u32 pi = ((chbase + ch) * NST + lane * 2) * 2, dl = (chbase + ch) * 2;
        asm volatile("" : "+v"(pi), "+v"(dl));
        ROLE_ARGS();
#define GLD(T, ptr, idx) __builtin_nontemporal_load((const GAS T*)((const GAS char*)(ptr) + (idx)))
        const u32 are = GLD(u32, p.Are, pi), aim = GLD(u32, p.Aim, pi), bre = GLD(u32, p.Bre, pi), bim = GLD(u32, p.Bim, pi), cre = GLD(u32, p.Cre, pi), cim = GLD(u32, p.Cim, pi);
        const u16 ldt = *(const GAS u16*)((const GAS char*)p.logdt + dl), dsk = *(const GAS u16*)((const GAS char*)p.dskip + dl);
        SBAR();
        __builtin_amdgcn_s_barrier();
        SBAR();
        LAS char* kb = smem + ch * KCH;
        const int r32 = lane & 31, h = lane >> 5, q = lane >> 4, n = lane & 15;
        const int ai = 4 * (r32 >> 3) + (r32 & 3), hh = (r32 >> 2) & 1;
        const u64 tsel = ai < 8 ? (hh ? KROW_1LO : KROW_0LO) : (hh ? KROW_1HI : KROW_0HI);
        const int o = (int)((tsel >> (8 * (ai & 7))) & 0xff);
        LAS char* ap = kb + o * STG_ROW + h * 16;
        LAS char* bp = kb + r32 * STG_ROW + h * 16;
        u32 sg[14];
#pragma unroll
        for (int k = 0; k < 14; ++k) { sg[k] = (u32)(uintptr_t)(kb + lane * 8 + k * 4 * STG_ROW); asm volatile("" : "+v"(sg[k])); }
        bf16x8 b5 = band(((0x1fu << 6) << n) >> (8 * q), 0x3f803f80u), b3 = band(((0x7u << 11) << n) >> (8 * q), 0x3f803f80u);
        bf16x8 b9 = band(((0x1ffu << 4) << n) >> (8 * q), 0x3f803f80u), b7 = band(((0x7fu << 5) << n) >> (8 * q), 0x3f803f80u);
        bf16x8 pat = band(((0x3u << 12) << n) >> (8 * q), 0xffffffffu);
        LAS char* k0 = kb + 32 + lane * 16;
        LAS char* kt = kb + (lane & 3) * KCOPY + (lane < 16 ? 32 + 1024 + (lane >> 2) * 16 : ((lane >> 2) & 1) * 16);
        LAS char* a0 = kb + ((lane >> 2) & 3) * KCOPY + (28 - 8 * q + 4 * (lane & 3)) * 2;
        LAS char* bb = smem + LDS_U + ch * 4608 + (16 * n + 8 * q - 16) * 2;
        LAS char* yb = kb + CH_Y + (4 * n + q + (n >> 1)) * 16;
        LAS char* tp = smem + LDS_TAP + ch * 2304 + ((n & 1) ? 13 + n - 4 * q : 96 + 14 + n - 4 * q) * 4;
        LAS char* ta = smem + LDS_TAP + ch * 2304 + (h ? 0 : 768) + 64 + 2 * r32;
        LAS char* tb = smem + LDS_TAP + ch * 2304 + 1600 + 64 * h + 2 * r32;
        u32 ctl = (u32)(uintptr_t)(smem + LDS_CTL), ctc = ctl + C_Y + ch * 16 + (lane & 15), one = 1u, so = OFF_SEL + (8 + xcd) * 256 + (lane & 7) * 8;
        u32 m01 = lane < 2 ? ~0u : 0u, m2 = lane == 2 ? ~0u : 0u, m3 = lane == 3 ? ~0u : 0u, m0_ = lane == 0 ? ~0u : 0u;
        asm volatile("" : "+v"(b5), "+v"(b3), "+v"(b9), "+v"(b7), "+v"(pat), "+v"(ap), "+v"(bp), "+v"(m01), "+v"(m2), "+v"(m3), "+v"(m0_));
        asm volatile("" : "+v"(k0), "+v"(kt), "+v"(a0), "+v"(bb), "+v"(yb), "+v"(tp), "+v"(ta), "+v"(tb), "+v"(ctl), "+v"(ctc), "+v"(one), "+v"(so));
        const char* hint = p.ws + (OFF_PART + cw * PP + hline(p.ws, cw >> 1));
        asm volatile("" : "+s"(hint));
        __builtin_amdgcn_s_barrier();
        SBAR();
        const float dt = rbf(expf(bf2f(ldt)));
        STAMP(1);
        v16f acc;
        u32 stg;
        float kscale;
        {
            const v2f a2 = (v2f){bflo(are), bfhi(are)} * dt * 1.44269504088896341f;
            const v2f brev = (v2f){bflo(aim), bfhi(aim)} * dt * 0.15915494309189535f;
            const cf2 Bc = {{dt * bflo(bre), dt * bflo(bim)}, {dt * bfhi(bre), dt * bfhi(bim)}};
            const cf2 Cc = {{bflo(cre), bflo(cim)}, {bfhi(cre), bfhi(cim)}};
            cf2 W = cmul2(Cc, Bc);
            u32 mx = max(max(__float_as_uint(W.a.x) & 0x7f800000u, __float_as_uint(W.a.y) & 0x7f800000u), max(__float_as_uint(W.b.x) & 0x7f800000u, __float_as_uint(W.b.y) & 0x7f800000u));
            mx = dpp_umax<0x111, 0xf>(mx); mx = dpp_umax<0x112, 0xf>(mx); mx = dpp_umax<0x114, 0xf>(mx); mx = dpp_umax<0x118, 0xf>(mx);
            mx = dpp_umax<0x142, 0xa>(mx); mx = dpp_umax<0x143, 0xc>(mx);
            mx = min(max((u32)__builtin_amdgcn_readlane((int)mx, 63), 0x00800000u), 0x7e800000u);
            kscale = __uint_as_float(mx);
            const float inv = __uint_as_float(0x7f000000u - mx);
            W.a *= inv; W.b *= inv;
            const cf2 E1 = cexp2(a2, brev, 1.f), E8 = cexp2(a2, brev, 8.f), E256 = cexp2(a2, brev, 256.f);
            asm volatile("s_nop 0\n\t" KGEN_ASM :: "{v[48:49]}"(W.a), "{v[50:51]}"(W.b), "{v[52:53]}"(E1.a), "{v[54:55]}"(E1.b), "{v[56:57]}"(E8.a), "{v[58:59]}"(E8.b), "{v[60:61]}"(E256.a), "{v[62:63]}"(E256.b),
                         [b0] "v"(sg[0]), [b1] "v"(sg[1]), [b2] "v"(sg[2]), [b3] "v"(sg[3]), [b4] "v"(sg[4]), [b5] "v"(sg[5]), [b6] "v"(sg[6]), [b7] "v"(sg[7]), [b8] "v"(sg[8]), [b9] "v"(sg[9]),
                         [b10] "v"(sg[10]), [b11] "v"(sg[11]), [b12] "v"(sg[12]), [b13] "v"(sg[13]) : "memory", KGEN_CLOBBER);
            STAMP(2);
            asm volatile(KMFMA_ASM : "=&{v[48:63]}"(acc), [stg] "=&v"(stg) : [ap] "v"(ap), [bp] "v"(bp), [ctl] "v"(ctl) : "memory", KMFMA_CLOBBER);
            acc *= kscale;
        }
        STAMP(3);
        acc[0] += __uint_as_float(((u32)dsk << 16) & m0_);
        u32 t78 = cvtpk(acc[7], acc[8]), t9a = cvtpk(acc[9], acc[10]);
        {
            const auto s0 = __builtin_amdgcn_permlane32_swap(cvtpk(acc[0], acc[1]), cvtpk(acc[4], acc[4]), false, false);
            const auto s1 = __builtin_amdgcn_permlane32_swap(cvtpk(acc[2], acc[3]), cvtpk(acc[5], acc[6]), false, false);
            const v4u ww = {s0[0], s1[0], s0[1], s1[1]};
            const u32 pz = (u32)__builtin_amdgcn_update_dpp(0, (int)ww.z, 0x138, 0xf, 0xf, true), pw = (u32)__builtin_amdgcn_update_dpp(0, (int)ww.w, 0x138, 0xf, 0xf, true);
            const v4u c1 = {__builtin_amdgcn_alignbit(ww.x, pw, 16), __builtin_amdgcn_alignbit(ww.y, ww.x, 16), __builtin_amdgcn_alignbit(ww.z, ww.y, 16), __builtin_amdgcn_alignbit(ww.w, ww.z, 16)};
            *(LAS v4u*)(k0) = ww;
            *(LAS v4u*)(k0 + KCOPY) = c1;
            *(LAS v4u*)(k0 + 2 * KCOPY) = (v4u){pw, ww.x, ww.y, ww.z};
            *(LAS v4u*)(k0 + 3 * KCOPY) = (v4u){__builtin_amdgcn_alignbit(pw, pz, 16), c1.x, c1.y, c1.z};
            const u32 w0 = (u32)__builtin_amdgcn_readlane((int)ww.z, 63), w1 = (u32)__builtin_amdgcn_readlane((int)ww.w, 63);
            const u32 w3 = w1 >> 16, w2 = w3 * 0x10001u, w10 = (w0 >> 16) | (w1 << 16);
            *(LAS v4u*)kt = (v4u){(w2 & m01) | (w1 & m2) | (w10 & m3), (w3 & m0_) | (w2 & ((m01 ^ m0_) | m2 | m3)), (w3 & m2) | (w2 & m3), 0u};
        }
        asm volatile("" ::: "memory");
        pat = __builtin_bit_cast(bf16x8, __builtin_bit_cast(v4u, pat) & (((u32)__builtin_amdgcn_readlane((int)t78, 63) >> 16) * 0x10001u));
#define POLL_SEL "global_load_dwordx2 v[240:241], %[so], %[ws] sc1\n\t"
        v2u sq;
        const v4f zero4 = {0.f, 0.f, 0.f, 0.f};
        if (MPRIO) __builtin_amdgcn_s_setprio(MPRIO);
        STAMP(4);
        {
            u32 s_, c_;
            asm volatile(CONV_ASM : "=&{v[240:241]}"(sq), [s] "=&s"(s_), [c] "=&s"(c_)
                         : "{v[4:7]}"(b5), "{v[8:11]}"(b3), "{v[12:15]}"(b9), "{v[16:19]}"(b7), "{v[20:23]}"(pat), [a0] "v"(a0), [bb] "v"(bb), [yb] "v"(yb), [tp] "v"(tp), [ta] "v"(ta), [tb] "v"(tb),
                           [ctl] "v"(ctl), [ctc] "v"(ctc), [one] "v"(one), [so] "v"(so), [t78] "v"(t78), [t9a] "v"(t9a), [stg] "v"(stg), [ws] "s"(p.ws) : "memory", "scc", CONV_CLOBBER);
        }
        STAMP(9);
        asm volatile("s_cmp_eq_u32 %0, 0\n\ts_cbranch_scc1 1f\n\ts_endpgm\n1:" :: "s"(wave) : "memory", "scc");
        {
            u32 h_;
            asm volatile("s_load_dword %0, %1, 0x4 glc" : "=&s"(h_) : "s"(hint) : "memory");
            const u32 tag = __builtin_amdgcn_readfirstlane(*(LAS u32*)(smem + LDS_CTL + C_TAG));
            u32 c_;
            u32 pb[9];
#pragma unroll
            for (int k = 0; k < 9; ++k) { pb[k] = OFF_PART + cw * PP + hline(p.ws, 8 * k + (lane >> 3)) + (lane & 7) * 16; asm volatile("" : "+v"(pb[k])); }
            {
                asm volatile("s_mov_b32 %[c], 64\n1:\n\ts_waitcnt lgkmcnt(0)\n\ts_cmp_eq_u32 %[h], %[tag]\n\ts_cbranch_scc1 2f\n\ts_load_dword %[h], %[a], 0x4 glc\n\ts_sub_u32 %[c], %[c], 1\n\ts_cbranch_scc0 1b\n\ts_waitcnt lgkmcnt(0)\n2:\n\ts_sleep %[hs]"
                             : [h] "+s"(h_), [c] "=&s"(c_) : [a] "s"(hint), [tag] "s"(tag), [hs] "n"(HSLEEP) : "memory", "scc");
            }
            STAMP(10);
            typedef __attribute__((ext_vector_type(32))) u32 v32u;
            v32u ra, rb; v4u rc, rd;
            asm volatile("global_load_dwordx4 v[64:67], %[pb0], %[ws] sc1\n\tglobal_load_dwordx4 v[68:71], %[pb1], %[ws] sc1\n\tglobal_load_dwordx4 v[72:75], %[pb2], %[ws] sc1\n\tglobal_load_dwordx4 v[76:79], %[pb3], %[ws] sc1\n\tglobal_load_dwordx4 v[80:83], %[pb4], %[ws] sc1\n\tglobal_load_dwordx4 v[84:87], %[pb5], %[ws] sc1\n\tglobal_load_dwordx4 v[88:91], %[pb6], %[ws] sc1\n\tglobal_load_dwordx4 v[92:95], %[pb7], %[ws] sc1\n\tglobal_load_dwordx4 v[96:99], %[pb8], %[ws] sc1\n\t" : "=&{v[64:95]}"(ra), "=&{v[96:99]}"(rc) : [pb0] "v"(pb[0]), [pb1] "v"(pb[1]), [pb2] "v"(pb[2]), [pb3] "v"(pb[3]), [pb4] "v"(pb[4]), [pb5] "v"(pb[5]), [pb6] "v"(pb[6]), [pb7] "v"(pb[7]), [pb8] "v"(pb[8]), [ws] "s"(p.ws) : "memory");
            asm volatile("s_mov_b32 %[c], 0x80000\n\ts_waitcnt vmcnt(9)\n1:\n\tv_cmpx_eq_u32 %[tag], v241\n\t"
                         "s_cmp_eq_u64 exec, -1\n\ts_mov_b64 exec, -1\n\ts_cbranch_scc1 2f\n\t" POLL_SEL "s_waitcnt vmcnt(0)\n\ts_sub_u32 %[c], %[c], 1\n\ts_cbranch_scc0 1b\n2:\n\ts_nop 4"
                         : "+{v[240:241]}"(sq), [c] "=&s"(c_) : [so] "v"(so), [ws] "s"(p.ws), [tag] "s"(tag) : "memory", "scc", "vcc");
            const int t = (int)(0xffffu - (sq.x & 0xffffu)) & 2047;
            const float rs = rbf(sigmoidf_(key_to_r(sq.x)));
            LWAIT(ctl, C_ACK, 4);
            const float g = bf2f(*(LAS u16*)(smem + LDS_G + ((t >> 8) & 3) * 3072 + (t >> 10) * 512 + (t & 255) * 2));
            const int uq = (t & 255) >> 2;
            float xv[4], av[4];
#pragma unroll
            for (int c = 0; c < 4; ++c) {
                xv[c] = bf2f(*(LAS u16*)(smem + LDS_U + c * 4608 + (256 + t) * 2));
                av[c] = g * *(LAS float*)(smem + c * KCH + CH_Y + (t >> 8) * 1152 + (uq + (uq >> 3)) * 16 + (t & 3) * 4);
            }
            const u32 e1 = n < 4 ? ((n & 1) ? 0x3f800000u : 0x00003f80u) : 0u;
            bf16x8 sel4 = __builtin_bit_cast(bf16x8, (n & 2) ? (v4u){0u, 0u, e1, e1} : (v4u){e1, e1, 0u, 0u});
            u32 oo = (u32)(t * D + chbase) * 2;
            float omg = 1.f - g;
            asm volatile("" : "+v"(sel4), "+v"(oo), "+v"(omg), "+v"(xv[0]), "+v"(xv[1]), "+v"(xv[2]), "+v"(xv[3]), "+v"(av[0]), "+v"(av[1]), "+v"(av[2]), "+v"(av[3]));
            v4f s0, s1;
            asm volatile("s_mov_b32 %[c], 0x40000\n\ts_sleep %[bs]\n\tglobal_load_dwordx4 v[100:103], %[pb0], %[ws] sc1\n\tglobal_load_dwordx4 v[104:107], %[pb1], %[ws] sc1\n\tglobal_load_dwordx4 v[108:111], %[pb2], %[ws] sc1\n\tglobal_load_dwordx4 v[112:115], %[pb3], %[ws] sc1\n\tglobal_load_dwordx4 v[116:119], %[pb4], %[ws] sc1\n\tglobal_load_dwordx4 v[120:123], %[pb5], %[ws] sc1\n\tglobal_load_dwordx4 v[124:127], %[pb6], %[ws] sc1\n\tglobal_load_dwordx4 v[128:131], %[pb7], %[ws] sc1\n\tglobal_load_dwordx4 v[132:135], %[pb8], %[ws] sc1\n\t"
                         "1:\n\ts_waitcnt vmcnt(9)\n\tv_cmpx_eq_u32 %[tag], v65\n\tv_cmpx_eq_u32 %[tag], v67\n\tv_cmpx_eq_u32 %[tag], v69\n\tv_cmpx_eq_u32 %[tag], v71\n\tv_cmpx_eq_u32 %[tag], v73\n\tv_cmpx_eq_u32 %[tag], v75\n\tv_cmpx_eq_u32 %[tag], v77\n\tv_cmpx_eq_u32 %[tag], v79\n\tv_cmpx_eq_u32 %[tag], v81\n\tv_cmpx_eq_u32 %[tag], v83\n\tv_cmpx_eq_u32 %[tag], v85\n\tv_cmpx_eq_u32 %[tag], v87\n\tv_cmpx_eq_u32 %[tag], v89\n\tv_cmpx_eq_u32 %[tag], v91\n\tv_cmpx_eq_u32 %[tag], v93\n\tv_cmpx_eq_u32 %[tag], v95\n\tv_cmpx_eq_u32 %[tag], v97\n\tv_cmpx_eq_u32 %[tag], v99\n\ts_cmp_eq_u64 exec, -1\n\ts_mov_b64 exec, -1\n\ts_cbranch_scc1 3f\n\tglobal_load_dwordx4 v[64:67], %[pb0], %[ws] sc1\n\tglobal_load_dwordx4 v[68:71], %[pb1], %[ws] sc1\n\tglobal_load_dwordx4 v[72:75], %[pb2], %[ws] sc1\n\tglobal_load_dwordx4 v[76:79], %[pb3], %[ws] sc1\n\tglobal_load_dwordx4 v[80:83], %[pb4], %[ws] sc1\n\tglobal_load_dwordx4 v[84:87], %[pb5], %[ws] sc1\n\tglobal_load_dwordx4 v[88:91], %[pb6], %[ws] sc1\n\tglobal_load_dwordx4 v[92:95], %[pb7], %[ws] sc1\n\tglobal_load_dwordx4 v[96:99], %[pb8], %[ws] sc1\n\t"
                         "s_waitcnt vmcnt(9)\n\tv_cmpx_eq_u32 %[tag], v101\n\tv_cmpx_eq_u32 %[tag], v103\n\tv_cmpx_eq_u32 %[tag], v105\n\tv_cmpx_eq_u32 %[tag], v107\n\tv_cmpx_eq_u32 %[tag], v109\n\tv_cmpx_eq_u32 %[tag], v111\n\tv_cmpx_eq_u32 %[tag], v113\n\tv_cmpx_eq_u32 %[tag], v115\n\tv_cmpx_eq_u32 %[tag], v117\n\tv_cmpx_eq_u32 %[tag], v119\n\tv_cmpx_eq_u32 %[tag], v121\n\tv_cmpx_eq_u32 %[tag], v123\n\tv_cmpx_eq_u32 %[tag], v125\n\tv_cmpx_eq_u32 %[tag], v127\n\tv_cmpx_eq_u32 %[tag], v129\n\tv_cmpx_eq_u32 %[tag], v131\n\tv_cmpx_eq_u32 %[tag], v133\n\tv_cmpx_eq_u32 %[tag], v135\n\ts_cmp_eq_u64 exec, -1\n\ts_mov_b64 exec, -1\n\ts_cbranch_scc1 2f\n\tglobal_load_dwordx4 v[100:103], %[pb0], %[ws] sc1\n\tglobal_load_dwordx4 v[104:107], %[pb1], %[ws] sc1\n\tglobal_load_dwordx4 v[108:111], %[pb2], %[ws] sc1\n\tglobal_load_dwordx4 v[112:115], %[pb3], %[ws] sc1\n\tglobal_load_dwordx4 v[116:119], %[pb4], %[ws] sc1\n\tglobal_load_dwordx4 v[120:123], %[pb5], %[ws] sc1\n\tglobal_load_dwordx4 v[124:127], %[pb6], %[ws] sc1\n\tglobal_load_dwordx4 v[128:131], %[pb7], %[ws] sc1\n\tglobal_load_dwordx4 v[132:135], %[pb8], %[ws] sc1\n\t"
                         "s_sub_u32 %[c], %[c], 1\n\ts_cbranch_scc0 1b\n\ts_waitcnt vmcnt(0)\n"
                         "2:\n\tv_mov_b32 v101, v104\n\tv_mov_b32 v103, v106\n\tv_mov_b32 v109, v112\n\tv_mov_b32 v111, v114\n\tv_mov_b32 v117, v120\n\tv_mov_b32 v119, v122\n\tv_mov_b32 v125, v128\n\tv_mov_b32 v127, v130\n\tv_mov_b32 v133, 0\n\tv_mov_b32 v135, 0\n\tv_mfma_f32_16x16x32_bf16 v[136:139], %[sel], v[100:103], 0\n\tv_mfma_f32_16x16x32_bf16 v[140:143], %[sel], v[108:111], 0\n\tv_mfma_f32_16x16x32_bf16 v[136:139], %[sel], v[116:119], v[136:139]\n\tv_mfma_f32_16x16x32_bf16 v[140:143], %[sel], v[124:127], v[140:143]\n\tv_mfma_f32_16x16x32_bf16 v[136:139], %[sel], v[132:135], v[136:139]\n\ts_branch 4f\n"
                         "3:\n\tv_mov_b32 v65, v68\n\tv_mov_b32 v67, v70\n\tv_mov_b32 v73, v76\n\tv_mov_b32 v75, v78\n\tv_mov_b32 v81, v84\n\tv_mov_b32 v83, v86\n\tv_mov_b32 v89, v92\n\tv_mov_b32 v91, v94\n\tv_mov_b32 v97, 0\n\tv_mov_b32 v99, 0\n\tv_mfma_f32_16x16x32_bf16 v[136:139], %[sel], v[64:67], 0\n\tv_mfma_f32_16x16x32_bf16 v[140:143], %[sel], v[72:75], 0\n\tv_mfma_f32_16x16x32_bf16 v[136:139], %[sel], v[80:83], v[136:139]\n\tv_mfma_f32_16x16x32_bf16 v[140:143], %[sel], v[88:91], v[140:143]\n\tv_mfma_f32_16x16x32_bf16 v[136:139], %[sel], v[96:99], v[136:139]\n\t\n4:\n\ts_nop 7"
                         : "+{v[64:95]}"(ra), "+{v[96:99]}"(rc), "=&{v[100:131]}"(rb), "=&{v[132:135]}"(rd), "=&{v[136:139]}"(s0), "=&{v[140:143]}"(s1), [c] "=&s"(c_)
                         : [pb0] "v"(pb[0]), [pb1] "v"(pb[1]), [pb2] "v"(pb[2]), [pb3] "v"(pb[3]), [pb4] "v"(pb[4]), [pb5] "v"(pb[5]), [pb6] "v"(pb[6]), [pb7] "v"(pb[7]), [pb8] "v"(pb[8]), [ws] "s"(p.ws), [tag] "s"(tag), [sel] "v"(sel4), [bs] "n"(BSLEEP) : "memory", "scc", "vcc");
            STAMP(11);
            float o[4];
#pragma unroll
            for (int c = 0; c < 4; ++c) o[c] = fmaf(omg, xv[c] + rbf(rbf(dpp_add<0x128, 0xf>(s0[c] + s1[c])) * rs), av[c]);
            asm volatile("s_mov_b64 exec, 0xff\n\tglobal_store_dwordx2 %0, %1, %2\n\ts_mov_b64 exec, -1" :: "v"(oo), "v"((v2u){cvtpk(o[0], o[1]), cvtpk(o[2], o[3])}), "s"(p.out) : "memory");
            STAMP(12);
            asm volatile("s_waitcnt vmcnt(0)" :: "v"(ra), "v"(rb), "v"(rc), "v"(rd) : "memory");
        }
        ROLE_END();
    }
    }
}

extern "C" void kernel_launch(void* const* d_in, const int* in_sizes, int n_in,
                              void* d_out, int out_size, void* d_ws, size_t ws_size,
                              hipStream_t stream) {
    Params p;
    p.x = (const u16*)d_in[0]; p.Are = (const u16*)d_in[1]; p.Aim = (const u16*)d_in[2]; p.Bre = (const u16*)d_in[3];
    p.Bim = (const u16*)d_in[4]; p.Cre = (const u16*)d_in[5]; p.Cim = (const u16*)d_in[6]; p.logdt = (const u16*)d_in[7];
    p.dskip = (const u16*)d_in[8]; p.wr = (const u16*)d_in[10]; p.wg = (const u16*)d_in[11]; p.wu = (const u16*)d_in[12];
    p.wd = (const u16*)d_in[13]; p.gw = (const u16*)d_in[14]; p.out = (u16*)d_out; p.ws = (char*)d_ws;
    (void)hipFuncSetAttribute((const void*)fused_kernel, hipFuncAttributeMaxDynamicSharedMemorySize, LDS_ALLOC);
    fused_kernel<<<256, 512, LDS_ALLOC, stream>>>(p);
}
```

```cpp
#include <hip/hip_runtime.h>
#include <stdint.h>

typedef unsigned short u16;
typedef unsigned int u32;
typedef unsigned long long u64;
typedef __attribute__((ext_vector_type(4))) u32 v4u;
typedef __attribute__((ext_vector_type(2))) u32 v2u;
typedef __attribute__((ext_vector_type(2))) float v2f;
typedef __attribute__((ext_vector_type(4))) float v4f;
typedef __attribute__((ext_vector_type(16))) float v16f;
typedef __attribute__((ext_vector_type(8))) short bf16x8;
typedef __attribute__((ext_vector_type(4))) short s16x4;
typedef __attribute__((ext_vector_type(2))) __bf16 bf16x2_t;
#define LAS __attribute__((address_space(3)))
#define GAS __attribute__((address_space(1)))
#define RLX_AGENT __ATOMIC_RELAXED, __HIP_MEMORY_SCOPE_AGENT
#define SBAR() __builtin_amdgcn_sched_barrier(0)
#define BAR() asm volatile("s_waitcnt lgkmcnt(0)\n\ts_barrier" ::: "memory")
#define WSR() __builtin_amdgcn_make_buffer_rsrc(p.ws, 0, 0x7fffffff, 0x00020000)
#define REREAD() asm volatile("" ::: "memory")

#define WNT " nt"
constexpr int CODE_KB = 24;
constexpr int NB1 = 2;
constexpr int REPG = 2;
constexpr int PFG0 = 528;
constexpr int WPF = 2;
constexpr int SELWAIT = 50;

constexpr int L = 2048, D = 576, NST = 128, F = 4608;
constexpr int NFFN = 72;
constexpr u32 OFF_RG = 4096;
constexpr u32 OFF_G = OFF_RG + 20480;
constexpr u32 OFF_SEL = OFF_G + REPG * 10240;
constexpr u32 OFF_CI = OFF_SEL + 16 * 256;
constexpr u32 OFF_PART = OFF_CI + 4096;
constexpr u32 PP = 11 * 1024;
constexpr u32 SPIN_CAP = 1u << 19;


struct Params {
    const u16 *x, *Are, *Aim, *Bre, *Bim, *Cre, *Cim, *logdt, *dskip, *wr, *wg, *wu, *wd, *gw;
    u16* out;
    char* ws;
};

__device__ __forceinline__ float bflo(u32 w) { return __uint_as_float(w << 16); }
__device__ __forceinline__ float bfhi(u32 w) { return __uint_as_float(w & 0xffff0000u); }
__device__ __forceinline__ float bf2f(u16 h) { return __uint_as_float((u32)h << 16); }
__device__ __forceinline__ u32 cvtpk(float lo, float hi) { v2f v = {lo, hi}; bf16x2_t r = __builtin_convertvector(v, bf16x2_t); return __builtin_bit_cast(u32, r); }
__device__ __forceinline__ float rbf(float f) { return bflo(cvtpk(f, f)); }
__device__ __forceinline__ float sigmoidf_(float s) { return __builtin_amdgcn_rcpf(1.f + __expf(-s)); }

template <int CTRL, int RM> __device__ __forceinline__ float dpp_add(float v) {
    return v + __builtin_bit_cast(float, __builtin_amdgcn_update_dpp(0, __builtin_bit_cast(int, v), CTRL, RM, 0xf, true));
}
template <int CTRL, int RM> __device__ __forceinline__ u32 dpp_umax(u32 v) {
    u32 o = (u32)__builtin_amdgcn_update_dpp(0, (int)v, CTRL, RM, 0xf, true);
    return v > o ? v : o;
}

typedef v2f cf;
__device__ __forceinline__ cf cmul(cf a, cf b) { cf t = a.yy * (cf){-b.y, b.x}; return __builtin_elementwise_fma(a.xx, b, t); }
__device__ __forceinline__ cf cexp_(float a2, float brev, float rho) {
    float m = __builtin_amdgcn_exp2f(a2 * rho);
    float ph = __builtin_amdgcn_fractf(brev * rho);
    cf c; c.x = m * __builtin_amdgcn_cosf(ph); c.y = m * __builtin_amdgcn_sinf(ph); return c;
}

struct cf2 { cf a, b; };
__device__ __forceinline__ cf2 cmul2(cf2 x, cf2 y) { return {cmul(x.a, y.a), cmul(x.b, y.b)}; }
__device__ __forceinline__ cf2 cexp2(v2f a2, v2f brev, float rho) { return {cexp_(a2.x, brev.x, rho), cexp_(a2.y, brev.y, rho)}; }

__device__ __forceinline__ u32 make_payload(float r, float g) {
    const u32 w = cvtpk(g, r);
    return w ^ (((u32)((int)w >> 31) & 0x7fff0000u) | 0x80000000u);
}
__device__ __forceinline__ float key_to_r(u32 k) {
    u32 hi = k & 0xffff0000u;
    return __uint_as_float((hi & 0x80000000u) ? (hi ^ 0x80000000u) : (~hi & 0xffff0000u));
}


constexpr int STG_ROW = 528;
constexpr int NA = 21;
constexpr int KCH = (NA + 32) * STG_ROW;
constexpr int KCOPY = 1344;
constexpr int CH_Y = 4 * KCOPY;
constexpr int LDS_U = 4 * KCH;
constexpr int LDS_TAP = LDS_U + 4 * 4608;
constexpr int LDS_CTL = LDS_TAP + 4 * 2304;
constexpr int C_TAG = 0, C_STG = 4, C_PUB = 8, C_ACK = 12, C_ST2 = 20, C_Y = 64;
constexpr int LDS_RW = LDS_CTL + 1024;
constexpr int LDS_G = LDS_RW;
constexpr int LDS_DUMMY = LDS_RW + 4 * 3072;
constexpr int LDS_TOTAL = LDS_DUMMY + 1024;
static_assert(CH_Y + 8 * 1152 <= KCH && LDS_TOTAL <= 160 * 1024, "lds");
constexpr int WDP = 1184;
constexpr int XSP = 1168;
constexpr int FL_XS = 144 * 1024;
constexpr int FL_H = FL_XS + 8 * XSP;
constexpr int FL_Z = FL_H + 8 * 144;
constexpr int FL_TAG = FL_Z + 256;
constexpr int LDS_ALLOC = FL_TAG + 64 > LDS_TOTAL ? FL_TAG + 64 : LDS_TOTAL;
static_assert(LDS_ALLOC <= 160 * 1024, "lds");

__device__ __forceinline__ u32 top8_d(LAS char* t8, int lane, int n) {
    LAS u32* cand = (LAS u32*)(t8 + 256);
    u32 res = 0;
    _Pragma("unroll 1") for (int i0 = 0; i0 < n; i0 += 64) {
        const int i = i0 + lane;
        const u32 k = i < n ? cand[i] : 0u;
        int rk = 0;
        _Pragma("unroll 1") for (int j0 = 0; j0 < n; j0 += 16) {
            v4u c[4];
#pragma unroll
            for (int e = 0; e < 4; ++e) c[e] = *(LAS v4u*)(cand + j0 + 4 * e);
#pragma unroll
            for (int e = 0; e < 16; ++e) rk += (c[e >> 2][e & 3] > k) & (j0 + e < n);
        }
        const bool hit = i < n && rk < 8;
        const u32 got = (u32)__builtin_amdgcn_ds_permute((hit ? rk : 63) * 4, (int)(hit ? k : 0u));
        if (lane < 8) res |= got;
    }
    return res;
}


#define LWAIT_BODY "1:\n\tds_read_b32 %0, %3 offset:%4\n\ts_waitcnt lgkmcnt(0)\n\tv_readfirstlane_b32 %1, %0\n\ts_cmp_ge_u32 %1, %5\n\ts_cbranch_scc1 2f\n\ts_sleep 1\n\ts_sub_u32 %2, %2, 1\n\ts_cbranch_scc0 1b\n2:"
#define LWAIT(addr, off, want) do { u32 t_, s_, c_; asm volatile("s_mov_b32 %2, 0x400000\n" LWAIT_BODY : "=&v"(t_), "=&s"(s_), "=&s"(c_) : "v"(addr), "n"(off), "n"(want) : "memory", "scc"); } while (0)
#define LDS_ADD1(addr, one, off) asm volatile("s_mov_b64 exec, 1\n\tds_add_u32 %0, %1 offset:%2\n\ts_mov_b64 exec, -1" :: "v"(addr), "v"(one), "n"(off) : "memory")
__device__ __forceinline__ bf16x8 band(u32 bits, u32 val) {
    v4u r;
#pragma unroll
    for (int j = 0; j < 4; ++j) r[j] = __builtin_amdgcn_perm((u32)__builtin_amdgcn_sbfe((int)bits, 2 * j + 1, 1), (u32)__builtin_amdgcn_sbfe((int)bits, 2 * j, 1), 0x05040100u) & val;
    return __builtin_bit_cast(bf16x8, r);
}

__device__ __forceinline__ u32 hline(const char* ws, u32 i) { const u32 q = i / 7u, r = i - 7u * q; return (8u * q + r + (r >= ((3u - (u32)((uintptr_t)ws >> 7)) & 7u))) * 128u; }
__device__ __forceinline__ u32 wd_line(u32 xcd, u32 i) {
    const u32 k = i / 9u, r = k / 9u;
    return ((xcd * 9u + k - 9u * r) * 64u + (r >> 1) + 32u * (r & 1)) * 1152u + (i - 9u * k) * 128u;
}
__device__ __forceinline__ bool touch(const Params& p, int xcd, int e, int lane, u32 dst) {
    const int wa = xcd * 9 + ((3 - (int)(((uintptr_t)p.wg >> 7) & 7) - xcd * 9) & 7), two = wa == xcd * 9;
    const int g0 = two ? 576 : PFG0, na = (g0 * 2) << two, nb = (576 - g0) * 18, nc = WPF * 1728;
    if (e * 64 >= na + nb + nc) return false;
    const u32 i = min(e * 64 + lane, na + nb + nc - 1);
    const u32 j = i - na, k = j / 9u;
    const bool first = i < (u32)na, last = i >= (u32)(na + nb);
    const u32 seg = first ? i >> two : k + 2 * g0, col = first ? (wa + 8 * (i & two)) * 128u : xcd * 1152u + (j - 9u * k) * 128u;
    const char* a = (const char*)(last ? p.wd : ((seg & 1) ? p.wu : p.wg)) + (last ? wd_line(xcd, j - nb) : (seg >> 1) * (F * 2) + col);
    asm volatile("s_mov_b32 m0, %1\n\ts_nop 0\n\tglobal_load_lds_dword %0, off" :: "v"(a), "s"(dst) : "memory");
    return true;
}

#define KGEN_ASM ".p2align 3\n\tv_mov_b32 v64, 0x3c00\n\tv_mov_b32 v65, 0x3c00\n\tv_pk_mul_f32 v[66:67], v[56:57], v[56:57] op_sel:[1,1] op_sel_hi:[1,0] neg_lo:[0,1]\n\tv_pk_mul_f32 v[68:69], v[58:59], v[58:59] op_sel:[1,1] op_sel_hi:[1,0] neg_lo:[0,1]\n\tv_pk_mul_f32 v[70:71], v[52:53], v[52:53] op_sel:[1,1] op_sel_hi:[1,0] neg_lo:[0,1]\n\tv_pk_mul_f32 v[72:73], v[54:55], v[54:55] op_sel:[1,1] op_sel_hi:[1,0] neg_lo:[0,1]\n\tv_pk_fma_f32 v[66:67], v[56:57], v[56:57], v[66:67] op_sel:[0,0,0] op_sel_hi:[0,1,1]\n\tv_pk_fma_f32 v[68:69], v[58:59], v[58:59], v[68:69] op_sel:[0,0,0] op_sel_hi:[0,1,1]\n\tv_pk_fma_f32 v[70:71], v[52:53], v[52:53], v[70:71] op_sel:[0,0,0] op_sel_hi:[0,1,1]\n\tv_pk_fma_f32 v[72:73], v[54:55], v[54:55], v[72:73] op_sel:[0,0,0] op_sel_hi:[0,1,1]\n\tv_cvt_pk_f16_f32 v74, v56, -v57\n\tv_cvt_pk_f16_f32 v75, v58, -v59\n\tv_cvt_pk_f16_f32 v76, v48, v49\n\tv_cvt_pk_f16_f32 v77, v50, v51\n\tv_pk_mul_f32 v[78:79], v[48:49], v[60:61] op_sel:[1,1] op_sel_hi:[1,0] neg_lo:[0,1]\n\tv_pk_mul_f32 v[80:81], v[50:51], v[62:63] op_sel:[1,1] op_sel_hi:[1,0] neg_lo:[0,1]\n\tv_pk_mul_f32 v[82:83], v[48:49], v[52:53] op_sel:[1,1] op_sel_hi:[1,0] neg_lo:[0,1]\n\tv_pk_mul_f32 v[84:85], v[50:51], v[54:55] op_sel:[1,1] op_sel_hi:[1,0] neg_lo:[0,1]\n\tds_write2_b64 %[b0], v[64:65], v[74:75] offset0:0 offset1:66\n\tv_pk_fma_f32 v[78:79], v[48:49], v[60:61], v[78:79] op_sel:[0,0,0] op_sel_hi:[0,1,1]\n\tv_pk_fma_f32 v[80:81], v[50:51], v[62:63], v[80:81] op_sel:[0,0,0] op_sel_hi:[0,1,1]\n\tv_pk_fma_f32 v[82:83], v[48:49], v[52:53], v[82:83] op_sel:[0,0,0] op_sel_hi:[0,1,1]\n\tv_pk_fma_f32 v[84:85], v[50:51], v[54:55], v[84:85] op_sel:[0,0,0] op_sel_hi:[0,1,1]\n\tv_cvt_pk_f16_f32 v86, v66, -v67\n\tv_cvt_pk_f16_f32 v87, v68, -v69\n\tv_pk_mul_f32 v[88:89], v[66:67], v[66:67] op_sel:[1,1] op_sel_hi:[1,0] neg_lo:[0,1]\n\tv_pk_mul_f32 v[90:91], v[68:69], v[68:69] op_sel:[1,1] op_sel_hi:[1,0] neg_lo:[0,1]\n\tv_pk_mul_f32 v[92:93], v[66:67], v[56:57] op_sel:[1,1] op_sel_hi:[1,0] neg_lo:[0,1]\n\tv_pk_mul_f32 v[94:95], v[68:69], v[58:59] op_sel:[1,1] op_sel_hi:[1,0] neg_lo:[0,1]\n\tv_pk_fma_f32 v[88:89], v[66:67], v[66:67], v[88:89] op_sel:[0,0,0] op_sel_hi:[0,1,1]\n\tv_pk_fma_f32 v[90:91], v[68:69], v[68:69], v[90:91] op_sel:[0,0,0] op_sel_hi:[0,1,1]\n\tv_pk_fma_f32 v[92:93], v[66:67], v[56:57], v[92:93] op_sel:[0,0,0] op_sel_hi:[0,1,1]\n\tv_pk_fma_f32 v[94:95], v[68:69], v[58:59], v[94:95] op_sel:[0,0,0] op_sel_hi:[0,1,1]\n\tv_cvt_pk_f16_f32 v64, v78, v79\n\tv_cvt_pk_f16_f32 v65, v80, v81\n\tv_cvt_pk_f16_f32 v96, v82, v83\n\tv_cvt_pk_f16_f32 v97, v84, v85\n\tv_pk_mul_f32 v[98:99], v[70:71], v[70:71] op_sel:[1,1] op_sel_hi:[1,0] neg_lo:[0,1]\n\tv_pk_mul_f32 v[100:101], v[72:73], v[72:73] op_sel:[1,1] op_sel_hi:[1,0] neg_lo:[0,1]\n\tv_pk_mul_f32 v[102:103], v[78:79], v[56:57] op_sel:[1,1] op_sel_hi:[1,0] neg_lo:[0,1]\n\tv_pk_mul_f32 v[104:105], v[80:81], v[58:59] op_sel:[1,1] op_sel_hi:[1,0] neg_lo:[0,1]\n\tds_write2_b64 %[b8], v[76:77], v[96:97] offset0:0 offset1:66\n\tv_pk_fma_f32 v[98:99], v[70:71], v[70:71], v[98:99] op_sel:[0,0,0] op_sel_hi:[0,1,1]\n\tv_pk_fma_f32 v[100:101], v[72:73], v[72:73], v[100:101] op_sel:[0,0,0] op_sel_hi:[0,1,1]\n\tv_pk_fma_f32 v[102:103], v[78:79], v[56:57], v[102:103] op_sel:[0,0,0] op_sel_hi:[0,1,1]\n\tv_pk_fma_f32 v[104:105], v[80:81], v[58:59], v[104:105] op_sel:[0,0,0] op_sel_hi:[0,1,1]\n\tv_cvt_pk_f16_f32 v106, v88, -v89\n\tv_cvt_pk_f16_f32 v107, v90, -v91\n\tv_cvt_pk_f16_f32 v108, v92, -v93\n\tv_cvt_pk_f16_f32 v109, v94, -v95\n\tv_pk_mul_f32 v[110:111], v[78:79], v[70:71] op_sel:[1,1] op_sel_hi:[1,0] neg_lo:[0,1]\n\tv_pk_mul_f32 v[112:113], v[80:81], v[72:73] op_sel:[1,1] op_sel_hi:[1,0] neg_lo:[0,1]\n\tv_pk_mul_f32 v[114:115], v[48:49], v[70:71] op_sel:[1,1] op_sel_hi:[1,0] neg_lo:[0,1]\n\tv_pk_mul_f32 v[116:117], v[50:51], v[72:73] op_sel:[1,1] op_sel_hi:[1,0] neg_lo:[0,1]\n\tds_write2_b64 %[b0], v[86:87], v[108:109] offset0:132 offset1:198\n\tv_pk_fma_f32 v[110:111], v[78:79], v[70:71], v[110:111] op_sel:[0,0,0] op_sel_hi:[0,1,1]\n\tv_pk_fma_f32 v[112:113], v[80:81], v[72:73], v[112:113] op_sel:[0,0,0] op_sel_hi:[0,1,1]\n\tv_pk_fma_f32 v[114:115], v[48:49], v[70:71], v[114:115] op_sel:[0,0,0] op_sel_hi:[0,1,1]\n\tv_pk_fma_f32 v[116:117], v[50:51], v[72:73], v[116:117] op_sel:[0,0,0] op_sel_hi:[0,1,1]\n\tv_cvt_pk_f16_f32 v76, v102, v103\n\tv_cvt_pk_f16_f32 v77, v104, v105\n\tv_pk_mul_f32 v[96:97], v[82:83], v[70:71] op_sel:[1,1] op_sel_hi:[1,0] neg_lo:[0,1]\n\tv_pk_mul_f32 v[118:119], v[84:85], v[72:73] op_sel:[1,1] op_sel_hi:[1,0] neg_lo:[0,1]\n\tv_pk_mul_f32 v[120:121], v[88:89], v[88:89] op_sel:[1,1] op_sel_hi:[1,0] neg_lo:[0,1]\n\tv_pk_mul_f32 v[122:123], v[90:91], v[90:91] op_sel:[1,1] op_sel_hi:[1,0] neg_lo:[0,1]\n\tv_pk_fma_f32 v[96:97], v[82:83], v[70:71], v[96:97] op_sel:[0,0,0] op_sel_hi:[0,1,1]\n\tv_pk_fma_f32 v[118:119], v[84:85], v[72:73], v[118:119] op_sel:[0,0,0] op_sel_hi:[0,1,1]\n\tv_pk_fma_f32 v[120:121], v[88:89], v[88:89], v[120:121] op_sel:[0,0,0] op_sel_hi:[0,1,1]\n\tv_pk_fma_f32 v[122:123], v[90:91], v[90:91], v[122:123] op_sel:[0,0,0] op_sel_hi:[0,1,1]\n\tv_cvt_pk_f16_f32 v124, v110, v111\n\tv_cvt_pk_f16_f32 v125, v112, v113\n\tv_cvt_pk_f16_f32 v126, v114, v115\n\tv_cvt_pk_f16_f32 v127, v116, v117\n\tv_pk_mul_f32 v[70:71], v[88:89], v[56:57] op_sel:[1,1] op_sel_hi:[1,0] neg_lo:[0,1]\n\tv_pk_mul_f32 v[72:73], v[90:91], v[58:59] op_sel:[1,1] op_sel_hi:[1,0] neg_lo:[0,1]\n\tv_pk_mul_f32 v[128:129], v[102:103], v[60:61] op_sel:[1,1] op_sel_hi:[1,0] neg_lo:[0,1]\n\tv_pk_mul_f32 v[130:131], v[104:105], v[62:63] op_sel:[1,1] op_sel_hi:[1,0] neg_lo:[0,1]\n\tds_write2_b64 %[b11], v[64:65], v[124:125] offset0:0 offset1:66\n\tv_pk_fma_f32 v[70:71], v[88:89], v[56:57], v[70:71] op_sel:[0,0,0] op_sel_hi:[0,1,1]\n\tv_pk_fma_f32 v[72:73], v[90:91], v[58:59], v[72:73] op_sel:[0,0,0] op_sel_hi:[0,1,1]\n\tv_pk_fma_f32 v[128:129], v[102:103], v[60:61], v[128:129] op_sel:[0,0,0] op_sel_hi:[0,1,1]\n\tv_pk_fma_f32 v[130:131], v[104:105], v[62:63], v[130:131] op_sel:[0,0,0] op_sel_hi:[0,1,1]\n\tv_cvt_pk_f16_f32 v132, v96, v97\n\tv_cvt_pk_f16_f32 v133, v118, v119\n\tv_cvt_pk_f16_f32 v134, v120, -v121\n\tv_cvt_pk_f16_f32 v135, v122, -v123\n\tv_pk_mul_f32 v[102:103], v[78:79], v[98:99] op_sel:[1,1] op_sel_hi:[1,0] neg_lo:[0,1]\n\tv_pk_mul_f32 v[104:105], v[80:81], v[100:101] op_sel:[1,1] op_sel_hi:[1,0] neg_lo:[0,1]\n\tv_pk_mul_f32 v[136:137], v[88:89], v[66:67] op_sel:[1,1] op_sel_hi:[1,0] neg_lo:[0,1]\n\tv_pk_mul_f32 v[138:139], v[90:91], v[68:69] op_sel:[1,1] op_sel_hi:[1,0] neg_lo:[0,1]\n\tds_write2_b64 %[b8], v[126:127], v[132:133] offset0:132 offset1:198\n\tv_pk_fma_f32 v[102:103], v[78:79], v[98:99], v[102:103] op_sel:[0,0,0] op_sel_hi:[0,1,1]\n\tv_pk_fma_f32 v[104:105], v[80:81], v[100:101], v[104:105] op_sel:[0,0,0] op_sel_hi:[0,1,1]\n\tv_pk_fma_f32 v[136:137], v[88:89], v[66:67], v[136:137] op_sel:[0,0,0] op_sel_hi:[0,1,1]\n\tv_pk_fma_f32 v[138:139], v[90:91], v[68:69], v[138:139] op_sel:[0,0,0] op_sel_hi:[0,1,1]\n\tv_cvt_pk_f16_f32 v64, v70, -v71\n\tv_cvt_pk_f16_f32 v65, v72, -v73\n\tv_cvt_pk_f16_f32 v124, v128, v129\n\tv_cvt_pk_f16_f32 v125, v130, v131\n\tv_pk_mul_f32 v[78:79], v[88:89], v[92:93] op_sel:[1,1] op_sel_hi:[1,0] neg_lo:[0,1]\n\tv_pk_mul_f32 v[80:81], v[90:91], v[94:95] op_sel:[1,1] op_sel_hi:[1,0] neg_lo:[0,1]\n\tv_pk_mul_f32 v[140:141], v[120:121], v[120:121] op_sel:[1,1] op_sel_hi:[1,0] neg_lo:[0,1]\n\tv_pk_mul_f32 v[142:143], v[122:123], v[122:123] op_sel:[1,1] op_sel_hi:[1,0] neg_lo:[0,1]\n\tds_write2_b64 %[b1], v[106:107], v[64:65] offset0:0 offset1:66\n\tv_pk_fma_f32 v[78:79], v[88:89], v[92:93], v[78:79] op_sel:[0,0,0] op_sel_hi:[0,1,1]\n\tv_pk_fma_f32 v[80:81], v[90:91], v[94:95], v[80:81] op_sel:[0,0,0] op_sel_hi:[0,1,1]\n\tv_pk_fma_f32 v[140:141], v[120:121], v[120:121], v[140:141] op_sel:[0,0,0] op_sel_hi:[0,1,1]\n\tv_pk_fma_f32 v[142:143], v[122:123], v[122:123], v[142:143] op_sel:[0,0,0] op_sel_hi:[0,1,1]\n\tv_cvt_pk_f16_f32 v126, v102, v103\n\tv_cvt_pk_f16_f32 v127, v104, v105\n\tv_cvt_pk_f16_f32 v132, v136, -v137\n\tv_cvt_pk_f16_f32 v133, v138, -v139\n\tv_pk_mul_f32 v[144:145], v[120:121], v[56:57] op_sel:[1,1] op_sel_hi:[1,0] neg_lo:[0,1]\n\tv_pk_mul_f32 v[146:147], v[122:123], v[58:59] op_sel:[1,1] op_sel_hi:[1,0] neg_lo:[0,1]\n\tv_pk_mul_f32 v[148:149], v[128:129], v[60:61] op_sel:[1,1] op_sel_hi:[1,0] neg_lo:[0,1]\n\tv_pk_mul_f32 v[150:151], v[130:131], v[62:63] op_sel:[1,1] op_sel_hi:[1,0] neg_lo:[0,1]\n\tds_write2_b64 %[b9], v[76:77], v[124:125] offset0:0 offset1:66\n\tv_pk_fma_f32 v[144:145], v[120:121], v[56:57], v[144:145] op_sel:[0,0,0] op_sel_hi:[0,1,1]\n\tv_pk_fma_f32 v[146:147], v[122:123], v[58:59], v[146:147] op_sel:[0,0,0] op_sel_hi:[0,1,1]\n\tv_pk_fma_f32 v[148:149], v[128:129], v[60:61], v[148:149] op_sel:[0,0,0] op_sel_hi:[0,1,1]\n\tv_pk_fma_f32 v[150:151], v[130:131], v[62:63], v[150:151] op_sel:[0,0,0] op_sel_hi:[0,1,1]\n\tv_cvt_pk_f16_f32 v152, v78, -v79\n\tv_cvt_pk_f16_f32 v153, v80, -v81\n\tv_cvt_pk_f16_f32 v154, v140, -v141\n\tv_cvt_pk_f16_f32 v155, v142, -v143\n\tv_pk_mul_f32 v[128:129], v[102:103], v[60:61] op_sel:[1,1] op_sel_hi:[1,0] neg_lo:[0,1]\n\tv_pk_mul_f32 v[130:131], v[104:105], v[62:63] op_sel:[1,1] op_sel_hi:[1,0] neg_lo:[0,1]\n\tv_pk_mul_f32 v[140:141], v[110:111], v[98:99] op_sel:[1,1] op_sel_hi:[1,0] neg_lo:[0,1]\n\tv_pk_mul_f32 v[142:143], v[112:113], v[100:101] op_sel:[1,1] op_sel_hi:[1,0] neg_lo:[0,1]\n\tds_write2_b64 %[b1], v[132:133], v[152:153] offset0:132 offset1:198\n\tv_pk_fma_f32 v[128:129], v[102:103], v[60:61], v[128:129] op_sel:[0,0,0] op_sel_hi:[0,1,1]\n\tv_pk_fma_f32 v[130:131], v[104:105], v[62:63], v[130:131] op_sel:[0,0,0] op_sel_hi:[0,1,1]\n\tv_pk_fma_f32 v[140:141], v[110:111], v[98:99], v[140:141] op_sel:[0,0,0] op_sel_hi:[0,1,1]\n\tv_pk_fma_f32 v[142:143], v[112:113], v[100:101], v[142:143] op_sel:[0,0,0] op_sel_hi:[0,1,1]\n\tv_cvt_pk_f16_f32 v76, v144, -v145\n\tv_cvt_pk_f16_f32 v77, v146, -v147\n\tv_cvt_pk_f16_f32 v124, v148, v149\n\tv_cvt_pk_f16_f32 v125, v150, v151\n\tv_pk_mul_f32 v[102:103], v[120:121], v[66:67] op_sel:[1,1] op_sel_hi:[1,0] neg_lo:[0,1]\n\tv_pk_mul_f32 v[104:105], v[122:123], v[68:69] op_sel:[1,1] op_sel_hi:[1,0] neg_lo:[0,1]\n\tv_pk_mul_f32 v[110:111], v[120:121], v[92:93] op_sel:[1,1] op_sel_hi:[1,0] neg_lo:[0,1]\n\tv_pk_mul_f32 v[112:113], v[122:123], v[94:95] op_sel:[1,1] op_sel_hi:[1,0] neg_lo:[0,1]\n\tds_write2_b64 %[b2], v[134:135], v[76:77] offset0:0 offset1:66\n\tv_pk_fma_f32 v[102:103], v[120:121], v[66:67], v[102:103] op_sel:[0,0,0] op_sel_hi:[0,1,1]\n\tv_pk_fma_f32 v[104:105], v[122:123], v[68:69], v[104:105] op_sel:[0,0,0] op_sel_hi:[0,1,1]\n\tv_pk_fma_f32 v[110:111], v[120:121], v[92:93], v[110:111] op_sel:[0,0,0] op_sel_hi:[0,1,1]\n\tv_pk_fma_f32 v[112:113], v[122:123], v[94:95], v[112:113] op_sel:[0,0,0] op_sel_hi:[0,1,1]\n\tv_cvt_pk_f16_f32 v144, v128, v129\n\tv_cvt_pk_f16_f32 v145, v130, v131\n\tv_cvt_pk_f16_f32 v146, v140, v141\n\tv_cvt_pk_f16_f32 v147, v142, v143\n\tv_pk_mul_f32 v[66:67], v[148:149], v[60:61] op_sel:[1,1] op_sel_hi:[1,0] neg_lo:[0,1]\n\tv_pk_mul_f32 v[68:69], v[150:151], v[62:63] op_sel:[1,1] op_sel_hi:[1,0] neg_lo:[0,1]\n\tv_pk_mul_f32 v[92:93], v[128:129], v[60:61] op_sel:[1,1] op_sel_hi:[1,0] neg_lo:[0,1]\n\tv_pk_mul_f32 v[94:95], v[130:131], v[62:63] op_sel:[1,1] op_sel_hi:[1,0] neg_lo:[0,1]\n\tds_write2_b64 %[b9], v[124:125], v[146:147] offset0:132 offset1:198\n\tv_pk_fma_f32 v[66:67], v[148:149], v[60:61], v[66:67] op_sel:[0,0,0] op_sel_hi:[0,1,1]\n\tv_pk_fma_f32 v[68:69], v[150:151], v[62:63], v[68:69] op_sel:[0,0,0] op_sel_hi:[0,1,1]\n\tv_pk_fma_f32 v[92:93], v[128:129], v[60:61], v[92:93] op_sel:[0,0,0] op_sel_hi:[0,1,1]\n\tv_pk_fma_f32 v[94:95], v[130:131], v[62:63], v[94:95] op_sel:[0,0,0] op_sel_hi:[0,1,1]\n\tv_cvt_pk_f16_f32 v140, v102, -v103\n\tv_cvt_pk_f16_f32 v141, v104, -v105\n\tv_cvt_pk_f16_f32 v142, v110, -v111\n\tv_cvt_pk_f16_f32 v143, v112, -v113\n\tv_pk_mul_f32 v[102:103], v[120:121], v[88:89] op_sel:[1,1] op_sel_hi:[1,0] neg_lo:[0,1]\n\tv_pk_mul_f32 v[104:105], v[122:123], v[90:91] op_sel:[1,1] op_sel_hi:[1,0] neg_lo:[0,1]\n\tv_pk_mul_f32 v[110:111], v[120:121], v[70:71] op_sel:[1,1] op_sel_hi:[1,0] neg_lo:[0,1]\n\tv_pk_mul_f32 v[112:113], v[122:123], v[72:73] op_sel:[1,1] op_sel_hi:[1,0] neg_lo:[0,1]\n\tds_write2_b64 %[b2], v[140:141], v[142:143] offset0:132 offset1:198\n\tv_pk_fma_f32 v[102:103], v[120:121], v[88:89], v[102:103] op_sel:[0,0,0] op_sel_hi:[0,1,1]\n\tv_pk_fma_f32 v[104:105], v[122:123], v[90:91], v[104:105] op_sel:[0,0,0] op_sel_hi:[0,1,1]\n\tv_pk_fma_f32 v[110:111], v[120:121], v[70:71], v[110:111] op_sel:[0,0,0] op_sel_hi:[0,1,1]\n\tv_pk_fma_f32 v[112:113], v[122:123], v[72:73], v[112:113] op_sel:[0,0,0] op_sel_hi:[0,1,1]\n\tv_cvt_pk_f16_f32 v124, v66, v67\n\tv_cvt_pk_f16_f32 v125, v68, v69\n\tv_cvt_pk_f16_f32 v128, v92, v93\n\tv_cvt_pk_f16_f32 v129, v94, v95\n\tv_pk_mul_f32 v[70:71], v[48:49], v[98:99] op_sel:[1,1] op_sel_hi:[1,0] neg_lo:[0,1]\n\tv_pk_mul_f32 v[72:73], v[50:51], v[100:101] op_sel:[1,1] op_sel_hi:[1,0] neg_lo:[0,1]\n\tv_pk_mul_f32 v[88:89], v[82:83], v[98:99] op_sel:[1,1] op_sel_hi:[1,0] neg_lo:[0,1]\n\tv_pk_mul_f32 v[90:91], v[84:85], v[100:101] op_sel:[1,1] op_sel_hi:[1,0] neg_lo:[0,1]\n\tds_write2_b64 %[b10], v[126:127], v[144:145] offset0:132 offset1:198\n\tv_pk_fma_f32 v[70:71], v[48:49], v[98:99], v[70:71] op_sel:[0,0,0] op_sel_hi:[0,1,1]\n\tv_pk_fma_f32 v[72:73], v[50:51], v[100:101], v[72:73] op_sel:[0,0,0] op_sel_hi:[0,1,1]\n\tv_pk_fma_f32 v[88:89], v[82:83], v[98:99], v[88:89] op_sel:[0,0,0] op_sel_hi:[0,1,1]\n\tv_pk_fma_f32 v[90:91], v[84:85], v[100:101], v[90:91] op_sel:[0,0,0] op_sel_hi:[0,1,1]\n\tv_cvt_pk_f16_f32 v92, v102, -v103\n\tv_cvt_pk_f16_f32 v93, v104, -v105\n\tv_cvt_pk_f16_f32 v94, v110, -v111\n\tv_cvt_pk_f16_f32 v95, v112, -v113\n\tv_pk_mul_f32 v[82:83], v[66:67], v[60:61] op_sel:[1,1] op_sel_hi:[1,0] neg_lo:[0,1]\n\tv_pk_mul_f32 v[84:85], v[68:69], v[62:63] op_sel:[1,1] op_sel_hi:[1,0] neg_lo:[0,1]\n\tv_pk_mul_f32 v[102:103], v[120:121], v[136:137] op_sel:[1,1] op_sel_hi:[1,0] neg_lo:[0,1]\n\tv_pk_mul_f32 v[104:105], v[122:123], v[138:139] op_sel:[1,1] op_sel_hi:[1,0] neg_lo:[0,1]\n\tds_write2_b64 %[b3], v[92:93], v[94:95] offset0:0 offset1:66\n\tv_pk_fma_f32 v[82:83], v[66:67], v[60:61], v[82:83] op_sel:[0,0,0] op_sel_hi:[0,1,1]\n\tv_pk_fma_f32 v[84:85], v[68:69], v[62:63], v[84:85] op_sel:[0,0,0] op_sel_hi:[0,1,1]\n\tv_pk_fma_f32 v[102:103], v[120:121], v[136:137], v[102:103] op_sel:[0,0,0] op_sel_hi:[0,1,1]\n\tv_pk_fma_f32 v[104:105], v[122:123], v[138:139], v[104:105] op_sel:[0,0,0] op_sel_hi:[0,1,1]\n\tv_cvt_pk_f16_f32 v110, v70, v71\n\tv_cvt_pk_f16_f32 v111, v72, v73\n\tv_cvt_pk_f16_f32 v112, v88, v89\n\tv_cvt_pk_f16_f32 v113, v90, v91\n\tv_pk_mul_f32 v[66:67], v[120:121], v[78:79] op_sel:[1,1] op_sel_hi:[1,0] neg_lo:[0,1]\n\tv_pk_mul_f32 v[68:69], v[122:123], v[80:81] op_sel:[1,1] op_sel_hi:[1,0] neg_lo:[0,1]\n\tv_pk_mul_f32 v[70:71], v[114:115], v[98:99] op_sel:[1,1] op_sel_hi:[1,0] neg_lo:[0,1]\n\tv_pk_mul_f32 v[72:73], v[116:117], v[100:101] op_sel:[1,1] op_sel_hi:[1,0] neg_lo:[0,1]\n\tds_write2_b64 %[b11], v[110:111], v[112:113] offset0:132 offset1:198\n\tv_pk_fma_f32 v[66:67], v[120:121], v[78:79], v[66:67] op_sel:[0,0,0] op_sel_hi:[0,1,1]\n\tv_pk_fma_f32 v[68:69], v[122:123], v[80:81], v[68:69] op_sel:[0,0,0] op_sel_hi:[0,1,1]\n\tv_pk_fma_f32 v[70:71], v[114:115], v[98:99], v[70:71] op_sel:[0,0,0] op_sel_hi:[0,1,1]\n\tv_pk_fma_f32 v[72:73], v[116:117], v[100:101], v[72:73] op_sel:[0,0,0] op_sel_hi:[0,1,1]\n\tv_cvt_pk_f16_f32 v88, v82, v83\n\tv_cvt_pk_f16_f32 v89, v84, v85\n\tv_cvt_pk_f16_f32 v90, v102, -v103\n\tv_cvt_pk_f16_f32 v91, v104, -v105\n\tv_pk_mul_f32 v[78:79], v[96:97], v[98:99] op_sel:[1,1] op_sel_hi:[1,0] neg_lo:[0,1]\n\tv_pk_mul_f32 v[80:81], v[118:119], v[100:101] op_sel:[1,1] op_sel_hi:[1,0] neg_lo:[0,1]\n\tv_pk_mul_f32 v[102:103], v[82:83], v[60:61] op_sel:[1,1] op_sel_hi:[1,0] neg_lo:[0,1]\n\tv_pk_mul_f32 v[104:105], v[84:85], v[62:63] op_sel:[1,1] op_sel_hi:[1,0] neg_lo:[0,1]\n\tds_write2_b64 %[b12], v[128:129], v[124:125] offset0:132 offset1:198\n\tv_pk_fma_f32 v[78:79], v[96:97], v[98:99], v[78:79] op_sel:[0,0,0] op_sel_hi:[0,1,1]\n\tv_pk_fma_f32 v[80:81], v[118:119], v[100:101], v[80:81] op_sel:[0,0,0] op_sel_hi:[0,1,1]\n\tv_pk_fma_f32 v[102:103], v[82:83], v[60:61], v[102:103] op_sel:[0,0,0] op_sel_hi:[0,1,1]\n\tv_pk_fma_f32 v[104:105], v[84:85], v[62:63], v[104:105] op_sel:[0,0,0] op_sel_hi:[0,1,1]\n\tv_cvt_pk_f16_f32 v110, v66, -v67\n\tv_cvt_pk_f16_f32 v111, v68, -v69\n\tv_cvt_pk_f16_f32 v112, v70, v71\n\tv_cvt_pk_f16_f32 v113, v72, v73\n\tv_pk_mul_f16 v66, v154, v74 op_sel:[1,1] op_sel_hi:[1,0] neg_lo:[0,1]\n\tv_pk_mul_f16 v67, v155, v75 op_sel:[1,1] op_sel_hi:[1,0] neg_lo:[0,1]\n\tv_pk_mul_f16 v68, v154, v86 op_sel:[1,1] op_sel_hi:[1,0] neg_lo:[0,1]\n\tv_pk_mul_f16 v69, v155, v87 op_sel:[1,1] op_sel_hi:[1,0] neg_lo:[0,1]\n\tds_write2_b64 %[b3], v[90:91], v[110:111] offset0:132 offset1:198\n\tv_pk_fma_f16 v66, v154, v74, v66 op_sel:[0,0,0] op_sel_hi:[0,1,1]\n\tv_pk_fma_f16 v67, v155, v75, v67 op_sel:[0,0,0] op_sel_hi:[0,1,1]\n\tv_pk_fma_f16 v68, v154, v86, v68 op_sel:[0,0,0] op_sel_hi:[0,1,1]\n\tv_pk_fma_f16 v69, v155, v87, v69 op_sel:[0,0,0] op_sel_hi:[0,1,1]\n\tv_cvt_pk_f16_f32 v70, v78, v79\n\tv_cvt_pk_f16_f32 v71, v80, v81\n\tv_cvt_pk_f16_f32 v72, v102, v103\n\tv_cvt_pk_f16_f32 v73, v104, v105\n\tv_pk_mul_f16 v78, v154, v108 op_sel:[1,1] op_sel_hi:[1,0] neg_lo:[0,1]\n\tv_pk_mul_f16 v79, v155, v109 op_sel:[1,1] op_sel_hi:[1,0] neg_lo:[0,1]\n\tv_pk_mul_f32 v[80:81], v[102:103], v[60:61] op_sel:[1,1] op_sel_hi:[1,0] neg_lo:[0,1]\n\tv_pk_mul_f32 v[82:83], v[104:105], v[62:63] op_sel:[1,1] op_sel_hi:[1,0] neg_lo:[0,1]\n\tds_write2_b64 %[b4], v[154:155], v[66:67] offset0:0 offset1:66\n\tv_pk_fma_f16 v78, v154, v108, v78 op_sel:[0,0,0] op_sel_hi:[0,1,1]\n\tv_pk_fma_f16 v79, v155, v109, v79 op_sel:[0,0,0] op_sel_hi:[0,1,1]\n\tv_pk_fma_f32 v[80:81], v[102:103], v[60:61], v[80:81] op_sel:[0,0,0] op_sel_hi:[0,1,1]\n\tv_pk_fma_f32 v[82:83], v[104:105], v[62:63], v[82:83] op_sel:[0,0,0] op_sel_hi:[0,1,1]\n\tv_pk_mul_f16 v74, v154, v106 op_sel:[1,1] op_sel_hi:[1,0] neg_lo:[0,1]\n\tv_pk_mul_f16 v75, v155, v107 op_sel:[1,1] op_sel_hi:[1,0] neg_lo:[0,1]\n\tv_pk_mul_f16 v84, v154, v64 op_sel:[1,1] op_sel_hi:[1,0] neg_lo:[0,1]\n\tv_pk_mul_f16 v85, v155, v65 op_sel:[1,1] op_sel_hi:[1,0] neg_lo:[0,1]\n\tds_write2_b64 %[b4], v[68:69], v[78:79] offset0:132 offset1:198\n\tv_pk_fma_f16 v74, v154, v106, v74 op_sel:[0,0,0] op_sel_hi:[0,1,1]\n\tv_pk_fma_f16 v75, v155, v107, v75 op_sel:[0,0,0] op_sel_hi:[0,1,1]\n\tv_pk_fma_f16 v84, v154, v64, v84 op_sel:[0,0,0] op_sel_hi:[0,1,1]\n\tv_pk_fma_f16 v85, v155, v65, v85 op_sel:[0,0,0] op_sel_hi:[0,1,1]\n\tv_cvt_pk_f16_f32 v66, v80, v81\n\tv_cvt_pk_f16_f32 v67, v82, v83\n\tv_pk_mul_f16 v80, v154, v132 op_sel:[1,1] op_sel_hi:[1,0] neg_lo:[0,1]\n\tv_pk_mul_f16 v81, v155, v133 op_sel:[1,1] op_sel_hi:[1,0] neg_lo:[0,1]\n\tv_pk_mul_f16 v82, v154, v152 op_sel:[1,1] op_sel_hi:[1,0] neg_lo:[0,1]\n\tv_pk_mul_f16 v83, v155, v153 op_sel:[1,1] op_sel_hi:[1,0] neg_lo:[0,1]\n\tds_write2_b64 %[b5], v[74:75], v[84:85] offset0:0 offset1:66\n\tv_pk_fma_f16 v80, v154, v132, v80 op_sel:[0,0,0] op_sel_hi:[0,1,1]\n\tv_pk_fma_f16 v81, v155, v133, v81 op_sel:[0,0,0] op_sel_hi:[0,1,1]\n\tv_pk_fma_f16 v82, v154, v152, v82 op_sel:[0,0,0] op_sel_hi:[0,1,1]\n\tv_pk_fma_f16 v83, v155, v153, v83 op_sel:[0,0,0] op_sel_hi:[0,1,1]\n\tv_pk_mul_f16 v64, v154, v134 op_sel:[1,1] op_sel_hi:[1,0] neg_lo:[0,1]\n\tv_pk_mul_f16 v65, v155, v135 op_sel:[1,1] op_sel_hi:[1,0] neg_lo:[0,1]\n\tv_pk_mul_f16 v68, v154, v76 op_sel:[1,1] op_sel_hi:[1,0] neg_lo:[0,1]\n\tv_pk_mul_f16 v69, v155, v77 op_sel:[1,1] op_sel_hi:[1,0] neg_lo:[0,1]\n\tds_write2_b64 %[b5], v[80:81], v[82:83] offset0:132 offset1:198\n\tv_pk_fma_f16 v64, v154, v134, v64 op_sel:[0,0,0] op_sel_hi:[0,1,1]\n\tv_pk_fma_f16 v65, v155, v135, v65 op_sel:[0,0,0] op_sel_hi:[0,1,1]\n\tv_pk_fma_f16 v68, v154, v76, v68 op_sel:[0,0,0] op_sel_hi:[0,1,1]\n\tv_pk_fma_f16 v69, v155, v77, v69 op_sel:[0,0,0] op_sel_hi:[0,1,1]\n\tv_pk_mul_f16 v74, v154, v140 op_sel:[1,1] op_sel_hi:[1,0] neg_lo:[0,1]\n\tv_pk_mul_f16 v75, v155, v141 op_sel:[1,1] op_sel_hi:[1,0] neg_lo:[0,1]\n\tv_pk_mul_f16 v78, v154, v142 op_sel:[1,1] op_sel_hi:[1,0] neg_lo:[0,1]\n\tv_pk_mul_f16 v79, v155, v143 op_sel:[1,1] op_sel_hi:[1,0] neg_lo:[0,1]\n\tds_write2_b64 %[b6], v[64:65], v[68:69] offset0:0 offset1:66\n\tv_pk_fma_f16 v74, v154, v140, v74 op_sel:[0,0,0] op_sel_hi:[0,1,1]\n\tv_pk_fma_f16 v75, v155, v141, v75 op_sel:[0,0,0] op_sel_hi:[0,1,1]\n\tv_pk_fma_f16 v78, v154, v142, v78 op_sel:[0,0,0] op_sel_hi:[0,1,1]\n\tv_pk_fma_f16 v79, v155, v143, v79 op_sel:[0,0,0] op_sel_hi:[0,1,1]\n\tv_pk_mul_f16 v76, v154, v92 op_sel:[1,1] op_sel_hi:[1,0] neg_lo:[0,1]\n\tv_pk_mul_f16 v77, v155, v93 op_sel:[1,1] op_sel_hi:[1,0] neg_lo:[0,1]\n\tv_pk_mul_f16 v80, v154, v94 op_sel:[1,1] op_sel_hi:[1,0] neg_lo:[0,1]\n\tv_pk_mul_f16 v81, v155, v95 op_sel:[1,1] op_sel_hi:[1,0] neg_lo:[0,1]\n\tds_write2_b64 %[b6], v[74:75], v[78:79] offset0:132 offset1:198\n\tv_pk_fma_f16 v76, v154, v92, v76 op_sel:[0,0,0] op_sel_hi:[0,1,1]\n\tv_pk_fma_f16 v77, v155, v93, v77 op_sel:[0,0,0] op_sel_hi:[0,1,1]\n\tv_pk_fma_f16 v80, v154, v94, v80 op_sel:[0,0,0] op_sel_hi:[0,1,1]\n\tv_pk_fma_f16 v81, v155, v95, v81 op_sel:[0,0,0] op_sel_hi:[0,1,1]\n\tv_pk_mul_f16 v64, v154, v90 op_sel:[1,1] op_sel_hi:[1,0] neg_lo:[0,1]\n\tv_pk_mul_f16 v65, v155, v91 op_sel:[1,1] op_sel_hi:[1,0] neg_lo:[0,1]\n\tv_pk_mul_f16 v68, v154, v110 op_sel:[1,1] op_sel_hi:[1,0] neg_lo:[0,1]\n\tv_pk_mul_f16 v69, v155, v111 op_sel:[1,1] op_sel_hi:[1,0] neg_lo:[0,1]\n\tds_write2_b64 %[b7], v[76:77], v[80:81] offset0:0 offset1:66\n\tv_pk_fma_f16 v64, v154, v90, v64 op_sel:[0,0,0] op_sel_hi:[0,1,1]\n\tv_pk_fma_f16 v65, v155, v91, v65 op_sel:[0,0,0] op_sel_hi:[0,1,1]\n\tv_pk_fma_f16 v68, v154, v110, v68 op_sel:[0,0,0] op_sel_hi:[0,1,1]\n\tv_pk_fma_f16 v69, v155, v111, v69 op_sel:[0,0,0] op_sel_hi:[0,1,1]\n\ts_nop 0\n\tds_write2_b64 %[b7], v[64:65], v[68:69] offset0:132 offset1:198\n\tds_write2_b64 %[b10], v[88:89], v[66:67] offset0:0 offset1:66\n\tds_write2_b64 %[b12], v[112:113], v[70:71] offset0:0 offset1:66\n\tds_write_b64 %[b13], v[72:73] offset:0"
#define KGEN_CLOBBER "v64", "v65", "v66", "v67", "v68", "v69", "v70", "v71", "v72", "v73", "v74", "v75", "v76", "v77", "v78", "v79", "v80", "v81", "v82", "v83", "v84", "v85", "v86", "v87", "v88", "v89", "v90", "v91", "v92", "v93", "v94", "v95", "v96", "v97", "v98", "v99", "v100", "v101", "v102", "v103", "v104", "v105", "v106", "v107", "v108", "v109", "v110", "v111", "v112", "v113", "v114", "v115", "v116", "v117", "v118", "v119", "v120", "v121", "v122", "v123", "v124", "v125", "v126", "v127", "v128", "v129", "v130", "v131", "v132", "v133", "v134", "v135", "v136", "v137", "v138", "v139", "v140", "v141", "v142", "v143", "v144", "v145", "v146", "v147", "v148", "v149", "v150", "v151", "v152", "v153", "v154", "v155"
#define KROW_0LO 0x2b2a2d2c23222120ull
#define KROW_0HI 0x202020202b343332ull
#define KROW_1LO 0x2524272a31302f2eull
#define KROW_1HI 0x2b2b2b2b20292826ull
#define KMFMA_ASM "ds_read_b128 v[64:67], %[ap] offset:0\n\tds_read_b128 v[68:71], %[bp] offset:0\n\tds_read_b128 v[72:75], %[ap] offset:32\n\tds_read_b128 v[76:79], %[bp] offset:32\n\tds_read_b128 v[80:83], %[ap] offset:64\n\tds_read_b128 v[84:87], %[bp] offset:64\n\tds_read_b128 v[88:91], %[ap] offset:96\n\tds_read_b128 v[92:95], %[bp] offset:96\n\ts_waitcnt lgkmcnt(6)\n\tv_mfma_f32_32x32x16_f16 v[48:63], v[64:67], v[68:71], 0\n\tds_read_b128 v[96:99], %[ap] offset:128\n\tds_read_b128 v[100:103], %[bp] offset:128\n\ts_waitcnt lgkmcnt(6)\n\tv_mfma_f32_32x32x16_f16 v[48:63], v[72:75], v[76:79], v[48:63]\n\tds_read_b128 v[104:107], %[ap] offset:160\n\tds_read_b128 v[108:111], %[bp] offset:160\n\ts_waitcnt lgkmcnt(6)\n\tv_mfma_f32_32x32x16_f16 v[48:63], v[80:83], v[84:87], v[48:63]\n\tds_read_b128 v[64:67], %[ap] offset:192\n\tds_read_b128 v[68:71], %[bp] offset:192\n\ts_waitcnt lgkmcnt(6)\n\tv_mfma_f32_32x32x16_f16 v[48:63], v[88:91], v[92:95], v[48:63]\n\tds_read_b128 v[72:75], %[ap] offset:224\n\tds_read_b128 v[76:79], %[bp] offset:224\n\ts_waitcnt lgkmcnt(6)\n\tv_mfma_f32_32x32x16_f16 v[48:63], v[96:99], v[100:103], v[48:63]\n\tds_read_b128 v[80:83], %[ap] offset:256\n\tds_read_b128 v[84:87], %[bp] offset:256\n\ts_waitcnt lgkmcnt(6)\n\tv_mfma_f32_32x32x16_f16 v[48:63], v[104:107], v[108:111], v[48:63]\n\tds_read_b128 v[88:91], %[ap] offset:288\n\tds_read_b128 v[92:95], %[bp] offset:288\n\ts_waitcnt lgkmcnt(6)\n\tv_mfma_f32_32x32x16_f16 v[48:63], v[64:67], v[68:71], v[48:63]\n\tds_read_b128 v[96:99], %[ap] offset:320\n\tds_read_b128 v[100:103], %[bp] offset:320\n\ts_waitcnt lgkmcnt(6)\n\tv_mfma_f32_32x32x16_f16 v[48:63], v[72:75], v[76:79], v[48:63]\n\tds_read_b128 v[104:107], %[ap] offset:352\n\tds_read_b128 v[108:111], %[bp] offset:352\n\ts_waitcnt lgkmcnt(6)\n\tv_mfma_f32_32x32x16_f16 v[48:63], v[80:83], v[84:87], v[48:63]\n\tds_read_b128 v[64:67], %[ap] offset:384\n\tds_read_b128 v[68:71], %[bp] offset:384\n\ts_waitcnt lgkmcnt(6)\n\tv_mfma_f32_32x32x16_f16 v[48:63], v[88:91], v[92:95], v[48:63]\n\tds_read_b128 v[72:75], %[ap] offset:416\n\tds_read_b128 v[76:79], %[bp] offset:416\n\ts_waitcnt lgkmcnt(6)\n\tv_mfma_f32_32x32x16_f16 v[48:63], v[96:99], v[100:103], v[48:63]\n\tds_read_b128 v[80:83], %[ap] offset:448\n\tds_read_b128 v[84:87], %[bp] offset:448\n\ts_waitcnt lgkmcnt(6)\n\tv_mfma_f32_32x32x16_f16 v[48:63], v[104:107], v[108:111], v[48:63]\n\tds_read_b128 v[88:91], %[ap] offset:480\n\tds_read_b128 v[92:95], %[bp] offset:480\n\ts_waitcnt lgkmcnt(6)\n\tv_mfma_f32_32x32x16_f16 v[48:63], v[64:67], v[68:71], v[48:63]\n\tds_read_b32 %[stg], %[ctl] offset:4\n\ts_waitcnt lgkmcnt(4)\n\tv_mfma_f32_32x32x16_f16 v[48:63], v[72:75], v[76:79], v[48:63]\n\ts_waitcnt lgkmcnt(2)\n\tv_mfma_f32_32x32x16_f16 v[48:63], v[80:83], v[84:87], v[48:63]\n\ts_waitcnt lgkmcnt(0)\n\tv_mfma_f32_32x32x16_f16 v[48:63], v[88:91], v[92:95], v[48:63]\n\ts_nop 7\n\ts_nop 3\n\ts_waitcnt lgkmcnt(0)"
#define KMFMA_CLOBBER "v64", "v65", "v66", "v67", "v68", "v69", "v70", "v71", "v72", "v73", "v74", "v75", "v76", "v77", "v78", "v79", "v80", "v81", "v82", "v83", "v84", "v85", "v86", "v87", "v88", "v89", "v90", "v91", "v92", "v93", "v94", "v95", "v96", "v97", "v98", "v99", "v100", "v101", "v102", "v103", "v104", "v105", "v106", "v107", "v108", "v109", "v110", "v111"
#define CONV_ASM ".p2align 3\n\tds_read_b64_tr_b16 v[52:53], %[a0] offset:456\n\tds_read_b64_tr_b16 v[54:55], %[a0] offset:448\n\tds_read_b64_tr_b16 v[84:85], %[a0] offset:968\n\tds_read_b64_tr_b16 v[86:87], %[a0] offset:960\n\tds_read_b64_tr_b16 v[48:49], %[a0] offset:392\n\tds_read_b64_tr_b16 v[50:51], %[a0] offset:384\n\tds_read_b64_tr_b16 v[80:81], %[a0] offset:904\n\tds_read_b64_tr_b16 v[82:83], %[a0] offset:896\n\tds_read_b64_tr_b16 v[44:45], %[a0] offset:328\n\tds_read_b64_tr_b16 v[46:47], %[a0] offset:320\n\tds_read_b64_tr_b16 v[76:77], %[a0] offset:840\n\tds_read_b64_tr_b16 v[78:79], %[a0] offset:832\n\tv_readfirstlane_b32 %[s], %[stg]\n\ts_mov_b32 %[c], 0x400000\n\ts_cmp_ge_u32 %[s], 4\n\ts_cbranch_scc1 2f\n\t1:\n\tds_read_b32 v216, %[ctl] offset:4\n\ts_waitcnt lgkmcnt(0)\n\tv_readfirstlane_b32 %[s], v216\n\ts_cmp_ge_u32 %[s], 4\n\ts_cbranch_scc1 2f\n\ts_sleep 1\n\ts_sub_u32 %[c], %[c], 1\n\ts_cbranch_scc0 1b\n\t.p2align 3\n\t2:\n\tds_read_b128 v[164:167], %[bb] offset:64\n\tds_read_b128 v[168:171], %[bb] offset:128\n\tds_read_b128 v[172:175], %[bb] offset:192\n\tds_read_b64_tr_b16 v[40:41], %[a0] offset:264\n\tds_read_b64_tr_b16 v[42:43], %[a0] offset:256\n\tds_read_b128 v[176:179], %[bb] offset:256\n\ts_waitcnt lgkmcnt(5)\n\ts_nop 0\n\tv_mfma_f32_16x16x32_bf16 v[132:135], v[52:55], v[164:167], 0\n\tds_read_b64_tr_b16 v[72:73], %[a0] offset:776\n\tds_read_b64_tr_b16 v[74:75], %[a0] offset:768\n\tds_read_b64_tr_b16 v[36:37], %[a0] offset:200\n\tv_mfma_f32_16x16x32_bf16 v[136:139], v[84:87], v[164:167], 0\n\tds_read_b64_tr_b16 v[38:39], %[a0] offset:192\n\ts_waitcnt lgkmcnt(8)\n\ts_nop 0\n\tv_mfma_f32_16x16x32_bf16 v[132:135], v[48:51], v[168:171], v[132:135]\n\tds_read_b128 v[180:183], %[bb] offset:320\n\tds_read_b64_tr_b16 v[68:69], %[a0] offset:712\n\tds_read_b64_tr_b16 v[70:71], %[a0] offset:704\n\tv_mfma_f32_16x16x32_bf16 v[136:139], v[80:83], v[168:171], v[136:139]\n\tds_read_b64_tr_b16 v[32:33], %[a0] offset:136\n\ts_waitcnt lgkmcnt(8)\n\ts_nop 0\n\tv_mfma_f32_16x16x32_bf16 v[132:135], v[44:47], v[172:175], v[132:135]\n\tds_read_b64_tr_b16 v[34:35], %[a0] offset:128\n\tds_read_b128 v[184:187], %[bb] offset:384\n\tds_read_b64_tr_b16 v[64:65], %[a0] offset:648\n\tv_mfma_f32_16x16x32_bf16 v[136:139], v[76:79], v[172:175], v[136:139]\n\tds_read_b64_tr_b16 v[66:67], %[a0] offset:640\n\tds_read_b64_tr_b16 v[28:29], %[a0] offset:72\n\tds_read_b64_tr_b16 v[30:31], %[a0] offset:64\n\tv_mfma_f32_16x16x32_bf16 v[132:135], v[40:43], v[176:179], v[132:135]\n\tds_read_b128 v[164:167], %[bb] offset:448\n\ts_waitcnt lgkmcnt(8)\n\ts_nop 0\n\tv_mfma_f32_16x16x32_bf16 v[136:139], v[72:75], v[176:179], v[136:139]\n\tds_read_b64_tr_b16 v[60:61], %[a0] offset:584\n\tds_read_b64_tr_b16 v[62:63], %[a0] offset:576\n\tds_read_b64_tr_b16 v[24:25], %[a0] offset:8\n\tv_mfma_f32_16x16x32_bf16 v[132:135], v[36:39], v[180:183], v[132:135]\n\tds_read_b64_tr_b16 v[26:27], %[a0] offset:0\n\tds_read_b128 v[168:171], %[bb] offset:512\n\tds_read_b64_tr_b16 v[56:57], %[a0] offset:520\n\tv_mfma_f32_16x16x32_bf16 v[136:139], v[68:71], v[180:183], v[136:139]\n\tds_read_b64_tr_b16 v[58:59], %[a0] offset:512\n\ts_waitcnt lgkmcnt(8)\n\ts_nop 0\n\tv_mfma_f32_16x16x32_bf16 v[132:135], v[32:35], v[184:187], v[132:135]\n\tds_read_b64_tr_b16 v[88:89], %[a0] offset:1032\n\tds_read_b64_tr_b16 v[90:91], %[a0] offset:1024\n\tds_read_b128 v[188:191], %[bb] offset:496\n\tv_mfma_f32_16x16x32_bf16 v[136:139], v[64:67], v[184:187], v[136:139]\n\tds_read_b128 v[172:175], %[bb] offset:576\n\ts_waitcnt lgkmcnt(8)\n\ts_nop 0\n\tv_mfma_f32_16x16x32_bf16 v[132:135], v[28:31], v[164:167], v[132:135]\n\tds_read_b128 v[176:179], %[bb] offset:640\n\tv_mfma_f32_16x16x32_bf16 v[136:139], v[60:63], v[164:167], v[136:139]\n\ts_waitcnt lgkmcnt(5)\n\ts_nop 0\n\tv_mfma_f32_16x16x32_bf16 v[132:135], v[24:27], v[168:171], v[132:135]\n\tds_write_b16 %[ta], %[t78]\n\tds_read_b128 v[180:183], %[bb] offset:704\n\tv_mfma_f32_16x16x32_bf16 v[136:139], v[56:59], v[168:171], v[136:139]\n\ts_waitcnt lgkmcnt(3)\n\ts_nop 0\n\tv_mfma_f32_16x16x32_bf16 v[140:143], v[88:91], v[168:171], 0\n\tds_write_b16_d16_hi %[ta], %[t78] offset:64\n\tds_read_b128 v[184:187], %[bb] offset:768\n\tv_mfma_f32_16x16x32_bf16 v[204:207], v[188:191], v[4:7], 0\n\tds_write_b128 %[yb], v[132:135] offset:0\n\tv_mfma_f32_16x16x32_bf16 v[218:221], v[168:171], v[4:7], 0\n\tds_write_b16 %[ta], %[t78] offset:388\n\tds_read_b128 v[164:167], %[bb] offset:832\n\tv_mfma_f32_16x16x32_bf16 v[208:211], v[188:191], v[8:11], 0\n\tds_write_b8 %[ctc], %[one] offset:0\n\tds_write_b16_d16_hi %[ta], %[t78] offset:452\n\tv_mfma_f32_16x16x32_bf16 v[222:225], v[168:171], v[8:11], 0\n\tds_write_b16 %[tb], %[t9a]\n\tv_mfma_f32_16x16x32_bf16 v[212:215], v[188:191], v[12:15], 0\n\tds_write_b16_d16_hi %[tb], %[t9a] offset:128\n\tv_cvt_pk_bf16_f32 v192, v221, v207\n\tv_mfma_f32_16x16x32_bf16 v[226:229], v[168:171], v[16:19], 0\n\tds_write_b16 %[tb], %[t9a] offset:388\n\tv_cvt_pk_bf16_f32 v193, v220, v206\n\tv_mfma_f32_16x16x32_bf16 v[148:151], v[20:23], v[168:171], 0\n\tds_write_b16_d16_hi %[tb], %[t9a] offset:516\n\tds_read_b128 v[168:171], %[bb] offset:896\n\tv_mfma_f32_16x16x32_bf16 v[136:139], v[52:55], v[172:175], v[136:139]\n\tds_read_b64 v[92:93], %[tp] offset:0\n\tds_read_b64 v[94:95], %[tp] offset:8\n\tds_read_b64 v[96:97], %[tp] offset:64\n\tv_mfma_f32_16x16x32_bf16 v[140:143], v[84:87], v[172:175], v[140:143]\n\tds_read_b128 v[172:175], %[bb] offset:960\n\ts_waitcnt lgkmcnt(7)\n\ts_nop 0\n\tv_mfma_f32_16x16x32_bf16 v[136:139], v[48:51], v[176:179], v[136:139]\n\tds_read_b64 v[98:99], %[tp] offset:72\n\tds_read_b64 v[100:101], %[tp] offset:128\n\tds_read_b64 v[102:103], %[tp] offset:136\n\tv_mfma_f32_16x16x32_bf16 v[140:143], v[80:83], v[176:179], v[140:143]\n\tds_read_b128 v[176:179], %[bb] offset:1024\n\tds_read_b64 v[104:105], %[tp] offset:768\n\tds_read_b64 v[106:107], %[tp] offset:776\n\tv_mfma_f32_16x16x32_bf16 v[136:139], v[44:47], v[180:183], v[136:139]\n\tds_read_b64 v[108:109], %[tp] offset:832\n\tds_read_b64 v[110:111], %[tp] offset:840\n\tds_read_b64 v[112:113], %[tp] offset:896\n\tv_mfma_f32_16x16x32_bf16 v[140:143], v[76:79], v[180:183], v[140:143]\n\tds_read_b64 v[114:115], %[tp] offset:904\n\tds_read_b64 v[116:117], %[tp] offset:1536\n\tds_read_b64 v[118:119], %[tp] offset:1544\n\tv_mfma_f32_16x16x32_bf16 v[136:139], v[40:43], v[184:187], v[136:139]\n\tds_read_b64 v[120:121], %[tp] offset:1600\n\tds_read_b64 v[122:123], %[tp] offset:1608\n\tv_cvt_pk_bf16_f32 v194, v219, v205\n\tv_mfma_f32_16x16x32_bf16 v[140:143], v[72:75], v[184:187], v[140:143]\n\tv_cvt_pk_bf16_f32 v195, v218, v204\n\tds_read_b64 v[124:125], %[tp] offset:1664\n\tds_read_b64 v[126:127], %[tp] offset:1672\n\tv_mfma_f32_16x16x32_bf16 v[136:139], v[36:39], v[164:167], v[136:139]\n\tds_read_b64 v[128:129], %[tp] offset:1728\n\tds_read_b64 v[130:131], %[tp] offset:1736\n\tv_cvt_pk_bf16_f32 v196, v225, v211\n\tv_mfma_f32_16x16x32_bf16 v[140:143], v[68:71], v[164:167], v[140:143]\n\tv_cvt_pk_bf16_f32 v197, v224, v210\n\ts_waitcnt lgkmcnt(6)\n\ts_nop 0\n\tv_mfma_f32_16x16x32_bf16 v[136:139], v[32:35], v[168:171], v[136:139]\n\tv_cvt_pk_bf16_f32 v198, v223, v209\n\tv_mfma_f32_16x16x32_bf16 v[140:143], v[64:67], v[168:171], v[140:143]\n\tv_cvt_pk_bf16_f32 v199, v222, v208\n\tv_mfma_f32_16x16x32_bf16 v[136:139], v[28:31], v[172:175], v[136:139]\n\tv_cvt_pk_bf16_f32 v200, v229, v215\n\tv_mfma_f32_16x16x32_bf16 v[140:143], v[60:63], v[172:175], v[140:143]\n\tds_read_b128 v[188:191], %[bb] offset:1008\n\tv_cvt_pk_bf16_f32 v201, v228, v214\n\tv_mfma_f32_16x16x32_bf16 v[136:139], v[24:27], v[176:179], v[136:139]\n\tv_cvt_pk_bf16_f32 v202, v227, v213\n\tv_mfma_f32_16x16x32_bf16 v[140:143], v[56:59], v[176:179], v[140:143]\n\tv_cvt_pk_bf16_f32 v203, v226, v212\n\tv_mfma_f32_16x16x32_bf16 v[144:147], v[88:91], v[176:179], 0\n\tv_mfma_f32_16x16x32_bf16 v[140:143], v[192:195], v[92:95], v[140:143]\n\tv_mfma_f32_16x16x32_bf16 v[144:147], v[192:195], v[96:99], v[144:147]\n\tv_mfma_f32_16x16x32_bf16 v[148:151], v[192:195], v[100:103], v[148:151]\n\tv_mfma_f32_16x16x32_bf16 v[140:143], v[196:199], v[104:107], v[140:143]\n\tds_write_b128 %[yb], v[136:139] offset:1152\n\tds_read_b128 v[180:183], %[bb] offset:1088\n\tv_mfma_f32_16x16x32_bf16 v[144:147], v[196:199], v[108:111], v[144:147]\n\tv_mfma_f32_16x16x32_bf16 v[148:151], v[196:199], v[112:115], v[148:151]\n\tds_write_b8 %[ctc], %[one] offset:64\n\tds_read_b128 v[184:187], %[bb] offset:1152\n\tv_mfma_f32_16x16x32_bf16 v[148:151], v[200:203], v[116:119], v[148:151]\n\ts_waitcnt lgkmcnt(4)\n\ts_nop 0\n\tv_mfma_f32_16x16x32_bf16 v[152:155], v[200:203], v[120:123], 0\n\tds_read_b128 v[164:167], %[bb] offset:1216\n\tv_mfma_f32_16x16x32_bf16 v[156:159], v[200:203], v[124:127], 0\n\tv_mfma_f32_16x16x32_bf16 v[160:163], v[200:203], v[128:131], 0\n\tds_read_b128 v[168:171], %[bb] offset:1280\n\tv_mfma_f32_16x16x32_bf16 v[204:207], v[188:191], v[4:7], 0\n\tv_mfma_f32_16x16x32_bf16 v[218:221], v[176:179], v[4:7], 0\n\tds_read_b128 v[172:175], %[bb] offset:1344\n\tv_mfma_f32_16x16x32_bf16 v[208:211], v[188:191], v[8:11], 0\n\tv_mfma_f32_16x16x32_bf16 v[222:225], v[176:179], v[8:11], 0\n\tv_mfma_f32_16x16x32_bf16 v[212:215], v[188:191], v[12:15], 0\n\tv_mfma_f32_16x16x32_bf16 v[226:229], v[176:179], v[16:19], 0\n\tv_mfma_f32_16x16x32_bf16 v[152:155], v[20:23], v[176:179], v[152:155]\n\ts_waitcnt lgkmcnt(0)\n\ts_nop 0\n\tv_mfma_f32_16x16x32_bf16 v[140:143], v[52:55], v[180:183], v[140:143]\n\tds_read_b128 v[176:179], %[bb] offset:1408\n\tv_cvt_pk_bf16_f32 v192, v221, v207\n\tv_mfma_f32_16x16x32_bf16 v[144:147], v[84:87], v[180:183], v[144:147]\n\tds_read_b128 v[180:183], %[bb] offset:1472\n\tv_cvt_pk_bf16_f32 v193, v220, v206\n\tv_mfma_f32_16x16x32_bf16 v[140:143], v[48:51], v[184:187], v[140:143]\n\tv_cvt_pk_bf16_f32 v194, v219, v205\n\tv_mfma_f32_16x16x32_bf16 v[144:147], v[80:83], v[184:187], v[144:147]\n\tds_read_b128 v[184:187], %[bb] offset:1536\n\tv_cvt_pk_bf16_f32 v195, v218, v204\n\tv_mfma_f32_16x16x32_bf16 v[140:143], v[44:47], v[164:167], v[140:143]\n\tv_cvt_pk_bf16_f32 v196, v225, v211\n\tv_mfma_f32_16x16x32_bf16 v[144:147], v[76:79], v[164:167], v[144:147]\n\tv_cvt_pk_bf16_f32 v197, v224, v210\n\tv_mfma_f32_16x16x32_bf16 v[140:143], v[40:43], v[168:171], v[140:143]\n\tv_cvt_pk_bf16_f32 v198, v223, v209\n\tv_mfma_f32_16x16x32_bf16 v[144:147], v[72:75], v[168:171], v[144:147]\n\tv_cvt_pk_bf16_f32 v199, v222, v208\n\tv_mfma_f32_16x16x32_bf16 v[140:143], v[36:39], v[172:175], v[140:143]\n\tv_cvt_pk_bf16_f32 v200, v229, v215\n\tv_mfma_f32_16x16x32_bf16 v[144:147], v[68:71], v[172:175], v[144:147]\n\ts_waitcnt lgkmcnt(0)\n\ts_nop 0\n\tv_mfma_f32_16x16x32_bf16 v[140:143], v[32:35], v[176:179], v[140:143]\n\tv_cvt_pk_bf16_f32 v201, v228, v214\n\tv_mfma_f32_16x16x32_bf16 v[144:147], v[64:67], v[176:179], v[144:147]\n\tv_cvt_pk_bf16_f32 v202, v227, v213\n\tv_mfma_f32_16x16x32_bf16 v[140:143], v[28:31], v[180:183], v[140:143]\n\tds_read_b128 v[188:191], %[bb] offset:1520\n\tv_cvt_pk_bf16_f32 v203, v226, v212\n\tv_mfma_f32_16x16x32_bf16 v[144:147], v[60:63], v[180:183], v[144:147]\n\tv_mfma_f32_16x16x32_bf16 v[140:143], v[24:27], v[184:187], v[140:143]\n\tv_mfma_f32_16x16x32_bf16 v[144:147], v[56:59], v[184:187], v[144:147]\n\tv_mfma_f32_16x16x32_bf16 v[148:151], v[88:91], v[184:187], v[148:151]\n\tv_mfma_f32_16x16x32_bf16 v[144:147], v[192:195], v[92:95], v[144:147]\n\tv_mfma_f32_16x16x32_bf16 v[148:151], v[192:195], v[96:99], v[148:151]\n\tv_mfma_f32_16x16x32_bf16 v[152:155], v[192:195], v[100:103], v[152:155]\n\tds_read_b128 v[164:167], %[bb] offset:1600\n\tv_mfma_f32_16x16x32_bf16 v[144:147], v[196:199], v[104:107], v[144:147]\n\tv_mfma_f32_16x16x32_bf16 v[148:151], v[196:199], v[108:111], v[148:151]\n\tds_write_b128 %[yb], v[140:143] offset:2304\n\tv_mfma_f32_16x16x32_bf16 v[152:155], v[196:199], v[112:115], v[152:155]\n\tds_read_b128 v[168:171], %[bb] offset:1664\n\tv_mfma_f32_16x16x32_bf16 v[152:155], v[200:203], v[116:119], v[152:155]\n\tds_write_b8 %[ctc], %[one] offset:128\n\tds_read_b128 v[172:175], %[bb] offset:1728\n\tv_mfma_f32_16x16x32_bf16 v[156:159], v[200:203], v[120:123], v[156:159]\n\tv_mfma_f32_16x16x32_bf16 v[160:163], v[200:203], v[124:127], v[160:163]\n\ts_waitcnt lgkmcnt(3)\n\ts_nop 0\n\tv_mfma_f32_16x16x32_bf16 v[204:207], v[188:191], v[4:7], 0\n\tds_read_b128 v[176:179], %[bb] offset:1792\n\tv_mfma_f32_16x16x32_bf16 v[218:221], v[184:187], v[4:7], 0\n\tds_read_b128 v[180:183], %[bb] offset:1856\n\tv_mfma_f32_16x16x32_bf16 v[208:211], v[188:191], v[8:11], 0\n\tv_mfma_f32_16x16x32_bf16 v[222:225], v[184:187], v[8:11], 0\n\tv_mfma_f32_16x16x32_bf16 v[212:215], v[188:191], v[12:15], 0\n\tv_mfma_f32_16x16x32_bf16 v[226:229], v[184:187], v[16:19], 0\n\tv_mfma_f32_16x16x32_bf16 v[156:159], v[20:23], v[184:187], v[156:159]\n\tds_read_b128 v[184:187], %[bb] offset:1920\n\tv_mfma_f32_16x16x32_bf16 v[144:147], v[52:55], v[164:167], v[144:147]\n\tv_cvt_pk_bf16_f32 v192, v221, v207\n\tv_mfma_f32_16x16x32_bf16 v[148:151], v[84:87], v[164:167], v[148:151]\n\ts_waitcnt lgkmcnt(1)\n\ts_nop 0\n\tv_mfma_f32_16x16x32_bf16 v[144:147], v[48:51], v[168:171], v[144:147]\n\tds_read_b128 v[164:167], %[bb] offset:1984\n\tv_cvt_pk_bf16_f32 v193, v220, v206\n\tv_mfma_f32_16x16x32_bf16 v[148:151], v[80:83], v[168:171], v[148:151]\n\tds_read_b128 v[168:171], %[bb] offset:2048\n\tv_cvt_pk_bf16_f32 v194, v219, v205\n\tv_mfma_f32_16x16x32_bf16 v[144:147], v[44:47], v[172:175], v[144:147]\n\tds_read_b128 v[188:191], %[bb] offset:2032\n\tv_cvt_pk_bf16_f32 v195, v218, v204\n\tv_mfma_f32_16x16x32_bf16 v[148:151], v[76:79], v[172:175], v[148:151]\n\tv_cvt_pk_bf16_f32 v196, v225, v211\n\tv_mfma_f32_16x16x32_bf16 v[144:147], v[40:43], v[176:179], v[144:147]\n\tv_cvt_pk_bf16_f32 v197, v224, v210\n\tv_mfma_f32_16x16x32_bf16 v[148:151], v[72:75], v[176:179], v[148:151]\n\tv_cvt_pk_bf16_f32 v198, v223, v209\n\tv_mfma_f32_16x16x32_bf16 v[144:147], v[36:39], v[180:183], v[144:147]\n\tv_cvt_pk_bf16_f32 v199, v222, v208\n\tv_mfma_f32_16x16x32_bf16 v[148:151], v[68:71], v[180:183], v[148:151]\n\ts_waitcnt lgkmcnt(0)\n\ts_nop 0\n\tv_mfma_f32_16x16x32_bf16 v[144:147], v[32:35], v[184:187], v[144:147]\n\tv_cvt_pk_bf16_f32 v200, v229, v215\n\tv_mfma_f32_16x16x32_bf16 v[148:151], v[64:67], v[184:187], v[148:151]\n\tv_cvt_pk_bf16_f32 v201, v228, v214\n\tv_mfma_f32_16x16x32_bf16 v[144:147], v[28:31], v[164:167], v[144:147]\n\tv_cvt_pk_bf16_f32 v202, v227, v213\n\tv_mfma_f32_16x16x32_bf16 v[148:151], v[60:63], v[164:167], v[148:151]\n\tv_cvt_pk_bf16_f32 v203, v226, v212\n\tv_mfma_f32_16x16x32_bf16 v[144:147], v[24:27], v[168:171], v[144:147]\n\tv_mfma_f32_16x16x32_bf16 v[148:151], v[56:59], v[168:171], v[148:151]\n\tv_mfma_f32_16x16x32_bf16 v[152:155], v[88:91], v[168:171], v[152:155]\n\tv_mfma_f32_16x16x32_bf16 v[204:207], v[188:191], v[4:7], 0\n\tv_mfma_f32_16x16x32_bf16 v[218:221], v[168:171], v[4:7], 0\n\tv_mfma_f32_16x16x32_bf16 v[208:211], v[188:191], v[8:11], 0\n\tv_mfma_f32_16x16x32_bf16 v[222:225], v[168:171], v[8:11], 0\n\tv_mfma_f32_16x16x32_bf16 v[212:215], v[188:191], v[12:15], 0\n\tv_mfma_f32_16x16x32_bf16 v[226:229], v[168:171], v[16:19], 0\n\tds_write_b128 %[yb], v[144:147] offset:3456\n\tv_mfma_f32_16x16x32_bf16 v[160:163], v[20:23], v[168:171], v[160:163]\n\tds_read_b32 v216, %[ctl] offset:20\n\tv_mfma_f32_16x16x32_bf16 v[148:151], v[192:195], v[92:95], v[148:151]\n\tds_write_b8 %[ctc], %[one] offset:192\n\tv_mfma_f32_16x16x32_bf16 v[152:155], v[192:195], v[96:99], v[152:155]\n\tv_mfma_f32_16x16x32_bf16 v[156:159], v[192:195], v[100:103], v[156:159]\n\tv_mfma_f32_16x16x32_bf16 v[148:151], v[196:199], v[104:107], v[148:151]\n\tv_cvt_pk_bf16_f32 v192, v221, v207\n\tv_mfma_f32_16x16x32_bf16 v[152:155], v[196:199], v[108:111], v[152:155]\n\tv_cvt_pk_bf16_f32 v193, v220, v206\n\tv_mfma_f32_16x16x32_bf16 v[156:159], v[196:199], v[112:115], v[156:159]\n\tv_cvt_pk_bf16_f32 v194, v219, v205\n\tv_mfma_f32_16x16x32_bf16 v[156:159], v[200:203], v[116:119], v[156:159]\n\tv_cvt_pk_bf16_f32 v195, v218, v204\n\tv_mfma_f32_16x16x32_bf16 v[160:163], v[200:203], v[120:123], v[160:163]\n\ts_waitcnt lgkmcnt(0)\n\ts_nop 0\n\tv_readfirstlane_b32 %[s], v216\n\ts_mov_b32 %[c], 0x400000\n\ts_cmp_ge_u32 %[s], 2\n\ts_cbranch_scc1 2f\n\t1:\n\tds_read_b32 v216, %[ctl] offset:20\n\ts_waitcnt lgkmcnt(0)\n\tv_readfirstlane_b32 %[s], v216\n\ts_cmp_ge_u32 %[s], 2\n\ts_cbranch_scc1 2f\n\ts_sleep 1\n\ts_sub_u32 %[c], %[c], 1\n\ts_cbranch_scc0 1b\n\t.p2align 3\n\t2:\n\tv_mfma_f32_16x16x32_bf16 v[152:155], v[192:195], v[92:95], v[152:155]\n\tds_read_b128 v[172:175], %[bb] offset:2112\n\tv_cvt_pk_bf16_f32 v196, v225, v211\n\tv_mfma_f32_16x16x32_bf16 v[156:159], v[192:195], v[96:99], v[156:159]\n\tds_read_b128 v[176:179], %[bb] offset:2176\n\tv_cvt_pk_bf16_f32 v197, v224, v210\n\tv_mfma_f32_16x16x32_bf16 v[160:163], v[192:195], v[100:103], v[160:163]\n\tv_cvt_pk_bf16_f32 v198, v223, v209\n\tds_read_b128 v[180:183], %[bb] offset:2240\n\tv_cvt_pk_bf16_f32 v199, v222, v208\n\ts_nop 1\n\ts_nop 0\n\tv_mfma_f32_16x16x32_bf16 v[152:155], v[196:199], v[104:107], v[152:155]\n\tds_read_b128 v[184:187], %[bb] offset:2304\n\tv_cvt_pk_bf16_f32 v200, v229, v215\n\tv_mfma_f32_16x16x32_bf16 v[156:159], v[196:199], v[108:111], v[156:159]\n\tv_cvt_pk_bf16_f32 v201, v228, v214\n\tds_read_b128 v[164:167], %[bb] offset:2368\n\tv_mfma_f32_16x16x32_bf16 v[160:163], v[196:199], v[112:115], v[160:163]\n\tv_cvt_pk_bf16_f32 v202, v227, v213\n\tds_read_b128 v[168:171], %[bb] offset:2432\n\tv_cvt_pk_bf16_f32 v203, v226, v212\n\ts_nop 1\n\ts_nop 0\n\tv_mfma_f32_16x16x32_bf16 v[160:163], v[200:203], v[116:119], v[160:163]\n\ts_waitcnt lgkmcnt(3)\n\ts_nop 0\n\tv_mfma_f32_16x16x32_bf16 v[148:151], v[52:55], v[172:175], v[148:151]\n\tv_mfma_f32_16x16x32_bf16 v[152:155], v[84:87], v[172:175], v[152:155]\n\tds_read_b128 v[172:175], %[bb] offset:2496\n\tv_mfma_f32_16x16x32_bf16 v[148:151], v[48:51], v[176:179], v[148:151]\n\tv_mfma_f32_16x16x32_bf16 v[152:155], v[80:83], v[176:179], v[152:155]\n\tds_read_b128 v[176:179], %[bb] offset:2560\n\tv_mfma_f32_16x16x32_bf16 v[148:151], v[44:47], v[180:183], v[148:151]\n\tds_read_b128 v[188:191], %[bb] offset:2544\n\tv_mfma_f32_16x16x32_bf16 v[152:155], v[76:79], v[180:183], v[152:155]\n\ts_waitcnt lgkmcnt(2)\n\ts_nop 0\n\tv_mfma_f32_16x16x32_bf16 v[148:151], v[40:43], v[184:187], v[148:151]\n\tv_mfma_f32_16x16x32_bf16 v[152:155], v[72:75], v[184:187], v[152:155]\n\tds_read_b128 v[180:183], %[bb] offset:2624\n\tv_mfma_f32_16x16x32_bf16 v[148:151], v[36:39], v[164:167], v[148:151]\n\tv_mfma_f32_16x16x32_bf16 v[152:155], v[68:71], v[164:167], v[152:155]\n\tds_read_b128 v[184:187], %[bb] offset:2688\n\tv_mfma_f32_16x16x32_bf16 v[148:151], v[32:35], v[168:171], v[148:151]\n\tv_mfma_f32_16x16x32_bf16 v[152:155], v[64:67], v[168:171], v[152:155]\n\tds_read_b128 v[164:167], %[bb] offset:2752\n\tv_mfma_f32_16x16x32_bf16 v[148:151], v[28:31], v[172:175], v[148:151]\n\tv_mfma_f32_16x16x32_bf16 v[152:155], v[60:63], v[172:175], v[152:155]\n\ts_waitcnt lgkmcnt(1)\n\ts_nop 0\n\tv_mfma_f32_16x16x32_bf16 v[148:151], v[24:27], v[176:179], v[148:151]\n\tds_read_b128 v[168:171], %[bb] offset:2816\n\tv_mfma_f32_16x16x32_bf16 v[152:155], v[56:59], v[176:179], v[152:155]\n\tds_read_b128 v[172:175], %[bb] offset:2880\n\tv_mfma_f32_16x16x32_bf16 v[156:159], v[88:91], v[176:179], v[156:159]\n\tv_mfma_f32_16x16x32_bf16 v[204:207], v[188:191], v[4:7], 0\n\tv_mfma_f32_16x16x32_bf16 v[218:221], v[176:179], v[4:7], 0\n\tv_mfma_f32_16x16x32_bf16 v[208:211], v[188:191], v[8:11], 0\n\tv_mfma_f32_16x16x32_bf16 v[222:225], v[176:179], v[8:11], 0\n\tds_write_b128 %[yb], v[148:151] offset:4608\n\tv_mfma_f32_16x16x32_bf16 v[152:155], v[52:55], v[180:183], v[152:155]\n\tds_read_b128 v[176:179], %[bb] offset:2944\n\tv_mfma_f32_16x16x32_bf16 v[156:159], v[84:87], v[180:183], v[156:159]\n\tds_write_b8 %[ctc], %[one] offset:256\n\tds_read_b128 v[180:183], %[bb] offset:3008\n\tv_mfma_f32_16x16x32_bf16 v[152:155], v[48:51], v[184:187], v[152:155]\n\tv_cvt_pk_bf16_f32 v192, v221, v207\n\tv_mfma_f32_16x16x32_bf16 v[156:159], v[80:83], v[184:187], v[156:159]\n\ts_waitcnt lgkmcnt(3)\n\ts_nop 0\n\tv_mfma_f32_16x16x32_bf16 v[152:155], v[44:47], v[164:167], v[152:155]\n\tds_read_b128 v[184:187], %[bb] offset:3072\n\tv_cvt_pk_bf16_f32 v193, v220, v206\n\tv_mfma_f32_16x16x32_bf16 v[156:159], v[76:79], v[164:167], v[156:159]\n\tv_cvt_pk_bf16_f32 v194, v219, v205\n\tv_mfma_f32_16x16x32_bf16 v[152:155], v[40:43], v[168:171], v[152:155]\n\tv_cvt_pk_bf16_f32 v195, v218, v204\n\tv_mfma_f32_16x16x32_bf16 v[156:159], v[72:75], v[168:171], v[156:159]\n\tds_read_b128 v[188:191], %[bb] offset:3056\n\tv_cvt_pk_bf16_f32 v196, v225, v211\n\tv_mfma_f32_16x16x32_bf16 v[152:155], v[36:39], v[172:175], v[152:155]\n\tv_cvt_pk_bf16_f32 v197, v224, v210\n\tv_mfma_f32_16x16x32_bf16 v[156:159], v[68:71], v[172:175], v[156:159]\n\ts_waitcnt lgkmcnt(1)\n\ts_nop 0\n\tv_mfma_f32_16x16x32_bf16 v[152:155], v[32:35], v[176:179], v[152:155]\n\tds_read_b32 v216, %[ctl] offset:24\n\tv_cvt_pk_bf16_f32 v198, v223, v209\n\tv_mfma_f32_16x16x32_bf16 v[156:159], v[64:67], v[176:179], v[156:159]\n\tv_cvt_pk_bf16_f32 v199, v222, v208\n\tv_mfma_f32_16x16x32_bf16 v[152:155], v[28:31], v[180:183], v[152:155]\n\tv_mfma_f32_16x16x32_bf16 v[156:159], v[60:63], v[180:183], v[156:159]\n\tv_mfma_f32_16x16x32_bf16 v[152:155], v[24:27], v[184:187], v[152:155]\n\tv_mfma_f32_16x16x32_bf16 v[156:159], v[56:59], v[184:187], v[156:159]\n\tv_mfma_f32_16x16x32_bf16 v[160:163], v[88:91], v[184:187], v[160:163]\n\tv_mfma_f32_16x16x32_bf16 v[156:159], v[192:195], v[92:95], v[156:159]\n\ts_waitcnt lgkmcnt(0)\n\ts_nop 0\n\tv_readfirstlane_b32 %[s], v216\n\ts_mov_b32 %[c], 0x400000\n\ts_cmp_ge_u32 %[s], 2\n\ts_cbranch_scc1 2f\n\t1:\n\tds_read_b32 v216, %[ctl] offset:24\n\ts_waitcnt lgkmcnt(0)\n\tv_readfirstlane_b32 %[s], v216\n\ts_cmp_ge_u32 %[s], 2\n\ts_cbranch_scc1 2f\n\ts_sleep 1\n\ts_sub_u32 %[c], %[c], 1\n\ts_cbranch_scc0 1b\n\t.p2align 3\n\t2:\n\tv_mfma_f32_16x16x32_bf16 v[160:163], v[192:195], v[96:99], v[160:163]\n\tds_write_b128 %[yb], v[152:155] offset:5760\n\tv_mfma_f32_16x16x32_bf16 v[156:159], v[196:199], v[104:107], v[156:159]\n\tds_read_b128 v[164:167], %[bb] offset:3136\n\tv_mfma_f32_16x16x32_bf16 v[160:163], v[196:199], v[108:111], v[160:163]\n\tds_write_b8 %[ctc], %[one] offset:320\n\tds_read_b128 v[168:171], %[bb] offset:3200\n\tv_mfma_f32_16x16x32_bf16 v[204:207], v[188:191], v[4:7], 0\n\tds_read_b128 v[172:175], %[bb] offset:3264\n\tv_mfma_f32_16x16x32_bf16 v[218:221], v[184:187], v[4:7], 0\n\tds_read_b128 v[176:179], %[bb] offset:3328\n\tv_mfma_f32_16x16x32_bf16 v[208:211], v[188:191], v[8:11], 0\n\tds_read_b128 v[180:183], %[bb] offset:3392\n\tv_mfma_f32_16x16x32_bf16 v[222:225], v[184:187], v[8:11], 0\n\ts_waitcnt lgkmcnt(3)\n\ts_nop 0\n\tv_mfma_f32_16x16x32_bf16 v[156:159], v[52:55], v[164:167], v[156:159]\n\tds_read_b128 v[184:187], %[bb] offset:3456\n\tv_cvt_pk_bf16_f32 v192, v221, v207\n\tv_mfma_f32_16x16x32_bf16 v[160:163], v[84:87], v[164:167], v[160:163]\n\tds_read_b128 v[164:167], %[bb] offset:3520\n\tv_cvt_pk_bf16_f32 v193, v220, v206\n\tv_mfma_f32_16x16x32_bf16 v[156:159], v[48:51], v[168:171], v[156:159]\n\tv_cvt_pk_bf16_f32 v194, v219, v205\n\tv_mfma_f32_16x16x32_bf16 v[160:163], v[80:83], v[168:171], v[160:163]\n\ts_waitcnt lgkmcnt(2)\n\ts_nop 0\n\tv_mfma_f32_16x16x32_bf16 v[156:159], v[44:47], v[172:175], v[156:159]\n\tds_read_b128 v[168:171], %[bb] offset:3584\n\tv_cvt_pk_bf16_f32 v195, v218, v204\n\tv_mfma_f32_16x16x32_bf16 v[160:163], v[76:79], v[172:175], v[160:163]\n\tds_read_b128 v[172:175], %[bb] offset:3648\n\tv_cvt_pk_bf16_f32 v196, v225, v211\n\tv_mfma_f32_16x16x32_bf16 v[156:159], v[40:43], v[176:179], v[156:159]\n\tglobal_load_dwordx2 v[240:241], %[so], %[ws] sc1\n\tv_cvt_pk_bf16_f32 v197, v224, v210\n\tv_mfma_f32_16x16x32_bf16 v[160:163], v[72:75], v[176:179], v[160:163]\n\tds_read_b128 v[176:179], %[bb] offset:3712\n\tv_cvt_pk_bf16_f32 v198, v223, v209\n\tv_mfma_f32_16x16x32_bf16 v[156:159], v[36:39], v[180:183], v[156:159]\n\tv_cvt_pk_bf16_f32 v199, v222, v208\n\tv_mfma_f32_16x16x32_bf16 v[160:163], v[68:71], v[180:183], v[160:163]\n\ts_waitcnt lgkmcnt(1)\n\ts_nop 0\n\tv_mfma_f32_16x16x32_bf16 v[156:159], v[32:35], v[184:187], v[156:159]\n\tds_read_b128 v[180:183], %[bb] offset:3776\n\tv_mfma_f32_16x16x32_bf16 v[160:163], v[64:67], v[184:187], v[160:163]\n\tds_read_b128 v[184:187], %[bb] offset:3840\n\tv_mfma_f32_16x16x32_bf16 v[156:159], v[28:31], v[164:167], v[156:159]\n\tv_mfma_f32_16x16x32_bf16 v[160:163], v[60:63], v[164:167], v[160:163]\n\tds_read_b128 v[164:167], %[bb] offset:3904\n\tv_mfma_f32_16x16x32_bf16 v[156:159], v[24:27], v[168:171], v[156:159]\n\tv_mfma_f32_16x16x32_bf16 v[160:163], v[56:59], v[168:171], v[160:163]\n\tds_read_b128 v[168:171], %[bb] offset:3968\n\tv_mfma_f32_16x16x32_bf16 v[160:163], v[192:195], v[92:95], v[160:163]\n\tv_mfma_f32_16x16x32_bf16 v[160:163], v[196:199], v[104:107], v[160:163]\n\tv_mfma_f32_16x16x32_bf16 v[160:163], v[52:55], v[172:175], v[160:163]\n\ts_waitcnt lgkmcnt(1)\n\ts_nop 0\n\tv_mfma_f32_16x16x32_bf16 v[160:163], v[48:51], v[176:179], v[160:163]\n\tds_write_b128 %[yb], v[156:159] offset:6912\n\tv_mfma_f32_16x16x32_bf16 v[160:163], v[44:47], v[180:183], v[160:163]\n\tds_read_b128 v[172:175], %[bb] offset:4032\n\tv_mfma_f32_16x16x32_bf16 v[160:163], v[40:43], v[184:187], v[160:163]\n\tds_write_b8 %[ctc], %[one] offset:384\n\tds_read_b128 v[176:179], %[bb] offset:4096\n\tv_mfma_f32_16x16x32_bf16 v[160:163], v[36:39], v[164:167], v[160:163]\n\ts_waitcnt lgkmcnt(4)\n\ts_nop 0\n\tv_mfma_f32_16x16x32_bf16 v[160:163], v[32:35], v[168:171], v[160:163]\n\ts_waitcnt lgkmcnt(2)\n\ts_nop 0\n\tv_mfma_f32_16x16x32_bf16 v[160:163], v[28:31], v[172:175], v[160:163]\n\ts_waitcnt lgkmcnt(0)\n\ts_nop 0\n\tv_mfma_f32_16x16x32_bf16 v[160:163], v[24:27], v[176:179], v[160:163]\n\ts_nop 7\n\tds_write_b128 %[yb], v[160:163] offset:8064\n\tds_write_b8 %[ctc], %[one] offset:448"
#define CONV_CLOBBER "v24", "v25", "v26", "v27", "v28", "v29", "v30", "v31", "v32", "v33", "v34", "v35", "v36", "v37", "v38", "v39", "v40", "v41", "v42", "v43", "v44", "v45", "v46", "v47", "v48", "v49", "v50", "v51", "v52", "v53", "v54", "v55", "v56", "v57", "v58", "v59", "v60", "v61", "v62", "v63", "v64", "v65", "v66", "v67", "v68", "v69", "v70", "v71", "v72", "v73", "v74", "v75", "v76", "v77", "v78", "v79", "v80", "v81", "v82", "v83", "v84", "v85", "v86", "v87", "v88", "v89", "v90", "v91", "v92", "v93", "v94", "v95", "v96", "v97", "v98", "v99", "v100", "v101", "v102", "v103", "v104", "v105", "v106", "v107", "v108", "v109", "v110", "v111", "v112", "v113", "v114", "v115", "v116", "v117", "v118", "v119", "v120", "v121", "v122", "v123", "v124", "v125", "v126", "v127", "v128", "v129", "v130", "v131", "v132", "v133", "v134", "v135", "v136", "v137", "v138", "v139", "v140", "v141", "v142", "v143", "v144", "v145", "v146", "v147", "v148", "v149", "v150", "v151", "v152", "v153", "v154", "v155", "v156", "v157", "v158", "v159", "v160", "v161", "v162", "v163", "v164", "v165", "v166", "v167", "v168", "v169", "v170", "v171", "v172", "v173", "v174", "v175", "v176", "v177", "v178", "v179", "v180", "v181", "v182", "v183", "v184", "v185", "v186", "v187", "v188", "v189", "v190", "v191", "v192", "v193", "v194", "v195", "v196", "v197", "v198", "v199", "v200", "v201", "v202", "v203", "v204", "v205", "v206", "v207", "v208", "v209", "v210", "v211", "v212", "v213", "v214", "v215", "v216", "v217", "v218", "v219", "v220", "v221", "v222", "v223", "v224", "v225", "v226", "v227", "v228", "v229"

typedef __attribute__((address_space(4))) const Params* KArg;
#define ROLE_BEGIN(N) int b, tid; \
    asm volatile("fk_role" #N ":\n\tv_mov_b32 %0, v0" : "=&v"(tid) : "{s2}"(b0), "{v0}"(tid0) : "memory"); \
    asm volatile("s_mov_b32 %0, s2" : "=&s"(b) : "{s2}"(b0)); \
    const int lane = tid & 63, wave = __builtin_amdgcn_readfirstlane(tid >> 6), xcd = b & 7, slot = b >> 3; \
    const int cw = slot < 16 ? xcd * 16 + slot : 112 + xcd * 2 + slot, chbase = cw * 4, w = xcd * 9 + (slot - 18); (void)chbase; (void)w; (void)lane
#define ROLE_ARGS() Params p; SBAR(); \
    asm volatile("s_waitcnt lgkmcnt(0)" : "={s[56:57]}"(p.x), "={s[58:59]}"(p.Are), "={s[60:61]}"(p.Aim), "={s[62:63]}"(p.Bre), "={s[64:65]}"(p.Bim), "={s[66:67]}"(p.Cre), "={s[68:69]}"(p.Cim), "={s[70:71]}"(p.logdt), \
                 "={s[72:73]}"(p.dskip), "={s[74:75]}"(p.wr), "={s[76:77]}"(p.wg), "={s[78:79]}"(p.wu), "={s[80:81]}"(p.wd), "={s[82:83]}"(p.gw), "={s[84:85]}"(p.out), "={s[86:87]}"(p.ws))
#define ROLE_END() do { __builtin_amdgcn_s_waitcnt(0x0070); asm volatile("s_endpgm" ::: "memory"); } while (0)
#define MF(A_, B_, C_) __builtin_amdgcn_mfma_f32_16x16x32_bf16(A_, B_, C_, 0, 0, 0)

#define FFN_PRE() u32 gof = w * 128 + ((wave >> 1) * 8 + (lane >> 3)) * (F * 2) + ((lane & 7) ^ (((lane >> 3) & 2) | ((wave & 2) << 1))) * 16; asm volatile("" : "+v"(gof)); ROLE_ARGS()
#define FFN_FILL() \
    { const char* gsrc = (const char*)((wave & 1) ? p.wu : p.wg); \
      _Pragma("unroll") for (int i = 0; i < 18; ++i) { \
          asm volatile("s_mov_b32 m0, %2\n\ts_nop 0\n\tglobal_load_lds_dwordx4 %0, %1" WNT :: "v"(gof), "s"(gsrc + i * (32 * F * 2)), "s"((8 * i + wave) * 1024) : "memory"); \
          if (i == 5 || i == 11) __builtin_amdgcn_s_barrier(); } }
#define FFN_WDFRAG(k, pr) do { LAS char* t_ = ta + (pr) * 64; \
    _Pragma("unroll") for (int e_ = 0; e_ < 8; ++e_) { const int e = (e_ & 1) | ((e_ & 2) << 1) | ((e_ & 4) >> 1); a[k][e] = __builtin_amdgcn_ds_read_tr16_b64_v4i16((LAS s16x4*)(t_ + (e >> 2) * 32 + (e & 3) * 16 * WDP)); } SBAR(); } while (0)
#define FFN_DOWN(k, pr) do { v4f y_ = MF(__builtin_shufflevector(a[k][0], a[k][1], 0, 1, 2, 3, 4, 5, 6, 7), hf[0], ((v4f){0.f, 0.f, 0.f, 0.f})); \
    y_ = MF(__builtin_shufflevector(a[k][4], a[k][5], 0, 1, 2, 3, 4, 5, 6, 7), hf[1], y_); \
    y_ = MF(__builtin_shufflevector(a[k][2], a[k][3], 0, 1, 2, 3, 4, 5, 6, 7), hf[2], y_); \
    ya[k] = MF(__builtin_shufflevector(a[k][6], a[k][7], 0, 1, 2, 3, 4, 5, 6, 7), hf[3], y_); } while (0)
#define FFN_ADDR() const int n = lane & 15, q = lane >> 4; \
    LAS char* ta = smem + (lane >> 2) * WDP + (lane & 3) * 8; \
    LAS char* hrow = smem + FL_H + (n & 7) * 144 + q * 16; \
    LAS char* ha = n < 8 ? hrow : smem + FL_Z + q * 16; \
    LAS char* hb = n < 8 ? smem + FL_Z + q * 16 : hrow; \
    u32 obase = OFF_PART + (4 * (n >> 3) + q) * PP + hline(p.ws, w) + (n & 7) * 16; \
    asm volatile("" : "+v"(ta), "+v"(ha), "+v"(hb), "+v"(obase))
#define FFN_HFRAG() const bf16x8 hf[4] = {*(LAS bf16x8*)(ha), *(LAS bf16x8*)(hb), *(LAS bf16x8*)(ha + 64), *(LAS bf16x8*)(hb + 64)}
#define FFN_PUB(k, pr) __builtin_amdgcn_raw_buffer_store_b128((v4u){cvtpk(ya[k][0], ya[k][1]), tag, cvtpk(ya[k][2], ya[k][3]), tag}, WSR(), obase + (pr) * (8 * PP), 0, 16)

__global__ __launch_bounds__(512, 2) void fused_kernel(Params p_unused) {
    extern __shared__ __attribute__((aligned(16))) char smem_[];
    LAS char* smem = (LAS char*)smem_;
    const KArg kp0 = (KArg)__builtin_amdgcn_kernarg_segment_ptr();
    const int tid0 = threadIdx.x, b0 = blockIdx.x;
    asm volatile("s_load_dwordx16 s[56:71], s[0:1], 0x0\n\ts_load_dwordx16 s[72:87], s[0:1], 0x40\n\tv_readfirstlane_b32 s92, v0\n\t"
                 "s_cmp_ge_u32 s2, 32\n\ts_cbranch_scc1 1f\n\t"
                 "s_getpc_b64 s[90:91]\n\ts_and_b32 s90, s90, 0xffffff00\n\ts_and_b32 s93, s2, -8\n\ts_lshr_b32 s89, s92, 6\n\ts_add_u32 s93, s93, s89\n\ts_min_u32 s93, s93, %3\n\ts_lshl_b32 s93, s93, 10\n\t"
                 "v_and_b32 v1, 63, v0\n\tv_lshlrev_b32 v1, 4, v1\n\tv_add_u32 v1, s93, v1\n\ts_mov_b32 m0, %4\n\ts_nop 0\n\tglobal_load_lds_dwordx4 v1, s[90:91]\n"
                 "1:\n\ts_cmp_lt_u32 s2, 144\n\ts_cbranch_scc0 2f\n\ts_cmp_lt_u32 s92, 256\n\ts_cbranch_scc1 fk_role0\n\ts_branch fk_role1\n"
                 "2:\n\ts_cmp_lt_u32 s2, 216\n\ts_cbranch_scc0 3f\n\ts_cmp_lt_u32 s92, 256\n\ts_cbranch_scc1 fk_role2\n\ts_branch fk_role3\n"
                 "3:\n\ts_cmp_lt_u32 s2, 224\n\ts_cbranch_scc1 fk_role4\n\ts_branch fk_role5"
                 :: "{s[0:1]}"(kp0), "{s2}"(b0), "{v0}"(tid0), "n"(CODE_KB - 1), "n"(LDS_DUMMY) : "memory", "scc", "s89", "s90", "s91", "s92", "s93", "v1");
    const int slot = b0 >> 3, wave = __builtin_amdgcn_readfirstlane(tid0 >> 6);
    if (slot >= 18) {
    if (slot >= 27 || wave >= 4) {
    if (slot < 28) {
    if (slot == 27)
    {
        ROLE_BEGIN(4);
        ROLE_ARGS();
        u32 tagv;
        asm volatile("v_mov_b32 %0, 0\n\tglobal_load_dword %0, %0, %1 sc1\n\ts_waitcnt vmcnt(0)" : "=&v"(tagv) : "s"(p.ws) : "memory");
        const u32 tag = __builtin_amdgcn_readfirstlane(tagv) + 1u;
        if (tid < 128) ((LAS u32*)smem)[tid] = 0;
        u32 ga = (u32)(uintptr_t)(smem + (lane & 15) * 4), gv = (u32)(uintptr_t)(smem + (lane >> 2) * 4), gq_ = (u32)(uintptr_t)(smem + (lane & 3) * 16), zero = (u32)(uintptr_t)smem;
        u32 ro = OFF_RG + hline(p.ws, tid >> 3) + (tid & 7) * 16, ro2 = OFF_RG + hline(p.ws, 64 + (tid >> 3)) + (tid & 7) * 16, so = OFF_SEL + ((lane & 1) * 8 + xcd) * 256, xo = (lane & 3) * 128;
        asm volatile("" : "+v"(ga), "+v"(gv), "+v"(gq_), "+v"(zero), "+v"(ro), "+v"(ro2), "+v"(so), "+v"(xo));
        BAR();
        __builtin_amdgcn_s_sleep(SELWAIT);
        v4u qa, qb;
        u32 c_;
#define POLL_A "global_load_dwordx4 v[20:23], %[ro], %[ws] sc1\n\tglobal_load_dwordx4 v[24:27], %[r2], %[ws] sc1\n\t"
#define POLL_B "global_load_dwordx4 v[28:31], %[ro], %[ws] sc1\n\tglobal_load_dwordx4 v[32:35], %[r2], %[ws] sc1\n\t"
        asm volatile("s_mov_b32 %[c], 0x40000\n\t" POLL_A "s_sleep 8\n\t" POLL_B
                     "1:\n\ts_waitcnt vmcnt(2)\n\tv_cmpx_eq_u32 %[tag], v21\n\tv_cmpx_eq_u32 %[tag], v23\n\tv_cmpx_eq_u32 %[tag], v25\n\tv_cmpx_eq_u32 %[tag], v27\n\t"
                     "s_cmp_eq_u64 exec, -1\n\ts_mov_b64 exec, -1\n\ts_cbranch_scc1 3f\n\t" POLL_A
                     "s_waitcnt vmcnt(2)\n\tv_cmpx_eq_u32 %[tag], v29\n\tv_cmpx_eq_u32 %[tag], v31\n\tv_cmpx_eq_u32 %[tag], v33\n\tv_cmpx_eq_u32 %[tag], v35\n\t"
                     "s_cmp_eq_u64 exec, -1\n\ts_mov_b64 exec, -1\n\ts_cbranch_scc1 2f\n\t" POLL_B
                     "s_sub_u32 %[c], %[c], 1\n\ts_cbranch_scc0 1b\n\ts_waitcnt vmcnt(0)\n"
                     "2:\n\tv_mov_b32 v36, v28\n\tv_mov_b32 v37, v30\n\tv_mov_b32 v38, v32\n\tv_mov_b32 v39, v34\n\ts_branch 4f\n"
                     "3:\n\tv_mov_b32 v36, v20\n\tv_mov_b32 v37, v22\n\tv_mov_b32 v38, v24\n\tv_mov_b32 v39, v26\n"
                     "4:"
                     : "=&{v[36:39]}"(qa), [c] "=&s"(c_) : [ro] "v"(ro), [r2] "v"(ro2), [ws] "s"(p.ws), [tag] "s"(tag)
                     : "memory", "scc", "vcc", "v20", "v21", "v22", "v23", "v24", "v25", "v26", "v27", "v28", "v29", "v30", "v31", "v32", "v33", "v34", "v35");
        asm volatile("" : "=&{v[20:23]}"(qb));
        v4u qc, qd, qe;
        asm volatile("" : "=&{v[24:27]}"(qc), "=&{v[28:31]}"(qd), "=&{v[32:35]}"(qe));
        u32 rank, tau, cnt;
        u64 m0_, m1_, m2_, m3_;
        {
            u32 v, nv; v4u q4;
            asm volatile("ds_max_u32 %0, %1\n\ts_waitcnt lgkmcnt(0)\n\ts_barrier" :: "v"(ga), "v"(max(max(qa.x, qa.y), max(qa.z, qa.w))) : "memory");
#define RANK16(off) asm volatile("ds_read_b32 %[v], %[gv] offset:" #off "\n\tds_read_b128 v[40:43], %[gq] offset:" #off "\n\tds_read_b32 %[n], %[z] offset:64\n\tv_mov_b32 %[r], 0\n\ts_waitcnt lgkmcnt(0)\n\t" \
                         "v_cmp_gt_u32 %[a], v40, %[v]\n\tv_cmp_gt_u32 %[b], v41, %[v]\n\tv_cmp_gt_u32 %[c], v42, %[v]\n\tv_cmp_gt_u32 %[d], v43, %[v]\n\t" \
                         "v_addc_co_u32 %[r], vcc, 0, %[r], %[a]\n\tv_addc_co_u32 %[r], vcc, 0, %[r], %[b]\n\tv_addc_co_u32 %[r], vcc, 0, %[r], %[c]\n\tv_addc_co_u32 %[r], vcc, 0, %[r], %[d]\n\ts_nop 1\n\t" \
                         "v_add_u32_dpp %[r], %[r], %[r] quad_perm:[1,0,3,2] row_mask:0xf bank_mask:0xf\n\ts_nop 1\n\tv_add_u32_dpp %[r], %[r], %[r] quad_perm:[2,3,0,1] row_mask:0xf bank_mask:0xf\n\ts_nop 0" \
                         : [v] "=&v"(v), [r] "=&v"(rank), [n] "=&v"(nv), "=&{v[40:43]}"(q4), [a] "=&s"(m0_), [b] "=&s"(m1_), [c] "=&s"(m2_), [d] "=&s"(m3_) : [gv] "v"(gv), [gq] "v"(gq_), [z] "v"(zero) : "memory", "vcc")
            RANK16(0);
            tau = (u32)__builtin_amdgcn_readlane((int)v, (int)__builtin_ctzll(__ballot(rank == 7)));
            u32 pos;
            asm volatile("s_nop 1\n\tv_mov_b32 %[n], 0\n\tv_cmp_le_u32 %[a], %[t], v36\n\tv_cmp_le_u32 %[b], %[t], v37\n\tv_cmp_le_u32 %[c], %[t], v38\n\tv_cmp_le_u32 %[d], %[t], v39\n\t"
                         "v_addc_co_u32 %[n], vcc, 0, %[n], %[a]\n\tv_addc_co_u32 %[n], vcc, 0, %[n], %[b]\n\tv_addc_co_u32 %[n], vcc, 0, %[n], %[c]\n\tv_addc_co_u32 %[n], vcc, 0, %[n], %[d]\n\t"
                         "v_lshlrev_b32 %[n], 2, %[n]\n\tv_cmpx_ne_u32 0, %[n]\n\tds_add_rtn_u32 %[p], %[z], %[n] offset:64\n\ts_waitcnt lgkmcnt(0)\n\t"
                         "s_mov_b64 exec, %[a]\n\tds_write_b32 %[p], v36 offset:256\n\tv_add_u32 %[p], 4, %[p]\n\t"
                         "s_mov_b64 exec, %[b]\n\tds_write_b32 %[p], v37 offset:256\n\tv_add_u32 %[p], 4, %[p]\n\t"
                         "s_mov_b64 exec, %[c]\n\tds_write_b32 %[p], v38 offset:256\n\tv_add_u32 %[p], 4, %[p]\n\t"
                         "s_mov_b64 exec, %[d]\n\tds_write_b32 %[p], v39 offset:256\n\t"
                         "s_mov_b64 exec, -1\n\ts_waitcnt lgkmcnt(0)\n\ts_barrier"
                         : [n] "=&v"(cnt), [p] "=&v"(pos), [a] "=&s"(m0_), [b] "=&s"(m1_), [c] "=&s"(m2_), [d] "=&s"(m3_) : [t] "s"(tau), [z] "v"(zero), "{v[36:39]}"(qa) : "memory", "vcc");
            if (wave == 0) {
                RANK16(256);
                const int nc = __builtin_amdgcn_readfirstlane((int)nv) >> 2;
                if (__builtin_expect(nc > 16, 0)) {
                    v = (u32)__shfl((int)top8_d(smem, lane, nc), (lane >> 2) & 7);
                    rank = lane >> 2;
                }
                const u64 m8 = __ballot(rank < 8);
                asm volatile("s_and_b64 exec, %[m], %[k2]\n\tglobal_store_dwordx2 %[so], %[d], %[ws] sc1\n\ts_mov_b64 exec, %[m]\n\ts_mov_b32 m0, 0x8000\n\ts_nop 0\n\t"
                             "global_load_lds_dword %[xo], %[x]\n\tglobal_load_lds_dword %[xo], %[x] offset:512\n\ts_and_b64 exec, %[m], %[k1]\n\tglobal_load_lds_dword %[xo], %[x] offset:1024\n\ts_mov_b64 exec, -1"
                             :: [m] "s"(m8), [k2] "s"(0x3333333333333333ull), [k1] "s"(0x1111111111111111ull), [so] "v"(so + rank * 8), [d] "v"((v2u){v, tag}), [ws] "s"(p.ws),
                                [xo] "v"(xo + (~v & 2047u) * 1152u), [x] "s"(p.x) : "memory", "scc");
                asm volatile("s_mov_b64 exec, 1\n\tglobal_store_dwordx2 %0, %1, %2 sc1\n\ts_mov_b64 exec, -1" :: "v"(OFF_CI + (416 + xcd) * 8), "v"((v2u){tag, tag}), "s"(p.ws) : "memory");
            }
        }
        asm volatile("s_waitcnt vmcnt(0)" :: "v"(qb), "v"(qc), "v"(qd), "v"(qe) : "memory");
        if (xcd == 0) {
            const u32 cio = OFF_CI + (tid < 212 ? tid : 0) * 16;
            for (u32 spins = 0;; ++spins) {
                REREAD();
                const v4u q2 = __builtin_amdgcn_raw_buffer_load_b128(WSR(), cio, 0, 16);
                if (__all((q2.y == tag) & (q2.w == tag)) || spins > SPIN_CAP) break;
                __builtin_amdgcn_s_sleep(16);
            }
            BAR();
            if (tid == 0) __hip_atomic_store((GAS u32*)p.ws, tag, RLX_AGENT);
        }
        ROLE_END();
    }
    else
        {
            ROLE_BEGIN(3);
            FFN_PRE();
            const int pw = wave - 4;
            u32 tagv;
            asm volatile("v_mov_b32 %0, 0\n\tglobal_load_dword %0, %0, %1 sc1" : "=&v"(tagv) : "s"(p.ws) : "memory");
            FFN_FILL();
            v2u sel;
            const u32 so = OFF_SEL + xcd * 256 + (2 * pw + (lane & 1)) * 8, l16 = lane * 16;
#define POLL_S "global_load_dwordx2 v[10:11], %[so], %[ws] sc1\n\t"
            asm volatile("s_waitcnt vmcnt(18)" : "+v"(tagv) :: "memory");
            const u32 tag = __builtin_amdgcn_readfirstlane(tagv) + 1u;
            *(LAS v4u*)(smem + FL_Z + min(l16, 256u)) = (v4u){lane >= 16 ? tag : 0u, 0u, 0u, 0u};
            FFN_ADDR();
            const char* dsrc = (const char*)(p.wd + (size_t)(64 * w + 32 * (pw >> 1)) * D);
            const u32 ddst = (pw >> 1) * 32 * WDP + (pw & 1) * 1024;
            u32 dof[19];
#pragma unroll
            for (int i = 0; i < 19; ++i) {
                const u32 id = min(128u * i + 64u * (pw & 1) + lane, 2367u), r0 = id / 74u;
                dof[i] = r0 * 1152u + min(id - 74u * r0, 71u) * 16u;
                asm volatile("" : "+v"(dof[i]));
            }
#define LD_WD(i) asm volatile("s_mov_b32 m0, %2\n\ts_nop 0\n\tglobal_load_lds_dwordx4 %0, %1" :: "v"(dof[i]), "s"(dsrc), "s"(ddst + (i) * 2048) : "memory")
            asm volatile("s_waitcnt vmcnt(12) lgkmcnt(0)\n\ts_barrier\n\ts_waitcnt vmcnt(6)\n\ts_barrier\n\ts_waitcnt vmcnt(2)\n\t" POLL_S "s_waitcnt vmcnt(1)\n\ts_barrier" : "=&{v[10:11]}"(sel) : [so] "v"(so), [ws] "s"(p.ws) : "memory");
            u32 c_, early, mw = ddst;
            asm volatile("s_mov_b32 %[e], 0\n\ts_waitcnt vmcnt(0)\n\tv_cmp_ne_u32 vcc, %[tag], v11\n\ts_cmp_eq_u64 vcc, 0\n\ts_cbranch_scc1 2f\n\t"
                         "s_mov_b32 %[e], 1\n\ts_mov_b32 %[c], 0x80000\n\ts_mov_b32 m0, %[m]\n\ts_add_u32 %[m], %[m], 0x800\n\tglobal_load_lds_dwordx4 %[d0], %[src]\n\ts_mov_b32 m0, %[m]\n\ts_add_u32 %[m], %[m], 0x800\n\tglobal_load_lds_dwordx4 %[d1], %[src]\n\ts_mov_b32 m0, %[m]\n\ts_add_u32 %[m], %[m], 0x800\n\tglobal_load_lds_dwordx4 %[d2], %[src]\n\ts_mov_b32 m0, %[m]\n\ts_add_u32 %[m], %[m], 0x800\n\tglobal_load_lds_dwordx4 %[d3], %[src]\n\ts_mov_b32 m0, %[m]\n\ts_add_u32 %[m], %[m], 0x800\n\tglobal_load_lds_dwordx4 %[d4], %[src]\n\ts_mov_b32 m0, %[m]\n\ts_add_u32 %[m], %[m], 0x800\n\tglobal_load_lds_dwordx4 %[d5], %[src]\n\t"
                         "1:\n\t" POLL_S "s_waitcnt vmcnt(0)\n\tv_cmp_ne_u32 vcc, %[tag], v11\n\ts_cmp_eq_u64 vcc, 0\n\ts_cbranch_scc1 2f\n\ts_sub_u32 %[c], %[c], 1\n\ts_cbranch_scc0 1b\n2:"
                         : "+{v[10:11]}"(sel), [c] "=&s"(c_), [e] "=&s"(early), [m] "+s"(mw) : [so] "v"(so), [ws] "s"(p.ws), [tag] "s"(tag), [src] "s"(dsrc), [d0] "v"(dof[0]), [d1] "v"(dof[1]), [d2] "v"(dof[2]), [d3] "v"(dof[3]), [d4] "v"(dof[4]), [d5] "v"(dof[5]) : "memory", "scc", "vcc");
#pragma unroll
            for (int k = 0; k < 2; ++k) {
                const u32 t = ~(u32)__builtin_amdgcn_readlane((int)sel.x, k) & 2047u;
                const char* xr = (const char*)p.x + t * (D * 2);
                asm volatile("s_mov_b32 m0, %2\n\ts_nop 0\n\tglobal_load_lds_dwordx4 %0, %1\n\ts_mov_b64 exec, 0xff\n\tglobal_load_lds_dwordx4 %0, %1 offset:1024\n\ts_mov_b64 exec, -1"
                             :: "v"(l16), "s"(xr), "s"(FL_XS + (2 * pw + k) * XSP) : "memory");
            }
            asm volatile("s_barrier\n\ts_cmp_eq_u32 %[e], 0\n\ts_cbranch_scc0 1f\n\ts_mov_b32 m0, %[m]\n\ts_add_u32 %[m], %[m], 0x800\n\tglobal_load_lds_dwordx4 %[d0], %[src]\n\ts_mov_b32 m0, %[m]\n\ts_add_u32 %[m], %[m], 0x800\n\tglobal_load_lds_dwordx4 %[d1], %[src]\n\ts_mov_b32 m0, %[m]\n\ts_add_u32 %[m], %[m], 0x800\n\tglobal_load_lds_dwordx4 %[d2], %[src]\n\ts_mov_b32 m0, %[m]\n\ts_add_u32 %[m], %[m], 0x800\n\tglobal_load_lds_dwordx4 %[d3], %[src]\n\ts_mov_b32 m0, %[m]\n\ts_add_u32 %[m], %[m], 0x800\n\tglobal_load_lds_dwordx4 %[d4], %[src]\n\ts_mov_b32 m0, %[m]\n\ts_add_u32 %[m], %[m], 0x800\n\tglobal_load_lds_dwordx4 %[d5], %[src]\n\ts_mov_b32 m0, %[m]\n\ts_add_u32 %[m], %[m], 0x800\n\tglobal_load_lds_dwordx4 %[d6], %[src]\n\ts_mov_b32 m0, %[m]\n\ts_add_u32 %[m], %[m], 0x800\n\tglobal_load_lds_dwordx4 %[d7], %[src]\n\ts_mov_b32 m0, %[m]\n\ts_add_u32 %[m], %[m], 0x800\n\tglobal_load_lds_dwordx4 %[d8], %[src]\n\ts_mov_b32 m0, %[m]\n\ts_add_u32 %[m], %[m], 0x800\n\tglobal_load_lds_dwordx4 %[d9], %[src]\n\ts_mov_b32 m0, %[m]\n\ts_add_u32 %[m], %[m], 0x800\n\tglobal_load_lds_dwordx4 %[d10], %[src]\n\ts_mov_b32 m0, %[m]\n\ts_add_u32 %[m], %[m], 0x800\n\tglobal_load_lds_dwordx4 %[d11], %[src]\n\ts_waitcnt vmcnt(12)\n\ts_branch 2f\n1:\n\ts_mov_b32 m0, %[m]\n\ts_add_u32 %[m], %[m], 0x800\n\tglobal_load_lds_dwordx4 %[d6], %[src]\n\ts_mov_b32 m0, %[m]\n\ts_add_u32 %[m], %[m], 0x800\n\tglobal_load_lds_dwordx4 %[d7], %[src]\n\ts_mov_b32 m0, %[m]\n\ts_add_u32 %[m], %[m], 0x800\n\tglobal_load_lds_dwordx4 %[d8], %[src]\n\ts_mov_b32 m0, %[m]\n\ts_add_u32 %[m], %[m], 0x800\n\tglobal_load_lds_dwordx4 %[d9], %[src]\n\ts_mov_b32 m0, %[m]\n\ts_add_u32 %[m], %[m], 0x800\n\tglobal_load_lds_dwordx4 %[d10], %[src]\n\ts_mov_b32 m0, %[m]\n\ts_add_u32 %[m], %[m], 0x800\n\tglobal_load_lds_dwordx4 %[d11], %[src]\n\ts_waitcnt vmcnt(6)\n2:\n\ts_barrier"
                         : [m] "+s"(mw) : [e] "s"(early), [src] "s"(dsrc), [d0] "v"(dof[0]), [d1] "v"(dof[1]), [d2] "v"(dof[2]), [d3] "v"(dof[3]), [d4] "v"(dof[4]), [d5] "v"(dof[5]), [d6] "v"(dof[6]), [d7] "v"(dof[7]), [d8] "v"(dof[8]), [d9] "v"(dof[9]), [d10] "v"(dof[10]), [d11] "v"(dof[11]) : "memory", "scc");
#pragma unroll
            for (int i = 12; i < 18; ++i) LD_WD(i);
            if (!(pw & 1)) LD_WD(18);
            asm volatile("s_mov_b64 exec, 1\n\tglobal_store_dwordx2 %0, %1, %2 sc1\n\ts_mov_b64 exec, -1\n\ts_barrier" :: "v"(OFF_CI + (128 + 4 * w + pw) * 8), "v"((v2u){tag, tag}), "s"(p.ws) : "memory");
            FFN_HFRAG();
            asm volatile("s_waitcnt vmcnt(1)\n\ts_barrier" ::: "memory");
            s16x4 a[3][8];
            FFN_WDFRAG(0, 8 + pw); FFN_WDFRAG(1, 12 + pw); FFN_WDFRAG(2, 16 + (pw & 1));
            v4f ya[3];
            FFN_DOWN(0, 8 + pw); FFN_DOWN(1, 12 + pw);
            if (pw < 2) { FFN_DOWN(2, 16 + pw); FFN_PUB(2, 16 + pw); }
            FFN_PUB(0, 8 + pw); FFN_PUB(1, 12 + pw);
            ROLE_END();
        }
    } else
    {
        ROLE_BEGIN(5);
        ROLE_ARGS();
        _Pragma("unroll 1") for (int e = (slot - 28) + 4 * wave; touch(p, xcd, e, lane, (u32)(uintptr_t)(smem + wave * 256)); e += 32) {}
        asm volatile("s_waitcnt vmcnt(0)" ::: "memory");
        ROLE_END();
    }
    } else
        {
            ROLE_BEGIN(2);
            FFN_PRE();
            FFN_FILL();
            FFN_ADDR();
            LAS char* gp = smem + q * 2048 + ((lane >> 2) & 3) * 128 + ((2 * wave + ((lane >> 1) & 1)) ^ (((lane >> 2) & 2) | ((q & 1) << 2))) * 16 + (lane & 1) * 8;
            LAS char* xp = smem + FL_XS + (n & 7) * XSP + q * 16;
            const int hc = 16 * wave + n;
            LAS char* hw_ = smem + FL_H + (4 * (q & 1) + 2 * (q >> 1)) * 144 + ((hc & 35) | ((hc & 12) << 1) | ((hc & 16) >> 2)) * 2;
            asm volatile("" : "+v"(gp), "+v"(xp), "+v"(hw_));
            bf16x8 gf[18], uf[18];
#pragma unroll
            for (int sl = 0; sl < 3; ++sl) {
                if (sl == 0) asm volatile("s_waitcnt vmcnt(12)\n\ts_barrier" ::: "memory");
                if (sl == 1) asm volatile("s_waitcnt vmcnt(6)\n\ts_barrier" ::: "memory");
                if (sl == 2) asm volatile("s_waitcnt vmcnt(0) lgkmcnt(0)\n\ts_barrier" ::: "memory");
                LAS char* g_ = gp + sl * 49152;
                asm volatile("" : "+v"(g_));
#pragma unroll
                for (int i = 0; i < 6; ++i) {
                    gf[6 * sl + i] = __builtin_shufflevector(__builtin_amdgcn_ds_read_tr16_b64_v4i16((LAS s16x4*)(g_ + i * 8192)), __builtin_amdgcn_ds_read_tr16_b64_v4i16((LAS s16x4*)(g_ + i * 8192 + 512)), 0, 1, 2, 3, 4, 5, 6, 7);
                    uf[6 * sl + i] = __builtin_shufflevector(__builtin_amdgcn_ds_read_tr16_b64_v4i16((LAS s16x4*)(g_ + i * 8192 + 1024)), __builtin_amdgcn_ds_read_tr16_b64_v4i16((LAS s16x4*)(g_ + i * 8192 + 1536)), 0, 1, 2, 3, 4, 5, 6, 7);
                }
#pragma unroll
                for (int i = 0; i < 6; ++i) asm volatile("" : "+v"(gf[6 * sl + i]), "+v"(uf[6 * sl + i]));
            }
            u32 tag = *(LAS u32*)(smem + FL_TAG);
            asm volatile("s_barrier\n\ts_barrier" : "+v"(tag) :: "memory");
            bf16x8 xf[18];
            v4f ag = {0.f, 0.f, 0.f, 0.f}, au = ag;
#pragma unroll
            for (int s = 0; s < 4; ++s) xf[s] = *(LAS bf16x8*)(xp + s * 64);
            SBAR();
#pragma unroll
            for (int s = 0; s < 18; ++s) {
                ag = MF(xf[s], gf[s], ag);
                if (s + 4 < 18) xf[s + 4] = *(LAS bf16x8*)(xp + (s + 4) * 64);
                au = MF(xf[s], uf[s], au);
                SBAR();
            }
            {
                const u32 ab = cvtpk(q < 2 ? ag[0] : ag[2], q < 2 ? ag[1] : ag[3]), ub = cvtpk(q < 2 ? au[0] : au[2], q < 2 ? au[1] : au[3]);
                const u32 sl = cvtpk(bflo(ab) * sigmoidf_(bflo(ab)), bfhi(ab) * sigmoidf_(bfhi(ab)));
                const u32 hh = cvtpk(bflo(sl) * bflo(ub), bfhi(sl) * bfhi(ub));
                *(LAS u16*)(hw_) = (u16)hh; *(LAS u16*)(hw_ + 144) = (u16)(hh >> 16);
            }
            BAR();
            FFN_HFRAG();
            asm volatile("s_barrier" ::: "memory");
            s16x4 a[2][8];
            FFN_WDFRAG(0, 2 * wave); FFN_WDFRAG(1, 2 * wave + 1);
            v4f ya[2];
            FFN_DOWN(0, 2 * wave); FFN_DOWN(1, 2 * wave + 1);
            FFN_PUB(0, 2 * wave); FFN_PUB(1, 2 * wave + 1);
            ROLE_END();
        }
    } else {
    if (wave >= 4)
    {
        ROLE_BEGIN(1);
        const int hw = wave - 4, ht = tid - 256;
        const v4u z4 = {0, 0, 0, 0};
        const int sub = lane & 15;
        v4u xr[5];
        u32 tagv;
        const int rrow = 16 * (cw & 127) + 4 * hw;
        u32 dst = (u32)(uintptr_t)(smem + LDS_RW) + hw * 3072, l16 = lane * 16;
        u32 ro = (u32)(((rrow + (lane >> 4)) & ~((127 - cw) >> 31)) * D + sub * 8) * 2, ro4 = ro - (sub & 8) * 16;
        u32 uo0 = (u32)((256 * hw + 4 * lane) * D + chbase) * 2;
        asm volatile("" : "+s"(dst), "+v"(l16), "+v"(ro), "+v"(ro4), "+v"(uo0));
        ROLE_ARGS();
        {
            asm volatile("s_mov_b32 m0, %5\n\tv_mov_b32 %0, 0\n\tglobal_load_dword %0, %0, %2 sc1\n\tglobal_load_lds_dwordx4 %3, %4\n\ts_mov_b64 exec, 0xff\n\tglobal_load_lds_dwordx4 %3, %4 offset:1024\n\t"
                         "s_add_u32 m0, m0, 1280\n\ts_mov_b64 exec, -1\n\tglobal_load_lds_dwordx4 %3, %6\n\ts_mov_b64 exec, 0xff\n\tglobal_load_lds_dwordx4 %3, %6 offset:1024\n\ts_mov_b64 exec, -1"
                         : "=&v"(tagv) : "n"(0), "s"(p.ws), "v"(l16), "s"(p.wr), "s"(dst), "s"(p.gw) : "memory", "scc");
#pragma unroll
            for (int j = 0; j < 4; ++j) asm volatile("global_load_dwordx4 %0, %1, %2 offset:%3" : "=v"(xr[j]) : "v"(ro), "s"(p.x), "n"(j * 256));
            asm volatile("global_load_dwordx4 %0, %1, %2 offset:1024" : "=v"(xr[4]) : "v"(ro4), "s"(p.x));
        }
        __builtin_amdgcn_s_barrier();
        v2u uu[8];
        const u32 uo1 = uo0 + 1024 * D * 2;
#define LD_U(i) asm volatile("global_load_dwordx2 %0, %1, %2 offset:%3" : "=v"(uu[i]) : "v"((i) < 4 ? uo0 : uo1), "s"(p.x), "n"(((i) & 3) * D * 2))
#pragma unroll
        for (int i = 0; i < NB1; ++i) LD_U(i);
        *(LAS v4u*)(smem + LDS_TAP + ht * 16) = z4; *(LAS v4u*)(smem + LDS_TAP + 4096 + ht * 16) = z4;
        *(LAS v4u*)(smem + LDS_TAP + 8192 + ht * 16 + (ht >> 7) * (LDS_U - LDS_TAP - 8192 + ((ht >> 5) & 3) * 4608 + (ht & 31) * 16 - ht * 16)) = z4;
        u32 ctl = (u32)(uintptr_t)(smem + LDS_CTL), one = 1u, cty = ctl + 64 * hw + (lane & 3) * 16;
        const u32 late = (u32)((127 - cw) >> 31), pa_ = OFF_RG + hline(p.ws, cw & 127) + hw * 32, pk_ = pa_ ^ ((pa_ ^ (OFF_CI + ((cw - 128) * 4 + hw) * 16)) & late);
        u32 po = pk_ + (lane >> 1) + (sub != 0) * (OFF_G + hline(p.ws, ((lane - 1) & 3) * 64 + ((cw & 127) >> 1)) + ((4 * cw + hw) & 7) * 16 - pk_ - (lane >> 1));
        const u64 pmask = 1ull | ((u64)(~late & 1u) << 32) | (~late & ((1u << (1 + REPG)) - 1));
        u32 nrow = 0xffffu - (rrow + (lane >> 4));
        u32 go = OFF_G + hline(p.ws, (cw % REPG) * 64 + 8 * hw + (lane >> 3)) + (lane & 7) * 16, go2 = OFF_G + hline(p.ws, (cw % REPG) * 64 + 32 + 8 * hw + (lane >> 3)) + (lane & 7) * 16;
        LAS char* up = smem + LDS_U + (256 + 256 * hw + 4 * lane) * 2;
        LAS char* yp = smem + CH_Y + hw * 1152 + (lane + (lane >> 3)) * 16;
        LAS char* rw = smem + LDS_RW + hw * 3072 + sub * 16;
        LAS char* rw4 = rw - (sub & 8) * 16;
        asm volatile("" : "+v"(ctl), "+v"(one), "+v"(cty), "+v"(po), "+v"(go), "+v"(go2), "+v"(up), "+v"(yp), "+v"(rw), "+v"(rw4), "+v"(nrow));
        u32 wt[3];
#pragma unroll
        for (int k = WPF; k < 3; ++k) {
            wt[k] = wd_line(xcd, (36 * k + slot * 2 + (hw & 1)) * 48 + min(lane, 47));
            asm volatile("" : "+v"(wt[k]));
        }
        BAR();
        __builtin_amdgcn_s_setprio(3);
        u32 tag;
        {
            asm volatile("s_waitcnt vmcnt(%1)" : "+v"(tagv) : "n"(NB1 + 5) : "memory");
            tag = __builtin_amdgcn_readfirstlane(tagv) + 1u;
            v4u wa[5], wg_[5];
#pragma unroll
            for (int j = 0; j < 5; ++j) { wa[j] = *(LAS v4u*)((j < 4 ? rw : rw4) + j * 256); wg_[j] = *(LAS v4u*)((j < 4 ? rw : rw4) + 1280 + j * 256); }
#pragma unroll
            for (int j = 0; j < 5; ++j) asm volatile("" : "+v"(wa[j]), "+v"(wg_[j]));
            asm volatile("s_waitcnt vmcnt(%5)" : "+v"(xr[0]), "+v"(xr[1]), "+v"(xr[2]), "+v"(xr[3]), "+v"(xr[4]) : "n"(NB1) : "memory");
            if (sub >= 8) xr[4] = z4;
            float r = 0.f, s = 0.f;
#pragma unroll
            for (int j = 0; j < 5; ++j) {
                const v4u a = wa[j], g = wg_[j];
#pragma unroll
                for (int e = 0; e < 4; ++e) {
                    const u32 xe = xr[j][e], ae = a[e], ge = g[e];
                    r = __builtin_amdgcn_fdot2_f32_bf16(__builtin_bit_cast(bf16x2_t, xe), __builtin_bit_cast(bf16x2_t, ae), r, false);
                    s = __builtin_amdgcn_fdot2_f32_bf16(__builtin_bit_cast(bf16x2_t, xe), __builtin_bit_cast(bf16x2_t, ge), s, false);
                }
            }
            r = dpp_add<0x128, 0xf>(r); s = dpp_add<0x128, 0xf>(s); r = dpp_add<0x124, 0xf>(r); s = dpp_add<0x124, 0xf>(s);
            r = dpp_add<0x122, 0xf>(r); s = dpp_add<0x122, 0xf>(s); r = dpp_add<0x121, 0xf>(r); s = dpp_add<0x121, 0xf>(s);
            const u32 pay = make_payload(r, sigmoidf_(rbf(s))), kk = (pay & 0xffff0000u) | nrow;
            const u32 p0 = (u32)__builtin_amdgcn_readlane((int)pay, 0), p1 = (u32)__builtin_amdgcn_readlane((int)pay, 16), p2 = (u32)__builtin_amdgcn_readlane((int)pay, 32), p3 = (u32)__builtin_amdgcn_readlane((int)pay, 48);
            const auto sw = __builtin_amdgcn_permlane16_swap(kk, kk, false, false);
            const u32 g01 = (p0 & 0xffffu) | (p1 << 16), g23 = (p2 & 0xffffu) | (p3 << 16);
            const v4u pv = {sub ? g01 : sw[0], tag, sub ? g23 : sw[1], tag};
            asm volatile("s_mov_b64 exec, %3\n\tglobal_store_dwordx4 %0, %1, %2 sc1\n\ts_mov_b64 exec, -1\n\ts_nop 0" :: "v"(po), "v"(pv), "s"(p.ws), "s"(pmask) : "memory");
        }
        __builtin_amdgcn_s_setprio(0);
        *(LAS u32*)(smem + LDS_CTL + C_TAG) = tag;
        LDS_ADD1(ctl, one, C_PUB);
        LWAIT(ctl, C_PUB, 4);
#pragma unroll
        for (int i = NB1; i < 4; ++i) LD_U(i);
        LDS_ADD1(ctl, one, C_PUB);
        LWAIT(ctl, C_PUB, 8);
        v4u gq[2];
#define POLL_G "global_load_dwordx4 v[100:103], %[go], %[ws] sc1\n\tglobal_load_dwordx4 v[104:107], %[go2], %[ws] sc1\n\t"
#define LD_U4 "global_load_dwordx2 %[u4], %[uo], %[x]\n\tglobal_load_dwordx2 %[u5], %[uo], %[x] offset:1152\n\tglobal_load_dwordx2 %[u6], %[uo], %[x] offset:2304\n\tglobal_load_dwordx2 %[u7], %[uo], %[x] offset:3456\n\t"
        asm volatile("" : "=&{v[100:103]}"(gq[0]), "=&{v[104:107]}"(gq[1]), "=&v"(uu[4]), "=&v"(uu[5]), "=&v"(uu[6]), "=&v"(uu[7]));
        asm volatile("s_cmp_ge_u32 %[hw], 2\n\ts_cbranch_scc1 1f\n\t" LD_U4 POLL_G "s_waitcnt vmcnt(6)\n\ts_branch 2f\n1:\n\ts_waitcnt vmcnt(0)\n2:"
                     : "+{v[100:103]}"(gq[0]), "+{v[104:107]}"(gq[1]), [u4] "+v"(uu[4]), [u5] "+v"(uu[5]), [u6] "+v"(uu[6]), [u7] "+v"(uu[7]), "+v"(uu[0]), "+v"(uu[1]), "+v"(uu[2]), "+v"(uu[3])
                     : [go] "v"(go), [go2] "v"(go2), [ws] "s"(p.ws), [uo] "v"(uo1), [x] "s"(p.x), [hw] "s"(hw) : "memory", "scc");
        u32 ct2 = ctl + (hw & 2) * 2;
        auto stage = [&](int g) __attribute__((always_inline)) {
            const v2u* u4 = uu + 4 * g;
            LAS char* d = up + g * 2048;
            *(LAS v2u*)(d) = (v2u){__builtin_amdgcn_perm(u4[1].x, u4[0].x, 0x05040100u), __builtin_amdgcn_perm(u4[3].x, u4[2].x, 0x05040100u)};
            *(LAS v2u*)(d + 4608) = (v2u){__builtin_amdgcn_perm(u4[1].x, u4[0].x, 0x07060302u), __builtin_amdgcn_perm(u4[3].x, u4[2].x, 0x07060302u)};
            *(LAS v2u*)(d + 2 * 4608) = (v2u){__builtin_amdgcn_perm(u4[1].y, u4[0].y, 0x05040100u), __builtin_amdgcn_perm(u4[3].y, u4[2].y, 0x05040100u)};
            *(LAS v2u*)(d + 3 * 4608) = (v2u){__builtin_amdgcn_perm(u4[1].y, u4[0].y, 0x07060302u), __builtin_amdgcn_perm(u4[3].y, u4[2].y, 0x07060302u)};
            if (g) LDS_ADD1(ct2, one, C_ST2); else LDS_ADD1(ctl, one, C_STG);
        };
        stage(0);
        asm volatile("s_cmp_lt_u32 %[hw], 2\n\ts_cbranch_scc1 1f\n\t" POLL_G LD_U4 "\n1:"
                     : "+{v[100:103]}"(gq[0]), "+{v[104:107]}"(gq[1]), [u4] "+v"(uu[4]), [u5] "+v"(uu[5]), [u6] "+v"(uu[6]), [u7] "+v"(uu[7])
                     : [go] "v"(go), [go2] "v"(go2), [ws] "s"(p.ws), [uo] "v"(uo1), [x] "s"(p.x), [hw] "s"(hw) : "memory", "scc");
        if (hw < 2) {
            asm volatile("s_waitcnt vmcnt(2)" : "+v"(uu[4]), "+v"(uu[5]), "+v"(uu[6]), "+v"(uu[7]));
            stage(1);
        }
        {
            u32 c_;
            asm volatile("s_mov_b32 %[c], 0x80000\n\ts_waitcnt vmcnt(4)\n\ts_cmp_ge_u32 %[hw], 2\n\ts_cbranch_scc1 1f\n\ts_waitcnt vmcnt(0)\n1:\n\tv_cmpx_eq_u32 %[tag], v101\n\tv_cmpx_eq_u32 %[tag], v103\n\tv_cmpx_eq_u32 %[tag], v105\n\tv_cmpx_eq_u32 %[tag], v107\n\t"
                         "s_cmp_eq_u64 exec, -1\n\ts_mov_b64 exec, -1\n\ts_cbranch_scc1 2f\n\ts_sleep 4\n\t" POLL_G "s_waitcnt vmcnt(0)\n\ts_sub_u32 %[c], %[c], 1\n\ts_cbranch_scc0 1b\n2:\n\ts_nop 4"
                         : "+{v[100:103]}"(gq[0]), "+{v[104:107]}"(gq[1]), [c] "=&s"(c_) : [go] "v"(go), [go2] "v"(go2), [ws] "s"(p.ws), [tag] "s"(tag), [hw] "s"(hw) : "memory", "scc", "vcc");
        }
        *(LAS v2u*)(smem + LDS_G + hw * 3072 + lane * 8) = (v2u){gq[0].x, gq[0].z}; *(LAS v2u*)(smem + LDS_G + hw * 3072 + 512 + lane * 8) = (v2u){gq[1].x, gq[1].z};
        if (hw < 2) {
#pragma unroll
            for (int k = WPF; k < 3; ++k) asm volatile("s_mov_b32 m0, %2\n\ts_nop 0\n\tglobal_load_lds_dword %0, %1" :: "v"(wt[k]), "s"(p.wd), "n"(LDS_DUMMY) : "memory");
        }
#pragma unroll
        for (int g = 0; g < 2; ++g) {
            if (g == 1 && hw >= 2) {
                asm volatile("s_waitcnt vmcnt(4)" : "+v"(uu[4]), "+v"(uu[5]), "+v"(uu[6]), "+v"(uu[7]));
                stage(1);
            }
            const v2u* u4 = uu + 4 * g;
            const float gr[4] = {bflo(gq[g].x), bfhi(gq[g].x), bflo(gq[g].z), bfhi(gq[g].z)};
            float o[4][4];
#pragma unroll
            for (int r = 0; r < 4; ++r) {
                const float xv[4] = {bflo(u4[r].x), bfhi(u4[r].x), bflo(u4[r].y), bfhi(u4[r].y)};
#pragma unroll
                for (int c = 0; c < 4; ++c) asm("v_fma_f32 %0, -%1, %2, %2" : "=v"(o[r][c]) : "v"(gr[r]), "v"(xv[c]));
                asm volatile("" : "+v"(o[r][0]), "+v"(o[r][1]), "+v"(o[r][2]), "+v"(o[r][3]));
            }
            {
                u32 t_, c_;
                asm volatile("s_mov_b32 %1, 0x400000\n1:\n\tds_read_b32 %0, %2 offset:%3\n\ts_waitcnt lgkmcnt(0)\n\tv_cmp_eq_u32 vcc, 0, %0\n\ts_cmp_eq_u64 vcc, 0\n\ts_cbranch_scc1 2f\n\ts_sleep 1\n\ts_sub_u32 %1, %1, 1\n\ts_cbranch_scc0 1b\n2:"
                             : "=&v"(t_), "=&s"(c_) : "v"(cty), "n"(C_Y + 256 * g) : "memory", "scc", "vcc");
            }
            v4f y[4];
#pragma unroll
            for (int c = 0; c < 4; ++c) y[c] = *(LAS v4f*)(yp + c * KCH + g * 4608);
            SBAR();
#define ST_O(i, v) asm volatile("global_store_dwordx2 %0, %1, %2 offset:%3" :: "v"(g ? uo1 : uo0), "v"(v), "s"(p.out), "n"((i) * D * 2) : "memory")
#pragma unroll
            for (int r = 0; r < 4; ++r) {
#pragma unroll
                for (int c = 0; c < 4; ++c) { const float yv = y[c][r]; asm("v_fmac_f32 %0, %1, %2" : "+v"(o[r][c]) : "v"(gr[r]), "v"(yv)); }
                ST_O(r, ((v2u){cvtpk(o[r][0], o[r][1]), cvtpk(o[r][2], o[r][3])}));
            }
        }
        asm volatile("s_waitcnt vmcnt(0)" ::: "memory");
        LDS_ADD1(ctl, one, C_ACK);
        ROLE_END();
    }
    else
    {
        ROLE_BEGIN(0);
        const int ch = wave;
        u32 pi = ((chbase + ch) * NST + lane * 2) * 2, dl = (chbase + ch) * 2;
        asm volatile("" : "+v"(pi), "+v"(dl));
        ROLE_ARGS();
#define GLD(T, ptr, idx) __builtin_nontemporal_load((const GAS T*)((const GAS char*)(ptr) + (idx)))
        const u32 are = GLD(u32, p.Are, pi), aim = GLD(u32, p.Aim, pi), bre = GLD(u32, p.Bre, pi), bim = GLD(u32, p.Bim, pi), cre = GLD(u32, p.Cre, pi), cim = GLD(u32, p.Cim, pi);
        const u16 ldt = *(const GAS u16*)((const GAS char*)p.logdt + dl), dsk = *(const GAS u16*)((const GAS char*)p.dskip + dl);
        SBAR();
        __builtin_amdgcn_s_barrier();
        SBAR();
        LAS char* kb = smem + ch * KCH;
        const int r32 = lane & 31, h = lane >> 5, q = lane >> 4, n = lane & 15;
        const int ai = 4 * (r32 >> 3) + (r32 & 3), hh = (r32 >> 2) & 1;
        const u64 tsel = ai < 8 ? (hh ? KROW_1LO : KROW_0LO) : (hh ? KROW_1HI : KROW_0HI);
        const int o = (int)((tsel >> (8 * (ai & 7))) & 0xff);
        LAS char* ap = kb + o * STG_ROW + h * 16;
        LAS char* bp = kb + r32 * STG_ROW + h * 16;
        u32 sg[14];
#pragma unroll
        for (int k = 0; k < 14; ++k) { sg[k] = (u32)(uintptr_t)(kb + lane * 8 + k * 4 * STG_ROW); asm volatile("" : "+v"(sg[k])); }
        bf16x8 b5 = band(((0x1fu << 6) << n) >> (8 * q), 0x3f803f80u), b3 = band(((0x7u << 11) << n) >> (8 * q), 0x3f803f80u);
        bf16x8 b9 = band(((0x1ffu << 4) << n) >> (8 * q), 0x3f803f80u), b7 = band(((0x7fu << 5) << n) >> (8 * q), 0x3f803f80u);
        bf16x8 pat = band(((0x3u << 12) << n) >> (8 * q), 0xffffffffu);
        LAS char* k0 = kb + 32 + lane * 16;
        LAS char* kt = kb + (lane & 3) * KCOPY + (lane < 16 ? 32 + 1024 + (lane >> 2) * 16 : ((lane >> 2) & 1) * 16);
        LAS char* a0 = kb + ((lane >> 2) & 3) * KCOPY + (28 - 8 * q + 4 * (lane & 3)) * 2;
        LAS char* bb = smem + LDS_U + ch * 4608 + (16 * n + 8 * q - 16) * 2;
        LAS char* yb = kb + CH_Y + (4 * n + q + (n >> 1)) * 16;
        LAS char* tp = smem + LDS_TAP + ch * 2304 + ((n & 1) ? 13 + n - 4 * q : 96 + 14 + n - 4 * q) * 4;
        LAS char* ta = smem + LDS_TAP + ch * 2304 + (h ? 0 : 768) + 64 + 2 * r32;
        LAS char* tb = smem + LDS_TAP + ch * 2304 + 1600 + 64 * h + 2 * r32;
        u32 ctl = (u32)(uintptr_t)(smem + LDS_CTL), ctc = ctl + C_Y + ch * 16 + (lane & 15), one = 1u, so = OFF_SEL + (8 + xcd) * 256 + (lane & 7) * 8;
        u32 m01 = lane < 2 ? ~0u : 0u, m2 = lane == 2 ? ~0u : 0u, m3 = lane == 3 ? ~0u : 0u, m0_ = lane == 0 ? ~0u : 0u;
        asm volatile("" : "+v"(b5), "+v"(b3), "+v"(b9), "+v"(b7), "+v"(pat), "+v"(ap), "+v"(bp), "+v"(m01), "+v"(m2), "+v"(m3), "+v"(m0_));
        asm volatile("" : "+v"(k0), "+v"(kt), "+v"(a0), "+v"(bb), "+v"(yb), "+v"(tp), "+v"(ta), "+v"(tb), "+v"(ctl), "+v"(ctc), "+v"(one), "+v"(so));
        const char* hint = p.ws + (OFF_PART + cw * PP + hline(p.ws, cw >> 1));
        asm volatile("" : "+s"(hint));
        __builtin_amdgcn_s_barrier();
        SBAR();
        const float dt = rbf(expf(bf2f(ldt)));
        v16f acc;
        u32 stg;
        float kscale;
        {
            const v2f a2 = (v2f){bflo(are), bfhi(are)} * dt * 1.44269504088896341f;
            const v2f brev = (v2f){bflo(aim), bfhi(aim)} * dt * 0.15915494309189535f;
            const cf2 Bc = {{dt * bflo(bre), dt * bflo(bim)}, {dt * bfhi(bre), dt * bfhi(bim)}};
            const cf2 Cc = {{bflo(cre), bflo(cim)}, {bfhi(cre), bfhi(cim)}};
            cf2 W = cmul2(Cc, Bc);
            u32 mx = max(max(__float_as_uint(W.a.x) & 0x7f800000u, __float_as_uint(W.a.y) & 0x7f800000u), max(__float_as_uint(W.b.x) & 0x7f800000u, __float_as_uint(W.b.y) & 0x7f800000u));
            mx = dpp_umax<0x111, 0xf>(mx); mx = dpp_umax<0x112, 0xf>(mx); mx = dpp_umax<0x114, 0xf>(mx); mx = dpp_umax<0x118, 0xf>(mx);
            mx = dpp_umax<0x142, 0xa>(mx); mx = dpp_umax<0x143, 0xc>(mx);
            mx = min(max((u32)__builtin_amdgcn_readlane((int)mx, 63), 0x00800000u), 0x7e800000u);
            kscale = __uint_as_float(mx);
            const float inv = __uint_as_float(0x7f000000u - mx);
            W.a *= inv; W.b *= inv;
            const cf2 E1 = cexp2(a2, brev, 1.f), E8 = cexp2(a2, brev, 8.f), E256 = cexp2(a2, brev, 256.f);
            asm volatile("s_nop 0\n\t" KGEN_ASM :: "{v[48:49]}"(W.a), "{v[50:51]}"(W.b), "{v[52:53]}"(E1.a), "{v[54:55]}"(E1.b), "{v[56:57]}"(E8.a), "{v[58:59]}"(E8.b), "{v[60:61]}"(E256.a), "{v[62:63]}"(E256.b),
                         [b0] "v"(sg[0]), [b1] "v"(sg[1]), [b2] "v"(sg[2]), [b3] "v"(sg[3]), [b4] "v"(sg[4]), [b5] "v"(sg[5]), [b6] "v"(sg[6]), [b7] "v"(sg[7]), [b8] "v"(sg[8]), [b9] "v"(sg[9]),
                         [b10] "v"(sg[10]), [b11] "v"(sg[11]), [b12] "v"(sg[12]), [b13] "v"(sg[13]) : "memory", KGEN_CLOBBER);
            asm volatile(KMFMA_ASM : "=&{v[48:63]}"(acc), [stg] "=&v"(stg) : [ap] "v"(ap), [bp] "v"(bp), [ctl] "v"(ctl) : "memory", KMFMA_CLOBBER);
            acc *= kscale;
        }
        acc[0] += __uint_as_float(((u32)dsk << 16) & m0_);
        u32 t78 = cvtpk(acc[7], acc[8]), t9a = cvtpk(acc[9], acc[10]);
        {
            const auto s0 = __builtin_amdgcn_permlane32_swap(cvtpk(acc[0], acc[1]), cvtpk(acc[4], acc[4]), false, false);
            const auto s1 = __builtin_amdgcn_permlane32_swap(cvtpk(acc[2], acc[3]), cvtpk(acc[5], acc[6]), false, false);
            const v4u ww = {s0[0], s1[0], s0[1], s1[1]};
            const u32 pz = (u32)__builtin_amdgcn_update_dpp(0, (int)ww.z, 0x138, 0xf, 0xf, true), pw = (u32)__builtin_amdgcn_update_dpp(0, (int)ww.w, 0x138, 0xf, 0xf, true);
            const v4u c1 = {__builtin_amdgcn_alignbit(ww.x, pw, 16), __builtin_amdgcn_alignbit(ww.y, ww.x, 16), __builtin_amdgcn_alignbit(ww.z, ww.y, 16), __builtin_amdgcn_alignbit(ww.w, ww.z, 16)};
            *(LAS v4u*)(k0) = ww;
            *(LAS v4u*)(k0 + KCOPY) = c1;
            *(LAS v4u*)(k0 + 2 * KCOPY) = (v4u){pw, ww.x, ww.y, ww.z};
            *(LAS v4u*)(k0 + 3 * KCOPY) = (v4u){__builtin_amdgcn_alignbit(pw, pz, 16), c1.x, c1.y, c1.z};
            const u32 w0 = (u32)__builtin_amdgcn_readlane((int)ww.z, 63), w1 = (u32)__builtin_amdgcn_readlane((int)ww.w, 63);
            const u32 w3 = w1 >> 16, w2 = w3 * 0x10001u, w10 = (w0 >> 16) | (w1 << 16);
            *(LAS v4u*)kt = (v4u){(w2 & m01) | (w1 & m2) | (w10 & m3), (w3 & m0_) | (w2 & ((m01 ^ m0_) | m2 | m3)), (w3 & m2) | (w2 & m3), 0u};
        }
        asm volatile("" ::: "memory");
        pat = __builtin_bit_cast(bf16x8, __builtin_bit_cast(v4u, pat) & (((u32)__builtin_amdgcn_readlane((int)t78, 63) >> 16) * 0x10001u));
#define POLL_SEL "global_load_dwordx2 v[240:241], %[so], %[ws] sc1\n\t"
        v2u sq;
        const v4f zero4 = {0.f, 0.f, 0.f, 0.f};
        {
            u32 s_, c_;
            asm volatile(CONV_ASM : "=&{v[240:241]}"(sq), [s] "=&s"(s_), [c] "=&s"(c_)
                         : "{v[4:7]}"(b5), "{v[8:11]}"(b3), "{v[12:15]}"(b9), "{v[16:19]}"(b7), "{v[20:23]}"(pat), [a0] "v"(a0), [bb] "v"(bb), [yb] "v"(yb), [tp] "v"(tp), [ta] "v"(ta), [tb] "v"(tb),
                           [ctl] "v"(ctl), [ctc] "v"(ctc), [one] "v"(one), [so] "v"(so), [t78] "v"(t78), [t9a] "v"(t9a), [stg] "v"(stg), [ws] "s"(p.ws) : "memory", "scc", CONV_CLOBBER);
        }
        asm volatile("s_cmp_eq_u32 %0, 0\n\ts_cbranch_scc1 1f\n\ts_endpgm\n1:" :: "s"(wave) : "memory", "scc");
        {
            u32 h_;
            asm volatile("s_load_dword %0, %1, 0x4 glc" : "=&s"(h_) : "s"(hint) : "memory");
            const u32 tag = __builtin_amdgcn_readfirstlane(*(LAS u32*)(smem + LDS_CTL + C_TAG));
            u32 c_;
            u32 pb[9];
#pragma unroll
            for (int k = 0; k < 9; ++k) { pb[k] = OFF_PART + cw * PP + hline(p.ws, 8 * k + (lane >> 3)) + (lane & 7) * 16; asm volatile("" : "+v"(pb[k])); }
            {
                asm volatile("s_mov_b32 %[c], 64\n1:\n\ts_waitcnt lgkmcnt(0)\n\ts_cmp_eq_u32 %[h], %[tag]\n\ts_cbranch_scc1 2f\n\ts_load_dword %[h], %[a], 0x4 glc\n\ts_sub_u32 %[c], %[c], 1\n\ts_cbranch_scc0 1b\n\ts_waitcnt lgkmcnt(0)\n2:"
                             : [h] "+s"(h_), [c] "=&s"(c_) : [a] "s"(hint), [tag] "s"(tag) : "memory", "scc");
            }
            typedef __attribute__((ext_vector_type(32))) u32 v32u;
            v32u ra, rb; v4u rc, rd;
            asm volatile("global_load_dwordx4 v[64:67], %[pb0], %[ws] sc1\n\tglobal_load_dwordx4 v[68:71], %[pb1], %[ws] sc1\n\tglobal_load_dwordx4 v[72:75], %[pb2], %[ws] sc1\n\tglobal_load_dwordx4 v[76:79], %[pb3], %[ws] sc1\n\tglobal_load_dwordx4 v[80:83], %[pb4], %[ws] sc1\n\tglobal_load_dwordx4 v[84:87], %[pb5], %[ws] sc1\n\tglobal_load_dwordx4 v[88:91], %[pb6], %[ws] sc1\n\tglobal_load_dwordx4 v[92:95], %[pb7], %[ws] sc1\n\tglobal_load_dwordx4 v[96:99], %[pb8], %[ws] sc1\n\t" : "=&{v[64:95]}"(ra), "=&{v[96:99]}"(rc) : [pb0] "v"(pb[0]), [pb1] "v"(pb[1]), [pb2] "v"(pb[2]), [pb3] "v"(pb[3]), [pb4] "v"(pb[4]), [pb5] "v"(pb[5]), [pb6] "v"(pb[6]), [pb7] "v"(pb[7]), [pb8] "v"(pb[8]), [ws] "s"(p.ws) : "memory");
            asm volatile("s_mov_b32 %[c], 0x80000\n\ts_waitcnt vmcnt(9)\n1:\n\tv_cmpx_eq_u32 %[tag], v241\n\t"
                         "s_cmp_eq_u64 exec, -1\n\ts_mov_b64 exec, -1\n\ts_cbranch_scc1 2f\n\t" POLL_SEL "s_waitcnt vmcnt(0)\n\ts_sub_u32 %[c], %[c], 1\n\ts_cbranch_scc0 1b\n2:\n\ts_nop 4"
                         : "+{v[240:241]}"(sq), [c] "=&s"(c_) : [so] "v"(so), [ws] "s"(p.ws), [tag] "s"(tag) : "memory", "scc", "vcc");
            const int t = (int)(0xffffu - (sq.x & 0xffffu)) & 2047;
            const float rs = rbf(sigmoidf_(key_to_r(sq.x)));
            LWAIT(ctl, C_ACK, 4);
            const float g = bf2f(*(LAS u16*)(smem + LDS_G + ((t >> 8) & 3) * 3072 + (t >> 10) * 512 + (t & 255) * 2));
            const int uq = (t & 255) >> 2;
            float xv[4], av[4];
#pragma unroll
            for (int c = 0; c < 4; ++c) {
                xv[c] = bf2f(*(LAS u16*)(smem + LDS_U + c * 4608 + (256 + t) * 2));
                av[c] = g * *(LAS float*)(smem + c * KCH + CH_Y + (t >> 8) * 1152 + (uq + (uq >> 3)) * 16 + (t & 3) * 4);
            }
            const u32 e1 = n < 4 ? ((n & 1) ? 0x3f800000u : 0x00003f80u) : 0u;
            bf16x8 sel4 = __builtin_bit_cast(bf16x8, (n & 2) ? (v4u){0u, 0u, e1, e1} : (v4u){e1, e1, 0u, 0u});
            u32 oo = (u32)(t * D + chbase) * 2;
            float omg = 1.f - g;
            asm volatile("" : "+v"(sel4), "+v"(oo), "+v"(omg), "+v"(xv[0]), "+v"(xv[1]), "+v"(xv[2]), "+v"(xv[3]), "+v"(av[0]), "+v"(av[1]), "+v"(av[2]), "+v"(av[3]));
            v4f s0, s1;
            asm volatile("s_mov_b32 %[c], 0x40000\n\tglobal_load_dwordx4 v[100:103], %[pb0], %[ws] sc1\n\tglobal_load_dwordx4 v[104:107], %[pb1], %[ws] sc1\n\tglobal_load_dwordx4 v[108:111], %[pb2], %[ws] sc1\n\tglobal_load_dwordx4 v[112:115], %[pb3], %[ws] sc1\n\tglobal_load_dwordx4 v[116:119], %[pb4], %[ws] sc1\n\tglobal_load_dwordx4 v[120:123], %[pb5], %[ws] sc1\n\tglobal_load_dwordx4 v[124:127], %[pb6], %[ws] sc1\n\tglobal_load_dwordx4 v[128:131], %[pb7], %[ws] sc1\n\tglobal_load_dwordx4 v[132:135], %[pb8], %[ws] sc1\n\t"
                         "1:\n\ts_waitcnt vmcnt(9)\n\tv_cmpx_eq_u32 %[tag], v65\n\tv_cmpx_eq_u32 %[tag], v67\n\tv_cmpx_eq_u32 %[tag], v69\n\tv_cmpx_eq_u32 %[tag], v71\n\tv_cmpx_eq_u32 %[tag], v73\n\tv_cmpx_eq_u32 %[tag], v75\n\tv_cmpx_eq_u32 %[tag], v77\n\tv_cmpx_eq_u32 %[tag], v79\n\tv_cmpx_eq_u32 %[tag], v81\n\tv_cmpx_eq_u32 %[tag], v83\n\tv_cmpx_eq_u32 %[tag], v85\n\tv_cmpx_eq_u32 %[tag], v87\n\tv_cmpx_eq_u32 %[tag], v89\n\tv_cmpx_eq_u32 %[tag], v91\n\tv_cmpx_eq_u32 %[tag], v93\n\tv_cmpx_eq_u32 %[tag], v95\n\tv_cmpx_eq_u32 %[tag], v97\n\tv_cmpx_eq_u32 %[tag], v99\n\ts_cmp_eq_u64 exec, -1\n\ts_mov_b64 exec, -1\n\ts_cbranch_scc1 3f\n\tglobal_load_dwordx4 v[64:67], %[pb0], %[ws] sc1\n\tglobal_load_dwordx4 v[68:71], %[pb1], %[ws] sc1\n\tglobal_load_dwordx4 v[72:75], %[pb2], %[ws] sc1\n\tglobal_load_dwordx4 v[76:79], %[pb3], %[ws] sc1\n\tglobal_load_dwordx4 v[80:83], %[pb4], %[ws] sc1\n\tglobal_load_dwordx4 v[84:87], %[pb5], %[ws] sc1\n\tglobal_load_dwordx4 v[88:91], %[pb6], %[ws] sc1\n\tglobal_load_dwordx4 v[92:95], %[pb7], %[ws] sc1\n\tglobal_load_dwordx4 v[96:99], %[pb8], %[ws] sc1\n\t"
                         "s_waitcnt vmcnt(9)\n\tv_cmpx_eq_u32 %[tag], v101\n\tv_cmpx_eq_u32 %[tag], v103\n\tv_cmpx_eq_u32 %[tag], v105\n\tv_cmpx_eq_u32 %[tag], v107\n\tv_cmpx_eq_u32 %[tag], v109\n\tv_cmpx_eq_u32 %[tag], v111\n\tv_cmpx_eq_u32 %[tag], v113\n\tv_cmpx_eq_u32 %[tag], v115\n\tv_cmpx_eq_u32 %[tag], v117\n\tv_cmpx_eq_u32 %[tag], v119\n\tv_cmpx_eq_u32 %[tag], v121\n\tv_cmpx_eq_u32 %[tag], v123\n\tv_cmpx_eq_u32 %[tag], v125\n\tv_cmpx_eq_u32 %[tag], v127\n\tv_cmpx_eq_u32 %[tag], v129\n\tv_cmpx_eq_u32 %[tag], v131\n\tv_cmpx_eq_u32 %[tag], v133\n\tv_cmpx_eq_u32 %[tag], v135\n\ts_cmp_eq_u64 exec, -1\n\ts_mov_b64 exec, -1\n\ts_cbranch_scc1 2f\n\tglobal_load_dwordx4 v[100:103], %[pb0], %[ws] sc1\n\tglobal_load_dwordx4 v[104:107], %[pb1], %[ws] sc1\n\tglobal_load_dwordx4 v[108:111], %[pb2], %[ws] sc1\n\tglobal_load_dwordx4 v[112:115], %[pb3], %[ws] sc1\n\tglobal_load_dwordx4 v[116:119], %[pb4], %[ws] sc1\n\tglobal_load_dwordx4 v[120:123], %[pb5], %[ws] sc1\n\tglobal_load_dwordx4 v[124:127], %[pb6], %[ws] sc1\n\tglobal_load_dwordx4 v[128:131], %[pb7], %[ws] sc1\n\tglobal_load_dwordx4 v[132:135], %[pb8], %[ws] sc1\n\t"
                         "s_sub_u32 %[c], %[c], 1\n\ts_cbranch_scc0 1b\n\ts_waitcnt vmcnt(0)\n"
                         "2:\n\tv_mov_b32 v101, v104\n\tv_mov_b32 v103, v106\n\tv_mov_b32 v109, v112\n\tv_mov_b32 v111, v114\n\tv_mov_b32 v117, v120\n\tv_mov_b32 v119, v122\n\tv_mov_b32 v125, v128\n\tv_mov_b32 v127, v130\n\tv_mov_b32 v133, 0\n\tv_mov_b32 v135, 0\n\tv_mfma_f32_16x16x32_bf16 v[136:139], %[sel], v[100:103], 0\n\tv_mfma_f32_16x16x32_bf16 v[140:143], %[sel], v[108:111], 0\n\tv_mfma_f32_16x16x32_bf16 v[136:139], %[sel], v[116:119], v[136:139]\n\tv_mfma_f32_16x16x32_bf16 v[140:143], %[sel], v[124:127], v[140:143]\n\tv_mfma_f32_16x16x32_bf16 v[136:139], %[sel], v[132:135], v[136:139]\n\ts_branch 4f\n"
                         "3:\n\tv_mov_b32 v65, v68\n\tv_mov_b32 v67, v70\n\tv_mov_b32 v73, v76\n\tv_mov_b32 v75, v78\n\tv_mov_b32 v81, v84\n\tv_mov_b32 v83, v86\n\tv_mov_b32 v89, v92\n\tv_mov_b32 v91, v94\n\tv_mov_b32 v97, 0\n\tv_mov_b32 v99, 0\n\tv_mfma_f32_16x16x32_bf16 v[136:139], %[sel], v[64:67], 0\n\tv_mfma_f32_16x16x32_bf16 v[140:143], %[sel], v[72:75], 0\n\tv_mfma_f32_16x16x32_bf16 v[136:139], %[sel], v[80:83], v[136:139]\n\tv_mfma_f32_16x16x32_bf16 v[140:143], %[sel], v[88:91], v[140:143]\n\tv_mfma_f32_16x16x32_bf16 v[136:139], %[sel], v[96:99], v[136:139]\n\t\n4:\n\ts_nop 7"
                         : "+{v[64:95]}"(ra), "+{v[96:99]}"(rc), "=&{v[100:131]}"(rb), "=&{v[132:135]}"(rd), "=&{v[136:139]}"(s0), "=&{v[140:143]}"(s1), [c] "=&s"(c_)
                         : [pb0] "v"(pb[0]), [pb1] "v"(pb[1]), [pb2] "v"(pb[2]), [pb3] "v"(pb[3]), [pb4] "v"(pb[4]), [pb5] "v"(pb[5]), [pb6] "v"(pb[6]), [pb7] "v"(pb[7]), [pb8] "v"(pb[8]), [ws] "s"(p.ws), [tag] "s"(tag), [sel] "v"(sel4) : "memory", "scc", "vcc");
            float o[4];
#pragma unroll
            for (int c = 0; c < 4; ++c) o[c] = fmaf(omg, xv[c] + rbf(rbf(dpp_add<0x128, 0xf>(s0[c] + s1[c])) * rs), av[c]);
            asm volatile("s_mov_b64 exec, 0xff\n\tglobal_store_dwordx2 %0, %1, %2\n\ts_mov_b64 exec, -1" :: "v"(oo), "v"((v2u){cvtpk(o[0], o[1]), cvtpk(o[2], o[3])}), "s"(p.out) : "memory");
            asm volatile("s_waitcnt vmcnt(0)" :: "v"(ra), "v"(rb), "v"(rc), "v"(rd) : "memory");
        }
        ROLE_END();
    }
    }
}

extern "C" void kernel_launch(void* const* d_in, const int* in_sizes, int n_in,
                              void* d_out, int out_size, void* d_ws, size_t ws_size,
                              hipStream_t stream) {
    Params p;
    p.x = (const u16*)d_in[0]; p.Are = (const u16*)d_in[1]; p.Aim = (const u16*)d_in[2]; p.Bre = (const u16*)d_in[3];
    p.Bim = (const u16*)d_in[4]; p.Cre = (const u16*)d_in[5]; p.Cim = (const u16*)d_in[6]; p.logdt = (const u16*)d_in[7];
    p.dskip = (const u16*)d_in[8]; p.wr = (const u16*)d_in[10]; p.wg = (const u16*)d_in[11]; p.wu = (const u16*)d_in[12];
    p.wd = (const u16*)d_in[13]; p.gw = (const u16*)d_in[14]; p.out = (u16*)d_out; p.ws = (char*)d_ws;
    (void)hipFuncSetAttribute((const void*)fused_kernel, hipFuncAttributeMaxDynamicSharedMemorySize, LDS_ALLOC);
    fused_kernel<<<256, 512, LDS_ALLOC, stream>>>(p);
}
```

```cpp
#include <hip/hip_runtime.h>
#include <stdint.h>

typedef unsigned short u16;
typedef unsigned int u32;
typedef unsigned long long u64;
typedef __attribute__((ext_vector_type(4))) u32 v4u;
typedef __attribute__((ext_vector_type(2))) u32 v2u;
typedef __attribute__((ext_vector_type(2))) float v2f;
typedef __attribute__((ext_vector_type(4))) float v4f;
typedef __attribute__((ext_vector_type(16))) float v16f;
typedef __attribute__((ext_vector_type(8))) short bf16x8;
typedef __attribute__((ext_vector_type(4))) short s16x4;
typedef __attribute__((ext_vector_type(2))) __bf16 bf16x2_t;
#define LAS __attribute__((address_space(3)))
#define GAS __attribute__((address_space(1)))
#define RLX_AGENT __ATOMIC_RELAXED, __HIP_MEMORY_SCOPE_AGENT
#define SBAR() __builtin_amdgcn_sched_barrier(0)
#define BAR() asm volatile("s_waitcnt lgkmcnt(0)\n\ts_barrier" ::: "memory")
#define WSR() __builtin_amdgcn_make_buffer_rsrc(p.ws, 0, 0x7fffffff, 0x00020000)
#define REREAD() asm volatile("" ::: "memory")

#define WNT " nt"
constexpr int CODE_KB = 24;
constexpr int NB1 = 2;
constexpr int REPG = 2;
constexpr int PFG0 = 528;
constexpr int WPF = 2;
constexpr int SELWAIT = 50;

constexpr int L = 2048, D = 576, NST = 128, F = 4608;
constexpr int NFFN = 72;
constexpr u32 OFF_RG = 4096;
constexpr u32 OFF_G = OFF_RG + 20480;
constexpr u32 OFF_SEL = OFF_G + REPG * 10240;
constexpr u32 OFF_CI = OFF_SEL + 16 * 256;
constexpr u32 OFF_PART = OFF_CI + 4096;
constexpr u32 PP = 11 * 1024;
constexpr u32 SPIN_CAP = 1u << 19;


struct Params {
    const u16 *x, *Are, *Aim, *Bre, *Bim, *Cre, *Cim, *logdt, *dskip, *wr, *wg, *wu, *wd, *gw;
    u16* out;
    char* ws;
};

__device__ __forceinline__ float bflo(u32 w) { return __uint_as_float(w << 16); }
__device__ __forceinline__ float bfhi(u32 w) { return __uint_as_float(w & 0xffff0000u); }
__device__ __forceinline__ float bf2f(u16 h) { return __uint_as_float((u32)h << 16); }
__device__ __forceinline__ u32 cvtpk(float lo, float hi) { v2f v = {lo, hi}; bf16x2_t r = __builtin_convertvector(v, bf16x2_t); return __builtin_bit_cast(u32, r); }
__device__ __forceinline__ float rbf(float f) { return bflo(cvtpk(f, f)); }
__device__ __forceinline__ float sigmoidf_(float s) { return __builtin_amdgcn_rcpf(1.f + __expf(-s)); }

template <int CTRL, int RM> __device__ __forceinline__ float dpp_add(float v) {
    return v + __builtin_bit_cast(float, __builtin_amdgcn_update_dpp(0, __builtin_bit_cast(int, v), CTRL, RM, 0xf, true));
}
template <int CTRL, int RM> __device__ __forceinline__ u32 dpp_umax(u32 v) {
    u32 o = (u32)__builtin_amdgcn_update_dpp(0, (int)v, CTRL, RM, 0xf, true);
    return v > o ? v : o;
}

typedef v2f cf;
__device__ __forceinline__ cf cmul(cf a, cf b) { cf t = a.yy * (cf){-b.y, b.x}; return __builtin_elementwise_fma(a.xx, b, t); }
__device__ __forceinline__ cf cexp_(float a2, float brev, float rho) {
    float m = __builtin_amdgcn_exp2f(a2 * rho);
    float ph = __builtin_amdgcn_fractf(brev * rho);
    cf c; c.x = m * __builtin_amdgcn_cosf(ph); c.y = m * __builtin_amdgcn_sinf(ph); return c;
}

struct cf2 { cf a, b; };
__device__ __forceinline__ cf2 cmul2(cf2 x, cf2 y) { return {cmul(x.a, y.a), cmul(x.b, y.b)}; }
__device__ __forceinline__ cf2 cexp2(v2f a2, v2f brev, float rho) { return {cexp_(a2.x, brev.x, rho), cexp_(a2.y, brev.y, rho)}; }

__device__ __forceinline__ u32 make_payload(float r, float g) {
    const u32 w = cvtpk(g, r);
    return w ^ (((u32)((int)w >> 31) & 0x7fff0000u) | 0x80000000u);
}
__device__ __forceinline__ float key_to_r(u32 k) {
    u32 hi = k & 0xffff0000u;
    return __uint_as_float((hi & 0x80000000u) ? (hi ^ 0x80000000u) : (~hi & 0xffff0000u));
}


constexpr int STG_ROW = 528;
constexpr int NA = 21;
constexpr int KCH = (NA + 32) * STG_ROW;
constexpr int KCOPY = 1344;
constexpr int CH_Y = 4 * KCOPY;
constexpr int LDS_U = 4 * KCH;
constexpr int LDS_TAP = LDS_U + 4 * 4608;
constexpr int LDS_CTL = LDS_TAP + 4 * 2304;
constexpr int C_TAG = 0, C_STG = 4, C_PUB = 8, C_ACK = 12, C_GOK = 16, C_ST2 = 20, C_Y = 64;
constexpr int LDS_RW = LDS_CTL + 1024;
constexpr int LDS_G = LDS_RW;
constexpr int LDS_DUMMY = LDS_RW + 4 * 3072;
constexpr int LDS_TOTAL = LDS_DUMMY + 1024;
static_assert(CH_Y + 8 * 1152 <= KCH && LDS_TOTAL <= 160 * 1024, "lds");
constexpr int WDP = 1184;
constexpr int XSP = 1168;
constexpr int FL_XS = 144 * 1024;
constexpr int FL_H = FL_XS + 8 * XSP;
constexpr int FL_Z = FL_H + 8 * 144;
constexpr int FL_TAG = FL_Z + 256;
constexpr int LDS_ALLOC = FL_TAG + 64 > LDS_TOTAL ? FL_TAG + 64 : LDS_TOTAL;
static_assert(LDS_ALLOC <= 160 * 1024, "lds");

__device__ __forceinline__ u32 top8_d(LAS char* t8, int lane, int n) {
    LAS u32* cand = (LAS u32*)(t8 + 256);
    u32 res = 0;
    _Pragma("unroll 1") for (int i0 = 0; i0 < n; i0 += 64) {
        const int i = i0 + lane;
        const u32 k = i < n ? cand[i] : 0u;
        int rk = 0;
        _Pragma("unroll 1") for (int j0 = 0; j0 < n; j0 += 16) {
            v4u c[4];
#pragma unroll
            for (int e = 0; e < 4; ++e) c[e] = *(LAS v4u*)(cand + j0 + 4 * e);
#pragma unroll
            for (int e = 0; e < 16; ++e) rk += (c[e >> 2][e & 3] > k) & (j0 + e < n);
        }
        const bool hit = i < n && rk < 8;
        const u32 got = (u32)__builtin_amdgcn_ds_permute((hit ? rk : 63) * 4, (int)(hit ? k : 0u));
        if (lane < 8) res |= got;
    }
    return res;
}


#define LWAIT_BODY "1:\n\tds_read_b32 %0, %3 offset:%4\n\ts_waitcnt lgkmcnt(0)\n\tv_readfirstlane_b32 %1, %0\n\ts_cmp_ge_u32 %1, %5\n\ts_cbranch_scc1 2f\n\ts_sleep 1\n\ts_sub_u32 %2, %2, 1\n\ts_cbranch_scc0 1b\n2:"
#define LWAIT(addr, off, want) do { u32 t_, s_, c_; asm volatile("s_mov_b32 %2, 0x400000\n" LWAIT_BODY : "=&v"(t_), "=&s"(s_), "=&s"(c_) : "v"(addr), "n"(off), "n"(want) : "memory", "scc"); } while (0)
#define LDS_ADD1(addr, one, off) asm volatile("s_mov_b64 exec, 1\n\tds_add_u32 %0, %1 offset:%2\n\ts_mov_b64 exec, -1" :: "v"(addr), "v"(one), "n"(off) : "memory")
__device__ __forceinline__ bf16x8 band(u32 bits, u32 val) {
    v4u r;
#pragma unroll
    for (int j = 0; j < 4; ++j) r[j] = __builtin_amdgcn_perm((u32)__builtin_amdgcn_sbfe((int)bits, 2 * j + 1, 1), (u32)__builtin_amdgcn_sbfe((int)bits, 2 * j, 1), 0x05040100u) & val;
    return __builtin_bit_cast(bf16x8, r);
}

__device__ __forceinline__ u32 hline(const char* ws, u32 i) { const u32 q = i / 7u, r = i - 7u * q; return (8u * q + r + (r >= ((3u - (u32)((uintptr_t)ws >> 7)) & 7u))) * 128u; }
__device__ __forceinline__ u32 wd_line(u32 xcd, u32 i) {
    const u32 k = i / 9u, r = k / 9u;
    return ((xcd * 9u + k - 9u * r) * 64u + (r >> 1) + 32u * (r & 1)) * 1152u + (i - 9u * k) * 128u;
}
__device__ __forceinline__ bool touch(const Params& p, int xcd, int e, int lane, u32 dst) {
    const int wa = xcd * 9 + ((3 - (int)(((uintptr_t)p.wg >> 7) & 7) - xcd * 9) & 7), two = wa == xcd * 9;
    const int g0 = two ? 576 : PFG0, na = (g0 * 2) << two, nb = (576 - g0) * 18, nc = WPF * 1728;
    if (e * 64 >= na + nb + nc) return false;
    const u32 i = min(e * 64 + lane, na + nb + nc - 1);
    const u32 j = i - na, k = j / 9u;
    const bool first = i < (u32)na, last = i >= (u32)(na + nb);
    const u32 seg = first ? i >> two : k + 2 * g0, col = first ? (wa + 8 * (i & two)) * 128u : xcd * 1152u + (j - 9u * k) * 128u;
    const char* a = (const char*)(last ? p.wd : ((seg & 1) ? p.wu : p.wg)) + (last ? wd_line(xcd, j - nb) : (seg >> 1) * (F * 2) + col);
    asm volatile("s_mov_b32 m0, %1\n\ts_nop 0\n\tglobal_load_lds_dword %0, off" :: "v"(a), "s"(dst) : "memory");
    return true;
}

#define KGEN_ASM ".p2align 3\n\tv_mov_b32 v64, 0x3c00\n\tv_mov_b32 v65, 0x3c00\n\tv_pk_mul_f32 v[66:67], v[56:57], v[56:57] op_sel:[1,1] op_sel_hi:[1,0] neg_lo:[0,1]\n\tv_pk_mul_f32 v[68:69], v[58:59], v[58:59] op_sel:[1,1] op_sel_hi:[1,0] neg_lo:[0,1]\n\tv_pk_mul_f32 v[70:71], v[52:53], v[52:53] op_sel:[1,1] op_sel_hi:[1,0] neg_lo:[0,1]\n\tv_pk_mul_f32 v[72:73], v[54:55], v[54:55] op_sel:[1,1] op_sel_hi:[1,0] neg_lo:[0,1]\n\tv_pk_fma_f32 v[66:67], v[56:57], v[56:57], v[66:67] op_sel:[0,0,0] op_sel_hi:[0,1,1]\n\tv_pk_fma_f32 v[68:69], v[58:59], v[58:59], v[68:69] op_sel:[0,0,0] op_sel_hi:[0,1,1]\n\tv_pk_fma_f32 v[70:71], v[52:53], v[52:53], v[70:71] op_sel:[0,0,0] op_sel_hi:[0,1,1]\n\tv_pk_fma_f32 v[72:73], v[54:55], v[54:55], v[72:73] op_sel:[0,0,0] op_sel_hi:[0,1,1]\n\tv_cvt_pk_f16_f32 v74, v56, -v57\n\tv_cvt_pk_f16_f32 v75, v58, -v59\n\tv_cvt_pk_f16_f32 v76, v48, v49\n\tv_cvt_pk_f16_f32 v77, v50, v51\n\tv_pk_mul_f32 v[78:79], v[48:49], v[60:61] op_sel:[1,1] op_sel_hi:[1,0] neg_lo:[0,1]\n\tv_pk_mul_f32 v[80:81], v[50:51], v[62:63] op_sel:[1,1] op_sel_hi:[1,0] neg_lo:[0,1]\n\tv_pk_mul_f32 v[82:83], v[48:49], v[52:53] op_sel:[1,1] op_sel_hi:[1,0] neg_lo:[0,1]\n\tv_pk_mul_f32 v[84:85], v[50:51], v[54:55] op_sel:[1,1] op_sel_hi:[1,0] neg_lo:[0,1]\n\tds_write2_b64 %[b0], v[64:65], v[74:75] offset0:0 offset1:66\n\tv_pk_fma_f32 v[78:79], v[48:49], v[60:61], v[78:79] op_sel:[0,0,0] op_sel_hi:[0,1,1]\n\tv_pk_fma_f32 v[80:81], v[50:51], v[62:63], v[80:81] op_sel:[0,0,0] op_sel_hi:[0,1,1]\n\tv_pk_fma_f32 v[82:83], v[48:49], v[52:53], v[82:83] op_sel:[0,0,0] op_sel_hi:[0,1,1]\n\tv_pk_fma_f32 v[84:85], v[50:51], v[54:55], v[84:85] op_sel:[0,0,0] op_sel_hi:[0,1,1]\n\tv_cvt_pk_f16_f32 v86, v66, -v67\n\tv_cvt_pk_f16_f32 v87, v68, -v69\n\tv_pk_mul_f32 v[88:89], v[66:67], v[66:67] op_sel:[1,1] op_sel_hi:[1,0] neg_lo:[0,1]\n\tv_pk_mul_f32 v[90:91], v[68:69], v[68:69] op_sel:[1,1] op_sel_hi:[1,0] neg_lo:[0,1]\n\tv_pk_mul_f32 v[92:93], v[66:67], v[56:57] op_sel:[1,1] op_sel_hi:[1,0] neg_lo:[0,1]\n\tv_pk_mul_f32 v[94:95], v[68:69], v[58:59] op_sel:[1,1] op_sel_hi:[1,0] neg_lo:[0,1]\n\tv_pk_fma_f32 v[88:89], v[66:67], v[66:67], v[88:89] op_sel:[0,0,0] op_sel_hi:[0,1,1]\n\tv_pk_fma_f32 v[90:91], v[68:69], v[68:69], v[90:91] op_sel:[0,0,0] op_sel_hi:[0,1,1]\n\tv_pk_fma_f32 v[92:93], v[66:67], v[56:57], v[92:93] op_sel:[0,0,0] op_sel_hi:[0,1,1]\n\tv_pk_fma_f32 v[94:95], v[68:69], v[58:59], v[94:95] op_sel:[0,0,0] op_sel_hi:[0,1,1]\n\tv_cvt_pk_f16_f32 v64, v78, v79\n\tv_cvt_pk_f16_f32 v65, v80, v81\n\tv_cvt_pk_f16_f32 v96, v82, v83\n\tv_cvt_pk_f16_f32 v97, v84, v85\n\tv_pk_mul_f32 v[98:99], v[70:71], v[70:71] op_sel:[1,1] op_sel_hi:[1,0] neg_lo:[0,1]\n\tv_pk_mul_f32 v[100:101], v[72:73], v[72:73] op_sel:[1,1] op_sel_hi:[1,0] neg_lo:[0,1]\n\tv_pk_mul_f32 v[102:103], v[78:79], v[56:57] op_sel:[1,1] op_sel_hi:[1,0] neg_lo:[0,1]\n\tv_pk_mul_f32 v[104:105], v[80:81], v[58:59] op_sel:[1,1] op_sel_hi:[1,0] neg_lo:[0,1]\n\tds_write2_b64 %[b8], v[76:77], v[96:97] offset0:0 offset1:66\n\tv_pk_fma_f32 v[98:99], v[70:71], v[70:71], v[98:99] op_sel:[0,0,0] op_sel_hi:[0,1,1]\n\tv_pk_fma_f32 v[100:101], v[72:73], v[72:73], v[100:101] op_sel:[0,0,0] op_sel_hi:[0,1,1]\n\tv_pk_fma_f32 v[102:103], v[78:79], v[56:57], v[102:103] op_sel:[0,0,0] op_sel_hi:[0,1,1]\n\tv_pk_fma_f32 v[104:105], v[80:81], v[58:59], v[104:105] op_sel:[0,0,0] op_sel_hi:[0,1,1]\n\tv_cvt_pk_f16_f32 v106, v88, -v89\n\tv_cvt_pk_f16_f32 v107, v90, -v91\n\tv_cvt_pk_f16_f32 v108, v92, -v93\n\tv_cvt_pk_f16_f32 v109, v94, -v95\n\tv_pk_mul_f32 v[110:111], v[78:79], v[70:71] op_sel:[1,1] op_sel_hi:[1,0] neg_lo:[0,1]\n\tv_pk_mul_f32 v[112:113], v[80:81], v[72:73] op_sel:[1,1] op_sel_hi:[1,0] neg_lo:[0,1]\n\tv_pk_mul_f32 v[114:115], v[48:49], v[70:71] op_sel:[1,1] op_sel_hi:[1,0] neg_lo:[0,1]\n\tv_pk_mul_f32 v[116:117], v[50:51], v[72:73] op_sel:[1,1] op_sel_hi:[1,0] neg_lo:[0,1]\n\tds_write2_b64 %[b0], v[86:87], v[108:109] offset0:132 offset1:198\n\tv_pk_fma_f32 v[110:111], v[78:79], v[70:71], v[110:111] op_sel:[0,0,0] op_sel_hi:[0,1,1]\n\tv_pk_fma_f32 v[112:113], v[80:81], v[72:73], v[112:113] op_sel:[0,0,0] op_sel_hi:[0,1,1]\n\tv_pk_fma_f32 v[114:115], v[48:49], v[70:71], v[114:115] op_sel:[0,0,0] op_sel_hi:[0,1,1]\n\tv_pk_fma_f32 v[116:117], v[50:51], v[72:73], v[116:117] op_sel:[0,0,0] op_sel_hi:[0,1,1]\n\tv_cvt_pk_f16_f32 v76, v102, v103\n\tv_cvt_pk_f16_f32 v77, v104, v105\n\tv_pk_mul_f32 v[96:97], v[82:83], v[70:71] op_sel:[1,1] op_sel_hi:[1,0] neg_lo:[0,1]\n\tv_pk_mul_f32 v[118:119], v[84:85], v[72:73] op_sel:[1,1] op_sel_hi:[1,0] neg_lo:[0,1]\n\tv_pk_mul_f32 v[120:121], v[88:89], v[88:89] op_sel:[1,1] op_sel_hi:[1,0] neg_lo:[0,1]\n\tv_pk_mul_f32 v[122:123], v[90:91], v[90:91] op_sel:[1,1] op_sel_hi:[1,0] neg_lo:[0,1]\n\tv_pk_fma_f32 v[96:97], v[82:83], v[70:71], v[96:97] op_sel:[0,0,0] op_sel_hi:[0,1,1]\n\tv_pk_fma_f32 v[118:119], v[84:85], v[72:73], v[118:119] op_sel:[0,0,0] op_sel_hi:[0,1,1]\n\tv_pk_fma_f32 v[120:121], v[88:89], v[88:89], v[120:121] op_sel:[0,0,0] op_sel_hi:[0,1,1]\n\tv_pk_fma_f32 v[122:123], v[90:91], v[90:91], v[122:123] op_sel:[0,0,0] op_sel_hi:[0,1,1]\n\tv_cvt_pk_f16_f32 v124, v110, v111\n\tv_cvt_pk_f16_f32 v125, v112, v113\n\tv_cvt_pk_f16_f32 v126, v114, v115\n\tv_cvt_pk_f16_f32 v127, v116, v117\n\tv_pk_mul_f32 v[70:71], v[88:89], v[56:57] op_sel:[1,1] op_sel_hi:[1,0] neg_lo:[0,1]\n\tv_pk_mul_f32 v[72:73], v[90:91], v[58:59] op_sel:[1,1] op_sel_hi:[1,0] neg_lo:[0,1]\n\tv_pk_mul_f32 v[128:129], v[102:103], v[60:61] op_sel:[1,1] op_sel_hi:[1,0] neg_lo:[0,1]\n\tv_pk_mul_f32 v[130:131], v[104:105], v[62:63] op_sel:[1,1] op_sel_hi:[1,0] neg_lo:[0,1]\n\tds_write2_b64 %[b11], v[64:65], v[124:125] offset0:0 offset1:66\n\tv_pk_fma_f32 v[70:71], v[88:89], v[56:57], v[70:71] op_sel:[0,0,0] op_sel_hi:[0,1,1]\n\tv_pk_fma_f32 v[72:73], v[90:91], v[58:59], v[72:73] op_sel:[0,0,0] op_sel_hi:[0,1,1]\n\tv_pk_fma_f32 v[128:129], v[102:103], v[60:61], v[128:129] op_sel:[0,0,0] op_sel_hi:[0,1,1]\n\tv_pk_fma_f32 v[130:131], v[104:105], v[62:63], v[130:131] op_sel:[0,0,0] op_sel_hi:[0,1,1]\n\tv_cvt_pk_f16_f32 v132, v96, v97\n\tv_cvt_pk_f16_f32 v133, v118, v119\n\tv_cvt_pk_f16_f32 v134, v120, -v121\n\tv_cvt_pk_f16_f32 v135, v122, -v123\n\tv_pk_mul_f32 v[102:103], v[78:79], v[98:99] op_sel:[1,1] op_sel_hi:[1,0] neg_lo:[0,1]\n\tv_pk_mul_f32 v[104:105], v[80:81], v[100:101] op_sel:[1,1] op_sel_hi:[1,0] neg_lo:[0,1]\n\tv_pk_mul_f32 v[136:137], v[88:89], v[66:67] op_sel:[1,1] op_sel_hi:[1,0] neg_lo:[0,1]\n\tv_pk_mul_f32 v[138:139], v[90:91], v[68:69] op_sel:[1,1] op_sel_hi:[1,0] neg_lo:[0,1]\n\tds_write2_b64 %[b8], v[126:127], v[132:133] offset0:132 offset1:198\n\tv_pk_fma_f32 v[102:103], v[78:79], v[98:99], v[102:103] op_sel:[0,0,0] op_sel_hi:[0,1,1]\n\tv_pk_fma_f32 v[104:105], v[80:81], v[100:101], v[104:105] op_sel:[0,0,0] op_sel_hi:[0,1,1]\n\tv_pk_fma_f32 v[136:137], v[88:89], v[66:67], v[136:137] op_sel:[0,0,0] op_sel_hi:[0,1,1]\n\tv_pk_fma_f32 v[138:139], v[90:91], v[68:69], v[138:139] op_sel:[0,0,0] op_sel_hi:[0,1,1]\n\tv_cvt_pk_f16_f32 v64, v70, -v71\n\tv_cvt_pk_f16_f32 v65, v72, -v73\n\tv_cvt_pk_f16_f32 v124, v128, v129\n\tv_cvt_pk_f16_f32 v125, v130, v131\n\tv_pk_mul_f32 v[78:79], v[88:89], v[92:93] op_sel:[1,1] op_sel_hi:[1,0] neg_lo:[0,1]\n\tv_pk_mul_f32 v[80:81], v[90:91], v[94:95] op_sel:[1,1] op_sel_hi:[1,0] neg_lo:[0,1]\n\tv_pk_mul_f32 v[140:141], v[120:121], v[120:121] op_sel:[1,1] op_sel_hi:[1,0] neg_lo:[0,1]\n\tv_pk_mul_f32 v[142:143], v[122:123], v[122:123] op_sel:[1,1] op_sel_hi:[1,0] neg_lo:[0,1]\n\tds_write2_b64 %[b1], v[106:107], v[64:65] offset0:0 offset1:66\n\tv_pk_fma_f32 v[78:79], v[88:89], v[92:93], v[78:79] op_sel:[0,0,0] op_sel_hi:[0,1,1]\n\tv_pk_fma_f32 v[80:81], v[90:91], v[94:95], v[80:81] op_sel:[0,0,0] op_sel_hi:[0,1,1]\n\tv_pk_fma_f32 v[140:141], v[120:121], v[120:121], v[140:141] op_sel:[0,0,0] op_sel_hi:[0,1,1]\n\tv_pk_fma_f32 v[142:143], v[122:123], v[122:123], v[142:143] op_sel:[0,0,0] op_sel_hi:[0,1,1]\n\tv_cvt_pk_f16_f32 v126, v102, v103\n\tv_cvt_pk_f16_f32 v127, v104, v105\n\tv_cvt_pk_f16_f32 v132, v136, -v137\n\tv_cvt_pk_f16_f32 v133, v138, -v139\n\tv_pk_mul_f32 v[144:145], v[120:121], v[56:57] op_sel:[1,1] op_sel_hi:[1,0] neg_lo:[0,1]\n\tv_pk_mul_f32 v[146:147], v[122:123], v[58:59] op_sel:[1,1] op_sel_hi:[1,0] neg_lo:[0,1]\n\tv_pk_mul_f32 v[148:149], v[128:129], v[60:61] op_sel:[1,1] op_sel_hi:[1,0] neg_lo:[0,1]\n\tv_pk_mul_f32 v[150:151], v[130:131], v[62:63] op_sel:[1,1] op_sel_hi:[1,0] neg_lo:[0,1]\n\tds_write2_b64 %[b9], v[76:77], v[124:125] offset0:0 offset1:66\n\tv_pk_fma_f32 v[144:145], v[120:121], v[56:57], v[144:145] op_sel:[0,0,0] op_sel_hi:[0,1,1]\n\tv_pk_fma_f32 v[146:147], v[122:123], v[58:59], v[146:147] op_sel:[0,0,0] op_sel_hi:[0,1,1]\n\tv_pk_fma_f32 v[148:149], v[128:129], v[60:61], v[148:149] op_sel:[0,0,0] op_sel_hi:[0,1,1]\n\tv_pk_fma_f32 v[150:151], v[130:131], v[62:63], v[150:151] op_sel:[0,0,0] op_sel_hi:[0,1,1]\n\tv_cvt_pk_f16_f32 v152, v78, -v79\n\tv_cvt_pk_f16_f32 v153, v80, -v81\n\tv_cvt_pk_f16_f32 v154, v140, -v141\n\tv_cvt_pk_f16_f32 v155, v142, -v143\n\tv_pk_mul_f32 v[128:129], v[102:103], v[60:61] op_sel:[1,1] op_sel_hi:[1,0] neg_lo:[0,1]\n\tv_pk_mul_f32 v[130:131], v[104:105], v[62:63] op_sel:[1,1] op_sel_hi:[1,0] neg_lo:[0,1]\n\tv_pk_mul_f32 v[140:141], v[110:111], v[98:99] op_sel:[1,1] op_sel_hi:[1,0] neg_lo:[0,1]\n\tv_pk_mul_f32 v[142:143], v[112:113], v[100:101] op_sel:[1,1] op_sel_hi:[1,0] neg_lo:[0,1]\n\tds_write2_b64 %[b1], v[132:133], v[152:153] offset0:132 offset1:198\n\tv_pk_fma_f32 v[128:129], v[102:103], v[60:61], v[128:129] op_sel:[0,0,0] op_sel_hi:[0,1,1]\n\tv_pk_fma_f32 v[130:131], v[104:105], v[62:63], v[130:131] op_sel:[0,0,0] op_sel_hi:[0,1,1]\n\tv_pk_fma_f32 v[140:141], v[110:111], v[98:99], v[140:141] op_sel:[0,0,0] op_sel_hi:[0,1,1]\n\tv_pk_fma_f32 v[142:143], v[112:113], v[100:101], v[142:143] op_sel:[0,0,0] op_sel_hi:[0,1,1]\n\tv_cvt_pk_f16_f32 v76, v144, -v145\n\tv_cvt_pk_f16_f32 v77, v146, -v147\n\tv_cvt_pk_f16_f32 v124, v148, v149\n\tv_cvt_pk_f16_f32 v125, v150, v151\n\tv_pk_mul_f32 v[102:103], v[120:121], v[66:67] op_sel:[1,1] op_sel_hi:[1,0] neg_lo:[0,1]\n\tv_pk_mul_f32 v[104:105], v[122:123], v[68:69] op_sel:[1,1] op_sel_hi:[1,0] neg_lo:[0,1]\n\tv_pk_mul_f32 v[110:111], v[120:121], v[92:93] op_sel:[1,1] op_sel_hi:[1,0] neg_lo:[0,1]\n\tv_pk_mul_f32 v[112:113], v[122:123], v[94:95] op_sel:[1,1] op_sel_hi:[1,0] neg_lo:[0,1]\n\tds_write2_b64 %[b2], v[134:135], v[76:77] offset0:0 offset1:66\n\tv_pk_fma_f32 v[102:103], v[120:121], v[66:67], v[102:103] op_sel:[0,0,0] op_sel_hi:[0,1,1]\n\tv_pk_fma_f32 v[104:105], v[122:123], v[68:69], v[104:105] op_sel:[0,0,0] op_sel_hi:[0,1,1]\n\tv_pk_fma_f32 v[110:111], v[120:121], v[92:93], v[110:111] op_sel:[0,0,0] op_sel_hi:[0,1,1]\n\tv_pk_fma_f32 v[112:113], v[122:123], v[94:95], v[112:113] op_sel:[0,0,0] op_sel_hi:[0,1,1]\n\tv_cvt_pk_f16_f32 v144, v128, v129\n\tv_cvt_pk_f16_f32 v145, v130, v131\n\tv_cvt_pk_f16_f32 v146, v140, v141\n\tv_cvt_pk_f16_f32 v147, v142, v143\n\tv_pk_mul_f32 v[66:67], v[148:149], v[60:61] op_sel:[1,1] op_sel_hi:[1,0] neg_lo:[0,1]\n\tv_pk_mul_f32 v[68:69], v[150:151], v[62:63] op_sel:[1,1] op_sel_hi:[1,0] neg_lo:[0,1]\n\tv_pk_mul_f32 v[92:93], v[128:129], v[60:61] op_sel:[1,1] op_sel_hi:[1,0] neg_lo:[0,1]\n\tv_pk_mul_f32 v[94:95], v[130:131], v[62:63] op_sel:[1,1] op_sel_hi:[1,0] neg_lo:[0,1]\n\tds_write2_b64 %[b9], v[124:125], v[146:147] offset0:132 offset1:198\n\tv_pk_fma_f32 v[66:67], v[148:149], v[60:61], v[66:67] op_sel:[0,0,0] op_sel_hi:[0,1,1]\n\tv_pk_fma_f32 v[68:69], v[150:151], v[62:63], v[68:69] op_sel:[0,0,0] op_sel_hi:[0,1,1]\n\tv_pk_fma_f32 v[92:93], v[128:129], v[60:61], v[92:93] op_sel:[0,0,0] op_sel_hi:[0,1,1]\n\tv_pk_fma_f32 v[94:95], v[130:131], v[62:63], v[94:95] op_sel:[0,0,0] op_sel_hi:[0,1,1]\n\tv_cvt_pk_f16_f32 v140, v102, -v103\n\tv_cvt_pk_f16_f32 v141, v104, -v105\n\tv_cvt_pk_f16_f32 v142, v110, -v111\n\tv_cvt_pk_f16_f32 v143, v112, -v113\n\tv_pk_mul_f32 v[102:103], v[120:121], v[88:89] op_sel:[1,1] op_sel_hi:[1,0] neg_lo:[0,1]\n\tv_pk_mul_f32 v[104:105], v[122:123], v[90:91] op_sel:[1,1] op_sel_hi:[1,0] neg_lo:[0,1]\n\tv_pk_mul_f32 v[110:111], v[120:121], v[70:71] op_sel:[1,1] op_sel_hi:[1,0] neg_lo:[0,1]\n\tv_pk_mul_f32 v[112:113], v[122:123], v[72:73] op_sel:[1,1] op_sel_hi:[1,0] neg_lo:[0,1]\n\tds_write2_b64 %[b2], v[140:141], v[142:143] offset0:132 offset1:198\n\tv_pk_fma_f32 v[102:103], v[120:121], v[88:89], v[102:103] op_sel:[0,0,0] op_sel_hi:[0,1,1]\n\tv_pk_fma_f32 v[104:105], v[122:123], v[90:91], v[104:105] op_sel:[0,0,0] op_sel_hi:[0,1,1]\n\tv_pk_fma_f32 v[110:111], v[120:121], v[70:71], v[110:111] op_sel:[0,0,0] op_sel_hi:[0,1,1]\n\tv_pk_fma_f32 v[112:113], v[122:123], v[72:73], v[112:113] op_sel:[0,0,0] op_sel_hi:[0,1,1]\n\tv_cvt_pk_f16_f32 v124, v66, v67\n\tv_cvt_pk_f16_f32 v125, v68, v69\n\tv_cvt_pk_f16_f32 v128, v92, v93\n\tv_cvt_pk_f16_f32 v129, v94, v95\n\tv_pk_mul_f32 v[70:71], v[48:49], v[98:99] op_sel:[1,1] op_sel_hi:[1,0] neg_lo:[0,1]\n\tv_pk_mul_f32 v[72:73], v[50:51], v[100:101] op_sel:[1,1] op_sel_hi:[1,0] neg_lo:[0,1]\n\tv_pk_mul_f32 v[88:89], v[82:83], v[98:99] op_sel:[1,1] op_sel_hi:[1,0] neg_lo:[0,1]\n\tv_pk_mul_f32 v[90:91], v[84:85], v[100:101] op_sel:[1,1] op_sel_hi:[1,0] neg_lo:[0,1]\n\tds_write2_b64 %[b10], v[126:127], v[144:145] offset0:132 offset1:198\n\tv_pk_fma_f32 v[70:71], v[48:49], v[98:99], v[70:71] op_sel:[0,0,0] op_sel_hi:[0,1,1]\n\tv_pk_fma_f32 v[72:73], v[50:51], v[100:101], v[72:73] op_sel:[0,0,0] op_sel_hi:[0,1,1]\n\tv_pk_fma_f32 v[88:89], v[82:83], v[98:99], v[88:89] op_sel:[0,0,0] op_sel_hi:[0,1,1]\n\tv_pk_fma_f32 v[90:91], v[84:85], v[100:101], v[90:91] op_sel:[0,0,0] op_sel_hi:[0,1,1]\n\tv_cvt_pk_f16_f32 v92, v102, -v103\n\tv_cvt_pk_f16_f32 v93, v104, -v105\n\tv_cvt_pk_f16_f32 v94, v110, -v111\n\tv_cvt_pk_f16_f32 v95, v112, -v113\n\tv_pk_mul_f32 v[82:83], v[66:67], v[60:61] op_sel:[1,1] op_sel_hi:[1,0] neg_lo:[0,1]\n\tv_pk_mul_f32 v[84:85], v[68:69], v[62:63] op_sel:[1,1] op_sel_hi:[1,0] neg_lo:[0,1]\n\tv_pk_mul_f32 v[102:103], v[120:121], v[136:137] op_sel:[1,1] op_sel_hi:[1,0] neg_lo:[0,1]\n\tv_pk_mul_f32 v[104:105], v[122:123], v[138:139] op_sel:[1,1] op_sel_hi:[1,0] neg_lo:[0,1]\n\tds_write2_b64 %[b3], v[92:93], v[94:95] offset0:0 offset1:66\n\tv_pk_fma_f32 v[82:83], v[66:67], v[60:61], v[82:83] op_sel:[0,0,0] op_sel_hi:[0,1,1]\n\tv_pk_fma_f32 v[84:85], v[68:69], v[62:63], v[84:85] op_sel:[0,0,0] op_sel_hi:[0,1,1]\n\tv_pk_fma_f32 v[102:103], v[120:121], v[136:137], v[102:103] op_sel:[0,0,0] op_sel_hi:[0,1,1]\n\tv_pk_fma_f32 v[104:105], v[122:123], v[138:139], v[104:105] op_sel:[0,0,0] op_sel_hi:[0,1,1]\n\tv_cvt_pk_f16_f32 v110, v70, v71\n\tv_cvt_pk_f16_f32 v111, v72, v73\n\tv_cvt_pk_f16_f32 v112, v88, v89\n\tv_cvt_pk_f16_f32 v113, v90, v91\n\tv_pk_mul_f32 v[66:67], v[120:121], v[78:79] op_sel:[1,1] op_sel_hi:[1,0] neg_lo:[0,1]\n\tv_pk_mul_f32 v[68:69], v[122:123], v[80:81] op_sel:[1,1] op_sel_hi:[1,0] neg_lo:[0,1]\n\tv_pk_mul_f32 v[70:71], v[114:115], v[98:99] op_sel:[1,1] op_sel_hi:[1,0] neg_lo:[0,1]\n\tv_pk_mul_f32 v[72:73], v[116:117], v[100:101] op_sel:[1,1] op_sel_hi:[1,0] neg_lo:[0,1]\n\tds_write2_b64 %[b11], v[110:111], v[112:113] offset0:132 offset1:198\n\tv_pk_fma_f32 v[66:67], v[120:121], v[78:79], v[66:67] op_sel:[0,0,0] op_sel_hi:[0,1,1]\n\tv_pk_fma_f32 v[68:69], v[122:123], v[80:81], v[68:69] op_sel:[0,0,0] op_sel_hi:[0,1,1]\n\tv_pk_fma_f32 v[70:71], v[114:115], v[98:99], v[70:71] op_sel:[0,0,0] op_sel_hi:[0,1,1]\n\tv_pk_fma_f32 v[72:73], v[116:117], v[100:101], v[72:73] op_sel:[0,0,0] op_sel_hi:[0,1,1]\n\tv_cvt_pk_f16_f32 v88, v82, v83\n\tv_cvt_pk_f16_f32 v89, v84, v85\n\tv_cvt_pk_f16_f32 v90, v102, -v103\n\tv_cvt_pk_f16_f32 v91, v104, -v105\n\tv_pk_mul_f32 v[78:79], v[96:97], v[98:99] op_sel:[1,1] op_sel_hi:[1,0] neg_lo:[0,1]\n\tv_pk_mul_f32 v[80:81], v[118:119], v[100:101] op_sel:[1,1] op_sel_hi:[1,0] neg_lo:[0,1]\n\tv_pk_mul_f32 v[102:103], v[82:83], v[60:61] op_sel:[1,1] op_sel_hi:[1,0] neg_lo:[0,1]\n\tv_pk_mul_f32 v[104:105], v[84:85], v[62:63] op_sel:[1,1] op_sel_hi:[1,0] neg_lo:[0,1]\n\tds_write2_b64 %[b12], v[128:129], v[124:125] offset0:132 offset1:198\n\tv_pk_fma_f32 v[78:79], v[96:97], v[98:99], v[78:79] op_sel:[0,0,0] op_sel_hi:[0,1,1]\n\tv_pk_fma_f32 v[80:81], v[118:119], v[100:101], v[80:81] op_sel:[0,0,0] op_sel_hi:[0,1,1]\n\tv_pk_fma_f32 v[102:103], v[82:83], v[60:61], v[102:103] op_sel:[0,0,0] op_sel_hi:[0,1,1]\n\tv_pk_fma_f32 v[104:105], v[84:85], v[62:63], v[104:105] op_sel:[0,0,0] op_sel_hi:[0,1,1]\n\tv_cvt_pk_f16_f32 v110, v66, -v67\n\tv_cvt_pk_f16_f32 v111, v68, -v69\n\tv_cvt_pk_f16_f32 v112, v70, v71\n\tv_cvt_pk_f16_f32 v113, v72, v73\n\tv_pk_mul_f16 v66, v154, v74 op_sel:[1,1] op_sel_hi:[1,0] neg_lo:[0,1]\n\tv_pk_mul_f16 v67, v155, v75 op_sel:[1,1] op_sel_hi:[1,0] neg_lo:[0,1]\n\tv_pk_mul_f16 v68, v154, v86 op_sel:[1,1] op_sel_hi:[1,0] neg_lo:[0,1]\n\tv_pk_mul_f16 v69, v155, v87 op_sel:[1,1] op_sel_hi:[1,0] neg_lo:[0,1]\n\tds_write2_b64 %[b3], v[90:91], v[110:111] offset0:132 offset1:198\n\tv_pk_fma_f16 v66, v154, v74, v66 op_sel:[0,0,0] op_sel_hi:[0,1,1]\n\tv_pk_fma_f16 v67, v155, v75, v67 op_sel:[0,0,0] op_sel_hi:[0,1,1]\n\tv_pk_fma_f16 v68, v154, v86, v68 op_sel:[0,0,0] op_sel_hi:[0,1,1]\n\tv_pk_fma_f16 v69, v155, v87, v69 op_sel:[0,0,0] op_sel_hi:[0,1,1]\n\tv_cvt_pk_f16_f32 v70, v78, v79\n\tv_cvt_pk_f16_f32 v71, v80, v81\n\tv_cvt_pk_f16_f32 v72, v102, v103\n\tv_cvt_pk_f16_f32 v73, v104, v105\n\tv_pk_mul_f16 v78, v154, v108 op_sel:[1,1] op_sel_hi:[1,0] neg_lo:[0,1]\n\tv_pk_mul_f16 v79, v155, v109 op_sel:[1,1] op_sel_hi:[1,0] neg_lo:[0,1]\n\tv_pk_mul_f32 v[80:81], v[102:103], v[60:61] op_sel:[1,1] op_sel_hi:[1,0] neg_lo:[0,1]\n\tv_pk_mul_f32 v[82:83], v[104:105], v[62:63] op_sel:[1,1] op_sel_hi:[1,0] neg_lo:[0,1]\n\tds_write2_b64 %[b4], v[154:155], v[66:67] offset0:0 offset1:66\n\tv_pk_fma_f16 v78, v154, v108, v78 op_sel:[0,0,0] op_sel_hi:[0,1,1]\n\tv_pk_fma_f16 v79, v155, v109, v79 op_sel:[0,0,0] op_sel_hi:[0,1,1]\n\tv_pk_fma_f32 v[80:81], v[102:103], v[60:61], v[80:81] op_sel:[0,0,0] op_sel_hi:[0,1,1]\n\tv_pk_fma_f32 v[82:83], v[104:105], v[62:63], v[82:83] op_sel:[0,0,0] op_sel_hi:[0,1,1]\n\tv_pk_mul_f16 v74, v154, v106 op_sel:[1,1] op_sel_hi:[1,0] neg_lo:[0,1]\n\tv_pk_mul_f16 v75, v155, v107 op_sel:[1,1] op_sel_hi:[1,0] neg_lo:[0,1]\n\tv_pk_mul_f16 v84, v154, v64 op_sel:[1,1] op_sel_hi:[1,0] neg_lo:[0,1]\n\tv_pk_mul_f16 v85, v155, v65 op_sel:[1,1] op_sel_hi:[1,0] neg_lo:[0,1]\n\tds_write2_b64 %[b4], v[68:69], v[78:79] offset0:132 offset1:198\n\tv_pk_fma_f16 v74, v154, v106, v74 op_sel:[0,0,0] op_sel_hi:[0,1,1]\n\tv_pk_fma_f16 v75, v155, v107, v75 op_sel:[0,0,0] op_sel_hi:[0,1,1]\n\tv_pk_fma_f16 v84, v154, v64, v84 op_sel:[0,0,0] op_sel_hi:[0,1,1]\n\tv_pk_fma_f16 v85, v155, v65, v85 op_sel:[0,0,0] op_sel_hi:[0,1,1]\n\tv_cvt_pk_f16_f32 v66, v80, v81\n\tv_cvt_pk_f16_f32 v67, v82, v83\n\tv_pk_mul_f16 v80, v154, v132 op_sel:[1,1] op_sel_hi:[1,0] neg_lo:[0,1]\n\tv_pk_mul_f16 v81, v155, v133 op_sel:[1,1] op_sel_hi:[1,0] neg_lo:[0,1]\n\tv_pk_mul_f16 v82, v154, v152 op_sel:[1,1] op_sel_hi:[1,0] neg_lo:[0,1]\n\tv_pk_mul_f16 v83, v155, v153 op_sel:[1,1] op_sel_hi:[1,0] neg_lo:[0,1]\n\tds_write2_b64 %[b5], v[74:75], v[84:85] offset0:0 offset1:66\n\tv_pk_fma_f16 v80, v154, v132, v80 op_sel:[0,0,0] op_sel_hi:[0,1,1]\n\tv_pk_fma_f16 v81, v155, v133, v81 op_sel:[0,0,0] op_sel_hi:[0,1,1]\n\tv_pk_fma_f16 v82, v154, v152, v82 op_sel:[0,0,0] op_sel_hi:[0,1,1]\n\tv_pk_fma_f16 v83, v155, v153, v83 op_sel:[0,0,0] op_sel_hi:[0,1,1]\n\tv_pk_mul_f16 v64, v154, v134 op_sel:[1,1] op_sel_hi:[1,0] neg_lo:[0,1]\n\tv_pk_mul_f16 v65, v155, v135 op_sel:[1,1] op_sel_hi:[1,0] neg_lo:[0,1]\n\tv_pk_mul_f16 v68, v154, v76 op_sel:[1,1] op_sel_hi:[1,0] neg_lo:[0,1]\n\tv_pk_mul_f16 v69, v155, v77 op_sel:[1,1] op_sel_hi:[1,0] neg_lo:[0,1]\n\tds_write2_b64 %[b5], v[80:81], v[82:83] offset0:132 offset1:198\n\tv_pk_fma_f16 v64, v154, v134, v64 op_sel:[0,0,0] op_sel_hi:[0,1,1]\n\tv_pk_fma_f16 v65, v155, v135, v65 op_sel:[0,0,0] op_sel_hi:[0,1,1]\n\tv_pk_fma_f16 v68, v154, v76, v68 op_sel:[0,0,0] op_sel_hi:[0,1,1]\n\tv_pk_fma_f16 v69, v155, v77, v69 op_sel:[0,0,0] op_sel_hi:[0,1,1]\n\tv_pk_mul_f16 v74, v154, v140 op_sel:[1,1] op_sel_hi:[1,0] neg_lo:[0,1]\n\tv_pk_mul_f16 v75, v155, v141 op_sel:[1,1] op_sel_hi:[1,0] neg_lo:[0,1]\n\tv_pk_mul_f16 v78, v154, v142 op_sel:[1,1] op_sel_hi:[1,0] neg_lo:[0,1]\n\tv_pk_mul_f16 v79, v155, v143 op_sel:[1,1] op_sel_hi:[1,0] neg_lo:[0,1]\n\tds_write2_b64 %[b6], v[64:65], v[68:69] offset0:0 offset1:66\n\tv_pk_fma_f16 v74, v154, v140, v74 op_sel:[0,0,0] op_sel_hi:[0,1,1]\n\tv_pk_fma_f16 v75, v155, v141, v75 op_sel:[0,0,0] op_sel_hi:[0,1,1]\n\tv_pk_fma_f16 v78, v154, v142, v78 op_sel:[0,0,0] op_sel_hi:[0,1,1]\n\tv_pk_fma_f16 v79, v155, v143, v79 op_sel:[0,0,0] op_sel_hi:[0,1,1]\n\tv_pk_mul_f16 v76, v154, v92 op_sel:[1,1] op_sel_hi:[1,0] neg_lo:[0,1]\n\tv_pk_mul_f16 v77, v155, v93 op_sel:[1,1] op_sel_hi:[1,0] neg_lo:[0,1]\n\tv_pk_mul_f16 v80, v154, v94 op_sel:[1,1] op_sel_hi:[1,0] neg_lo:[0,1]\n\tv_pk_mul_f16 v81, v155, v95 op_sel:[1,1] op_sel_hi:[1,0] neg_lo:[0,1]\n\tds_write2_b64 %[b6], v[74:75], v[78:79] offset0:132 offset1:198\n\tv_pk_fma_f16 v76, v154, v92, v76 op_sel:[0,0,0] op_sel_hi:[0,1,1]\n\tv_pk_fma_f16 v77, v155, v93, v77 op_sel:[0,0,0] op_sel_hi:[0,1,1]\n\tv_pk_fma_f16 v80, v154, v94, v80 op_sel:[0,0,0] op_sel_hi:[0,1,1]\n\tv_pk_fma_f16 v81, v155, v95, v81 op_sel:[0,0,0] op_sel_hi:[0,1,1]\n\tv_pk_mul_f16 v64, v154, v90 op_sel:[1,1] op_sel_hi:[1,0] neg_lo:[0,1]\n\tv_pk_mul_f16 v65, v155, v91 op_sel:[1,1] op_sel_hi:[1,0] neg_lo:[0,1]\n\tv_pk_mul_f16 v68, v154, v110 op_sel:[1,1] op_sel_hi:[1,0] neg_lo:[0,1]\n\tv_pk_mul_f16 v69, v155, v111 op_sel:[1,1] op_sel_hi:[1,0] neg_lo:[0,1]\n\tds_write2_b64 %[b7], v[76:77], v[80:81] offset0:0 offset1:66\n\tv_pk_fma_f16 v64, v154, v90, v64 op_sel:[0,0,0] op_sel_hi:[0,1,1]\n\tv_pk_fma_f16 v65, v155, v91, v65 op_sel:[0,0,0] op_sel_hi:[0,1,1]\n\tv_pk_fma_f16 v68, v154, v110, v68 op_sel:[0,0,0] op_sel_hi:[0,1,1]\n\tv_pk_fma_f16 v69, v155, v111, v69 op_sel:[0,0,0] op_sel_hi:[0,1,1]\n\ts_nop 0\n\tds_write2_b64 %[b7], v[64:65], v[68:69] offset0:132 offset1:198\n\tds_write2_b64 %[b10], v[88:89], v[66:67] offset0:0 offset1:66\n\tds_write2_b64 %[b12], v[112:113], v[70:71] offset0:0 offset1:66\n\tds_write_b64 %[b13], v[72:73] offset:0"
#define KGEN_CLOBBER "v64", "v65", "v66", "v67", "v68", "v69", "v70", "v71", "v72", "v73", "v74", "v75", "v76", "v77", "v78", "v79", "v80", "v81", "v82", "v83", "v84", "v85", "v86", "v87", "v88", "v89", "v90", "v91", "v92", "v93", "v94", "v95", "v96", "v97", "v98", "v99", "v100", "v101", "v102", "v103", "v104", "v105", "v106", "v107", "v108", "v109", "v110", "v111", "v112", "v113", "v114", "v115", "v116", "v117", "v118", "v119", "v120", "v121", "v122", "v123", "v124", "v125", "v126", "v127", "v128", "v129", "v130", "v131", "v132", "v133", "v134", "v135", "v136", "v137", "v138", "v139", "v140", "v141", "v142", "v143", "v144", "v145", "v146", "v147", "v148", "v149", "v150", "v151", "v152", "v153", "v154", "v155"
#define KROW_0LO 0x2b2a2d2c23222120ull
#define KROW_0HI 0x202020202b343332ull
#define KROW_1LO 0x2524272a31302f2eull
#define KROW_1HI 0x2b2b2b2b20292826ull
#define KMFMA_ASM "ds_read_b128 v[64:67], %[ap] offset:0\n\tds_read_b128 v[68:71], %[bp] offset:0\n\tds_read_b128 v[72:75], %[ap] offset:32\n\tds_read_b128 v[76:79], %[bp] offset:32\n\tds_read_b128 v[80:83], %[ap] offset:64\n\tds_read_b128 v[84:87], %[bp] offset:64\n\tds_read_b128 v[88:91], %[ap] offset:96\n\tds_read_b128 v[92:95], %[bp] offset:96\n\ts_waitcnt lgkmcnt(6)\n\tv_mfma_f32_32x32x16_f16 v[48:63], v[64:67], v[68:71], 0\n\tds_read_b128 v[96:99], %[ap] offset:128\n\tds_read_b128 v[100:103], %[bp] offset:128\n\ts_waitcnt lgkmcnt(6)\n\tv_mfma_f32_32x32x16_f16 v[48:63], v[72:75], v[76:79], v[48:63]\n\tds_read_b128 v[104:107], %[ap] offset:160\n\tds_read_b128 v[108:111], %[bp] offset:160\n\ts_waitcnt lgkmcnt(6)\n\tv_mfma_f32_32x32x16_f16 v[48:63], v[80:83], v[84:87], v[48:63]\n\tds_read_b128 v[64:67], %[ap] offset:192\n\tds_read_b128 v[68:71], %[bp] offset:192\n\ts_waitcnt lgkmcnt(6)\n\tv_mfma_f32_32x32x16_f16 v[48:63], v[88:91], v[92:95], v[48:63]\n\tds_read_b128 v[72:75], %[ap] offset:224\n\tds_read_b128 v[76:79], %[bp] offset:224\n\ts_waitcnt lgkmcnt(6)\n\tv_mfma_f32_32x32x16_f16 v[48:63], v[96:99], v[100:103], v[48:63]\n\tds_read_b128 v[80:83], %[ap] offset:256\n\tds_read_b128 v[84:87], %[bp] offset:256\n\ts_waitcnt lgkmcnt(6)\n\tv_mfma_f32_32x32x16_f16 v[48:63], v[104:107], v[108:111], v[48:63]\n\tds_read_b128 v[88:91], %[ap] offset:288\n\tds_read_b128 v[92:95], %[bp] offset:288\n\ts_waitcnt lgkmcnt(6)\n\tv_mfma_f32_32x32x16_f16 v[48:63], v[64:67], v[68:71], v[48:63]\n\tds_read_b128 v[96:99], %[ap] offset:320\n\tds_read_b128 v[100:103], %[bp] offset:320\n\ts_waitcnt lgkmcnt(6)\n\tv_mfma_f32_32x32x16_f16 v[48:63], v[72:75], v[76:79], v[48:63]\n\tds_read_b128 v[104:107], %[ap] offset:352\n\tds_read_b128 v[108:111], %[bp] offset:352\n\ts_waitcnt lgkmcnt(6)\n\tv_mfma_f32_32x32x16_f16 v[48:63], v[80:83], v[84:87], v[48:63]\n\tds_read_b128 v[64:67], %[ap] offset:384\n\tds_read_b128 v[68:71], %[bp] offset:384\n\ts_waitcnt lgkmcnt(6)\n\tv_mfma_f32_32x32x16_f16 v[48:63], v[88:91], v[92:95], v[48:63]\n\tds_read_b128 v[72:75], %[ap] offset:416\n\tds_read_b128 v[76:79], %[bp] offset:416\n\ts_waitcnt lgkmcnt(6)\n\tv_mfma_f32_32x32x16_f16 v[48:63], v[96:99], v[100:103], v[48:63]\n\tds_read_b128 v[80:83], %[ap] offset:448\n\tds_read_b128 v[84:87], %[bp] offset:448\n\ts_waitcnt lgkmcnt(6)\n\tv_mfma_f32_32x32x16_f16 v[48:63], v[104:107], v[108:111], v[48:63]\n\tds_read_b128 v[88:91], %[ap] offset:480\n\tds_read_b128 v[92:95], %[bp] offset:480\n\ts_waitcnt lgkmcnt(6)\n\tv_mfma_f32_32x32x16_f16 v[48:63], v[64:67], v[68:71], v[48:63]\n\tds_read_b32 %[stg], %[ctl] offset:4\n\ts_waitcnt lgkmcnt(4)\n\tv_mfma_f32_32x32x16_f16 v[48:63], v[72:75], v[76:79], v[48:63]\n\ts_waitcnt lgkmcnt(2)\n\tv_mfma_f32_32x32x16_f16 v[48:63], v[80:83], v[84:87], v[48:63]\n\ts_waitcnt lgkmcnt(0)\n\tv_mfma_f32_32x32x16_f16 v[48:63], v[88:91], v[92:95], v[48:63]\n\ts_nop 7\n\ts_nop 3\n\ts_waitcnt lgkmcnt(0)"
#define KMFMA_CLOBBER "v64", "v65", "v66", "v67", "v68", "v69", "v70", "v71", "v72", "v73", "v74", "v75", "v76", "v77", "v78", "v79", "v80", "v81", "v82", "v83", "v84", "v85", "v86", "v87", "v88", "v89", "v90", "v91", "v92", "v93", "v94", "v95", "v96", "v97", "v98", "v99", "v100", "v101", "v102", "v103", "v104", "v105", "v106", "v107", "v108", "v109", "v110", "v111"
#define CONV_ASM ".p2align 3\n\tds_read_b64_tr_b16 v[52:53], %[a0] offset:456\n\tds_read_b64_tr_b16 v[54:55], %[a0] offset:448\n\tds_read_b64_tr_b16 v[84:85], %[a0] offset:968\n\tds_read_b64_tr_b16 v[86:87], %[a0] offset:960\n\tds_read_b64_tr_b16 v[48:49], %[a0] offset:392\n\tds_read_b64_tr_b16 v[50:51], %[a0] offset:384\n\tds_read_b64_tr_b16 v[80:81], %[a0] offset:904\n\tds_read_b64_tr_b16 v[82:83], %[a0] offset:896\n\tds_read_b64_tr_b16 v[44:45], %[a0] offset:328\n\tds_read_b64_tr_b16 v[46:47], %[a0] offset:320\n\tds_read_b64_tr_b16 v[76:77], %[a0] offset:840\n\tds_read_b64_tr_b16 v[78:79], %[a0] offset:832\n\tv_readfirstlane_b32 %[s], %[stg]\n\ts_mov_b32 %[c], 0x400000\n\ts_cmp_ge_u32 %[s], 4\n\ts_cbranch_scc1 2f\n\t1:\n\tds_read_b32 v216, %[ctl] offset:4\n\ts_waitcnt lgkmcnt(0)\n\tv_readfirstlane_b32 %[s], v216\n\ts_cmp_ge_u32 %[s], 4\n\ts_cbranch_scc1 2f\n\ts_sleep 1\n\ts_sub_u32 %[c], %[c], 1\n\ts_cbranch_scc0 1b\n\t.p2align 3\n\t2:\n\tds_read_b128 v[164:167], %[bb] offset:64\n\tds_read_b128 v[168:171], %[bb] offset:128\n\tds_read_b128 v[172:175], %[bb] offset:192\n\tds_read_b64_tr_b16 v[40:41], %[a0] offset:264\n\tds_read_b64_tr_b16 v[42:43], %[a0] offset:256\n\tds_read_b128 v[176:179], %[bb] offset:256\n\ts_waitcnt lgkmcnt(5)\n\ts_nop 0\n\tv_mfma_f32_16x16x32_bf16 v[132:135], v[52:55], v[164:167], 0\n\tds_read_b64_tr_b16 v[72:73], %[a0] offset:776\n\tds_read_b64_tr_b16 v[74:75], %[a0] offset:768\n\tds_read_b64_tr_b16 v[36:37], %[a0] offset:200\n\tv_mfma_f32_16x16x32_bf16 v[136:139], v[84:87], v[164:167], 0\n\tds_read_b64_tr_b16 v[38:39], %[a0] offset:192\n\ts_waitcnt lgkmcnt(8)\n\ts_nop 0\n\tv_mfma_f32_16x16x32_bf16 v[132:135], v[48:51], v[168:171], v[132:135]\n\tds_read_b128 v[180:183], %[bb] offset:320\n\tds_read_b64_tr_b16 v[68:69], %[a0] offset:712\n\tds_read_b64_tr_b16 v[70:71], %[a0] offset:704\n\tv_mfma_f32_16x16x32_bf16 v[136:139], v[80:83], v[168:171], v[136:139]\n\tds_read_b64_tr_b16 v[32:33], %[a0] offset:136\n\ts_waitcnt lgkmcnt(8)\n\ts_nop 0\n\tv_mfma_f32_16x16x32_bf16 v[132:135], v[44:47], v[172:175], v[132:135]\n\tds_read_b64_tr_b16 v[34:35], %[a0] offset:128\n\tds_read_b128 v[184:187], %[bb] offset:384\n\tds_read_b64_tr_b16 v[64:65], %[a0] offset:648\n\tv_mfma_f32_16x16x32_bf16 v[136:139], v[76:79], v[172:175], v[136:139]\n\tds_read_b64_tr_b16 v[66:67], %[a0] offset:640\n\tds_read_b64_tr_b16 v[28:29], %[a0] offset:72\n\tds_read_b64_tr_b16 v[30:31], %[a0] offset:64\n\tv_mfma_f32_16x16x32_bf16 v[132:135], v[40:43], v[176:179], v[132:135]\n\tds_read_b128 v[164:167], %[bb] offset:448\n\ts_waitcnt lgkmcnt(8)\n\ts_nop 0\n\tv_mfma_f32_16x16x32_bf16 v[136:139], v[72:75], v[176:179], v[136:139]\n\tds_read_b64_tr_b16 v[60:61], %[a0] offset:584\n\tds_read_b64_tr_b16 v[62:63], %[a0] offset:576\n\tds_read_b64_tr_b16 v[24:25], %[a0] offset:8\n\tv_mfma_f32_16x16x32_bf16 v[132:135], v[36:39], v[180:183], v[132:135]\n\tds_read_b64_tr_b16 v[26:27], %[a0] offset:0\n\tds_read_b128 v[168:171], %[bb] offset:512\n\tds_read_b64_tr_b16 v[56:57], %[a0] offset:520\n\tv_mfma_f32_16x16x32_bf16 v[136:139], v[68:71], v[180:183], v[136:139]\n\tds_read_b64_tr_b16 v[58:59], %[a0] offset:512\n\ts_waitcnt lgkmcnt(8)\n\ts_nop 0\n\tv_mfma_f32_16x16x32_bf16 v[132:135], v[32:35], v[184:187], v[132:135]\n\tds_read_b64_tr_b16 v[88:89], %[a0] offset:1032\n\tds_read_b64_tr_b16 v[90:91], %[a0] offset:1024\n\tds_read_b128 v[188:191], %[bb] offset:496\n\tv_mfma_f32_16x16x32_bf16 v[136:139], v[64:67], v[184:187], v[136:139]\n\tds_read_b128 v[172:175], %[bb] offset:576\n\ts_waitcnt lgkmcnt(8)\n\ts_nop 0\n\tv_mfma_f32_16x16x32_bf16 v[132:135], v[28:31], v[164:167], v[132:135]\n\tds_read_b128 v[176:179], %[bb] offset:640\n\tv_mfma_f32_16x16x32_bf16 v[136:139], v[60:63], v[164:167], v[136:139]\n\ts_waitcnt lgkmcnt(5)\n\ts_nop 0\n\tv_mfma_f32_16x16x32_bf16 v[132:135], v[24:27], v[168:171], v[132:135]\n\tds_write_b16 %[ta], %[t78]\n\tds_read_b128 v[180:183], %[bb] offset:704\n\tv_mfma_f32_16x16x32_bf16 v[136:139], v[56:59], v[168:171], v[136:139]\n\ts_waitcnt lgkmcnt(3)\n\ts_nop 0\n\tv_mfma_f32_16x16x32_bf16 v[140:143], v[88:91], v[168:171], 0\n\tds_write_b16_d16_hi %[ta], %[t78] offset:64\n\tds_read_b128 v[184:187], %[bb] offset:768\n\tv_mfma_f32_16x16x32_bf16 v[204:207], v[188:191], v[4:7], 0\n\tds_write_b128 %[yb], v[132:135] offset:0\n\tv_mfma_f32_16x16x32_bf16 v[218:221], v[168:171], v[4:7], 0\n\tds_write_b16 %[ta], %[t78] offset:388\n\tds_read_b128 v[164:167], %[bb] offset:832\n\tv_mfma_f32_16x16x32_bf16 v[208:211], v[188:191], v[8:11], 0\n\tds_write_b8 %[ctc], %[one] offset:0\n\tds_write_b16_d16_hi %[ta], %[t78] offset:452\n\tv_mfma_f32_16x16x32_bf16 v[222:225], v[168:171], v[8:11], 0\n\tds_write_b16 %[tb], %[t9a]\n\tv_mfma_f32_16x16x32_bf16 v[212:215], v[188:191], v[12:15], 0\n\tds_write_b16_d16_hi %[tb], %[t9a] offset:128\n\tv_cvt_pk_bf16_f32 v192, v221, v207\n\tv_mfma_f32_16x16x32_bf16 v[226:229], v[168:171], v[16:19], 0\n\tds_write_b16 %[tb], %[t9a] offset:388\n\tv_cvt_pk_bf16_f32 v193, v220, v206\n\tv_mfma_f32_16x16x32_bf16 v[148:151], v[20:23], v[168:171], 0\n\tds_write_b16_d16_hi %[tb], %[t9a] offset:516\n\tds_read_b128 v[168:171], %[bb] offset:896\n\tv_mfma_f32_16x16x32_bf16 v[136:139], v[52:55], v[172:175], v[136:139]\n\tds_read_b64 v[92:93], %[tp] offset:0\n\tds_read_b64 v[94:95], %[tp] offset:8\n\tds_read_b64 v[96:97], %[tp] offset:64\n\tv_mfma_f32_16x16x32_bf16 v[140:143], v[84:87], v[172:175], v[140:143]\n\tds_read_b128 v[172:175], %[bb] offset:960\n\ts_waitcnt lgkmcnt(7)\n\ts_nop 0\n\tv_mfma_f32_16x16x32_bf16 v[136:139], v[48:51], v[176:179], v[136:139]\n\tds_read_b64 v[98:99], %[tp] offset:72\n\tds_read_b64 v[100:101], %[tp] offset:128\n\tds_read_b64 v[102:103], %[tp] offset:136\n\tv_mfma_f32_16x16x32_bf16 v[140:143], v[80:83], v[176:179], v[140:143]\n\tds_read_b128 v[176:179], %[bb] offset:1024\n\tds_read_b64 v[104:105], %[tp] offset:768\n\tds_read_b64 v[106:107], %[tp] offset:776\n\tv_mfma_f32_16x16x32_bf16 v[136:139], v[44:47], v[180:183], v[136:139]\n\tds_read_b64 v[108:109], %[tp] offset:832\n\tds_read_b64 v[110:111], %[tp] offset:840\n\tds_read_b64 v[112:113], %[tp] offset:896\n\tv_mfma_f32_16x16x32_bf16 v[140:143], v[76:79], v[180:183], v[140:143]\n\tds_read_b64 v[114:115], %[tp] offset:904\n\tds_read_b64 v[116:117], %[tp] offset:1536\n\tds_read_b64 v[118:119], %[tp] offset:1544\n\tv_mfma_f32_16x16x32_bf16 v[136:139], v[40:43], v[184:187], v[136:139]\n\tds_read_b64 v[120:121], %[tp] offset:1600\n\tds_read_b64 v[122:123], %[tp] offset:1608\n\tv_cvt_pk_bf16_f32 v194, v219, v205\n\tv_mfma_f32_16x16x32_bf16 v[140:143], v[72:75], v[184:187], v[140:143]\n\tv_cvt_pk_bf16_f32 v195, v218, v204\n\tds_read_b64 v[124:125], %[tp] offset:1664\n\tds_read_b64 v[126:127], %[tp] offset:1672\n\tv_mfma_f32_16x16x32_bf16 v[136:139], v[36:39], v[164:167], v[136:139]\n\tds_read_b64 v[128:129], %[tp] offset:1728\n\tds_read_b64 v[130:131], %[tp] offset:1736\n\tv_cvt_pk_bf16_f32 v196, v225, v211\n\tv_mfma_f32_16x16x32_bf16 v[140:143], v[68:71], v[164:167], v[140:143]\n\tv_cvt_pk_bf16_f32 v197, v224, v210\n\ts_waitcnt lgkmcnt(6)\n\ts_nop 0\n\tv_mfma_f32_16x16x32_bf16 v[136:139], v[32:35], v[168:171], v[136:139]\n\tv_cvt_pk_bf16_f32 v198, v223, v209\n\tv_mfma_f32_16x16x32_bf16 v[140:143], v[64:67], v[168:171], v[140:143]\n\tv_cvt_pk_bf16_f32 v199, v222, v208\n\tv_mfma_f32_16x16x32_bf16 v[136:139], v[28:31], v[172:175], v[136:139]\n\tv_cvt_pk_bf16_f32 v200, v229, v215\n\tv_mfma_f32_16x16x32_bf16 v[140:143], v[60:63], v[172:175], v[140:143]\n\tds_read_b128 v[188:191], %[bb] offset:1008\n\tv_cvt_pk_bf16_f32 v201, v228, v214\n\tv_mfma_f32_16x16x32_bf16 v[136:139], v[24:27], v[176:179], v[136:139]\n\tv_cvt_pk_bf16_f32 v202, v227, v213\n\tv_mfma_f32_16x16x32_bf16 v[140:143], v[56:59], v[176:179], v[140:143]\n\tv_cvt_pk_bf16_f32 v203, v226, v212\n\tv_mfma_f32_16x16x32_bf16 v[144:147], v[88:91], v[176:179], 0\n\tv_mfma_f32_16x16x32_bf16 v[140:143], v[192:195], v[92:95], v[140:143]\n\tv_mfma_f32_16x16x32_bf16 v[144:147], v[192:195], v[96:99], v[144:147]\n\tv_mfma_f32_16x16x32_bf16 v[148:151], v[192:195], v[100:103], v[148:151]\n\tv_mfma_f32_16x16x32_bf16 v[140:143], v[196:199], v[104:107], v[140:143]\n\tds_write_b128 %[yb], v[136:139] offset:1152\n\tds_read_b128 v[180:183], %[bb] offset:1088\n\tv_mfma_f32_16x16x32_bf16 v[144:147], v[196:199], v[108:111], v[144:147]\n\tv_mfma_f32_16x16x32_bf16 v[148:151], v[196:199], v[112:115], v[148:151]\n\tds_write_b8 %[ctc], %[one] offset:64\n\tds_read_b128 v[184:187], %[bb] offset:1152\n\tv_mfma_f32_16x16x32_bf16 v[148:151], v[200:203], v[116:119], v[148:151]\n\ts_waitcnt lgkmcnt(4)\n\ts_nop 0\n\tv_mfma_f32_16x16x32_bf16 v[152:155], v[200:203], v[120:123], 0\n\tds_read_b128 v[164:167], %[bb] offset:1216\n\tv_mfma_f32_16x16x32_bf16 v[156:159], v[200:203], v[124:127], 0\n\tv_mfma_f32_16x16x32_bf16 v[160:163], v[200:203], v[128:131], 0\n\tds_read_b128 v[168:171], %[bb] offset:1280\n\tv_mfma_f32_16x16x32_bf16 v[204:207], v[188:191], v[4:7], 0\n\tv_mfma_f32_16x16x32_bf16 v[218:221], v[176:179], v[4:7], 0\n\tds_read_b128 v[172:175], %[bb] offset:1344\n\tv_mfma_f32_16x16x32_bf16 v[208:211], v[188:191], v[8:11], 0\n\tv_mfma_f32_16x16x32_bf16 v[222:225], v[176:179], v[8:11], 0\n\tv_mfma_f32_16x16x32_bf16 v[212:215], v[188:191], v[12:15], 0\n\tv_mfma_f32_16x16x32_bf16 v[226:229], v[176:179], v[16:19], 0\n\tv_mfma_f32_16x16x32_bf16 v[152:155], v[20:23], v[176:179], v[152:155]\n\ts_waitcnt lgkmcnt(0)\n\ts_nop 0\n\tv_mfma_f32_16x16x32_bf16 v[140:143], v[52:55], v[180:183], v[140:143]\n\tds_read_b128 v[176:179], %[bb] offset:1408\n\tv_cvt_pk_bf16_f32 v192, v221, v207\n\tv_mfma_f32_16x16x32_bf16 v[144:147], v[84:87], v[180:183], v[144:147]\n\tds_read_b128 v[180:183], %[bb] offset:1472\n\tv_cvt_pk_bf16_f32 v193, v220, v206\n\tv_mfma_f32_16x16x32_bf16 v[140:143], v[48:51], v[184:187], v[140:143]\n\tv_cvt_pk_bf16_f32 v194, v219, v205\n\tv_mfma_f32_16x16x32_bf16 v[144:147], v[80:83], v[184:187], v[144:147]\n\tds_read_b128 v[184:187], %[bb] offset:1536\n\tv_cvt_pk_bf16_f32 v195, v218, v204\n\tv_mfma_f32_16x16x32_bf16 v[140:143], v[44:47], v[164:167], v[140:143]\n\tv_cvt_pk_bf16_f32 v196, v225, v211\n\tv_mfma_f32_16x16x32_bf16 v[144:147], v[76:79], v[164:167], v[144:147]\n\tv_cvt_pk_bf16_f32 v197, v224, v210\n\tv_mfma_f32_16x16x32_bf16 v[140:143], v[40:43], v[168:171], v[140:143]\n\tv_cvt_pk_bf16_f32 v198, v223, v209\n\tv_mfma_f32_16x16x32_bf16 v[144:147], v[72:75], v[168:171], v[144:147]\n\tv_cvt_pk_bf16_f32 v199, v222, v208\n\tv_mfma_f32_16x16x32_bf16 v[140:143], v[36:39], v[172:175], v[140:143]\n\tv_cvt_pk_bf16_f32 v200, v229, v215\n\tv_mfma_f32_16x16x32_bf16 v[144:147], v[68:71], v[172:175], v[144:147]\n\ts_waitcnt lgkmcnt(0)\n\ts_nop 0\n\tv_mfma_f32_16x16x32_bf16 v[140:143], v[32:35], v[176:179], v[140:143]\n\tv_cvt_pk_bf16_f32 v201, v228, v214\n\tv_mfma_f32_16x16x32_bf16 v[144:147], v[64:67], v[176:179], v[144:147]\n\tv_cvt_pk_bf16_f32 v202, v227, v213\n\tv_mfma_f32_16x16x32_bf16 v[140:143], v[28:31], v[180:183], v[140:143]\n\tds_read_b128 v[188:191], %[bb] offset:1520\n\tv_cvt_pk_bf16_f32 v203, v226, v212\n\tv_mfma_f32_16x16x32_bf16 v[144:147], v[60:63], v[180:183], v[144:147]\n\tv_mfma_f32_16x16x32_bf16 v[140:143], v[24:27], v[184:187], v[140:143]\n\tv_mfma_f32_16x16x32_bf16 v[144:147], v[56:59], v[184:187], v[144:147]\n\tv_mfma_f32_16x16x32_bf16 v[148:151], v[88:91], v[184:187], v[148:151]\n\tv_mfma_f32_16x16x32_bf16 v[144:147], v[192:195], v[92:95], v[144:147]\n\tv_mfma_f32_16x16x32_bf16 v[148:151], v[192:195], v[96:99], v[148:151]\n\tv_mfma_f32_16x16x32_bf16 v[152:155], v[192:195], v[100:103], v[152:155]\n\tds_read_b128 v[164:167], %[bb] offset:1600\n\tv_mfma_f32_16x16x32_bf16 v[144:147], v[196:199], v[104:107], v[144:147]\n\tv_mfma_f32_16x16x32_bf16 v[148:151], v[196:199], v[108:111], v[148:151]\n\tds_write_b128 %[yb], v[140:143] offset:2304\n\tv_mfma_f32_16x16x32_bf16 v[152:155], v[196:199], v[112:115], v[152:155]\n\tds_read_b128 v[168:171], %[bb] offset:1664\n\tv_mfma_f32_16x16x32_bf16 v[152:155], v[200:203], v[116:119], v[152:155]\n\tds_write_b8 %[ctc], %[one] offset:128\n\tds_read_b128 v[172:175], %[bb] offset:1728\n\tv_mfma_f32_16x16x32_bf16 v[156:159], v[200:203], v[120:123], v[156:159]\n\tv_mfma_f32_16x16x32_bf16 v[160:163], v[200:203], v[124:127], v[160:163]\n\ts_waitcnt lgkmcnt(3)\n\ts_nop 0\n\tv_mfma_f32_16x16x32_bf16 v[204:207], v[188:191], v[4:7], 0\n\tds_read_b128 v[176:179], %[bb] offset:1792\n\tv_mfma_f32_16x16x32_bf16 v[218:221], v[184:187], v[4:7], 0\n\tds_read_b128 v[180:183], %[bb] offset:1856\n\tv_mfma_f32_16x16x32_bf16 v[208:211], v[188:191], v[8:11], 0\n\tv_mfma_f32_16x16x32_bf16 v[222:225], v[184:187], v[8:11], 0\n\tv_mfma_f32_16x16x32_bf16 v[212:215], v[188:191], v[12:15], 0\n\tv_mfma_f32_16x16x32_bf16 v[226:229], v[184:187], v[16:19], 0\n\tv_mfma_f32_16x16x32_bf16 v[156:159], v[20:23], v[184:187], v[156:159]\n\tds_read_b128 v[184:187], %[bb] offset:1920\n\tv_mfma_f32_16x16x32_bf16 v[144:147], v[52:55], v[164:167], v[144:147]\n\tv_cvt_pk_bf16_f32 v192, v221, v207\n\tv_mfma_f32_16x16x32_bf16 v[148:151], v[84:87], v[164:167], v[148:151]\n\ts_waitcnt lgkmcnt(1)\n\ts_nop 0\n\tv_mfma_f32_16x16x32_bf16 v[144:147], v[48:51], v[168:171], v[144:147]\n\tds_read_b128 v[164:167], %[bb] offset:1984\n\tv_cvt_pk_bf16_f32 v193, v220, v206\n\tv_mfma_f32_16x16x32_bf16 v[148:151], v[80:83], v[168:171], v[148:151]\n\tds_read_b128 v[168:171], %[bb] offset:2048\n\tv_cvt_pk_bf16_f32 v194, v219, v205\n\tv_mfma_f32_16x16x32_bf16 v[144:147], v[44:47], v[172:175], v[144:147]\n\tds_read_b128 v[188:191], %[bb] offset:2032\n\tv_cvt_pk_bf16_f32 v195, v218, v204\n\tv_mfma_f32_16x16x32_bf16 v[148:151], v[76:79], v[172:175], v[148:151]\n\tv_cvt_pk_bf16_f32 v196, v225, v211\n\tv_mfma_f32_16x16x32_bf16 v[144:147], v[40:43], v[176:179], v[144:147]\n\tv_cvt_pk_bf16_f32 v197, v224, v210\n\tv_mfma_f32_16x16x32_bf16 v[148:151], v[72:75], v[176:179], v[148:151]\n\tv_cvt_pk_bf16_f32 v198, v223, v209\n\tv_mfma_f32_16x16x32_bf16 v[144:147], v[36:39], v[180:183], v[144:147]\n\tv_cvt_pk_bf16_f32 v199, v222, v208\n\tv_mfma_f32_16x16x32_bf16 v[148:151], v[68:71], v[180:183], v[148:151]\n\ts_waitcnt lgkmcnt(0)\n\ts_nop 0\n\tv_mfma_f32_16x16x32_bf16 v[144:147], v[32:35], v[184:187], v[144:147]\n\tv_cvt_pk_bf16_f32 v200, v229, v215\n\tv_mfma_f32_16x16x32_bf16 v[148:151], v[64:67], v[184:187], v[148:151]\n\tv_cvt_pk_bf16_f32 v201, v228, v214\n\tv_mfma_f32_16x16x32_bf16 v[144:147], v[28:31], v[164:167], v[144:147]\n\tv_cvt_pk_bf16_f32 v202, v227, v213\n\tv_mfma_f32_16x16x32_bf16 v[148:151], v[60:63], v[164:167], v[148:151]\n\tv_cvt_pk_bf16_f32 v203, v226, v212\n\tv_mfma_f32_16x16x32_bf16 v[144:147], v[24:27], v[168:171], v[144:147]\n\tv_mfma_f32_16x16x32_bf16 v[148:151], v[56:59], v[168:171], v[148:151]\n\tv_mfma_f32_16x16x32_bf16 v[152:155], v[88:91], v[168:171], v[152:155]\n\tv_mfma_f32_16x16x32_bf16 v[204:207], v[188:191], v[4:7], 0\n\tv_mfma_f32_16x16x32_bf16 v[218:221], v[168:171], v[4:7], 0\n\tv_mfma_f32_16x16x32_bf16 v[208:211], v[188:191], v[8:11], 0\n\tv_mfma_f32_16x16x32_bf16 v[222:225], v[168:171], v[8:11], 0\n\tv_mfma_f32_16x16x32_bf16 v[212:215], v[188:191], v[12:15], 0\n\tv_mfma_f32_16x16x32_bf16 v[226:229], v[168:171], v[16:19], 0\n\tds_write_b128 %[yb], v[144:147] offset:3456\n\tv_mfma_f32_16x16x32_bf16 v[160:163], v[20:23], v[168:171], v[160:163]\n\tds_read_b32 v216, %[ctl] offset:20\n\tv_mfma_f32_16x16x32_bf16 v[148:151], v[192:195], v[92:95], v[148:151]\n\tds_write_b8 %[ctc], %[one] offset:192\n\tv_mfma_f32_16x16x32_bf16 v[152:155], v[192:195], v[96:99], v[152:155]\n\tv_mfma_f32_16x16x32_bf16 v[156:159], v[192:195], v[100:103], v[156:159]\n\tv_mfma_f32_16x16x32_bf16 v[148:151], v[196:199], v[104:107], v[148:151]\n\tv_cvt_pk_bf16_f32 v192, v221, v207\n\tv_mfma_f32_16x16x32_bf16 v[152:155], v[196:199], v[108:111], v[152:155]\n\tv_cvt_pk_bf16_f32 v193, v220, v206\n\tv_mfma_f32_16x16x32_bf16 v[156:159], v[196:199], v[112:115], v[156:159]\n\tv_cvt_pk_bf16_f32 v194, v219, v205\n\tv_mfma_f32_16x16x32_bf16 v[156:159], v[200:203], v[116:119], v[156:159]\n\tv_cvt_pk_bf16_f32 v195, v218, v204\n\tv_mfma_f32_16x16x32_bf16 v[160:163], v[200:203], v[120:123], v[160:163]\n\ts_waitcnt lgkmcnt(0)\n\ts_nop 0\n\tv_readfirstlane_b32 %[s], v216\n\ts_mov_b32 %[c], 0x400000\n\ts_cmp_ge_u32 %[s], 2\n\ts_cbranch_scc1 2f\n\t1:\n\tds_read_b32 v216, %[ctl] offset:20\n\ts_waitcnt lgkmcnt(0)\n\tv_readfirstlane_b32 %[s], v216\n\ts_cmp_ge_u32 %[s], 2\n\ts_cbranch_scc1 2f\n\ts_sleep 1\n\ts_sub_u32 %[c], %[c], 1\n\ts_cbranch_scc0 1b\n\t.p2align 3\n\t2:\n\tv_mfma_f32_16x16x32_bf16 v[152:155], v[192:195], v[92:95], v[152:155]\n\tds_read_b128 v[172:175], %[bb] offset:2112\n\tv_cvt_pk_bf16_f32 v196, v225, v211\n\tv_mfma_f32_16x16x32_bf16 v[156:159], v[192:195], v[96:99], v[156:159]\n\tds_read_b128 v[176:179], %[bb] offset:2176\n\tv_cvt_pk_bf16_f32 v197, v224, v210\n\tv_mfma_f32_16x16x32_bf16 v[160:163], v[192:195], v[100:103], v[160:163]\n\tv_cvt_pk_bf16_f32 v198, v223, v209\n\tds_read_b128 v[180:183], %[bb] offset:2240\n\tv_cvt_pk_bf16_f32 v199, v222, v208\n\ts_nop 1\n\ts_nop 0\n\tv_mfma_f32_16x16x32_bf16 v[152:155], v[196:199], v[104:107], v[152:155]\n\tds_read_b128 v[184:187], %[bb] offset:2304\n\tv_cvt_pk_bf16_f32 v200, v229, v215\n\tv_mfma_f32_16x16x32_bf16 v[156:159], v[196:199], v[108:111], v[156:159]\n\tv_cvt_pk_bf16_f32 v201, v228, v214\n\tds_read_b128 v[164:167], %[bb] offset:2368\n\tv_mfma_f32_16x16x32_bf16 v[160:163], v[196:199], v[112:115], v[160:163]\n\tv_cvt_pk_bf16_f32 v202, v227, v213\n\tds_read_b128 v[168:171], %[bb] offset:2432\n\tv_cvt_pk_bf16_f32 v203, v226, v212\n\ts_nop 1\n\ts_nop 0\n\tv_mfma_f32_16x16x32_bf16 v[160:163], v[200:203], v[116:119], v[160:163]\n\ts_waitcnt lgkmcnt(3)\n\ts_nop 0\n\tv_mfma_f32_16x16x32_bf16 v[148:151], v[52:55], v[172:175], v[148:151]\n\tv_mfma_f32_16x16x32_bf16 v[152:155], v[84:87], v[172:175], v[152:155]\n\tds_read_b128 v[172:175], %[bb] offset:2496\n\tv_mfma_f32_16x16x32_bf16 v[148:151], v[48:51], v[176:179], v[148:151]\n\tv_mfma_f32_16x16x32_bf16 v[152:155], v[80:83], v[176:179], v[152:155]\n\tds_read_b128 v[176:179], %[bb] offset:2560\n\tv_mfma_f32_16x16x32_bf16 v[148:151], v[44:47], v[180:183], v[148:151]\n\tds_read_b128 v[188:191], %[bb] offset:2544\n\tv_mfma_f32_16x16x32_bf16 v[152:155], v[76:79], v[180:183], v[152:155]\n\ts_waitcnt lgkmcnt(2)\n\ts_nop 0\n\tv_mfma_f32_16x16x32_bf16 v[148:151], v[40:43], v[184:187], v[148:151]\n\tv_mfma_f32_16x16x32_bf16 v[152:155], v[72:75], v[184:187], v[152:155]\n\tds_read_b128 v[180:183], %[bb] offset:2624\n\tv_mfma_f32_16x16x32_bf16 v[148:151], v[36:39], v[164:167], v[148:151]\n\tv_mfma_f32_16x16x32_bf16 v[152:155], v[68:71], v[164:167], v[152:155]\n\tds_read_b128 v[184:187], %[bb] offset:2688\n\tv_mfma_f32_16x16x32_bf16 v[148:151], v[32:35], v[168:171], v[148:151]\n\tv_mfma_f32_16x16x32_bf16 v[152:155], v[64:67], v[168:171], v[152:155]\n\tds_read_b128 v[164:167], %[bb] offset:2752\n\tv_mfma_f32_16x16x32_bf16 v[148:151], v[28:31], v[172:175], v[148:151]\n\tv_mfma_f32_16x16x32_bf16 v[152:155], v[60:63], v[172:175], v[152:155]\n\ts_waitcnt lgkmcnt(1)\n\ts_nop 0\n\tv_mfma_f32_16x16x32_bf16 v[148:151], v[24:27], v[176:179], v[148:151]\n\tds_read_b128 v[168:171], %[bb] offset:2816\n\tv_mfma_f32_16x16x32_bf16 v[152:155], v[56:59], v[176:179], v[152:155]\n\tds_read_b128 v[172:175], %[bb] offset:2880\n\tv_mfma_f32_16x16x32_bf16 v[156:159], v[88:91], v[176:179], v[156:159]\n\tv_mfma_f32_16x16x32_bf16 v[204:207], v[188:191], v[4:7], 0\n\tv_mfma_f32_16x16x32_bf16 v[218:221], v[176:179], v[4:7], 0\n\tv_mfma_f32_16x16x32_bf16 v[208:211], v[188:191], v[8:11], 0\n\tv_mfma_f32_16x16x32_bf16 v[222:225], v[176:179], v[8:11], 0\n\tds_write_b128 %[yb], v[148:151] offset:4608\n\tv_mfma_f32_16x16x32_bf16 v[152:155], v[52:55], v[180:183], v[152:155]\n\tds_read_b128 v[176:179], %[bb] offset:2944\n\tv_mfma_f32_16x16x32_bf16 v[156:159], v[84:87], v[180:183], v[156:159]\n\tds_write_b8 %[ctc], %[one] offset:256\n\tds_read_b128 v[180:183], %[bb] offset:3008\n\tv_mfma_f32_16x16x32_bf16 v[152:155], v[48:51], v[184:187], v[152:155]\n\tv_cvt_pk_bf16_f32 v192, v221, v207\n\tv_mfma_f32_16x16x32_bf16 v[156:159], v[80:83], v[184:187], v[156:159]\n\ts_waitcnt lgkmcnt(3)\n\ts_nop 0\n\tv_mfma_f32_16x16x32_bf16 v[152:155], v[44:47], v[164:167], v[152:155]\n\tds_read_b128 v[184:187], %[bb] offset:3072\n\tv_cvt_pk_bf16_f32 v193, v220, v206\n\tv_mfma_f32_16x16x32_bf16 v[156:159], v[76:79], v[164:167], v[156:159]\n\tv_cvt_pk_bf16_f32 v194, v219, v205\n\tv_mfma_f32_16x16x32_bf16 v[152:155], v[40:43], v[168:171], v[152:155]\n\tv_cvt_pk_bf16_f32 v195, v218, v204\n\tv_mfma_f32_16x16x32_bf16 v[156:159], v[72:75], v[168:171], v[156:159]\n\tds_read_b128 v[188:191], %[bb] offset:3056\n\tv_cvt_pk_bf16_f32 v196, v225, v211\n\tv_mfma_f32_16x16x32_bf16 v[152:155], v[36:39], v[172:175], v[152:155]\n\tv_cvt_pk_bf16_f32 v197, v224, v210\n\tv_mfma_f32_16x16x32_bf16 v[156:159], v[68:71], v[172:175], v[156:159]\n\ts_waitcnt lgkmcnt(1)\n\ts_nop 0\n\tv_mfma_f32_16x16x32_bf16 v[152:155], v[32:35], v[176:179], v[152:155]\n\tds_read_b32 v216, %[ctl] offset:24\n\tv_cvt_pk_bf16_f32 v198, v223, v209\n\tv_mfma_f32_16x16x32_bf16 v[156:159], v[64:67], v[176:179], v[156:159]\n\tv_cvt_pk_bf16_f32 v199, v222, v208\n\tv_mfma_f32_16x16x32_bf16 v[152:155], v[28:31], v[180:183], v[152:155]\n\tv_mfma_f32_16x16x32_bf16 v[156:159], v[60:63], v[180:183], v[156:159]\n\tv_mfma_f32_16x16x32_bf16 v[152:155], v[24:27], v[184:187], v[152:155]\n\tv_mfma_f32_16x16x32_bf16 v[156:159], v[56:59], v[184:187], v[156:159]\n\tv_mfma_f32_16x16x32_bf16 v[160:163], v[88:91], v[184:187], v[160:163]\n\tv_mfma_f32_16x16x32_bf16 v[156:159], v[192:195], v[92:95], v[156:159]\n\ts_waitcnt lgkmcnt(0)\n\ts_nop 0\n\tv_readfirstlane_b32 %[s], v216\n\ts_mov_b32 %[c], 0x400000\n\ts_cmp_ge_u32 %[s], 2\n\ts_cbranch_scc1 2f\n\t1:\n\tds_read_b32 v216, %[ctl] offset:24\n\ts_waitcnt lgkmcnt(0)\n\tv_readfirstlane_b32 %[s], v216\n\ts_cmp_ge_u32 %[s], 2\n\ts_cbranch_scc1 2f\n\ts_sleep 1\n\ts_sub_u32 %[c], %[c], 1\n\ts_cbranch_scc0 1b\n\t.p2align 3\n\t2:\n\tv_mfma_f32_16x16x32_bf16 v[160:163], v[192:195], v[96:99], v[160:163]\n\tds_write_b128 %[yb], v[152:155] offset:5760\n\tv_mfma_f32_16x16x32_bf16 v[156:159], v[196:199], v[104:107], v[156:159]\n\tds_read_b128 v[164:167], %[bb] offset:3136\n\tv_mfma_f32_16x16x32_bf16 v[160:163], v[196:199], v[108:111], v[160:163]\n\tds_write_b8 %[ctc], %[one] offset:320\n\tds_read_b128 v[168:171], %[bb] offset:3200\n\tv_mfma_f32_16x16x32_bf16 v[204:207], v[188:191], v[4:7], 0\n\tds_read_b128 v[172:175], %[bb] offset:3264\n\tv_mfma_f32_16x16x32_bf16 v[218:221], v[184:187], v[4:7], 0\n\tds_read_b128 v[176:179], %[bb] offset:3328\n\tv_mfma_f32_16x16x32_bf16 v[208:211], v[188:191], v[8:11], 0\n\tds_read_b128 v[180:183], %[bb] offset:3392\n\tv_mfma_f32_16x16x32_bf16 v[222:225], v[184:187], v[8:11], 0\n\ts_waitcnt lgkmcnt(3)\n\ts_nop 0\n\tv_mfma_f32_16x16x32_bf16 v[156:159], v[52:55], v[164:167], v[156:159]\n\tds_read_b128 v[184:187], %[bb] offset:3456\n\tv_cvt_pk_bf16_f32 v192, v221, v207\n\tv_mfma_f32_16x16x32_bf16 v[160:163], v[84:87], v[164:167], v[160:163]\n\tds_read_b128 v[164:167], %[bb] offset:3520\n\tv_cvt_pk_bf16_f32 v193, v220, v206\n\tv_mfma_f32_16x16x32_bf16 v[156:159], v[48:51], v[168:171], v[156:159]\n\tv_cvt_pk_bf16_f32 v194, v219, v205\n\tv_mfma_f32_16x16x32_bf16 v[160:163], v[80:83], v[168:171], v[160:163]\n\ts_waitcnt lgkmcnt(2)\n\ts_nop 0\n\tv_mfma_f32_16x16x32_bf16 v[156:159], v[44:47], v[172:175], v[156:159]\n\tds_read_b128 v[168:171], %[bb] offset:3584\n\tv_cvt_pk_bf16_f32 v195, v218, v204\n\tv_mfma_f32_16x16x32_bf16 v[160:163], v[76:79], v[172:175], v[160:163]\n\tds_read_b128 v[172:175], %[bb] offset:3648\n\tv_cvt_pk_bf16_f32 v196, v225, v211\n\tv_mfma_f32_16x16x32_bf16 v[156:159], v[40:43], v[176:179], v[156:159]\n\tglobal_load_dwordx2 v[240:241], %[so], %[ws] sc1\n\tv_cvt_pk_bf16_f32 v197, v224, v210\n\tv_mfma_f32_16x16x32_bf16 v[160:163], v[72:75], v[176:179], v[160:163]\n\tds_read_b128 v[176:179], %[bb] offset:3712\n\tv_cvt_pk_bf16_f32 v198, v223, v209\n\tv_mfma_f32_16x16x32_bf16 v[156:159], v[36:39], v[180:183], v[156:159]\n\tv_cvt_pk_bf16_f32 v199, v222, v208\n\tv_mfma_f32_16x16x32_bf16 v[160:163], v[68:71], v[180:183], v[160:163]\n\ts_waitcnt lgkmcnt(1)\n\ts_nop 0\n\tv_mfma_f32_16x16x32_bf16 v[156:159], v[32:35], v[184:187], v[156:159]\n\tds_read_b128 v[180:183], %[bb] offset:3776\n\tv_mfma_f32_16x16x32_bf16 v[160:163], v[64:67], v[184:187], v[160:163]\n\tds_read_b128 v[184:187], %[bb] offset:3840\n\tv_mfma_f32_16x16x32_bf16 v[156:159], v[28:31], v[164:167], v[156:159]\n\tv_mfma_f32_16x16x32_bf16 v[160:163], v[60:63], v[164:167], v[160:163]\n\tds_read_b128 v[164:167], %[bb] offset:3904\n\tv_mfma_f32_16x16x32_bf16 v[156:159], v[24:27], v[168:171], v[156:159]\n\tv_mfma_f32_16x16x32_bf16 v[160:163], v[56:59], v[168:171], v[160:163]\n\tds_read_b128 v[168:171], %[bb] offset:3968\n\tv_mfma_f32_16x16x32_bf16 v[160:163], v[192:195], v[92:95], v[160:163]\n\tv_mfma_f32_16x16x32_bf16 v[160:163], v[196:199], v[104:107], v[160:163]\n\tv_mfma_f32_16x16x32_bf16 v[160:163], v[52:55], v[172:175], v[160:163]\n\ts_waitcnt lgkmcnt(1)\n\ts_nop 0\n\tv_mfma_f32_16x16x32_bf16 v[160:163], v[48:51], v[176:179], v[160:163]\n\tds_write_b128 %[yb], v[156:159] offset:6912\n\tv_mfma_f32_16x16x32_bf16 v[160:163], v[44:47], v[180:183], v[160:163]\n\tds_read_b128 v[172:175], %[bb] offset:4032\n\tv_mfma_f32_16x16x32_bf16 v[160:163], v[40:43], v[184:187], v[160:163]\n\tds_write_b8 %[ctc], %[one] offset:384\n\tds_read_b128 v[176:179], %[bb] offset:4096\n\tv_mfma_f32_16x16x32_bf16 v[160:163], v[36:39], v[164:167], v[160:163]\n\ts_waitcnt lgkmcnt(4)\n\ts_nop 0\n\tv_mfma_f32_16x16x32_bf16 v[160:163], v[32:35], v[168:171], v[160:163]\n\ts_waitcnt lgkmcnt(2)\n\ts_nop 0\n\tv_mfma_f32_16x16x32_bf16 v[160:163], v[28:31], v[172:175], v[160:163]\n\ts_waitcnt lgkmcnt(0)\n\ts_nop 0\n\tv_mfma_f32_16x16x32_bf16 v[160:163], v[24:27], v[176:179], v[160:163]\n\ts_nop 7\n\tds_write_b128 %[yb], v[160:163] offset:8064\n\tds_write_b8 %[ctc], %[one] offset:448"
#define CONV_CLOBBER "v24", "v25", "v26", "v27", "v28", "v29", "v30", "v31", "v32", "v33", "v34", "v35", "v36", "v37", "v38", "v39", "v40", "v41", "v42", "v43", "v44", "v45", "v46", "v47", "v48", "v49", "v50", "v51", "v52", "v53", "v54", "v55", "v56", "v57", "v58", "v59", "v60", "v61", "v62", "v63", "v64", "v65", "v66", "v67", "v68", "v69", "v70", "v71", "v72", "v73", "v74", "v75", "v76", "v77", "v78", "v79", "v80", "v81", "v82", "v83", "v84", "v85", "v86", "v87", "v88", "v89", "v90", "v91", "v92", "v93", "v94", "v95", "v96", "v97", "v98", "v99", "v100", "v101", "v102", "v103", "v104", "v105", "v106", "v107", "v108", "v109", "v110", "v111", "v112", "v113", "v114", "v115", "v116", "v117", "v118", "v119", "v120", "v121", "v122", "v123", "v124", "v125", "v126", "v127", "v128", "v129", "v130", "v131", "v132", "v133", "v134", "v135", "v136", "v137", "v138", "v139", "v140", "v141", "v142", "v143", "v144", "v145", "v146", "v147", "v148", "v149", "v150", "v151", "v152", "v153", "v154", "v155", "v156", "v157", "v158", "v159", "v160", "v161", "v162", "v163", "v164", "v165", "v166", "v167", "v168", "v169", "v170", "v171", "v172", "v173", "v174", "v175", "v176", "v177", "v178", "v179", "v180", "v181", "v182", "v183", "v184", "v185", "v186", "v187", "v188", "v189", "v190", "v191", "v192", "v193", "v194", "v195", "v196", "v197", "v198", "v199", "v200", "v201", "v202", "v203", "v204", "v205", "v206", "v207", "v208", "v209", "v210", "v211", "v212", "v213", "v214", "v215", "v216", "v217", "v218", "v219", "v220", "v221", "v222", "v223", "v224", "v225", "v226", "v227", "v228", "v229"

typedef __attribute__((address_space(4))) const Params* KArg;
#define ROLE_BEGIN(N) int b, tid; \
    asm volatile("fk_role" #N ":\n\tv_mov_b32 %0, v0" : "=&v"(tid) : "{s2}"(b0), "{v0}"(tid0) : "memory"); \
    asm volatile("s_mov_b32 %0, s2" : "=&s"(b) : "{s2}"(b0)); \
    const int lane = tid & 63, wave = __builtin_amdgcn_readfirstlane(tid >> 6), xcd = b & 7, slot = b >> 3; \
    const int cw = slot < 16 ? xcd * 16 + slot : 112 + xcd * 2 + slot, chbase = cw * 4, w = xcd * 9 + (slot - 18); (void)chbase; (void)w; (void)lane
#define ROLE_ARGS() Params p; SBAR(); \
    asm volatile("s_waitcnt lgkmcnt(0)" : "={s[56:57]}"(p.x), "={s[58:59]}"(p.Are), "={s[60:61]}"(p.Aim), "={s[62:63]}"(p.Bre), "={s[64:65]}"(p.Bim), "={s[66:67]}"(p.Cre), "={s[68:69]}"(p.Cim), "={s[70:71]}"(p.logdt), \
                 "={s[72:73]}"(p.dskip), "={s[74:75]}"(p.wr), "={s[76:77]}"(p.wg), "={s[78:79]}"(p.wu), "={s[80:81]}"(p.wd), "={s[82:83]}"(p.gw), "={s[84:85]}"(p.out), "={s[86:87]}"(p.ws))
#define ROLE_END() do { __builtin_amdgcn_s_waitcnt(0x0070); asm volatile("s_endpgm" ::: "memory"); } while (0)
#define MF(A_, B_, C_) __builtin_amdgcn_mfma_f32_16x16x32_bf16(A_, B_, C_, 0, 0, 0)

#define FFN_PRE() u32 gof = w * 128 + ((wave >> 1) * 8 + (lane >> 3)) * (F * 2) + ((lane & 7) ^ (((lane >> 3) & 2) | ((wave & 2) << 1))) * 16; asm volatile("" : "+v"(gof)); ROLE_ARGS()
#define FFN_FILL() \
    { const char* gsrc = (const char*)((wave & 1) ? p.wu : p.wg); \
      _Pragma("unroll") for (int i = 0; i < 18; ++i) { \
          asm volatile("s_mov_b32 m0, %2\n\ts_nop 0\n\tglobal_load_lds_dwordx4 %0, %1" WNT :: "v"(gof), "s"(gsrc + i * (32 * F * 2)), "s"((8 * i + wave) * 1024) : "memory"); \
          if (i == 5 || i == 11) __builtin_amdgcn_s_barrier(); } }
#define FFN_WDFRAG(k, pr) do { LAS char* t_ = ta + (pr) * 64; \
    _Pragma("unroll") for (int e_ = 0; e_ < 8; ++e_) { const int e = (e_ & 1) | ((e_ & 2) << 1) | ((e_ & 4) >> 1); a[k][e] = __builtin_amdgcn_ds_read_tr16_b64_v4i16((LAS s16x4*)(t_ + (e >> 2) * 32 + (e & 3) * 16 * WDP)); } SBAR(); } while (0)
#define FFN_DOWN(k, pr) do { v4f y_ = MF(__builtin_shufflevector(a[k][0], a[k][1], 0, 1, 2, 3, 4, 5, 6, 7), hf[0], ((v4f){0.f, 0.f, 0.f, 0.f})); \
    y_ = MF(__builtin_shufflevector(a[k][4], a[k][5], 0, 1, 2, 3, 4, 5, 6, 7), hf[1], y_); \
    y_ = MF(__builtin_shufflevector(a[k][2], a[k][3], 0, 1, 2, 3, 4, 5, 6, 7), hf[2], y_); \
    ya[k] = MF(__builtin_shufflevector(a[k][6], a[k][7], 0, 1, 2, 3, 4, 5, 6, 7), hf[3], y_); } while (0)
#define FFN_ADDR() const int n = lane & 15, q = lane >> 4; \
    LAS char* ta = smem + (lane >> 2) * WDP + (lane & 3) * 8; \
    LAS char* hrow = smem + FL_H + (n & 7) * 144 + q * 16; \
    LAS char* ha = n < 8 ? hrow : smem + FL_Z + q * 16; \
    LAS char* hb = n < 8 ? smem + FL_Z + q * 16 : hrow; \
    u32 obase = OFF_PART + (4 * (n >> 3) + q) * PP + hline(p.ws, w) + (n & 7) * 16; \
    asm volatile("" : "+v"(ta), "+v"(ha), "+v"(hb), "+v"(obase))
#define FFN_HFRAG() const bf16x8 hf[4] = {*(LAS bf16x8*)(ha), *(LAS bf16x8*)(hb), *(LAS bf16x8*)(ha + 64), *(LAS bf16x8*)(hb + 64)}
#define FFN_PUB(k, pr) __builtin_amdgcn_raw_buffer_store_b128((v4u){cvtpk(ya[k][0], ya[k][1]), tag, cvtpk(ya[k][2], ya[k][3]), tag}, WSR(), obase + (pr) * (8 * PP), 0, 16)

__global__ __launch_bounds__(512, 2) void fused_kernel(Params p_unused) {
    extern __shared__ __attribute__((aligned(16))) char smem_[];
    LAS char* smem = (LAS char*)smem_;
    const KArg kp0 = (KArg)__builtin_amdgcn_kernarg_segment_ptr();
    const int tid0 = threadIdx.x, b0 = blockIdx.x;
    asm volatile("s_load_dwordx16 s[56:71], s[0:1], 0x0\n\ts_load_dwordx16 s[72:87], s[0:1], 0x40\n\tv_readfirstlane_b32 s92, v0\n\t"
                 "s_cmp_ge_u32 s2, 32\n\ts_cbranch_scc1 1f\n\t"
                 "s_getpc_b64 s[90:91]\n\ts_and_b32 s90, s90, 0xffffff00\n\ts_and_b32 s93, s2, -8\n\ts_lshr_b32 s89, s92, 6\n\ts_add_u32 s93, s93, s89\n\ts_min_u32 s93, s93, %3\n\ts_lshl_b32 s93, s93, 10\n\t"
                 "v_and_b32 v1, 63, v0\n\tv_lshlrev_b32 v1, 4, v1\n\tv_add_u32 v1, s93, v1\n\ts_mov_b32 m0, %4\n\ts_nop 0\n\tglobal_load_lds_dwordx4 v1, s[90:91]\n"
                 "1:\n\ts_cmp_lt_u32 s2, 144\n\ts_cbranch_scc0 2f\n\ts_cmp_lt_u32 s92, 256\n\ts_cbranch_scc1 fk_role0\n\ts_branch fk_role1\n"
                 "2:\n\ts_cmp_lt_u32 s2, 216\n\ts_cbranch_scc0 3f\n\ts_cmp_lt_u32 s92, 256\n\ts_cbranch_scc1 fk_role2\n\ts_branch fk_role3\n"
                 "3:\n\ts_cmp_lt_u32 s2, 224\n\ts_cbranch_scc1 fk_role4\n\ts_branch fk_role5"
                 :: "{s[0:1]}"(kp0), "{s2}"(b0), "{v0}"(tid0), "n"(CODE_KB - 1), "n"(LDS_DUMMY) : "memory", "scc", "s89", "s90", "s91", "s92", "s93", "v1");
    const int slot = b0 >> 3, wave = __builtin_amdgcn_readfirstlane(tid0 >> 6);
    if (slot >= 18) {
    if (slot >= 27 || wave >= 4) {
    if (slot < 28) {
    if (slot == 27)
    {
        ROLE_BEGIN(4);
        ROLE_ARGS();
        u32 tagv;
        asm volatile("v_mov_b32 %0, 0\n\tglobal_load_dword %0, %0, %1 sc1\n\ts_waitcnt vmcnt(0)" : "=&v"(tagv) : "s"(p.ws) : "memory");
        const u32 tag = __builtin_amdgcn_readfirstlane(tagv) + 1u;
        if (tid < 128) ((LAS u32*)smem)[tid] = 0;
        u32 ga = (u32)(uintptr_t)(smem + (lane & 15) * 4), gv = (u32)(uintptr_t)(smem + (lane >> 2) * 4), gq_ = (u32)(uintptr_t)(smem + (lane & 3) * 16), zero = (u32)(uintptr_t)smem;
        u32 ro = OFF_RG + hline(p.ws, tid >> 3) + (tid & 7) * 16, ro2 = OFF_RG + hline(p.ws, 64 + (tid >> 3)) + (tid & 7) * 16, so = OFF_SEL + ((lane & 1) * 8 + xcd) * 256, xo = (lane & 3) * 128;
        asm volatile("" : "+v"(ga), "+v"(gv), "+v"(gq_), "+v"(zero), "+v"(ro), "+v"(ro2), "+v"(so), "+v"(xo));
        BAR();
        __builtin_amdgcn_s_sleep(SELWAIT);
        v4u qa, qb;
        u32 c_;
#define POLL_A "global_load_dwordx4 v[20:23], %[ro], %[ws] sc1\n\tglobal_load_dwordx4 v[24:27], %[r2], %[ws] sc1\n\t"
#define POLL_B "global_load_dwordx4 v[28:31], %[ro], %[ws] sc1\n\tglobal_load_dwordx4 v[32:35], %[r2], %[ws] sc1\n\t"
        asm volatile("s_mov_b32 %[c], 0x40000\n\t" POLL_A "s_sleep 8\n\t" POLL_B
                     "1:\n\ts_waitcnt vmcnt(2)\n\tv_cmpx_eq_u32 %[tag], v21\n\tv_cmpx_eq_u32 %[tag], v23\n\tv_cmpx_eq_u32 %[tag], v25\n\tv_cmpx_eq_u32 %[tag], v27\n\t"
                     "s_cmp_eq_u64 exec, -1\n\ts_mov_b64 exec, -1\n\ts_cbranch_scc1 3f\n\t" POLL_A
                     "s_waitcnt vmcnt(2)\n\tv_cmpx_eq_u32 %[tag], v29\n\tv_cmpx_eq_u32 %[tag], v31\n\tv_cmpx_eq_u32 %[tag], v33\n\tv_cmpx_eq_u32 %[tag], v35\n\t"
                     "s_cmp_eq_u64 exec, -1\n\ts_mov_b64 exec, -1\n\ts_cbranch_scc1 2f\n\t" POLL_B
                     "s_sub_u32 %[c], %[c], 1\n\ts_cbranch_scc0 1b\n\ts_waitcnt vmcnt(0)\n"
                     "2:\n\tv_mov_b32 v36, v28\n\tv_mov_b32 v37, v30\n\tv_mov_b32 v38, v32\n\tv_mov_b32 v39, v34\n\ts_branch 4f\n"
                     "3:\n\tv_mov_b32 v36, v20\n\tv_mov_b32 v37, v22\n\tv_mov_b32 v38, v24\n\tv_mov_b32 v39, v26\n"
                     "4:"
                     : "=&{v[36:39]}"(qa), [c] "=&s"(c_) : [ro] "v"(ro), [r2] "v"(ro2), [ws] "s"(p.ws), [tag] "s"(tag)
                     : "memory", "scc", "vcc", "v20", "v21", "v22", "v23", "v24", "v25", "v26", "v27", "v28", "v29", "v30", "v31", "v32", "v33", "v34", "v35");
        asm volatile("" : "=&{v[20:23]}"(qb));
        v4u qc, qd, qe;
        asm volatile("" : "=&{v[24:27]}"(qc), "=&{v[28:31]}"(qd), "=&{v[32:35]}"(qe));
        u32 rank, tau, cnt;
        u64 m0_, m1_, m2_, m3_;
        {
            u32 v, nv; v4u q4;
            asm volatile("ds_max_u32 %0, %1\n\ts_waitcnt lgkmcnt(0)\n\ts_barrier" :: "v"(ga), "v"(max(max(qa.x, qa.y), max(qa.z, qa.w))) : "memory");
#define RANK16(off) asm volatile("ds_read_b32 %[v], %[gv] offset:" #off "\n\tds_read_b128 v[40:43], %[gq] offset:" #off "\n\tds_read_b32 %[n], %[z] offset:64\n\tv_mov_b32 %[r], 0\n\ts_waitcnt lgkmcnt(0)\n\t" \
                         "v_cmp_gt_u32 %[a], v40, %[v]\n\tv_cmp_gt_u32 %[b], v41, %[v]\n\tv_cmp_gt_u32 %[c], v42, %[v]\n\tv_cmp_gt_u32 %[d], v43, %[v]\n\t" \
                         "v_addc_co_u32 %[r], vcc, 0, %[r], %[a]\n\tv_addc_co_u32 %[r], vcc, 0, %[r], %[b]\n\tv_addc_co_u32 %[r], vcc, 0, %[r], %[c]\n\tv_addc_co_u32 %[r], vcc, 0, %[r], %[d]\n\ts_nop 1\n\t" \
                         "v_add_u32_dpp %[r], %[r], %[r] quad_perm:[1,0,3,2] row_mask:0xf bank_mask:0xf\n\ts_nop 1\n\tv_add_u32_dpp %[r], %[r], %[r] quad_perm:[2,3,0,1] row_mask:0xf bank_mask:0xf\n\ts_nop 0" \
                         : [v] "=&v"(v), [r] "=&v"(rank), [n] "=&v"(nv), "=&{v[40:43]}"(q4), [a] "=&s"(m0_), [b] "=&s"(m1_), [c] "=&s"(m2_), [d] "=&s"(m3_) : [gv] "v"(gv), [gq] "v"(gq_), [z] "v"(zero) : "memory", "vcc")
            RANK16(0);
            tau = (u32)__builtin_amdgcn_readlane((int)v, (int)__builtin_ctzll(__ballot(rank == 7)));
            u32 pos;
            asm volatile("s_nop 1\n\tv_mov_b32 %[n], 0\n\tv_cmp_le_u32 %[a], %[t], v36\n\tv_cmp_le_u32 %[b], %[t], v37\n\tv_cmp_le_u32 %[c], %[t], v38\n\tv_cmp_le_u32 %[d], %[t], v39\n\t"
                         "v_addc_co_u32 %[n], vcc, 0, %[n], %[a]\n\tv_addc_co_u32 %[n], vcc, 0, %[n], %[b]\n\tv_addc_co_u32 %[n], vcc, 0, %[n], %[c]\n\tv_addc_co_u32 %[n], vcc, 0, %[n], %[d]\n\t"
                         "v_lshlrev_b32 %[n], 2, %[n]\n\tv_cmpx_ne_u32 0, %[n]\n\tds_add_rtn_u32 %[p], %[z], %[n] offset:64\n\ts_waitcnt lgkmcnt(0)\n\t"
                         "s_mov_b64 exec, %[a]\n\tds_write_b32 %[p], v36 offset:256\n\tv_add_u32 %[p], 4, %[p]\n\t"
                         "s_mov_b64 exec, %[b]\n\tds_write_b32 %[p], v37 offset:256\n\tv_add_u32 %[p], 4, %[p]\n\t"
                         "s_mov_b64 exec, %[c]\n\tds_write_b32 %[p], v38 offset:256\n\tv_add_u32 %[p], 4, %[p]\n\t"
                         "s_mov_b64 exec, %[d]\n\tds_write_b32 %[p], v39 offset:256\n\t"
                         "s_mov_b64 exec, -1\n\ts_waitcnt lgkmcnt(0)\n\ts_barrier"
                         : [n] "=&v"(cnt), [p] "=&v"(pos), [a] "=&s"(m0_), [b] "=&s"(m1_), [c] "=&s"(m2_), [d] "=&s"(m3_) : [t] "s"(tau), [z] "v"(zero), "{v[36:39]}"(qa) : "memory", "vcc");
            if (wave == 0) {
                RANK16(256);
                const int nc = __builtin_amdgcn_readfirstlane((int)nv) >> 2;
                if (__builtin_expect(nc > 16, 0)) {
                    v = (u32)__shfl((int)top8_d(smem, lane, nc), (lane >> 2) & 7);
                    rank = lane >> 2;
                }
                const u64 m8 = __ballot(rank < 8);
                asm volatile("s_and_b64 exec, %[m], %[k2]\n\tglobal_store_dwordx2 %[so], %[d], %[ws] sc1\n\ts_mov_b64 exec, %[m]\n\ts_mov_b32 m0, 0x8000\n\ts_nop 0\n\t"
                             "global_load_lds_dword %[xo], %[x]\n\tglobal_load_lds_dword %[xo], %[x] offset:512\n\ts_and_b64 exec, %[m], %[k1]\n\tglobal_load_lds_dword %[xo], %[x] offset:1024\n\ts_mov_b64 exec, -1"
                             :: [m] "s"(m8), [k2] "s"(0x3333333333333333ull), [k1] "s"(0x1111111111111111ull), [so] "v"(so + rank * 8), [d] "v"((v2u){v, tag}), [ws] "s"(p.ws),
                                [xo] "v"(xo + (~v & 2047u) * 1152u), [x] "s"(p.x) : "memory", "scc");
                asm volatile("s_mov_b64 exec, 1\n\tglobal_store_dwordx2 %0, %1, %2 sc1\n\ts_mov_b64 exec, -1" :: "v"(OFF_CI + (416 + xcd) * 8), "v"((v2u){tag, tag}), "s"(p.ws) : "memory");
            }
        }
        asm volatile("s_waitcnt vmcnt(0)" :: "v"(qb), "v"(qc), "v"(qd), "v"(qe) : "memory");
        if (xcd == 0) {
            const u32 cio = OFF_CI + (tid < 212 ? tid : 0) * 16;
            for (u32 spins = 0;; ++spins) {
                REREAD();
                const v4u q2 = __builtin_amdgcn_raw_buffer_load_b128(WSR(), cio, 0, 16);
                if (__all((q2.y == tag) & (q2.w == tag)) || spins > SPIN_CAP) break;
                __builtin_amdgcn_s_sleep(16);
            }
            BAR();
            if (tid == 0) __hip_atomic_store((GAS u32*)p.ws, tag, RLX_AGENT);
        }
        ROLE_END();
    }
    else
        {
            ROLE_BEGIN(3);
            FFN_PRE();
            const int pw = wave - 4;
            u32 tagv;
            asm volatile("v_mov_b32 %0, 0\n\tglobal_load_dword %0, %0, %1 sc1" : "=&v"(tagv) : "s"(p.ws) : "memory");
            FFN_FILL();
            v2u sel;
            const u32 so = OFF_SEL + xcd * 256 + (2 * pw + (lane & 1)) * 8, l16 = lane * 16;
#define POLL_S "global_load_dwordx2 v[10:11], %[so], %[ws] sc1\n\t"
            asm volatile("s_waitcnt vmcnt(18)" : "+v"(tagv) :: "memory");
            const u32 tag = __builtin_amdgcn_readfirstlane(tagv) + 1u;
            *(LAS v4u*)(smem + FL_Z + min(l16, 256u)) = (v4u){lane >= 16 ? tag : 0u, 0u, 0u, 0u};
            FFN_ADDR();
            const char* dsrc = (const char*)(p.wd + (size_t)(64 * w + 32 * (pw >> 1)) * D);
            const u32 ddst = (pw >> 1) * 32 * WDP + (pw & 1) * 1024;
            u32 dof[19];
#pragma unroll
            for (int i = 0; i < 19; ++i) {
                const u32 id = min(128u * i + 64u * (pw & 1) + lane, 2367u), r0 = id / 74u;
                dof[i] = r0 * 1152u + min(id - 74u * r0, 71u) * 16u;
                asm volatile("" : "+v"(dof[i]));
            }
#define LD_WD(i) asm volatile("s_mov_b32 m0, %2\n\ts_nop 0\n\tglobal_load_lds_dwordx4 %0, %1" :: "v"(dof[i]), "s"(dsrc), "s"(ddst + (i) * 2048) : "memory")
            asm volatile("s_waitcnt vmcnt(12) lgkmcnt(0)\n\ts_barrier\n\ts_waitcnt vmcnt(6)\n\ts_barrier\n\ts_waitcnt vmcnt(2)\n\t" POLL_S "s_waitcnt vmcnt(1)\n\ts_barrier" : "=&{v[10:11]}"(sel) : [so] "v"(so), [ws] "s"(p.ws) : "memory");
            u32 c_, early, mw = ddst;
            asm volatile("s_mov_b32 %[e], 0\n\ts_waitcnt vmcnt(0)\n\tv_cmp_ne_u32 vcc, %[tag], v11\n\ts_cmp_eq_u64 vcc, 0\n\ts_cbranch_scc1 2f\n\t"
                         "s_mov_b32 %[e], 1\n\ts_mov_b32 %[c], 0x80000\n\ts_mov_b32 m0, %[m]\n\ts_add_u32 %[m], %[m], 0x800\n\tglobal_load_lds_dwordx4 %[d0], %[src]\n\ts_mov_b32 m0, %[m]\n\ts_add_u32 %[m], %[m], 0x800\n\tglobal_load_lds_dwordx4 %[d1], %[src]\n\ts_mov_b32 m0, %[m]\n\ts_add_u32 %[m], %[m], 0x800\n\tglobal_load_lds_dwordx4 %[d2], %[src]\n\ts_mov_b32 m0, %[m]\n\ts_add_u32 %[m], %[m], 0x800\n\tglobal_load_lds_dwordx4 %[d3], %[src]\n\ts_mov_b32 m0, %[m]\n\ts_add_u32 %[m], %[m], 0x800\n\tglobal_load_lds_dwordx4 %[d4], %[src]\n\ts_mov_b32 m0, %[m]\n\ts_add_u32 %[m], %[m], 0x800\n\tglobal_load_lds_dwordx4 %[d5], %[src]\n\t"
                         "1:\n\t" POLL_S "s_waitcnt vmcnt(0)\n\tv_cmp_ne_u32 vcc, %[tag], v11\n\ts_cmp_eq_u64 vcc, 0\n\ts_cbranch_scc1 2f\n\ts_sub_u32 %[c], %[c], 1\n\ts_cbranch_scc0 1b\n2:"
                         : "+{v[10:11]}"(sel), [c] "=&s"(c_), [e] "=&s"(early), [m] "+s"(mw) : [so] "v"(so), [ws] "s"(p.ws), [tag] "s"(tag), [src] "s"(dsrc), [d0] "v"(dof[0]), [d1] "v"(dof[1]), [d2] "v"(dof[2]), [d3] "v"(dof[3]), [d4] "v"(dof[4]), [d5] "v"(dof[5]) : "memory", "scc", "vcc");
#pragma unroll
            for (int k = 0; k < 2; ++k) {
                const u32 t = ~(u32)__builtin_amdgcn_readlane((int)sel.x, k) & 2047u;
                const char* xr = (const char*)p.x + t * (D * 2);
                asm volatile("s_mov_b32 m0, %2\n\ts_nop 0\n\tglobal_load_lds_dwordx4 %0, %1\n\ts_mov_b64 exec, 0xff\n\tglobal_load_lds_dwordx4 %0, %1 offset:1024\n\ts_mov_b64 exec, -1"
                             :: "v"(l16), "s"(xr), "s"(FL_XS + (2 * pw + k) * XSP) : "memory");
            }
            asm volatile("s_barrier\n\ts_cmp_eq_u32 %[e], 0\n\ts_cbranch_scc0 1f\n\ts_mov_b32 m0, %[m]\n\ts_add_u32 %[m], %[m], 0x800\n\tglobal_load_lds_dwordx4 %[d0], %[src]\n\ts_mov_b32 m0, %[m]\n\ts_add_u32 %[m], %[m], 0x800\n\tglobal_load_lds_dwordx4 %[d1], %[src]\n\ts_mov_b32 m0, %[m]\n\ts_add_u32 %[m], %[m], 0x800\n\tglobal_load_lds_dwordx4 %[d2], %[src]\n\ts_mov_b32 m0, %[m]\n\ts_add_u32 %[m], %[m], 0x800\n\tglobal_load_lds_dwordx4 %[d3], %[src]\n\ts_mov_b32 m0, %[m]\n\ts_add_u32 %[m], %[m], 0x800\n\tglobal_load_lds_dwordx4 %[d4], %[src]\n\ts_mov_b32 m0, %[m]\n\ts_add_u32 %[m], %[m], 0x800\n\tglobal_load_lds_dwordx4 %[d5], %[src]\n\ts_mov_b32 m0, %[m]\n\ts_add_u32 %[m], %[m], 0x800\n\tglobal_load_lds_dwordx4 %[d6], %[src]\n\ts_mov_b32 m0, %[m]\n\ts_add_u32 %[m], %[m], 0x800\n\tglobal_load_lds_dwordx4 %[d7], %[src]\n\ts_mov_b32 m0, %[m]\n\ts_add_u32 %[m], %[m], 0x800\n\tglobal_load_lds_dwordx4 %[d8], %[src]\n\ts_mov_b32 m0, %[m]\n\ts_add_u32 %[m], %[m], 0x800\n\tglobal_load_lds_dwordx4 %[d9], %[src]\n\ts_mov_b32 m0, %[m]\n\ts_add_u32 %[m], %[m], 0x800\n\tglobal_load_lds_dwordx4 %[d10], %[src]\n\ts_mov_b32 m0, %[m]\n\ts_add_u32 %[m], %[m], 0x800\n\tglobal_load_lds_dwordx4 %[d11], %[src]\n\ts_waitcnt vmcnt(12)\n\ts_branch 2f\n1:\n\ts_mov_b32 m0, %[m]\n\ts_add_u32 %[m], %[m], 0x800\n\tglobal_load_lds_dwordx4 %[d6], %[src]\n\ts_mov_b32 m0, %[m]\n\ts_add_u32 %[m], %[m], 0x800\n\tglobal_load_lds_dwordx4 %[d7], %[src]\n\ts_mov_b32 m0, %[m]\n\ts_add_u32 %[m], %[m], 0x800\n\tglobal_load_lds_dwordx4 %[d8], %[src]\n\ts_mov_b32 m0, %[m]\n\ts_add_u32 %[m], %[m], 0x800\n\tglobal_load_lds_dwordx4 %[d9], %[src]\n\ts_mov_b32 m0, %[m]\n\ts_add_u32 %[m], %[m], 0x800\n\tglobal_load_lds_dwordx4 %[d10], %[src]\n\ts_mov_b32 m0, %[m]\n\ts_add_u32 %[m], %[m], 0x800\n\tglobal_load_lds_dwordx4 %[d11], %[src]\n\ts_waitcnt vmcnt(6)\n2:\n\ts_barrier"
                         : [m] "+s"(mw) : [e] "s"(early), [src] "s"(dsrc), [d0] "v"(dof[0]), [d1] "v"(dof[1]), [d2] "v"(dof[2]), [d3] "v"(dof[3]), [d4] "v"(dof[4]), [d5] "v"(dof[5]), [d6] "v"(dof[6]), [d7] "v"(dof[7]), [d8] "v"(dof[8]), [d9] "v"(dof[9]), [d10] "v"(dof[10]), [d11] "v"(dof[11]) : "memory", "scc");
#pragma unroll
            for (int i = 12; i < 18; ++i) LD_WD(i);
            if (!(pw & 1)) LD_WD(18);
            asm volatile("s_mov_b64 exec, 1\n\tglobal_store_dwordx2 %0, %1, %2 sc1\n\ts_mov_b64 exec, -1\n\ts_barrier" :: "v"(OFF_CI + (128 + 4 * w + pw) * 8), "v"((v2u){tag, tag}), "s"(p.ws) : "memory");
            FFN_HFRAG();
            asm volatile("s_waitcnt vmcnt(1)\n\ts_barrier" ::: "memory");
            s16x4 a[3][8];
            FFN_WDFRAG(0, 8 + pw); FFN_WDFRAG(1, 12 + pw); FFN_WDFRAG(2, 16 + (pw & 1));
            v4f ya[3];
            FFN_DOWN(0, 8 + pw); FFN_DOWN(1, 12 + pw);
            if (pw < 2) { FFN_DOWN(2, 16 + pw); FFN_PUB(2, 16 + pw); }
            FFN_PUB(0, 8 + pw); FFN_PUB(1, 12 + pw);
            ROLE_END();
        }
    } else
    {
        ROLE_BEGIN(5);
        ROLE_ARGS();
        _Pragma("unroll 1") for (int e = (slot - 28) + 4 * wave; touch(p, xcd, e, lane, (u32)(uintptr_t)(smem + wave * 256)); e += 32) {}
        asm volatile("s_waitcnt vmcnt(0)" ::: "memory");
        ROLE_END();
    }
    } else
        {
            ROLE_BEGIN(2);
            FFN_PRE();
            FFN_FILL();
            FFN_ADDR();
            LAS char* gp = smem + q * 2048 + ((lane >> 2) & 3) * 128 + ((2 * wave + ((lane >> 1) & 1)) ^ (((lane >> 2) & 2) | ((q & 1) << 2))) * 16 + (lane & 1) * 8;
            LAS char* xp = smem + FL_XS + (n & 7) * XSP + q * 16;
            const int hc = 16 * wave + n;
            LAS char* hw_ = smem + FL_H + (4 * (q & 1) + 2 * (q >> 1)) * 144 + ((hc & 35) | ((hc & 12) << 1) | ((hc & 16) >> 2)) * 2;
            asm volatile("" : "+v"(gp), "+v"(xp), "+v"(hw_));
            bf16x8 gf[18], uf[18];
#pragma unroll
            for (int sl = 0; sl < 3; ++sl) {
                if (sl == 0) asm volatile("s_waitcnt vmcnt(12)\n\ts_barrier" ::: "memory");
                if (sl == 1) asm volatile("s_waitcnt vmcnt(6)\n\ts_barrier" ::: "memory");
                if (sl == 2) asm volatile("s_waitcnt vmcnt(0) lgkmcnt(0)\n\ts_barrier" ::: "memory");
                LAS char* g_ = gp + sl * 49152;
                asm volatile("" : "+v"(g_));
#pragma unroll
                for (int i = 0; i < 6; ++i) {
                    gf[6 * sl + i] = __builtin_shufflevector(__builtin_amdgcn_ds_read_tr16_b64_v4i16((LAS s16x4*)(g_ + i * 8192)), __builtin_amdgcn_ds_read_tr16_b64_v4i16((LAS s16x4*)(g_ + i * 8192 + 512)), 0, 1, 2, 3, 4, 5, 6, 7);
                    uf[6 * sl + i] = __builtin_shufflevector(__builtin_amdgcn_ds_read_tr16_b64_v4i16((LAS s16x4*)(g_ + i * 8192 + 1024)), __builtin_amdgcn_ds_read_tr16_b64_v4i16((LAS s16x4*)(g_ + i * 8192 + 1536)), 0, 1, 2, 3, 4, 5, 6, 7);
                }
#pragma unroll
                for (int i = 0; i < 6; ++i) asm volatile("" : "+v"(gf[6 * sl + i]), "+v"(uf[6 * sl + i]));
            }
            u32 tag = *(LAS u32*)(smem + FL_TAG);
            asm volatile("s_barrier\n\ts_barrier" : "+v"(tag) :: "memory");
            bf16x8 xf[18];
            v4f ag = {0.f, 0.f, 0.f, 0.f}, au = ag;
#pragma unroll
            for (int s = 0; s < 4; ++s) xf[s] = *(LAS bf16x8*)(xp + s * 64);
            SBAR();
#pragma unroll
            for (int s = 0; s < 18; ++s) {
                ag = MF(xf[s], gf[s], ag);
                if (s + 4 < 18) xf[s + 4] = *(LAS bf16x8*)(xp + (s + 4) * 64);
                au = MF(xf[s], uf[s], au);
                SBAR();
            }
            {
                const u32 ab = cvtpk(q < 2 ? ag[0] : ag[2], q < 2 ? ag[1] : ag[3]), ub = cvtpk(q < 2 ? au[0] : au[2], q < 2 ? au[1] : au[3]);
                const u32 sl = cvtpk(bflo(ab) * sigmoidf_(bflo(ab)), bfhi(ab) * sigmoidf_(bfhi(ab)));
                const u32 hh = cvtpk(bflo(sl) * bflo(ub), bfhi(sl) * bfhi(ub));
                *(LAS u16*)(hw_) = (u16)hh; *(LAS u16*)(hw_ + 144) = (u16)(hh >> 16);
            }
            BAR();
            FFN_HFRAG();
            asm volatile("s_barrier" ::: "memory");
            s16x4 a[2][8];
            FFN_WDFRAG(0, 2 * wave); FFN_WDFRAG(1, 2 * wave + 1);
            v4f ya[2];
            FFN_DOWN(0, 2 * wave); FFN_DOWN(1, 2 * wave + 1);
            FFN_PUB(0, 2 * wave); FFN_PUB(1, 2 * wave + 1);
            ROLE_END();
        }
    } else {
    if (wave >= 4)
    {
        ROLE_BEGIN(1);
        const int hw = wave - 4, ht = tid - 256;
        const v4u z4 = {0, 0, 0, 0};
        const int sub = lane & 15;
        v4u xr[5];
        u32 tagv;
        const int rrow = 16 * (cw & 127) + 4 * hw;
        u32 dst = (u32)(uintptr_t)(smem + LDS_RW) + hw * 3072, l16 = lane * 16;
        u32 ro = (u32)(((rrow + (lane >> 4)) & ~((127 - cw) >> 31)) * D + sub * 8) * 2, ro4 = ro - (sub & 8) * 16;
        u32 uo0 = (u32)((256 * hw + 4 * lane) * D + chbase) * 2;
        asm volatile("" : "+s"(dst), "+v"(l16), "+v"(ro), "+v"(ro4), "+v"(uo0));
        ROLE_ARGS();
        {
            asm volatile("s_mov_b32 m0, %5\n\tv_mov_b32 %0, 0\n\tglobal_load_dword %0, %0, %2 sc1\n\tglobal_load_lds_dwordx4 %3, %4\n\ts_mov_b64 exec, 0xff\n\tglobal_load_lds_dwordx4 %3, %4 offset:1024\n\t"
                         "s_add_u32 m0, m0, 1280\n\ts_mov_b64 exec, -1\n\tglobal_load_lds_dwordx4 %3, %6\n\ts_mov_b64 exec, 0xff\n\tglobal_load_lds_dwordx4 %3, %6 offset:1024\n\ts_mov_b64 exec, -1"
                         : "=&v"(tagv) : "n"(0), "s"(p.ws), "v"(l16), "s"(p.wr), "s"(dst), "s"(p.gw) : "memory", "scc");
#pragma unroll
            for (int j = 0; j < 4; ++j) asm volatile("global_load_dwordx4 %0, %1, %2 offset:%3" : "=v"(xr[j]) : "v"(ro), "s"(p.x), "n"(j * 256));
            asm volatile("global_load_dwordx4 %0, %1, %2 offset:1024" : "=v"(xr[4]) : "v"(ro4), "s"(p.x));
        }
        __builtin_amdgcn_s_barrier();
        v2u uu[8];
        const u32 uo1 = uo0 + 1024 * D * 2;
#define LD_U(i) asm volatile("global_load_dwordx2 %0, %1, %2 offset:%3" : "=v"(uu[i]) : "v"((i) < 4 ? uo0 : uo1), "s"(p.x), "n"(((i) & 3) * D * 2))
#pragma unroll
        for (int i = 0; i < NB1; ++i) LD_U(i);
        *(LAS v4u*)(smem + LDS_TAP + ht * 16) = z4; *(LAS v4u*)(smem + LDS_TAP + 4096 + ht * 16) = z4;
        *(LAS v4u*)(smem + LDS_TAP + 8192 + ht * 16 + (ht >> 7) * (LDS_U - LDS_TAP - 8192 + ((ht >> 5) & 3) * 4608 + (ht & 31) * 16 - ht * 16)) = z4;
        u32 ctl = (u32)(uintptr_t)(smem + LDS_CTL), one = 1u, cty = ctl + 64 * hw + (lane & 3) * 16;
        const u32 late = (u32)((127 - cw) >> 31), pa_ = OFF_RG + hline(p.ws, cw & 127) + hw * 32, pk_ = pa_ ^ ((pa_ ^ (OFF_CI + ((cw - 128) * 4 + hw) * 16)) & late);
        u32 po = pk_ + (lane >> 1) + (sub != 0) * (OFF_G + hline(p.ws, ((lane - 1) & 3) * 64 + ((cw & 127) >> 1)) + ((4 * cw + hw) & 7) * 16 - pk_ - (lane >> 1));
        const u64 pmask = 1ull | ((u64)(~late & 1u) << 32) | (~late & ((1u << (1 + REPG)) - 1));
        u32 nrow = 0xffffu - (rrow + (lane >> 4));
        u32 go = OFF_G + hline(p.ws, (cw % REPG) * 64 + 8 * hw + (lane >> 3)) + (lane & 7) * 16, go2 = OFF_G + hline(p.ws, (cw % REPG) * 64 + 32 + 8 * hw + (lane >> 3)) + (lane & 7) * 16;
        LAS char* up = smem + LDS_U + (256 + 256 * hw + 4 * lane) * 2;
        LAS char* yp = smem + CH_Y + hw * 1152 + (lane + (lane >> 3)) * 16;
        LAS char* rw = smem + LDS_RW + hw * 3072 + sub * 16;
        LAS char* rw4 = rw - (sub & 8) * 16;
        asm volatile("" : "+v"(ctl), "+v"(one), "+v"(cty), "+v"(po), "+v"(go), "+v"(go2), "+v"(up), "+v"(yp), "+v"(rw), "+v"(rw4), "+v"(nrow));
        u32 wt[3];
#pragma unroll
        for (int k = WPF; k < 3; ++k) {
            wt[k] = wd_line(xcd, (36 * k + slot * 2 + (hw & 1)) * 48 + min(lane, 47));
            asm volatile("" : "+v"(wt[k]));
        }
        BAR();
        __builtin_amdgcn_s_setprio(3);
        u32 tag;
        {
            asm volatile("s_waitcnt vmcnt(%1)" : "+v"(tagv) : "n"(NB1 + 5) : "memory");
            tag = __builtin_amdgcn_readfirstlane(tagv) + 1u;
            v4u wa[5], wg_[5];
#pragma unroll
            for (int j = 0; j < 5; ++j) { wa[j] = *(LAS v4u*)((j < 4 ? rw : rw4) + j * 256); wg_[j] = *(LAS v4u*)((j < 4 ? rw : rw4) + 1280 + j * 256); }
#pragma unroll
            for (int j = 0; j < 5; ++j) asm volatile("" : "+v"(wa[j]), "+v"(wg_[j]));
            asm volatile("s_waitcnt vmcnt(%5)" : "+v"(xr[0]), "+v"(xr[1]), "+v"(xr[2]), "+v"(xr[3]), "+v"(xr[4]) : "n"(NB1) : "memory");
            if (sub >= 8) xr[4] = z4;
            float r = 0.f, s = 0.f;
#pragma unroll
            for (int j = 0; j < 5; ++j) {
                const v4u a = wa[j], g = wg_[j];
#pragma unroll
                for (int e = 0; e < 4; ++e) {
                    const u32 xe = xr[j][e], ae = a[e], ge = g[e];
                    r = __builtin_amdgcn_fdot2_f32_bf16(__builtin_bit_cast(bf16x2_t, xe), __builtin_bit_cast(bf16x2_t, ae), r, false);
                    s = __builtin_amdgcn_fdot2_f32_bf16(__builtin_bit_cast(bf16x2_t, xe), __builtin_bit_cast(bf16x2_t, ge), s, false);
                }
            }
            r = dpp_add<0x128, 0xf>(r); s = dpp_add<0x128, 0xf>(s); r = dpp_add<0x124, 0xf>(r); s = dpp_add<0x124, 0xf>(s);
            r = dpp_add<0x122, 0xf>(r); s = dpp_add<0x122, 0xf>(s); r = dpp_add<0x121, 0xf>(r); s = dpp_add<0x121, 0xf>(s);
            const u32 pay = make_payload(r, sigmoidf_(rbf(s))), kk = (pay & 0xffff0000u) | nrow;
            const u32 p0 = (u32)__builtin_amdgcn_readlane((int)pay, 0), p1 = (u32)__builtin_amdgcn_readlane((int)pay, 16), p2 = (u32)__builtin_amdgcn_readlane((int)pay, 32), p3 = (u32)__builtin_amdgcn_readlane((int)pay, 48);
            const auto sw = __builtin_amdgcn_permlane16_swap(kk, kk, false, false);
            const u32 g01 = (p0 & 0xffffu) | (p1 << 16), g23 = (p2 & 0xffffu) | (p3 << 16);
            const v4u pv = {sub ? g01 : sw[0], tag, sub ? g23 : sw[1], tag};
            asm volatile("s_mov_b64 exec, %3\n\tglobal_store_dwordx4 %0, %1, %2 sc1\n\ts_mov_b64 exec, -1\n\ts_nop 0" :: "v"(po), "v"(pv), "s"(p.ws), "s"(pmask) : "memory");
        }
        __builtin_amdgcn_s_setprio(0);
        *(LAS u32*)(smem + LDS_CTL + C_TAG) = tag;
        LDS_ADD1(ctl, one, C_PUB);
        LWAIT(ctl, C_PUB, 4);
#pragma unroll
        for (int i = NB1; i < 4; ++i) LD_U(i);
        LDS_ADD1(ctl, one, C_PUB);
        LWAIT(ctl, C_PUB, 8);
        v4u gq[2];
#define POLL_G "global_load_dwordx4 v[100:103], %[go], %[ws] sc1\n\tglobal_load_dwordx4 v[104:107], %[go2], %[ws] sc1\n\t"
#define LD_U4 "global_load_dwordx2 %[u4], %[uo], %[x]\n\tglobal_load_dwordx2 %[u5], %[uo], %[x] offset:1152\n\tglobal_load_dwordx2 %[u6], %[uo], %[x] offset:2304\n\tglobal_load_dwordx2 %[u7], %[uo], %[x] offset:3456\n\t"
        asm volatile("" : "=&{v[100:103]}"(gq[0]), "=&{v[104:107]}"(gq[1]), "=&v"(uu[4]), "=&v"(uu[5]), "=&v"(uu[6]), "=&v"(uu[7]));
        asm volatile("s_cmp_ge_u32 %[hw], 2\n\ts_cbranch_scc1 1f\n\t" LD_U4 POLL_G "s_waitcnt vmcnt(6)\n\ts_branch 2f\n1:\n\ts_waitcnt vmcnt(0)\n2:"
                     : "+{v[100:103]}"(gq[0]), "+{v[104:107]}"(gq[1]), [u4] "+v"(uu[4]), [u5] "+v"(uu[5]), [u6] "+v"(uu[6]), [u7] "+v"(uu[7]), "+v"(uu[0]), "+v"(uu[1]), "+v"(uu[2]), "+v"(uu[3])
                     : [go] "v"(go), [go2] "v"(go2), [ws] "s"(p.ws), [uo] "v"(uo1), [x] "s"(p.x), [hw] "s"(hw) : "memory", "scc");
        u32 ct2 = ctl + (hw & 2) * 2;
        auto stage = [&](int g) __attribute__((always_inline)) {
            const v2u* u4 = uu + 4 * g;
            LAS char* d = up + g * 2048;
            *(LAS v2u*)(d) = (v2u){__builtin_amdgcn_perm(u4[1].x, u4[0].x, 0x05040100u), __builtin_amdgcn_perm(u4[3].x, u4[2].x, 0x05040100u)};
            *(LAS v2u*)(d + 4608) = (v2u){__builtin_amdgcn_perm(u4[1].x, u4[0].x, 0x07060302u), __builtin_amdgcn_perm(u4[3].x, u4[2].x, 0x07060302u)};
            *(LAS v2u*)(d + 2 * 4608) = (v2u){__builtin_amdgcn_perm(u4[1].y, u4[0].y, 0x05040100u), __builtin_amdgcn_perm(u4[3].y, u4[2].y, 0x05040100u)};
            *(LAS v2u*)(d + 3 * 4608) = (v2u){__builtin_amdgcn_perm(u4[1].y, u4[0].y, 0x07060302u), __builtin_amdgcn_perm(u4[3].y, u4[2].y, 0x07060302u)};
            if (g) LDS_ADD1(ct2, one, C_ST2); else LDS_ADD1(ctl, one, C_STG);
        };
        stage(0);
        asm volatile("s_cmp_lt_u32 %[hw], 2\n\ts_cbranch_scc1 1f\n\t" POLL_G LD_U4 "\n1:"
                     : "+{v[100:103]}"(gq[0]), "+{v[104:107]}"(gq[1]), [u4] "+v"(uu[4]), [u5] "+v"(uu[5]), [u6] "+v"(uu[6]), [u7] "+v"(uu[7])
                     : [go] "v"(go), [go2] "v"(go2), [ws] "s"(p.ws), [uo] "v"(uo1), [x] "s"(p.x), [hw] "s"(hw) : "memory", "scc");
        if (hw < 2) {
            asm volatile("s_waitcnt vmcnt(2)" : "+v"(uu[4]), "+v"(uu[5]), "+v"(uu[6]), "+v"(uu[7]));
            stage(1);
        }
        {
            u32 c_;
            asm volatile("s_mov_b32 %[c], 0x80000\n\ts_waitcnt vmcnt(4)\n\ts_cmp_ge_u32 %[hw], 2\n\ts_cbranch_scc1 1f\n\ts_waitcnt vmcnt(0)\n1:\n\tv_cmpx_eq_u32 %[tag], v101\n\tv_cmpx_eq_u32 %[tag], v103\n\tv_cmpx_eq_u32 %[tag], v105\n\tv_cmpx_eq_u32 %[tag], v107\n\t"
                         "s_cmp_eq_u64 exec, -1\n\ts_mov_b64 exec, -1\n\ts_cbranch_scc1 2f\n\ts_sleep 4\n\t" POLL_G "s_waitcnt vmcnt(0)\n\ts_sub_u32 %[c], %[c], 1\n\ts_cbranch_scc0 1b\n2:\n\ts_nop 4"
                         : "+{v[100:103]}"(gq[0]), "+{v[104:107]}"(gq[1]), [c] "=&s"(c_) : [go] "v"(go), [go2] "v"(go2), [ws] "s"(p.ws), [tag] "s"(tag), [hw] "s"(hw) : "memory", "scc", "vcc");
        }
        *(LAS v2u*)(smem + LDS_G + hw * 3072 + lane * 8) = (v2u){gq[0].x, gq[0].z}; *(LAS v2u*)(smem + LDS_G + hw * 3072 + 512 + lane * 8) = (v2u){gq[1].x, gq[1].z};
        LDS_ADD1(ctl, one, C_GOK);
        if (hw < 2) {
#pragma unroll
            for (int k = WPF; k < 3; ++k) asm volatile("s_mov_b32 m0, %2\n\ts_nop 0\n\tglobal_load_lds_dword %0, %1" :: "v"(wt[k]), "s"(p.wd), "n"(LDS_DUMMY) : "memory");
        }
#pragma unroll
        for (int g = 0; g < 2; ++g) {
            if (g == 1 && hw >= 2) {
                asm volatile("s_waitcnt vmcnt(4)" : "+v"(uu[4]), "+v"(uu[5]), "+v"(uu[6]), "+v"(uu[7]));
                stage(1);
            }
            const v2u* u4 = uu + 4 * g;
            const float gr[4] = {bflo(gq[g].x), bfhi(gq[g].x), bflo(gq[g].z), bfhi(gq[g].z)};
            float o[4][4];
#pragma unroll
            for (int r = 0; r < 4; ++r) {
                const float xv[4] = {bflo(u4[r].x), bfhi(u4[r].x), bflo(u4[r].y), bfhi(u4[r].y)};
#pragma unroll
                for (int c = 0; c < 4; ++c) asm("v_fma_f32 %0, -%1, %2, %2" : "=v"(o[r][c]) : "v"(gr[r]), "v"(xv[c]));
                asm volatile("" : "+v"(o[r][0]), "+v"(o[r][1]), "+v"(o[r][2]), "+v"(o[r][3]));
            }
            {
                u32 t_, c_;
                asm volatile("s_mov_b32 %1, 0x400000\n1:\n\tds_read_b32 %0, %2 offset:%3\n\ts_waitcnt lgkmcnt(0)\n\tv_cmp_eq_u32 vcc, 0, %0\n\ts_cmp_eq_u64 vcc, 0\n\ts_cbranch_scc1 2f\n\ts_sleep 1\n\ts_sub_u32 %1, %1, 1\n\ts_cbranch_scc0 1b\n2:"
                             : "=&v"(t_), "=&s"(c_) : "v"(cty), "n"(C_Y + 256 * g) : "memory", "scc", "vcc");
            }
            v4f y[4];
#pragma unroll
            for (int c = 0; c < 4; ++c) y[c] = *(LAS v4f*)(yp + c * KCH + g * 4608);
            SBAR();
#define ST_O(i, v) asm volatile("global_store_dwordx2 %0, %1, %2 offset:%3" :: "v"(g ? uo1 : uo0), "v"(v), "s"(p.out), "n"((i) * D * 2) : "memory")
#pragma unroll
            for (int r = 0; r < 4; ++r) {
#pragma unroll
                for (int c = 0; c < 4; ++c) { const float yv = y[c][r]; asm("v_fmac_f32 %0, %1, %2" : "+v"(o[r][c]) : "v"(gr[r]), "v"(yv)); }
                ST_O(r, ((v2u){cvtpk(o[r][0], o[r][1]), cvtpk(o[r][2], o[r][3])}));
            }
        }
        asm volatile("s_waitcnt vmcnt(0)" ::: "memory");
        LDS_ADD1(ctl, one, C_ACK);
        ROLE_END();
    }
    else
    {
        ROLE_BEGIN(0);
        const int ch = wave;
        u32 pi = ((chbase + ch) * NST + lane * 2) * 2, dl = (chbase + ch) * 2;
        asm volatile("" : "+v"(pi), "+v"(dl));
        ROLE_ARGS();
#define GLD(T, ptr, idx) __builtin_nontemporal_load((const GAS T*)((const GAS char*)(ptr) + (idx)))
        const u32 are = GLD(u32, p.Are, pi), aim = GLD(u32, p.Aim, pi), bre = GLD(u32, p.Bre, pi), bim = GLD(u32, p.Bim, pi), cre = GLD(u32, p.Cre, pi), cim = GLD(u32, p.Cim, pi);
        const u16 ldt = *(const GAS u16*)((const GAS char*)p.logdt + dl), dsk = *(const GAS u16*)((const GAS char*)p.dskip + dl);
        SBAR();
        __builtin_amdgcn_s_barrier();
        SBAR();
        LAS char* kb = smem + ch * KCH;
        const int r32 = lane & 31, h = lane >> 5, q = lane >> 4, n = lane & 15;
        const int ai = 4 * (r32 >> 3) + (r32 & 3), hh = (r32 >> 2) & 1;
        const u64 tsel = ai < 8 ? (hh ? KROW_1LO : KROW_0LO) : (hh ? KROW_1HI : KROW_0HI);
        const int o = (int)((tsel >> (8 * (ai & 7))) & 0xff);
        LAS char* ap = kb + o * STG_ROW + h * 16;
        LAS char* bp = kb + r32 * STG_ROW + h * 16;
        u32 sg[14];
#pragma unroll
        for (int k = 0; k < 14; ++k) { sg[k] = (u32)(uintptr_t)(kb + lane * 8 + k * 4 * STG_ROW); asm volatile("" : "+v"(sg[k])); }
        bf16x8 b5 = band(((0x1fu << 6) << n) >> (8 * q), 0x3f803f80u), b3 = band(((0x7u << 11) << n) >> (8 * q), 0x3f803f80u);
        bf16x8 b9 = band(((0x1ffu << 4) << n) >> (8 * q), 0x3f803f80u), b7 = band(((0x7fu << 5) << n) >> (8 * q), 0x3f803f80u);
        bf16x8 pat = band(((0x3u << 12) << n) >> (8 * q), 0xffffffffu);
        LAS char* k0 = kb + 32 + lane * 16;
        LAS char* kt = kb + (lane & 3) * KCOPY + (lane < 16 ? 32 + 1024 + (lane >> 2) * 16 : ((lane >> 2) & 1) * 16);
        LAS char* a0 = kb + ((lane >> 2) & 3) * KCOPY + (28 - 8 * q + 4 * (lane & 3)) * 2;
        LAS char* bb = smem + LDS_U + ch * 4608 + (16 * n + 8 * q - 16) * 2;
        LAS char* yb = kb + CH_Y + (4 * n + q + (n >> 1)) * 16;
        LAS char* tp = smem + LDS_TAP + ch * 2304 + ((n & 1) ? 13 + n - 4 * q : 96 + 14 + n - 4 * q) * 4;
        LAS char* ta = smem + LDS_TAP + ch * 2304 + (h ? 0 : 768) + 64 + 2 * r32;
        LAS char* tb = smem + LDS_TAP + ch * 2304 + 1600 + 64 * h + 2 * r32;
        u32 ctl = (u32)(uintptr_t)(smem + LDS_CTL), ctc = ctl + C_Y + ch * 16 + (lane & 15), one = 1u, so = OFF_SEL + (8 + xcd) * 256 + (lane & 7) * 8;
        u32 m01 = lane < 2 ? ~0u : 0u, m2 = lane == 2 ? ~0u : 0u, m3 = lane == 3 ? ~0u : 0u, m0_ = lane == 0 ? ~0u : 0u;
        asm volatile("" : "+v"(b5), "+v"(b3), "+v"(b9), "+v"(b7), "+v"(pat), "+v"(ap), "+v"(bp), "+v"(m01), "+v"(m2), "+v"(m3), "+v"(m0_));
        asm volatile("" : "+v"(k0), "+v"(kt), "+v"(a0), "+v"(bb), "+v"(yb), "+v"(tp), "+v"(ta), "+v"(tb), "+v"(ctl), "+v"(ctc), "+v"(one), "+v"(so));
        const char* hint = p.ws + (OFF_PART + cw * PP + hline(p.ws, cw >> 1));
        asm volatile("" : "+s"(hint));
        __builtin_amdgcn_s_barrier();
        SBAR();
        const float dt = rbf(expf(bf2f(ldt)));
        v16f acc;
        u32 stg;
        float kscale;
        {
            const v2f a2 = (v2f){bflo(are), bfhi(are)} * dt * 1.44269504088896341f;
            const v2f brev = (v2f){bflo(aim), bfhi(aim)} * dt * 0.15915494309189535f;
            const cf2 Bc = {{dt * bflo(bre), dt * bflo(bim)}, {dt * bfhi(bre), dt * bfhi(bim)}};
            const cf2 Cc = {{bflo(cre), bflo(cim)}, {bfhi(cre), bfhi(cim)}};
            cf2 W = cmul2(Cc, Bc);
            u32 mx = max(max(__float_as_uint(W.a.x) & 0x7f800000u, __float_as_uint(W.a.y) & 0x7f800000u), max(__float_as_uint(W.b.x) & 0x7f800000u, __float_as_uint(W.b.y) & 0x7f800000u));
            mx = dpp_umax<0x111, 0xf>(mx); mx = dpp_umax<0x112, 0xf>(mx); mx = dpp_umax<0x114, 0xf>(mx); mx = dpp_umax<0x118, 0xf>(mx);
            mx = dpp_umax<0x142, 0xa>(mx); mx = dpp_umax<0x143, 0xc>(mx);
            mx = min(max((u32)__builtin_amdgcn_readlane((int)mx, 63), 0x00800000u), 0x7e800000u);
            kscale = __uint_as_float(mx);
            const float inv = __uint_as_float(0x7f000000u - mx);
            W.a *= inv; W.b *= inv;
            const cf2 E1 = cexp2(a2, brev, 1.f), E8 = cexp2(a2, brev, 8.f), E256 = cexp2(a2, brev, 256.f);
            asm volatile("s_nop 0\n\t" KGEN_ASM :: "{v[48:49]}"(W.a), "{v[50:51]}"(W.b), "{v[52:53]}"(E1.a), "{v[54:55]}"(E1.b), "{v[56:57]}"(E8.a), "{v[58:59]}"(E8.b), "{v[60:61]}"(E256.a), "{v[62:63]}"(E256.b),
                         [b0] "v"(sg[0]), [b1] "v"(sg[1]), [b2] "v"(sg[2]), [b3] "v"(sg[3]), [b4] "v"(sg[4]), [b5] "v"(sg[5]), [b6] "v"(sg[6]), [b7] "v"(sg[7]), [b8] "v"(sg[8]), [b9] "v"(sg[9]),
                         [b10] "v"(sg[10]), [b11] "v"(sg[11]), [b12] "v"(sg[12]), [b13] "v"(sg[13]) : "memory", KGEN_CLOBBER);
            asm volatile(KMFMA_ASM : "=&{v[48:63]}"(acc), [stg] "=&v"(stg) : [ap] "v"(ap), [bp] "v"(bp), [ctl] "v"(ctl) : "memory", KMFMA_CLOBBER);
            acc *= kscale;
        }
        acc[0] += __uint_as_float(((u32)dsk << 16) & m0_);
        u32 t78 = cvtpk(acc[7], acc[8]), t9a = cvtpk(acc[9], acc[10]);
        {
            const auto s0 = __builtin_amdgcn_permlane32_swap(cvtpk(acc[0], acc[1]), cvtpk(acc[4], acc[4]), false, false);
            const auto s1 = __builtin_amdgcn_permlane32_swap(cvtpk(acc[2], acc[3]), cvtpk(acc[5], acc[6]), false, false);
            const v4u ww = {s0[0], s1[0], s0[1], s1[1]};
            const u32 pz = (u32)__builtin_amdgcn_update_dpp(0, (int)ww.z, 0x138, 0xf, 0xf, true), pw = (u32)__builtin_amdgcn_update_dpp(0, (int)ww.w, 0x138, 0xf, 0xf, true);
            const v4u c1 = {__builtin_amdgcn_alignbit(ww.x, pw, 16), __builtin_amdgcn_alignbit(ww.y, ww.x, 16), __builtin_amdgcn_alignbit(ww.z, ww.y, 16), __builtin_amdgcn_alignbit(ww.w, ww.z, 16)};
            *(LAS v4u*)(k0) = ww;
            *(LAS v4u*)(k0 + KCOPY) = c1;
            *(LAS v4u*)(k0 + 2 * KCOPY) = (v4u){pw, ww.x, ww.y, ww.z};
            *(LAS v4u*)(k0 + 3 * KCOPY) = (v4u){__builtin_amdgcn_alignbit(pw, pz, 16), c1.x, c1.y, c1.z};
            const u32 w0 = (u32)__builtin_amdgcn_readlane((int)ww.z, 63), w1 = (u32)__builtin_amdgcn_readlane((int)ww.w, 63);
            const u32 w3 = w1 >> 16, w2 = w3 * 0x10001u, w10 = (w0 >> 16) | (w1 << 16);
            *(LAS v4u*)kt = (v4u){(w2 & m01) | (w1 & m2) | (w10 & m3), (w3 & m0_) | (w2 & ((m01 ^ m0_) | m2 | m3)), (w3 & m2) | (w2 & m3), 0u};
        }
        asm volatile("" ::: "memory");
        pat = __builtin_bit_cast(bf16x8, __builtin_bit_cast(v4u, pat) & (((u32)__builtin_amdgcn_readlane((int)t78, 63) >> 16) * 0x10001u));
#define POLL_SEL "global_load_dwordx2 v[240:241], %[so], %[ws] sc1\n\t"
        v2u sq;
        const v4f zero4 = {0.f, 0.f, 0.f, 0.f};
        {
            u32 s_, c_;
            asm volatile(CONV_ASM : "=&{v[240:241]}"(sq), [s] "=&s"(s_), [c] "=&s"(c_)
                         : "{v[4:7]}"(b5), "{v[8:11]}"(b3), "{v[12:15]}"(b9), "{v[16:19]}"(b7), "{v[20:23]}"(pat), [a0] "v"(a0), [bb] "v"(bb), [yb] "v"(yb), [tp] "v"(tp), [ta] "v"(ta), [tb] "v"(tb),
                           [ctl] "v"(ctl), [ctc] "v"(ctc), [one] "v"(one), [so] "v"(so), [t78] "v"(t78), [t9a] "v"(t9a), [stg] "v"(stg), [ws] "s"(p.ws) : "memory", "scc", CONV_CLOBBER);
        }
        asm volatile("s_cmp_eq_u32 %0, 0\n\ts_cbranch_scc1 1f\n\ts_endpgm\n1:" :: "s"(wave) : "memory", "scc");
        {
            u32 h_;
            asm volatile("s_load_dword %0, %1, 0x4 glc" : "=&s"(h_) : "s"(hint) : "memory");
            const u32 tag = __builtin_amdgcn_readfirstlane(*(LAS u32*)(smem + LDS_CTL + C_TAG));
            u32 c_;
            u32 pb[9];
#pragma unroll
            for (int k = 0; k < 9; ++k) { pb[k] = OFF_PART + cw * PP + hline(p.ws, 8 * k + (lane >> 3)) + (lane & 7) * 16; asm volatile("" : "+v"(pb[k])); }
            {
                asm volatile("s_mov_b32 %[c], 64\n1:\n\ts_waitcnt lgkmcnt(0)\n\ts_cmp_eq_u32 %[h], %[tag]\n\ts_cbranch_scc1 2f\n\ts_load_dword %[h], %[a], 0x4 glc\n\ts_sub_u32 %[c], %[c], 1\n\ts_cbranch_scc0 1b\n\ts_waitcnt lgkmcnt(0)\n2:"
                             : [h] "+s"(h_), [c] "=&s"(c_) : [a] "s"(hint), [tag] "s"(tag) : "memory", "scc");
            }
            typedef __attribute__((ext_vector_type(32))) u32 v32u;
            v32u ra, rb; v4u rc, rd;
            asm volatile("global_load_dwordx4 v[64:67], %[pb0], %[ws] sc1\n\tglobal_load_dwordx4 v[68:71], %[pb1], %[ws] sc1\n\tglobal_load_dwordx4 v[72:75], %[pb2], %[ws] sc1\n\tglobal_load_dwordx4 v[76:79], %[pb3], %[ws] sc1\n\tglobal_load_dwordx4 v[80:83], %[pb4], %[ws] sc1\n\tglobal_load_dwordx4 v[84:87], %[pb5], %[ws] sc1\n\tglobal_load_dwordx4 v[88:91], %[pb6], %[ws] sc1\n\tglobal_load_dwordx4 v[92:95], %[pb7], %[ws] sc1\n\tglobal_load_dwordx4 v[96:99], %[pb8], %[ws] sc1\n\t" : "=&{v[64:95]}"(ra), "=&{v[96:99]}"(rc) : [pb0] "v"(pb[0]), [pb1] "v"(pb[1]), [pb2] "v"(pb[2]), [pb3] "v"(pb[3]), [pb4] "v"(pb[4]), [pb5] "v"(pb[5]), [pb6] "v"(pb[6]), [pb7] "v"(pb[7]), [pb8] "v"(pb[8]), [ws] "s"(p.ws) : "memory");
            asm volatile("s_mov_b32 %[c], 0x80000\n\ts_waitcnt vmcnt(9)\n1:\n\tv_cmpx_eq_u32 %[tag], v241\n\t"
                         "s_cmp_eq_u64 exec, -1\n\ts_mov_b64 exec, -1\n\ts_cbranch_scc1 2f\n\t" POLL_SEL "s_waitcnt vmcnt(0)\n\ts_sub_u32 %[c], %[c], 1\n\ts_cbranch_scc0 1b\n2:\n\ts_nop 4"
                         : "+{v[240:241]}"(sq), [c] "=&s"(c_) : [so] "v"(so), [ws] "s"(p.ws), [tag] "s"(tag) : "memory", "scc", "vcc");
            const int t = (int)(0xffffu - (sq.x & 0xffffu)) & 2047;
            const float rs = rbf(sigmoidf_(key_to_r(sq.x)));
            LWAIT(ctl, C_GOK, 4);
            {
                u32 t_;
                asm volatile("s_mov_b32 %1, 0x400000\n1:\n\tds_read_b32 %0, %2 offset:%3\n\ts_waitcnt lgkmcnt(0)\n\tv_cmp_eq_u32 vcc, 0, %0\n\ts_cmp_eq_u64 vcc, 0\n\ts_cbranch_scc1 2f\n\ts_sub_u32 %1, %1, 1\n\ts_cbranch_scc0 1b\n2:"
                             : "=&v"(t_), "=&s"(c_) : "v"(ctl + (lane & 3) * 16), "n"(C_Y + 64 * 7) : "memory", "scc", "vcc");
            }
            const float g = bf2f(*(LAS u16*)(smem + LDS_G + ((t >> 8) & 3) * 3072 + (t >> 10) * 512 + (t & 255) * 2));
            const int uq = (t & 255) >> 2;
            float xv[4], av[4];
#pragma unroll
            for (int c = 0; c < 4; ++c) {
                xv[c] = bf2f(*(LAS u16*)(smem + LDS_U + c * 4608 + (256 + t) * 2));
                av[c] = g * *(LAS float*)(smem + c * KCH + CH_Y + (t >> 8) * 1152 + (uq + (uq >> 3)) * 16 + (t & 3) * 4);
            }
            const u32 e1 = n < 4 ? ((n & 1) ? 0x3f800000u : 0x00003f80u) : 0u;
            bf16x8 sel4 = __builtin_bit_cast(bf16x8, (n & 2) ? (v4u){0u, 0u, e1, e1} : (v4u){e1, e1, 0u, 0u});
            u32 oo = (u32)(t * D + chbase) * 2;
            float omg = 1.f - g;
            asm volatile("" : "+v"(sel4), "+v"(oo), "+v"(omg), "+v"(xv[0]), "+v"(xv[1]), "+v"(xv[2]), "+v"(xv[3]), "+v"(av[0]), "+v"(av[1]), "+v"(av[2]), "+v"(av[3]));
            v4f s0, s1;
            asm volatile("s_mov_b32 %[c], 0x40000\n\ts_waitcnt vmcnt(0)\n\tv_cmpx_eq_u32 %[tag], v65\n\tv_cmpx_eq_u32 %[tag], v67\n\tv_cmpx_eq_u32 %[tag], v69\n\tv_cmpx_eq_u32 %[tag], v71\n\tv_cmpx_eq_u32 %[tag], v73\n\tv_cmpx_eq_u32 %[tag], v75\n\tv_cmpx_eq_u32 %[tag], v77\n\tv_cmpx_eq_u32 %[tag], v79\n\tv_cmpx_eq_u32 %[tag], v81\n\tv_cmpx_eq_u32 %[tag], v83\n\tv_cmpx_eq_u32 %[tag], v85\n\tv_cmpx_eq_u32 %[tag], v87\n\tv_cmpx_eq_u32 %[tag], v89\n\tv_cmpx_eq_u32 %[tag], v91\n\tv_cmpx_eq_u32 %[tag], v93\n\tv_cmpx_eq_u32 %[tag], v95\n\tv_cmpx_eq_u32 %[tag], v97\n\tv_cmpx_eq_u32 %[tag], v99\n\ts_cmp_eq_u64 exec, -1\n\ts_mov_b64 exec, -1\n\ts_cbranch_scc1 3f\n\tglobal_load_dwordx4 v[64:67], %[pb0], %[ws] sc1\n\tglobal_load_dwordx4 v[68:71], %[pb1], %[ws] sc1\n\tglobal_load_dwordx4 v[72:75], %[pb2], %[ws] sc1\n\tglobal_load_dwordx4 v[76:79], %[pb3], %[ws] sc1\n\tglobal_load_dwordx4 v[80:83], %[pb4], %[ws] sc1\n\tglobal_load_dwordx4 v[84:87], %[pb5], %[ws] sc1\n\tglobal_load_dwordx4 v[88:91], %[pb6], %[ws] sc1\n\tglobal_load_dwordx4 v[92:95], %[pb7], %[ws] sc1\n\tglobal_load_dwordx4 v[96:99], %[pb8], %[ws] sc1\n\ts_sleep 6\n\tglobal_load_dwordx4 v[100:103], %[pb0], %[ws] sc1\n\tglobal_load_dwordx4 v[104:107], %[pb1], %[ws] sc1\n\tglobal_load_dwordx4 v[108:111], %[pb2], %[ws] sc1\n\tglobal_load_dwordx4 v[112:115], %[pb3], %[ws] sc1\n\tglobal_load_dwordx4 v[116:119], %[pb4], %[ws] sc1\n\tglobal_load_dwordx4 v[120:123], %[pb5], %[ws] sc1\n\tglobal_load_dwordx4 v[124:127], %[pb6], %[ws] sc1\n\tglobal_load_dwordx4 v[128:131], %[pb7], %[ws] sc1\n\tglobal_load_dwordx4 v[132:135], %[pb8], %[ws] sc1\n\t"
                         "1:\n\ts_waitcnt vmcnt(9)\n\tv_cmpx_eq_u32 %[tag], v65\n\tv_cmpx_eq_u32 %[tag], v67\n\tv_cmpx_eq_u32 %[tag], v69\n\tv_cmpx_eq_u32 %[tag], v71\n\tv_cmpx_eq_u32 %[tag], v73\n\tv_cmpx_eq_u32 %[tag], v75\n\tv_cmpx_eq_u32 %[tag], v77\n\tv_cmpx_eq_u32 %[tag], v79\n\tv_cmpx_eq_u32 %[tag], v81\n\tv_cmpx_eq_u32 %[tag], v83\n\tv_cmpx_eq_u32 %[tag], v85\n\tv_cmpx_eq_u32 %[tag], v87\n\tv_cmpx_eq_u32 %[tag], v89\n\tv_cmpx_eq_u32 %[tag], v91\n\tv_cmpx_eq_u32 %[tag], v93\n\tv_cmpx_eq_u32 %[tag], v95\n\tv_cmpx_eq_u32 %[tag], v97\n\tv_cmpx_eq_u32 %[tag], v99\n\ts_cmp_eq_u64 exec, -1\n\ts_mov_b64 exec, -1\n\ts_cbranch_scc1 3f\n\tglobal_load_dwordx4 v[64:67], %[pb0], %[ws] sc1\n\tglobal_load_dwordx4 v[68:71], %[pb1], %[ws] sc1\n\tglobal_load_dwordx4 v[72:75], %[pb2], %[ws] sc1\n\tglobal_load_dwordx4 v[76:79], %[pb3], %[ws] sc1\n\tglobal_load_dwordx4 v[80:83], %[pb4], %[ws] sc1\n\tglobal_load_dwordx4 v[84:87], %[pb5], %[ws] sc1\n\tglobal_load_dwordx4 v[88:91], %[pb6], %[ws] sc1\n\tglobal_load_dwordx4 v[92:95], %[pb7], %[ws] sc1\n\tglobal_load_dwordx4 v[96:99], %[pb8], %[ws] sc1\n\t"
                         "s_waitcnt vmcnt(9)\n\tv_cmpx_eq_u32 %[tag], v101\n\tv_cmpx_eq_u32 %[tag], v103\n\tv_cmpx_eq_u32 %[tag], v105\n\tv_cmpx_eq_u32 %[tag], v107\n\tv_cmpx_eq_u32 %[tag], v109\n\tv_cmpx_eq_u32 %[tag], v111\n\tv_cmpx_eq_u32 %[tag], v113\n\tv_cmpx_eq_u32 %[tag], v115\n\tv_cmpx_eq_u32 %[tag], v117\n\tv_cmpx_eq_u32 %[tag], v119\n\tv_cmpx_eq_u32 %[tag], v121\n\tv_cmpx_eq_u32 %[tag], v123\n\tv_cmpx_eq_u32 %[tag], v125\n\tv_cmpx_eq_u32 %[tag], v127\n\tv_cmpx_eq_u32 %[tag], v129\n\tv_cmpx_eq_u32 %[tag], v131\n\tv_cmpx_eq_u32 %[tag], v133\n\tv_cmpx_eq_u32 %[tag], v135\n\ts_cmp_eq_u64 exec, -1\n\ts_mov_b64 exec, -1\n\ts_cbranch_scc1 2f\n\tglobal_load_dwordx4 v[100:103], %[pb0], %[ws] sc1\n\tglobal_load_dwordx4 v[104:107], %[pb1], %[ws] sc1\n\tglobal_load_dwordx4 v[108:111], %[pb2], %[ws] sc1\n\tglobal_load_dwordx4 v[112:115], %[pb3], %[ws] sc1\n\tglobal_load_dwordx4 v[116:119], %[pb4], %[ws] sc1\n\tglobal_load_dwordx4 v[120:123], %[pb5], %[ws] sc1\n\tglobal_load_dwordx4 v[124:127], %[pb6], %[ws] sc1\n\tglobal_load_dwordx4 v[128:131], %[pb7], %[ws] sc1\n\tglobal_load_dwordx4 v[132:135], %[pb8], %[ws] sc1\n\t"
                         "s_sub_u32 %[c], %[c], 1\n\ts_cbranch_scc0 1b\n\ts_waitcnt vmcnt(0)\n"
                         "2:\n\tv_mov_b32 v101, v104\n\tv_mov_b32 v103, v106\n\tv_mov_b32 v109, v112\n\tv_mov_b32 v111, v114\n\tv_mov_b32 v117, v120\n\tv_mov_b32 v119, v122\n\tv_mov_b32 v125, v128\n\tv_mov_b32 v127, v130\n\tv_mov_b32 v133, 0\n\tv_mov_b32 v135, 0\n\tv_mfma_f32_16x16x32_bf16 v[136:139], %[sel], v[100:103], 0\n\tv_mfma_f32_16x16x32_bf16 v[140:143], %[sel], v[108:111], 0\n\tv_mfma_f32_16x16x32_bf16 v[136:139], %[sel], v[116:119], v[136:139]\n\tv_mfma_f32_16x16x32_bf16 v[140:143], %[sel], v[124:127], v[140:143]\n\tv_mfma_f32_16x16x32_bf16 v[136:139], %[sel], v[132:135], v[136:139]\n\ts_branch 4f\n"
                         "3:\n\tv_mov_b32 v65, v68\n\tv_mov_b32 v67, v70\n\tv_mov_b32 v73, v76\n\tv_mov_b32 v75, v78\n\tv_mov_b32 v81, v84\n\tv_mov_b32 v83, v86\n\tv_mov_b32 v89, v92\n\tv_mov_b32 v91, v94\n\tv_mov_b32 v97, 0\n\tv_mov_b32 v99, 0\n\tv_mfma_f32_16x16x32_bf16 v[136:139], %[sel], v[64:67], 0\n\tv_mfma_f32_16x16x32_bf16 v[140:143], %[sel], v[72:75], 0\n\tv_mfma_f32_16x16x32_bf16 v[136:139], %[sel], v[80:83], v[136:139]\n\tv_mfma_f32_16x16x32_bf16 v[140:143], %[sel], v[88:91], v[140:143]\n\tv_mfma_f32_16x16x32_bf16 v[136:139], %[sel], v[96:99], v[136:139]\n\t\n4:\n\ts_nop 7"
                         : "+{v[64:95]}"(ra), "+{v[96:99]}"(rc), "=&{v[100:131]}"(rb), "=&{v[132:135]}"(rd), "=&{v[136:139]}"(s0), "=&{v[140:143]}"(s1), [c] "=&s"(c_)
                         : [pb0] "v"(pb[0]), [pb1] "v"(pb[1]), [pb2] "v"(pb[2]), [pb3] "v"(pb[3]), [pb4] "v"(pb[4]), [pb5] "v"(pb[5]), [pb6] "v"(pb[6]), [pb7] "v"(pb[7]), [pb8] "v"(pb[8]), [ws] "s"(p.ws), [tag] "s"(tag), [sel] "v"(sel4) : "memory", "scc", "vcc");
            float o[4];
#pragma unroll
            for (int c = 0; c < 4; ++c) o[c] = fmaf(omg, xv[c] + rbf(rbf(dpp_add<0x128, 0xf>(s0[c] + s1[c])) * rs), av[c]);
            v2u ov = {cvtpk(o[0], o[1]), cvtpk(o[2], o[3])};
            asm volatile("" : "+v"(ov));
            LWAIT(ctl, C_ACK, 4);
            asm volatile("s_mov_b64 exec, 0xff\n\tglobal_store_dwordx2 %0, %1, %2\n\ts_mov_b64 exec, -1" :: "v"(oo), "v"(ov), "s"(p.out) : "memory");
            asm volatile("s_waitcnt vmcnt(0)" :: "v"(ra), "v"(rb), "v"(rc), "v"(rd) : "memory");
        }
        ROLE_END();
    }
    }
}

extern "C" void kernel_launch(void* const* d_in, const int* in_sizes, int n_in,
                              void* d_out, int out_size, void* d_ws, size_t ws_size,
                              hipStream_t stream) {
    Params p;
    p.x = (const u16*)d_in[0]; p.Are = (const u16*)d_in[1]; p.Aim = (const u16*)d_in[2]; p.Bre = (const u16*)d_in[3];
    p.Bim = (const u16*)d_in[4]; p.Cre = (const u16*)d_in[5]; p.Cim = (const u16*)d_in[6]; p.logdt = (const u16*)d_in[7];
    p.dskip = (const u16*)d_in[8]; p.wr = (const u16*)d_in[10]; p.wg = (const u16*)d_in[11]; p.wu = (const u16*)d_in[12];
    p.wd = (const u16*)d_in[13]; p.gw = (const u16*)d_in[14]; p.out = (u16*)d_out; p.ws = (char*)d_ws;
    (void)hipFuncSetAttribute((const void*)fused_kernel, hipFuncAttributeMaxDynamicSharedMemorySize, LDS_ALLOC);
    fused_kernel<<<256, 512, LDS_ALLOC, stream>>>(p);
}
```

```cpp
#include <hip/hip_runtime.h>
#include <stdint.h>

typedef unsigned short u16;
typedef unsigned int u32;
typedef unsigned long long u64;
typedef __attribute__((ext_vector_type(4))) u32 v4u;
typedef __attribute__((ext_vector_type(2))) u32 v2u;
typedef __attribute__((ext_vector_type(2))) float v2f;
typedef __attribute__((ext_vector_type(4))) float v4f;
typedef __attribute__((ext_vector_type(16))) float v16f;
typedef __attribute__((ext_vector_type(8))) short bf16x8;
typedef __attribute__((ext_vector_type(4))) short s16x4;
typedef __attribute__((ext_vector_type(2))) __bf16 bf16x2_t;
#define LAS __attribute__((address_space(3)))
#define GAS __attribute__((address_space(1)))
#define RLX_AGENT __ATOMIC_RELAXED, __HIP_MEMORY_SCOPE_AGENT
#define SBAR() __builtin_amdgcn_sched_barrier(0)
#define BAR() asm volatile("s_waitcnt lgkmcnt(0)\n\ts_barrier" ::: "memory")
#define WSR() __builtin_amdgcn_make_buffer_rsrc(p.ws, 0, 0x7fffffff, 0x00020000)
#define REREAD() asm volatile("" ::: "memory")

constexpr int NBX = 2, CB = 16 + 16 / NBX, FA = 11, FB = (72 - (8 - NBX) * FA) / NBX, WPFB = 3;
#define WNT " nt"
constexpr int CODE_KB = 25;
constexpr int NB1 = 2;
constexpr int REPG = 2;
constexpr int PFG0 = 528;
constexpr int WPF = 2;
constexpr int SELWAIT = 50;

constexpr int L = 2048, D = 576, NST = 128, F = 4608;
constexpr int NFFN = 72;
constexpr u32 OFF_RG = 4096;
constexpr u32 OFF_G = OFF_RG + 20480;
constexpr u32 OFF_SEL = OFF_G + REPG * 10240;
constexpr u32 OFF_CI = OFF_SEL + 16 * 256;
constexpr u32 OFF_PART = OFF_CI + 4096;
constexpr u32 PP = 11 * 1024;
constexpr u32 SPIN_CAP = 1u << 19;


struct Params {
    const u16 *x, *Are, *Aim, *Bre, *Bim, *Cre, *Cim, *logdt, *dskip, *wr, *wg, *wu, *wd, *gw;
    u16* out;
    char* ws;
};

__device__ __forceinline__ float bflo(u32 w) { return __uint_as_float(w << 16); }
__device__ __forceinline__ float bfhi(u32 w) { return __uint_as_float(w & 0xffff0000u); }
__device__ __forceinline__ float bf2f(u16 h) { return __uint_as_float((u32)h << 16); }
__device__ __forceinline__ u32 cvtpk(float lo, float hi) { v2f v = {lo, hi}; bf16x2_t r = __builtin_convertvector(v, bf16x2_t); return __builtin_bit_cast(u32, r); }
__device__ __forceinline__ float rbf(float f) { return bflo(cvtpk(f, f)); }
__device__ __forceinline__ float sigmoidf_(float s) { return __builtin_amdgcn_rcpf(1.f + __expf(-s)); }

template <int CTRL, int RM> __device__ __forceinline__ float dpp_add(float v) {
    return v + __builtin_bit_cast(float, __builtin_amdgcn_update_dpp(0, __builtin_bit_cast(int, v), CTRL, RM, 0xf, true));
}
template <int CTRL, int RM> __device__ __forceinline__ u32 dpp_umax(u32 v) {
    u32 o = (u32)__builtin_amdgcn_update_dpp(0, (int)v, CTRL, RM, 0xf, true);
    return v > o ? v : o;
}

typedef v2f cf;
__device__ __forceinline__ cf cmul(cf a, cf b) { cf t = a.yy * (cf){-b.y, b.x}; return __builtin_elementwise_fma(a.xx, b, t); }
__device__ __forceinline__ cf cexp_(float a2, float brev, float rho) {
    float m = __builtin_amdgcn_exp2f(a2 * rho);
    float ph = __builtin_amdgcn_fractf(brev * rho);
    cf c; c.x = m * __builtin_amdgcn_cosf(ph); c.y = m * __builtin_amdgcn_sinf(ph); return c;
}

struct cf2 { cf a, b; };
__device__ __forceinline__ cf2 cmul2(cf2 x, cf2 y) { return {cmul(x.a, y.a), cmul(x.b, y.b)}; }
__device__ __forceinline__ cf2 cexp2(v2f a2, v2f brev, float rho) { return {cexp_(a2.x, brev.x, rho), cexp_(a2.y, brev.y, rho)}; }

__device__ __forceinline__ u32 make_payload(float r, float g) {
    const u32 w = cvtpk(g, r);
    return w ^ (((u32)((int)w >> 31) & 0x7fff0000u) | 0x80000000u);
}
__device__ __forceinline__ float key_to_r(u32 k) {
    u32 hi = k & 0xffff0000u;
    return __uint_as_float((hi & 0x80000000u) ? (hi ^ 0x80000000u) : (~hi & 0xffff0000u));
}


constexpr int STG_ROW = 528;
constexpr int NA = 21;
constexpr int KCH = (NA + 32) * STG_ROW;
constexpr int KCOPY = 1344;
constexpr int CH_Y = 4 * KCOPY;
constexpr int LDS_U = 4 * KCH;
constexpr int LDS_TAP = LDS_U + 4 * 4608;
constexpr int LDS_CTL = LDS_TAP + 4 * 2304;
constexpr int C_TAG = 0, C_STG = 4, C_PUB = 8, C_ACK = 12, C_GOK = 16, C_ST2 = 20, C_Y = 64;
constexpr int LDS_RW = LDS_CTL + 1024;
constexpr int LDS_G = LDS_RW;
constexpr int LDS_DUMMY = LDS_RW + 4 * 3072;
constexpr int LDS_TOTAL = LDS_DUMMY + 1024;
static_assert(CH_Y + 8 * 1152 <= KCH && LDS_TOTAL <= 160 * 1024, "lds");
constexpr int WDP = 1184;
constexpr int XSP = 1168;
constexpr int FL_XS = 144 * 1024;
constexpr int FL_H = FL_XS + 8 * XSP;
constexpr int FL_Z = FL_H + 8 * 144;
constexpr int FL_TAG = FL_Z + 256;
constexpr int LDS_ALLOC = FL_TAG + 64 > LDS_TOTAL ? FL_TAG + 64 : LDS_TOTAL;
static_assert(LDS_ALLOC <= 160 * 1024, "lds");

__device__ __forceinline__ u32 top8_d(LAS char* t8, int lane, int n) {
    LAS u32* cand = (LAS u32*)(t8 + 256);
    u32 res = 0;
    _Pragma("unroll 1") for (int i0 = 0; i0 < n; i0 += 64) {
        const int i = i0 + lane;
        const u32 k = i < n ? cand[i] : 0u;
        int rk = 0;
        _Pragma("unroll 1") for (int j0 = 0; j0 < n; j0 += 16) {
            v4u c[4];
#pragma unroll
            for (int e = 0; e < 4; ++e) c[e] = *(LAS v4u*)(cand + j0 + 4 * e);
#pragma unroll
            for (int e = 0; e < 16; ++e) rk += (c[e >> 2][e & 3] > k) & (j0 + e < n);
        }
        const bool hit = i < n && rk < 8;
        const u32 got = (u32)__builtin_amdgcn_ds_permute((hit ? rk : 63) * 4, (int)(hit ? k : 0u));
        if (lane < 8) res |= got;
    }
    return res;
}


#define LWAIT_BODY "1:\n\tds_read_b32 %0, %3 offset:%4\n\ts_waitcnt lgkmcnt(0)\n\tv_readfirstlane_b32 %1, %0\n\ts_cmp_ge_u32 %1, %5\n\ts_cbranch_scc1 2f\n\ts_sleep 1\n\ts_sub_u32 %2, %2, 1\n\ts_cbranch_scc0 1b\n2:"
#define LWAIT(addr, off, want) do { u32 t_, s_, c_; asm volatile("s_mov_b32 %2, 0x400000\n" LWAIT_BODY : "=&v"(t_), "=&s"(s_), "=&s"(c_) : "v"(addr), "n"(off), "n"(want) : "memory", "scc"); } while (0)
#define LDS_ADD1(addr, one, off) asm volatile("s_mov_b64 exec, 1\n\tds_add_u32 %0, %1 offset:%2\n\ts_mov_b64 exec, -1" :: "v"(addr), "v"(one), "n"(off) : "memory")
__device__ __forceinline__ bf16x8 band(u32 bits, u32 val) {
    v4u r;
#pragma unroll
    for (int j = 0; j < 4; ++j) r[j] = __builtin_amdgcn_perm((u32)__builtin_amdgcn_sbfe((int)bits, 2 * j + 1, 1), (u32)__builtin_amdgcn_sbfe((int)bits, 2 * j, 1), 0x05040100u) & val;
    return __builtin_bit_cast(bf16x8, r);
}

__device__ __forceinline__ u32 hline(const char* ws, u32 i) { const u32 q = i / 7u, r = i - 7u * q; return (8u * q + r + (r >= ((3u - (u32)((uintptr_t)ws >> 7)) & 7u))) * 128u; }
struct Xcd { u32 nconv, nffn, w0, inv, wpf; };
__device__ __forceinline__ Xcd xcd_shares(int xcd) {
    const u32 m = (u32)((xcd - NBX) >> 31);
    return {16u + ((CB - 16) & m), FA - ((FA - FB) & m), (u32)(FA * xcd - (FA - FB) * min(xcd, NBX)), 65535u / FA + 1 + ((65535u / FB - 65535u / FA) & m), WPF - ((NBX < 8 ? WPF - WPFB : 0) & m)};
}
__device__ __forceinline__ u32 wd_line(const Xcd& x, u32 i) {
    const u32 k = i / 9u, r = (k * x.inv) >> 16;
    return ((x.w0 + k - x.nffn * r) * 64u + (r >> 1) + 32u * (r & 1)) * 1152u + (i - 9u * k) * 128u;
}
__device__ __forceinline__ bool touch(const Params& p, const Xcd& x, int e, int lane, u32 dst) {
    const u32 wa = x.w0 + ((3 - (u32)(((uintptr_t)p.wg >> 7) & 7) - x.w0) & 7), two = wa + 8 < x.w0 + x.nffn;
    const u32 g0 = two ? 576 : PFG0, na = wa < x.w0 + x.nffn ? (g0 * 2) << two : 0, nb = (576 - g0) * 2 * x.nffn, nc = x.wpf * 192 * x.nffn;
    if ((u32)e * 64 >= na + nb + nc) return false;
    const u32 i = min((u32)(e * 64 + lane), na + nb + nc - 1);
    const u32 j = i - na, k = (j * x.inv) >> 16;
    const bool first = i < na, last = i >= na + nb;
    const u32 seg = first ? i >> two : k + 2 * g0, col = first ? (wa + 8 * (i & two)) * 128u : (x.w0 + j - x.nffn * k) * 128u;
    const char* a = (const char*)(last ? p.wd : ((seg & 1) ? p.wu : p.wg)) + (last ? wd_line(x, j - nb) : (seg >> 1) * (F * 2) + col);
    asm volatile("s_mov_b32 m0, %1\n\ts_nop 0\n\tglobal_load_lds_dword %0, off" :: "v"(a), "s"(dst) : "memory");
    return true;
}

#define KGEN_ASM ".p2align 3\n\tv_mov_b32 v64, 0x3c00\n\tv_mov_b32 v65, 0x3c00\n\tv_pk_mul_f32 v[66:67], v[56:57], v[56:57] op_sel:[1,1] op_sel_hi:[1,0] neg_lo:[0,1]\n\tv_pk_mul_f32 v[68:69], v[58:59], v[58:59] op_sel:[1,1] op_sel_hi:[1,0] neg_lo:[0,1]\n\tv_pk_mul_f32 v[70:71], v[52:53], v[52:53] op_sel:[1,1] op_sel_hi:[1,0] neg_lo:[0,1]\n\tv_pk_mul_f32 v[72:73], v[54:55], v[54:55] op_sel:[1,1] op_sel_hi:[1,0] neg_lo:[0,1]\n\tv_pk_fma_f32 v[66:67], v[56:57], v[56:57], v[66:67] op_sel:[0,0,0] op_sel_hi:[0,1,1]\n\tv_pk_fma_f32 v[68:69], v[58:59], v[58:59], v[68:69] op_sel:[0,0,0] op_sel_hi:[0,1,1]\n\tv_pk_fma_f32 v[70:71], v[52:53], v[52:53], v[70:71] op_sel:[0,0,0] op_sel_hi:[0,1,1]\n\tv_pk_fma_f32 v[72:73], v[54:55], v[54:55], v[72:73] op_sel:[0,0,0] op_sel_hi:[0,1,1]\n\tv_cvt_pk_f16_f32 v74, v56, -v57\n\tv_cvt_pk_f16_f32 v75, v58, -v59\n\tv_cvt_pk_f16_f32 v76, v48, v49\n\tv_cvt_pk_f16_f32 v77, v50, v51\n\tv_pk_mul_f32 v[78:79], v[48:49], v[60:61] op_sel:[1,1] op_sel_hi:[1,0] neg_lo:[0,1]\n\tv_pk_mul_f32 v[80:81], v[50:51], v[62:63] op_sel:[1,1] op_sel_hi:[1,0] neg_lo:[0,1]\n\tv_pk_mul_f32 v[82:83], v[48:49], v[52:53] op_sel:[1,1] op_sel_hi:[1,0] neg_lo:[0,1]\n\tv_pk_mul_f32 v[84:85], v[50:51], v[54:55] op_sel:[1,1] op_sel_hi:[1,0] neg_lo:[0,1]\n\tds_write2_b64 %[b0], v[64:65], v[74:75] offset0:0 offset1:66\n\tv_pk_fma_f32 v[78:79], v[48:49], v[60:61], v[78:79] op_sel:[0,0,0] op_sel_hi:[0,1,1]\n\tv_pk_fma_f32 v[80:81], v[50:51], v[62:63], v[80:81] op_sel:[0,0,0] op_sel_hi:[0,1,1]\n\tv_pk_fma_f32 v[82:83], v[48:49], v[52:53], v[82:83] op_sel:[0,0,0] op_sel_hi:[0,1,1]\n\tv_pk_fma_f32 v[84:85], v[50:51], v[54:55], v[84:85] op_sel:[0,0,0] op_sel_hi:[0,1,1]\n\tv_cvt_pk_f16_f32 v86, v66, -v67\n\tv_cvt_pk_f16_f32 v87, v68, -v69\n\tv_pk_mul_f32 v[88:89], v[66:67], v[66:67] op_sel:[1,1] op_sel_hi:[1,0] neg_lo:[0,1]\n\tv_pk_mul_f32 v[90:91], v[68:69], v[68:69] op_sel:[1,1] op_sel_hi:[1,0] neg_lo:[0,1]\n\tv_pk_mul_f32 v[92:93], v[66:67], v[56:57] op_sel:[1,1] op_sel_hi:[1,0] neg_lo:[0,1]\n\tv_pk_mul_f32 v[94:95], v[68:69], v[58:59] op_sel:[1,1] op_sel_hi:[1,0] neg_lo:[0,1]\n\tv_pk_fma_f32 v[88:89], v[66:67], v[66:67], v[88:89] op_sel:[0,0,0] op_sel_hi:[0,1,1]\n\tv_pk_fma_f32 v[90:91], v[68:69], v[68:69], v[90:91] op_sel:[0,0,0] op_sel_hi:[0,1,1]\n\tv_pk_fma_f32 v[92:93], v[66:67], v[56:57], v[92:93] op_sel:[0,0,0] op_sel_hi:[0,1,1]\n\tv_pk_fma_f32 v[94:95], v[68:69], v[58:59], v[94:95] op_sel:[0,0,0] op_sel_hi:[0,1,1]\n\tv_cvt_pk_f16_f32 v64, v78, v79\n\tv_cvt_pk_f16_f32 v65, v80, v81\n\tv_cvt_pk_f16_f32 v96, v82, v83\n\tv_cvt_pk_f16_f32 v97, v84, v85\n\tv_pk_mul_f32 v[98:99], v[70:71], v[70:71] op_sel:[1,1] op_sel_hi:[1,0] neg_lo:[0,1]\n\tv_pk_mul_f32 v[100:101], v[72:73], v[72:73] op_sel:[1,1] op_sel_hi:[1,0] neg_lo:[0,1]\n\tv_pk_mul_f32 v[102:103], v[78:79], v[56:57] op_sel:[1,1] op_sel_hi:[1,0] neg_lo:[0,1]\n\tv_pk_mul_f32 v[104:105], v[80:81], v[58:59] op_sel:[1,1] op_sel_hi:[1,0] neg_lo:[0,1]\n\tds_write2_b64 %[b8], v[76:77], v[96:97] offset0:0 offset1:66\n\tv_pk_fma_f32 v[98:99], v[70:71], v[70:71], v[98:99] op_sel:[0,0,0] op_sel_hi:[0,1,1]\n\tv_pk_fma_f32 v[100:101], v[72:73], v[72:73], v[100:101] op_sel:[0,0,0] op_sel_hi:[0,1,1]\n\tv_pk_fma_f32 v[102:103], v[78:79], v[56:57], v[102:103] op_sel:[0,0,0] op_sel_hi:[0,1,1]\n\tv_pk_fma_f32 v[104:105], v[80:81], v[58:59], v[104:105] op_sel:[0,0,0] op_sel_hi:[0,1,1]\n\tv_cvt_pk_f16_f32 v106, v88, -v89\n\tv_cvt_pk_f16_f32 v107, v90, -v91\n\tv_cvt_pk_f16_f32 v108, v92, -v93\n\tv_cvt_pk_f16_f32 v109, v94, -v95\n\tv_pk_mul_f32 v[110:111], v[78:79], v[70:71] op_sel:[1,1] op_sel_hi:[1,0] neg_lo:[0,1]\n\tv_pk_mul_f32 v[112:113], v[80:81], v[72:73] op_sel:[1,1] op_sel_hi:[1,0] neg_lo:[0,1]\n\tv_pk_mul_f32 v[114:115], v[48:49], v[70:71] op_sel:[1,1] op_sel_hi:[1,0] neg_lo:[0,1]\n\tv_pk_mul_f32 v[116:117], v[50:51], v[72:73] op_sel:[1,1] op_sel_hi:[1,0] neg_lo:[0,1]\n\tds_write2_b64 %[b0], v[86:87], v[108:109] offset0:132 offset1:198\n\tv_pk_fma_f32 v[110:111], v[78:79], v[70:71], v[110:111] op_sel:[0,0,0] op_sel_hi:[0,1,1]\n\tv_pk_fma_f32 v[112:113], v[80:81], v[72:73], v[112:113] op_sel:[0,0,0] op_sel_hi:[0,1,1]\n\tv_pk_fma_f32 v[114:115], v[48:49], v[70:71], v[114:115] op_sel:[0,0,0] op_sel_hi:[0,1,1]\n\tv_pk_fma_f32 v[116:117], v[50:51], v[72:73], v[116:117] op_sel:[0,0,0] op_sel_hi:[0,1,1]\n\tv_cvt_pk_f16_f32 v76, v102, v103\n\tv_cvt_pk_f16_f32 v77, v104, v105\n\tv_pk_mul_f32 v[96:97], v[82:83], v[70:71] op_sel:[1,1] op_sel_hi:[1,0] neg_lo:[0,1]\n\tv_pk_mul_f32 v[118:119], v[84:85], v[72:73] op_sel:[1,1] op_sel_hi:[1,0] neg_lo:[0,1]\n\tv_pk_mul_f32 v[120:121], v[88:89], v[88:89] op_sel:[1,1] op_sel_hi:[1,0] neg_lo:[0,1]\n\tv_pk_mul_f32 v[122:123], v[90:91], v[90:91] op_sel:[1,1] op_sel_hi:[1,0] neg_lo:[0,1]\n\tv_pk_fma_f32 v[96:97], v[82:83], v[70:71], v[96:97] op_sel:[0,0,0] op_sel_hi:[0,1,1]\n\tv_pk_fma_f32 v[118:119], v[84:85], v[72:73], v[118:119] op_sel:[0,0,0] op_sel_hi:[0,1,1]\n\tv_pk_fma_f32 v[120:121], v[88:89], v[88:89], v[120:121] op_sel:[0,0,0] op_sel_hi:[0,1,1]\n\tv_pk_fma_f32 v[122:123], v[90:91], v[90:91], v[122:123] op_sel:[0,0,0] op_sel_hi:[0,1,1]\n\tv_cvt_pk_f16_f32 v124, v110, v111\n\tv_cvt_pk_f16_f32 v125, v112, v113\n\tv_cvt_pk_f16_f32 v126, v114, v115\n\tv_cvt_pk_f16_f32 v127, v116, v117\n\tv_pk_mul_f32 v[70:71], v[88:89], v[56:57] op_sel:[1,1] op_sel_hi:[1,0] neg_lo:[0,1]\n\tv_pk_mul_f32 v[72:73], v[90:91], v[58:59] op_sel:[1,1] op_sel_hi:[1,0] neg_lo:[0,1]\n\tv_pk_mul_f32 v[128:129], v[102:103], v[60:61] op_sel:[1,1] op_sel_hi:[1,0] neg_lo:[0,1]\n\tv_pk_mul_f32 v[130:131], v[104:105], v[62:63] op_sel:[1,1] op_sel_hi:[1,0] neg_lo:[0,1]\n\tds_write2_b64 %[b11], v[64:65], v[124:125] offset0:0 offset1:66\n\tv_pk_fma_f32 v[70:71], v[88:89], v[56:57], v[70:71] op_sel:[0,0,0] op_sel_hi:[0,1,1]\n\tv_pk_fma_f32 v[72:73], v[90:91], v[58:59], v[72:73] op_sel:[0,0,0] op_sel_hi:[0,1,1]\n\tv_pk_fma_f32 v[128:129], v[102:103], v[60:61], v[128:129] op_sel:[0,0,0] op_sel_hi:[0,1,1]\n\tv_pk_fma_f32 v[130:131], v[104:105], v[62:63], v[130:131] op_sel:[0,0,0] op_sel_hi:[0,1,1]\n\tv_cvt_pk_f16_f32 v132, v96, v97\n\tv_cvt_pk_f16_f32 v133, v118, v119\n\tv_cvt_pk_f16_f32 v134, v120, -v121\n\tv_cvt_pk_f16_f32 v135, v122, -v123\n\tv_pk_mul_f32 v[102:103], v[78:79], v[98:99] op_sel:[1,1] op_sel_hi:[1,0] neg_lo:[0,1]\n\tv_pk_mul_f32 v[104:105], v[80:81], v[100:101] op_sel:[1,1] op_sel_hi:[1,0] neg_lo:[0,1]\n\tv_pk_mul_f32 v[136:137], v[88:89], v[66:67] op_sel:[1,1] op_sel_hi:[1,0] neg_lo:[0,1]\n\tv_pk_mul_f32 v[138:139], v[90:91], v[68:69] op_sel:[1,1] op_sel_hi:[1,0] neg_lo:[0,1]\n\tds_write2_b64 %[b8], v[126:127], v[132:133] offset0:132 offset1:198\n\tv_pk_fma_f32 v[102:103], v[78:79], v[98:99], v[102:103] op_sel:[0,0,0] op_sel_hi:[0,1,1]\n\tv_pk_fma_f32 v[104:105], v[80:81], v[100:101], v[104:105] op_sel:[0,0,0] op_sel_hi:[0,1,1]\n\tv_pk_fma_f32 v[136:137], v[88:89], v[66:67], v[136:137] op_sel:[0,0,0] op_sel_hi:[0,1,1]\n\tv_pk_fma_f32 v[138:139], v[90:91], v[68:69], v[138:139] op_sel:[0,0,0] op_sel_hi:[0,1,1]\n\tv_cvt_pk_f16_f32 v64, v70, -v71\n\tv_cvt_pk_f16_f32 v65, v72, -v73\n\tv_cvt_pk_f16_f32 v124, v128, v129\n\tv_cvt_pk_f16_f32 v125, v130, v131\n\tv_pk_mul_f32 v[78:79], v[88:89], v[92:93] op_sel:[1,1] op_sel_hi:[1,0] neg_lo:[0,1]\n\tv_pk_mul_f32 v[80:81], v[90:91], v[94:95] op_sel:[1,1] op_sel_hi:[1,0] neg_lo:[0,1]\n\tv_pk_mul_f32 v[140:141], v[120:121], v[120:121] op_sel:[1,1] op_sel_hi:[1,0] neg_lo:[0,1]\n\tv_pk_mul_f32 v[142:143], v[122:123], v[122:123] op_sel:[1,1] op_sel_hi:[1,0] neg_lo:[0,1]\n\tds_write2_b64 %[b1], v[106:107], v[64:65] offset0:0 offset1:66\n\tv_pk_fma_f32 v[78:79], v[88:89], v[92:93], v[78:79] op_sel:[0,0,0] op_sel_hi:[0,1,1]\n\tv_pk_fma_f32 v[80:81], v[90:91], v[94:95], v[80:81] op_sel:[0,0,0] op_sel_hi:[0,1,1]\n\tv_pk_fma_f32 v[140:141], v[120:121], v[120:121], v[140:141] op_sel:[0,0,0] op_sel_hi:[0,1,1]\n\tv_pk_fma_f32 v[142:143], v[122:123], v[122:123], v[142:143] op_sel:[0,0,0] op_sel_hi:[0,1,1]\n\tv_cvt_pk_f16_f32 v126, v102, v103\n\tv_cvt_pk_f16_f32 v127, v104, v105\n\tv_cvt_pk_f16_f32 v132, v136, -v137\n\tv_cvt_pk_f16_f32 v133, v138, -v139\n\tv_pk_mul_f32 v[144:145], v[120:121], v[56:57] op_sel:[1,1] op_sel_hi:[1,0] neg_lo:[0,1]\n\tv_pk_mul_f32 v[146:147], v[122:123], v[58:59] op_sel:[1,1] op_sel_hi:[1,0] neg_lo:[0,1]\n\tv_pk_mul_f32 v[148:149], v[128:129], v[60:61] op_sel:[1,1] op_sel_hi:[1,0] neg_lo:[0,1]\n\tv_pk_mul_f32 v[150:151], v[130:131], v[62:63] op_sel:[1,1] op_sel_hi:[1,0] neg_lo:[0,1]\n\tds_write2_b64 %[b9], v[76:77], v[124:125] offset0:0 offset1:66\n\tv_pk_fma_f32 v[144:145], v[120:121], v[56:57], v[144:145] op_sel:[0,0,0] op_sel_hi:[0,1,1]\n\tv_pk_fma_f32 v[146:147], v[122:123], v[58:59], v[146:147] op_sel:[0,0,0] op_sel_hi:[0,1,1]\n\tv_pk_fma_f32 v[148:149], v[128:129], v[60:61], v[148:149] op_sel:[0,0,0] op_sel_hi:[0,1,1]\n\tv_pk_fma_f32 v[150:151], v[130:131], v[62:63], v[150:151] op_sel:[0,0,0] op_sel_hi:[0,1,1]\n\tv_cvt_pk_f16_f32 v152, v78, -v79\n\tv_cvt_pk_f16_f32 v153, v80, -v81\n\tv_cvt_pk_f16_f32 v154, v140, -v141\n\tv_cvt_pk_f16_f32 v155, v142, -v143\n\tv_pk_mul_f32 v[128:129], v[102:103], v[60:61] op_sel:[1,1] op_sel_hi:[1,0] neg_lo:[0,1]\n\tv_pk_mul_f32 v[130:131], v[104:105], v[62:63] op_sel:[1,1] op_sel_hi:[1,0] neg_lo:[0,1]\n\tv_pk_mul_f32 v[140:141], v[110:111], v[98:99] op_sel:[1,1] op_sel_hi:[1,0] neg_lo:[0,1]\n\tv_pk_mul_f32 v[142:143], v[112:113], v[100:101] op_sel:[1,1] op_sel_hi:[1,0] neg_lo:[0,1]\n\tds_write2_b64 %[b1], v[132:133], v[152:153] offset0:132 offset1:198\n\tv_pk_fma_f32 v[128:129], v[102:103], v[60:61], v[128:129] op_sel:[0,0,0] op_sel_hi:[0,1,1]\n\tv_pk_fma_f32 v[130:131], v[104:105], v[62:63], v[130:131] op_sel:[0,0,0] op_sel_hi:[0,1,1]\n\tv_pk_fma_f32 v[140:141], v[110:111], v[98:99], v[140:141] op_sel:[0,0,0] op_sel_hi:[0,1,1]\n\tv_pk_fma_f32 v[142:143], v[112:113], v[100:101], v[142:143] op_sel:[0,0,0] op_sel_hi:[0,1,1]\n\tv_cvt_pk_f16_f32 v76, v144, -v145\n\tv_cvt_pk_f16_f32 v77, v146, -v147\n\tv_cvt_pk_f16_f32 v124, v148, v149\n\tv_cvt_pk_f16_f32 v125, v150, v151\n\tv_pk_mul_f32 v[102:103], v[120:121], v[66:67] op_sel:[1,1] op_sel_hi:[1,0] neg_lo:[0,1]\n\tv_pk_mul_f32 v[104:105], v[122:123], v[68:69] op_sel:[1,1] op_sel_hi:[1,0] neg_lo:[0,1]\n\tv_pk_mul_f32 v[110:111], v[120:121], v[92:93] op_sel:[1,1] op_sel_hi:[1,0] neg_lo:[0,1]\n\tv_pk_mul_f32 v[112:113], v[122:123], v[94:95] op_sel:[1,1] op_sel_hi:[1,0] neg_lo:[0,1]\n\tds_write2_b64 %[b2], v[134:135], v[76:77] offset0:0 offset1:66\n\tv_pk_fma_f32 v[102:103], v[120:121], v[66:67], v[102:103] op_sel:[0,0,0] op_sel_hi:[0,1,1]\n\tv_pk_fma_f32 v[104:105], v[122:123], v[68:69], v[104:105] op_sel:[0,0,0] op_sel_hi:[0,1,1]\n\tv_pk_fma_f32 v[110:111], v[120:121], v[92:93], v[110:111] op_sel:[0,0,0] op_sel_hi:[0,1,1]\n\tv_pk_fma_f32 v[112:113], v[122:123], v[94:95], v[112:113] op_sel:[0,0,0] op_sel_hi:[0,1,1]\n\tv_cvt_pk_f16_f32 v144, v128, v129\n\tv_cvt_pk_f16_f32 v145, v130, v131\n\tv_cvt_pk_f16_f32 v146, v140, v141\n\tv_cvt_pk_f16_f32 v147, v142, v143\n\tv_pk_mul_f32 v[66:67], v[148:149], v[60:61] op_sel:[1,1] op_sel_hi:[1,0] neg_lo:[0,1]\n\tv_pk_mul_f32 v[68:69], v[150:151], v[62:63] op_sel:[1,1] op_sel_hi:[1,0] neg_lo:[0,1]\n\tv_pk_mul_f32 v[92:93], v[128:129], v[60:61] op_sel:[1,1] op_sel_hi:[1,0] neg_lo:[0,1]\n\tv_pk_mul_f32 v[94:95], v[130:131], v[62:63] op_sel:[1,1] op_sel_hi:[1,0] neg_lo:[0,1]\n\tds_write2_b64 %[b9], v[124:125], v[146:147] offset0:132 offset1:198\n\tv_pk_fma_f32 v[66:67], v[148:149], v[60:61], v[66:67] op_sel:[0,0,0] op_sel_hi:[0,1,1]\n\tv_pk_fma_f32 v[68:69], v[150:151], v[62:63], v[68:69] op_sel:[0,0,0] op_sel_hi:[0,1,1]\n\tv_pk_fma_f32 v[92:93], v[128:129], v[60:61], v[92:93] op_sel:[0,0,0] op_sel_hi:[0,1,1]\n\tv_pk_fma_f32 v[94:95], v[130:131], v[62:63], v[94:95] op_sel:[0,0,0] op_sel_hi:[0,1,1]\n\tv_cvt_pk_f16_f32 v140, v102, -v103\n\tv_cvt_pk_f16_f32 v141, v104, -v105\n\tv_cvt_pk_f16_f32 v142, v110, -v111\n\tv_cvt_pk_f16_f32 v143, v112, -v113\n\tv_pk_mul_f32 v[102:103], v[120:121], v[88:89] op_sel:[1,1] op_sel_hi:[1,0] neg_lo:[0,1]\n\tv_pk_mul_f32 v[104:105], v[122:123], v[90:91] op_sel:[1,1] op_sel_hi:[1,0] neg_lo:[0,1]\n\tv_pk_mul_f32 v[110:111], v[120:121], v[70:71] op_sel:[1,1] op_sel_hi:[1,0] neg_lo:[0,1]\n\tv_pk_mul_f32 v[112:113], v[122:123], v[72:73] op_sel:[1,1] op_sel_hi:[1,0] neg_lo:[0,1]\n\tds_write2_b64 %[b2], v[140:141], v[142:143] offset0:132 offset1:198\n\tv_pk_fma_f32 v[102:103], v[120:121], v[88:89], v[102:103] op_sel:[0,0,0] op_sel_hi:[0,1,1]\n\tv_pk_fma_f32 v[104:105], v[122:123], v[90:91], v[104:105] op_sel:[0,0,0] op_sel_hi:[0,1,1]\n\tv_pk_fma_f32 v[110:111], v[120:121], v[70:71], v[110:111] op_sel:[0,0,0] op_sel_hi:[0,1,1]\n\tv_pk_fma_f32 v[112:113], v[122:123], v[72:73], v[112:113] op_sel:[0,0,0] op_sel_hi:[0,1,1]\n\tv_cvt_pk_f16_f32 v124, v66, v67\n\tv_cvt_pk_f16_f32 v125, v68, v69\n\tv_cvt_pk_f16_f32 v128, v92, v93\n\tv_cvt_pk_f16_f32 v129, v94, v95\n\tv_pk_mul_f32 v[70:71], v[48:49], v[98:99] op_sel:[1,1] op_sel_hi:[1,0] neg_lo:[0,1]\n\tv_pk_mul_f32 v[72:73], v[50:51], v[100:101] op_sel:[1,1] op_sel_hi:[1,0] neg_lo:[0,1]\n\tv_pk_mul_f32 v[88:89], v[82:83], v[98:99] op_sel:[1,1] op_sel_hi:[1,0] neg_lo:[0,1]\n\tv_pk_mul_f32 v[90:91], v[84:85], v[100:101] op_sel:[1,1] op_sel_hi:[1,0] neg_lo:[0,1]\n\tds_write2_b64 %[b10], v[126:127], v[144:145] offset0:132 offset1:198\n\tv_pk_fma_f32 v[70:71], v[48:49], v[98:99], v[70:71] op_sel:[0,0,0] op_sel_hi:[0,1,1]\n\tv_pk_fma_f32 v[72:73], v[50:51], v[100:101], v[72:73] op_sel:[0,0,0] op_sel_hi:[0,1,1]\n\tv_pk_fma_f32 v[88:89], v[82:83], v[98:99], v[88:89] op_sel:[0,0,0] op_sel_hi:[0,1,1]\n\tv_pk_fma_f32 v[90:91], v[84:85], v[100:101], v[90:91] op_sel:[0,0,0] op_sel_hi:[0,1,1]\n\tv_cvt_pk_f16_f32 v92, v102, -v103\n\tv_cvt_pk_f16_f32 v93, v104, -v105\n\tv_cvt_pk_f16_f32 v94, v110, -v111\n\tv_cvt_pk_f16_f32 v95, v112, -v113\n\tv_pk_mul_f32 v[82:83], v[66:67], v[60:61] op_sel:[1,1] op_sel_hi:[1,0] neg_lo:[0,1]\n\tv_pk_mul_f32 v[84:85], v[68:69], v[62:63] op_sel:[1,1] op_sel_hi:[1,0] neg_lo:[0,1]\n\tv_pk_mul_f32 v[102:103], v[120:121], v[136:137] op_sel:[1,1] op_sel_hi:[1,0] neg_lo:[0,1]\n\tv_pk_mul_f32 v[104:105], v[122:123], v[138:139] op_sel:[1,1] op_sel_hi:[1,0] neg_lo:[0,1]\n\tds_write2_b64 %[b3], v[92:93], v[94:95] offset0:0 offset1:66\n\tv_pk_fma_f32 v[82:83], v[66:67], v[60:61], v[82:83] op_sel:[0,0,0] op_sel_hi:[0,1,1]\n\tv_pk_fma_f32 v[84:85], v[68:69], v[62:63], v[84:85] op_sel:[0,0,0] op_sel_hi:[0,1,1]\n\tv_pk_fma_f32 v[102:103], v[120:121], v[136:137], v[102:103] op_sel:[0,0,0] op_sel_hi:[0,1,1]\n\tv_pk_fma_f32 v[104:105], v[122:123], v[138:139], v[104:105] op_sel:[0,0,0] op_sel_hi:[0,1,1]\n\tv_cvt_pk_f16_f32 v110, v70, v71\n\tv_cvt_pk_f16_f32 v111, v72, v73\n\tv_cvt_pk_f16_f32 v112, v88, v89\n\tv_cvt_pk_f16_f32 v113, v90, v91\n\tv_pk_mul_f32 v[66:67], v[120:121], v[78:79] op_sel:[1,1] op_sel_hi:[1,0] neg_lo:[0,1]\n\tv_pk_mul_f32 v[68:69], v[122:123], v[80:81] op_sel:[1,1] op_sel_hi:[1,0] neg_lo:[0,1]\n\tv_pk_mul_f32 v[70:71], v[114:115], v[98:99] op_sel:[1,1] op_sel_hi:[1,0] neg_lo:[0,1]\n\tv_pk_mul_f32 v[72:73], v[116:117], v[100:101] op_sel:[1,1] op_sel_hi:[1,0] neg_lo:[0,1]\n\tds_write2_b64 %[b11], v[110:111], v[112:113] offset0:132 offset1:198\n\tv_pk_fma_f32 v[66:67], v[120:121], v[78:79], v[66:67] op_sel:[0,0,0] op_sel_hi:[0,1,1]\n\tv_pk_fma_f32 v[68:69], v[122:123], v[80:81], v[68:69] op_sel:[0,0,0] op_sel_hi:[0,1,1]\n\tv_pk_fma_f32 v[70:71], v[114:115], v[98:99], v[70:71] op_sel:[0,0,0] op_sel_hi:[0,1,1]\n\tv_pk_fma_f32 v[72:73], v[116:117], v[100:101], v[72:73] op_sel:[0,0,0] op_sel_hi:[0,1,1]\n\tv_cvt_pk_f16_f32 v88, v82, v83\n\tv_cvt_pk_f16_f32 v89, v84, v85\n\tv_cvt_pk_f16_f32 v90, v102, -v103\n\tv_cvt_pk_f16_f32 v91, v104, -v105\n\tv_pk_mul_f32 v[78:79], v[96:97], v[98:99] op_sel:[1,1] op_sel_hi:[1,0] neg_lo:[0,1]\n\tv_pk_mul_f32 v[80:81], v[118:119], v[100:101] op_sel:[1,1] op_sel_hi:[1,0] neg_lo:[0,1]\n\tv_pk_mul_f32 v[102:103], v[82:83], v[60:61] op_sel:[1,1] op_sel_hi:[1,0] neg_lo:[0,1]\n\tv_pk_mul_f32 v[104:105], v[84:85], v[62:63] op_sel:[1,1] op_sel_hi:[1,0] neg_lo:[0,1]\n\tds_write2_b64 %[b12], v[128:129], v[124:125] offset0:132 offset1:198\n\tv_pk_fma_f32 v[78:79], v[96:97], v[98:99], v[78:79] op_sel:[0,0,0] op_sel_hi:[0,1,1]\n\tv_pk_fma_f32 v[80:81], v[118:119], v[100:101], v[80:81] op_sel:[0,0,0] op_sel_hi:[0,1,1]\n\tv_pk_fma_f32 v[102:103], v[82:83], v[60:61], v[102:103] op_sel:[0,0,0] op_sel_hi:[0,1,1]\n\tv_pk_fma_f32 v[104:105], v[84:85], v[62:63], v[104:105] op_sel:[0,0,0] op_sel_hi:[0,1,1]\n\tv_cvt_pk_f16_f32 v110, v66, -v67\n\tv_cvt_pk_f16_f32 v111, v68, -v69\n\tv_cvt_pk_f16_f32 v112, v70, v71\n\tv_cvt_pk_f16_f32 v113, v72, v73\n\tv_pk_mul_f16 v66, v154, v74 op_sel:[1,1] op_sel_hi:[1,0] neg_lo:[0,1]\n\tv_pk_mul_f16 v67, v155, v75 op_sel:[1,1] op_sel_hi:[1,0] neg_lo:[0,1]\n\tv_pk_mul_f16 v68, v154, v86 op_sel:[1,1] op_sel_hi:[1,0] neg_lo:[0,1]\n\tv_pk_mul_f16 v69, v155, v87 op_sel:[1,1] op_sel_hi:[1,0] neg_lo:[0,1]\n\tds_write2_b64 %[b3], v[90:91], v[110:111] offset0:132 offset1:198\n\tv_pk_fma_f16 v66, v154, v74, v66 op_sel:[0,0,0] op_sel_hi:[0,1,1]\n\tv_pk_fma_f16 v67, v155, v75, v67 op_sel:[0,0,0] op_sel_hi:[0,1,1]\n\tv_pk_fma_f16 v68, v154, v86, v68 op_sel:[0,0,0] op_sel_hi:[0,1,1]\n\tv_pk_fma_f16 v69, v155, v87, v69 op_sel:[0,0,0] op_sel_hi:[0,1,1]\n\tv_cvt_pk_f16_f32 v70, v78, v79\n\tv_cvt_pk_f16_f32 v71, v80, v81\n\tv_cvt_pk_f16_f32 v72, v102, v103\n\tv_cvt_pk_f16_f32 v73, v104, v105\n\tv_pk_mul_f16 v78, v154, v108 op_sel:[1,1] op_sel_hi:[1,0] neg_lo:[0,1]\n\tv_pk_mul_f16 v79, v155, v109 op_sel:[1,1] op_sel_hi:[1,0] neg_lo:[0,1]\n\tv_pk_mul_f32 v[80:81], v[102:103], v[60:61] op_sel:[1,1] op_sel_hi:[1,0] neg_lo:[0,1]\n\tv_pk_mul_f32 v[82:83], v[104:105], v[62:63] op_sel:[1,1] op_sel_hi:[1,0] neg_lo:[0,1]\n\tds_write2_b64 %[b4], v[154:155], v[66:67] offset0:0 offset1:66\n\tv_pk_fma_f16 v78, v154, v108, v78 op_sel:[0,0,0] op_sel_hi:[0,1,1]\n\tv_pk_fma_f16 v79, v155, v109, v79 op_sel:[0,0,0] op_sel_hi:[0,1,1]\n\tv_pk_fma_f32 v[80:81], v[102:103], v[60:61], v[80:81] op_sel:[0,0,0] op_sel_hi:[0,1,1]\n\tv_pk_fma_f32 v[82:83], v[104:105], v[62:63], v[82:83] op_sel:[0,0,0] op_sel_hi:[0,1,1]\n\tv_pk_mul_f16 v74, v154, v106 op_sel:[1,1] op_sel_hi:[1,0] neg_lo:[0,1]\n\tv_pk_mul_f16 v75, v155, v107 op_sel:[1,1] op_sel_hi:[1,0] neg_lo:[0,1]\n\tv_pk_mul_f16 v84, v154, v64 op_sel:[1,1] op_sel_hi:[1,0] neg_lo:[0,1]\n\tv_pk_mul_f16 v85, v155, v65 op_sel:[1,1] op_sel_hi:[1,0] neg_lo:[0,1]\n\tds_write2_b64 %[b4], v[68:69], v[78:79] offset0:132 offset1:198\n\tv_pk_fma_f16 v74, v154, v106, v74 op_sel:[0,0,0] op_sel_hi:[0,1,1]\n\tv_pk_fma_f16 v75, v155, v107, v75 op_sel:[0,0,0] op_sel_hi:[0,1,1]\n\tv_pk_fma_f16 v84, v154, v64, v84 op_sel:[0,0,0] op_sel_hi:[0,1,1]\n\tv_pk_fma_f16 v85, v155, v65, v85 op_sel:[0,0,0] op_sel_hi:[0,1,1]\n\tv_cvt_pk_f16_f32 v66, v80, v81\n\tv_cvt_pk_f16_f32 v67, v82, v83\n\tv_pk_mul_f16 v80, v154, v132 op_sel:[1,1] op_sel_hi:[1,0] neg_lo:[0,1]\n\tv_pk_mul_f16 v81, v155, v133 op_sel:[1,1] op_sel_hi:[1,0] neg_lo:[0,1]\n\tv_pk_mul_f16 v82, v154, v152 op_sel:[1,1] op_sel_hi:[1,0] neg_lo:[0,1]\n\tv_pk_mul_f16 v83, v155, v153 op_sel:[1,1] op_sel_hi:[1,0] neg_lo:[0,1]\n\tds_write2_b64 %[b5], v[74:75], v[84:85] offset0:0 offset1:66\n\tv_pk_fma_f16 v80, v154, v132, v80 op_sel:[0,0,0] op_sel_hi:[0,1,1]\n\tv_pk_fma_f16 v81, v155, v133, v81 op_sel:[0,0,0] op_sel_hi:[0,1,1]\n\tv_pk_fma_f16 v82, v154, v152, v82 op_sel:[0,0,0] op_sel_hi:[0,1,1]\n\tv_pk_fma_f16 v83, v155, v153, v83 op_sel:[0,0,0] op_sel_hi:[0,1,1]\n\tv_pk_mul_f16 v64, v154, v134 op_sel:[1,1] op_sel_hi:[1,0] neg_lo:[0,1]\n\tv_pk_mul_f16 v65, v155, v135 op_sel:[1,1] op_sel_hi:[1,0] neg_lo:[0,1]\n\tv_pk_mul_f16 v68, v154, v76 op_sel:[1,1] op_sel_hi:[1,0] neg_lo:[0,1]\n\tv_pk_mul_f16 v69, v155, v77 op_sel:[1,1] op_sel_hi:[1,0] neg_lo:[0,1]\n\tds_write2_b64 %[b5], v[80:81], v[82:83] offset0:132 offset1:198\n\tv_pk_fma_f16 v64, v154, v134, v64 op_sel:[0,0,0] op_sel_hi:[0,1,1]\n\tv_pk_fma_f16 v65, v155, v135, v65 op_sel:[0,0,0] op_sel_hi:[0,1,1]\n\tv_pk_fma_f16 v68, v154, v76, v68 op_sel:[0,0,0] op_sel_hi:[0,1,1]\n\tv_pk_fma_f16 v69, v155, v77, v69 op_sel:[0,0,0] op_sel_hi:[0,1,1]\n\tv_pk_mul_f16 v74, v154, v140 op_sel:[1,1] op_sel_hi:[1,0] neg_lo:[0,1]\n\tv_pk_mul_f16 v75, v155, v141 op_sel:[1,1] op_sel_hi:[1,0] neg_lo:[0,1]\n\tv_pk_mul_f16 v78, v154, v142 op_sel:[1,1] op_sel_hi:[1,0] neg_lo:[0,1]\n\tv_pk_mul_f16 v79, v155, v143 op_sel:[1,1] op_sel_hi:[1,0] neg_lo:[0,1]\n\tds_write2_b64 %[b6], v[64:65], v[68:69] offset0:0 offset1:66\n\tv_pk_fma_f16 v74, v154, v140, v74 op_sel:[0,0,0] op_sel_hi:[0,1,1]\n\tv_pk_fma_f16 v75, v155, v141, v75 op_sel:[0,0,0] op_sel_hi:[0,1,1]\n\tv_pk_fma_f16 v78, v154, v142, v78 op_sel:[0,0,0] op_sel_hi:[0,1,1]\n\tv_pk_fma_f16 v79, v155, v143, v79 op_sel:[0,0,0] op_sel_hi:[0,1,1]\n\tv_pk_mul_f16 v76, v154, v92 op_sel:[1,1] op_sel_hi:[1,0] neg_lo:[0,1]\n\tv_pk_mul_f16 v77, v155, v93 op_sel:[1,1] op_sel_hi:[1,0] neg_lo:[0,1]\n\tv_pk_mul_f16 v80, v154, v94 op_sel:[1,1] op_sel_hi:[1,0] neg_lo:[0,1]\n\tv_pk_mul_f16 v81, v155, v95 op_sel:[1,1] op_sel_hi:[1,0] neg_lo:[0,1]\n\tds_write2_b64 %[b6], v[74:75], v[78:79] offset0:132 offset1:198\n\tv_pk_fma_f16 v76, v154, v92, v76 op_sel:[0,0,0] op_sel_hi:[0,1,1]\n\tv_pk_fma_f16 v77, v155, v93, v77 op_sel:[0,0,0] op_sel_hi:[0,1,1]\n\tv_pk_fma_f16 v80, v154, v94, v80 op_sel:[0,0,0] op_sel_hi:[0,1,1]\n\tv_pk_fma_f16 v81, v155, v95, v81 op_sel:[0,0,0] op_sel_hi:[0,1,1]\n\tv_pk_mul_f16 v64, v154, v90 op_sel:[1,1] op_sel_hi:[1,0] neg_lo:[0,1]\n\tv_pk_mul_f16 v65, v155, v91 op_sel:[1,1] op_sel_hi:[1,0] neg_lo:[0,1]\n\tv_pk_mul_f16 v68, v154, v110 op_sel:[1,1] op_sel_hi:[1,0] neg_lo:[0,1]\n\tv_pk_mul_f16 v69, v155, v111 op_sel:[1,1] op_sel_hi:[1,0] neg_lo:[0,1]\n\tds_write2_b64 %[b7], v[76:77], v[80:81] offset0:0 offset1:66\n\tv_pk_fma_f16 v64, v154, v90, v64 op_sel:[0,0,0] op_sel_hi:[0,1,1]\n\tv_pk_fma_f16 v65, v155, v91, v65 op_sel:[0,0,0] op_sel_hi:[0,1,1]\n\tv_pk_fma_f16 v68, v154, v110, v68 op_sel:[0,0,0] op_sel_hi:[0,1,1]\n\tv_pk_fma_f16 v69, v155, v111, v69 op_sel:[0,0,0] op_sel_hi:[0,1,1]\n\ts_nop 0\n\tds_write2_b64 %[b7], v[64:65], v[68:69] offset0:132 offset1:198\n\tds_write2_b64 %[b10], v[88:89], v[66:67] offset0:0 offset1:66\n\tds_write2_b64 %[b12], v[112:113], v[70:71] offset0:0 offset1:66\n\tds_write_b64 %[b13], v[72:73] offset:0"
#define KGEN_CLOBBER "v64", "v65", "v66", "v67", "v68", "v69", "v70", "v71", "v72", "v73", "v74", "v75", "v76", "v77", "v78", "v79", "v80", "v81", "v82", "v83", "v84", "v85", "v86", "v87", "v88", "v89", "v90", "v91", "v92", "v93", "v94", "v95", "v96", "v97", "v98", "v99", "v100", "v101", "v102", "v103", "v104", "v105", "v106", "v107", "v108", "v109", "v110", "v111", "v112", "v113", "v114", "v115", "v116", "v117", "v118", "v119", "v120", "v121", "v122", "v123", "v124", "v125", "v126", "v127", "v128", "v129", "v130", "v131", "v132", "v133", "v134", "v135", "v136", "v137", "v138", "v139", "v140", "v141", "v142", "v143", "v144", "v145", "v146", "v147", "v148", "v149", "v150", "v151", "v152", "v153", "v154", "v155"
#define KROW_0LO 0x2b2a2d2c23222120ull
#define KROW_0HI 0x202020202b343332ull
#define KROW_1LO 0x2524272a31302f2eull
#define KROW_1HI 0x2b2b2b2b20292826ull
#define KMFMA_ASM "ds_read_b128 v[64:67], %[ap] offset:0\n\tds_read_b128 v[68:71], %[bp] offset:0\n\tds_read_b128 v[72:75], %[ap] offset:32\n\tds_read_b128 v[76:79], %[bp] offset:32\n\tds_read_b128 v[80:83], %[ap] offset:64\n\tds_read_b128 v[84:87], %[bp] offset:64\n\tds_read_b128 v[88:91], %[ap] offset:96\n\tds_read_b128 v[92:95], %[bp] offset:96\n\ts_waitcnt lgkmcnt(6)\n\tv_mfma_f32_32x32x16_f16 v[48:63], v[64:67], v[68:71], 0\n\tds_read_b128 v[96:99], %[ap] offset:128\n\tds_read_b128 v[100:103], %[bp] offset:128\n\ts_waitcnt lgkmcnt(6)\n\tv_mfma_f32_32x32x16_f16 v[48:63], v[72:75], v[76:79], v[48:63]\n\tds_read_b128 v[104:107], %[ap] offset:160\n\tds_read_b128 v[108:111], %[bp] offset:160\n\ts_waitcnt lgkmcnt(6)\n\tv_mfma_f32_32x32x16_f16 v[48:63], v[80:83], v[84:87], v[48:63]\n\tds_read_b128 v[64:67], %[ap] offset:192\n\tds_read_b128 v[68:71], %[bp] offset:192\n\ts_waitcnt lgkmcnt(6)\n\tv_mfma_f32_32x32x16_f16 v[48:63], v[88:91], v[92:95], v[48:63]\n\tds_read_b128 v[72:75], %[ap] offset:224\n\tds_read_b128 v[76:79], %[bp] offset:224\n\ts_waitcnt lgkmcnt(6)\n\tv_mfma_f32_32x32x16_f16 v[48:63], v[96:99], v[100:103], v[48:63]\n\tds_read_b128 v[80:83], %[ap] offset:256\n\tds_read_b128 v[84:87], %[bp] offset:256\n\ts_waitcnt lgkmcnt(6)\n\tv_mfma_f32_32x32x16_f16 v[48:63], v[104:107], v[108:111], v[48:63]\n\tds_read_b128 v[88:91], %[ap] offset:288\n\tds_read_b128 v[92:95], %[bp] offset:288\n\ts_waitcnt lgkmcnt(6)\n\tv_mfma_f32_32x32x16_f16 v[48:63], v[64:67], v[68:71], v[48:63]\n\tds_read_b128 v[96:99], %[ap] offset:320\n\tds_read_b128 v[100:103], %[bp] offset:320\n\ts_waitcnt lgkmcnt(6)\n\tv_mfma_f32_32x32x16_f16 v[48:63], v[72:75], v[76:79], v[48:63]\n\tds_read_b128 v[104:107], %[ap] offset:352\n\tds_read_b128 v[108:111], %[bp] offset:352\n\ts_waitcnt lgkmcnt(6)\n\tv_mfma_f32_32x32x16_f16 v[48:63], v[80:83], v[84:87], v[48:63]\n\tds_read_b128 v[64:67], %[ap] offset:384\n\tds_read_b128 v[68:71], %[bp] offset:384\n\ts_waitcnt lgkmcnt(6)\n\tv_mfma_f32_32x32x16_f16 v[48:63], v[88:91], v[92:95], v[48:63]\n\tds_read_b128 v[72:75], %[ap] offset:416\n\tds_read_b128 v[76:79], %[bp] offset:416\n\ts_waitcnt lgkmcnt(6)\n\tv_mfma_f32_32x32x16_f16 v[48:63], v[96:99], v[100:103], v[48:63]\n\tds_read_b128 v[80:83], %[ap] offset:448\n\tds_read_b128 v[84:87], %[bp] offset:448\n\ts_waitcnt lgkmcnt(6)\n\tv_mfma_f32_32x32x16_f16 v[48:63], v[104:107], v[108:111], v[48:63]\n\tds_read_b128 v[88:91], %[ap] offset:480\n\tds_read_b128 v[92:95], %[bp] offset:480\n\ts_waitcnt lgkmcnt(6)\n\tv_mfma_f32_32x32x16_f16 v[48:63], v[64:67], v[68:71], v[48:63]\n\tds_read_b32 %[stg], %[ctl] offset:4\n\ts_waitcnt lgkmcnt(4)\n\tv_mfma_f32_32x32x16_f16 v[48:63], v[72:75], v[76:79], v[48:63]\n\ts_waitcnt lgkmcnt(2)\n\tv_mfma_f32_32x32x16_f16 v[48:63], v[80:83], v[84:87], v[48:63]\n\ts_waitcnt lgkmcnt(0)\n\tv_mfma_f32_32x32x16_f16 v[48:63], v[88:91], v[92:95], v[48:63]\n\ts_nop 7\n\ts_nop 3\n\ts_waitcnt lgkmcnt(0)"
#define KMFMA_CLOBBER "v64", "v65", "v66", "v67", "v68", "v69", "v70", "v71", "v72", "v73", "v74", "v75", "v76", "v77", "v78", "v79", "v80", "v81", "v82", "v83", "v84", "v85", "v86", "v87", "v88", "v89", "v90", "v91", "v92", "v93", "v94", "v95", "v96", "v97", "v98", "v99", "v100", "v101", "v102", "v103", "v104", "v105", "v106", "v107", "v108", "v109", "v110", "v111"
#define CONV_ASM ".p2align 3\n\tds_read_b64_tr_b16 v[52:53], %[a0] offset:456\n\tds_read_b64_tr_b16 v[54:55], %[a0] offset:448\n\tds_read_b64_tr_b16 v[84:85], %[a0] offset:968\n\tds_read_b64_tr_b16 v[86:87], %[a0] offset:960\n\tds_read_b64_tr_b16 v[48:49], %[a0] offset:392\n\tds_read_b64_tr_b16 v[50:51], %[a0] offset:384\n\tds_read_b64_tr_b16 v[80:81], %[a0] offset:904\n\tds_read_b64_tr_b16 v[82:83], %[a0] offset:896\n\tds_read_b64_tr_b16 v[44:45], %[a0] offset:328\n\tds_read_b64_tr_b16 v[46:47], %[a0] offset:320\n\tds_read_b64_tr_b16 v[76:77], %[a0] offset:840\n\tds_read_b64_tr_b16 v[78:79], %[a0] offset:832\n\tv_readfirstlane_b32 %[s], %[stg]\n\ts_mov_b32 %[c], 0x400000\n\ts_cmp_ge_u32 %[s], 4\n\ts_cbranch_scc1 2f\n\t1:\n\tds_read_b32 v216, %[ctl] offset:4\n\ts_waitcnt lgkmcnt(0)\n\tv_readfirstlane_b32 %[s], v216\n\ts_cmp_ge_u32 %[s], 4\n\ts_cbranch_scc1 2f\n\ts_sleep 1\n\ts_sub_u32 %[c], %[c], 1\n\ts_cbranch_scc0 1b\n\t.p2align 3\n\t2:\n\tds_read_b128 v[164:167], %[bb] offset:64\n\tds_read_b128 v[168:171], %[bb] offset:128\n\tds_read_b128 v[172:175], %[bb] offset:192\n\tds_read_b64_tr_b16 v[40:41], %[a0] offset:264\n\tds_read_b64_tr_b16 v[42:43], %[a0] offset:256\n\tds_read_b128 v[176:179], %[bb] offset:256\n\ts_waitcnt lgkmcnt(5)\n\ts_nop 0\n\tv_mfma_f32_16x16x32_bf16 v[132:135], v[52:55], v[164:167], 0\n\tds_read_b64_tr_b16 v[72:73], %[a0] offset:776\n\tds_read_b64_tr_b16 v[74:75], %[a0] offset:768\n\tds_read_b64_tr_b16 v[36:37], %[a0] offset:200\n\tv_mfma_f32_16x16x32_bf16 v[136:139], v[84:87], v[164:167], 0\n\tds_read_b64_tr_b16 v[38:39], %[a0] offset:192\n\ts_waitcnt lgkmcnt(8)\n\ts_nop 0\n\tv_mfma_f32_16x16x32_bf16 v[132:135], v[48:51], v[168:171], v[132:135]\n\tds_read_b128 v[180:183], %[bb] offset:320\n\tds_read_b64_tr_b16 v[68:69], %[a0] offset:712\n\tds_read_b64_tr_b16 v[70:71], %[a0] offset:704\n\tv_mfma_f32_16x16x32_bf16 v[136:139], v[80:83], v[168:171], v[136:139]\n\tds_read_b64_tr_b16 v[32:33], %[a0] offset:136\n\ts_waitcnt lgkmcnt(8)\n\ts_nop 0\n\tv_mfma_f32_16x16x32_bf16 v[132:135], v[44:47], v[172:175], v[132:135]\n\tds_read_b64_tr_b16 v[34:35], %[a0] offset:128\n\tds_read_b128 v[184:187], %[bb] offset:384\n\tds_read_b64_tr_b16 v[64:65], %[a0] offset:648\n\tv_mfma_f32_16x16x32_bf16 v[136:139], v[76:79], v[172:175], v[136:139]\n\tds_read_b64_tr_b16 v[66:67], %[a0] offset:640\n\tds_read_b64_tr_b16 v[28:29], %[a0] offset:72\n\tds_read_b64_tr_b16 v[30:31], %[a0] offset:64\n\tv_mfma_f32_16x16x32_bf16 v[132:135], v[40:43], v[176:179], v[132:135]\n\tds_read_b128 v[164:167], %[bb] offset:448\n\ts_waitcnt lgkmcnt(8)\n\ts_nop 0\n\tv_mfma_f32_16x16x32_bf16 v[136:139], v[72:75], v[176:179], v[136:139]\n\tds_read_b64_tr_b16 v[60:61], %[a0] offset:584\n\tds_read_b64_tr_b16 v[62:63], %[a0] offset:576\n\tds_read_b64_tr_b16 v[24:25], %[a0] offset:8\n\tv_mfma_f32_16x16x32_bf16 v[132:135], v[36:39], v[180:183], v[132:135]\n\tds_read_b64_tr_b16 v[26:27], %[a0] offset:0\n\tds_read_b128 v[168:171], %[bb] offset:512\n\tds_read_b64_tr_b16 v[56:57], %[a0] offset:520\n\tv_mfma_f32_16x16x32_bf16 v[136:139], v[68:71], v[180:183], v[136:139]\n\tds_read_b64_tr_b16 v[58:59], %[a0] offset:512\n\ts_waitcnt lgkmcnt(8)\n\ts_nop 0\n\tv_mfma_f32_16x16x32_bf16 v[132:135], v[32:35], v[184:187], v[132:135]\n\tds_read_b64_tr_b16 v[88:89], %[a0] offset:1032\n\tds_read_b64_tr_b16 v[90:91], %[a0] offset:1024\n\tds_read_b128 v[188:191], %[bb] offset:496\n\tv_mfma_f32_16x16x32_bf16 v[136:139], v[64:67], v[184:187], v[136:139]\n\tds_read_b128 v[172:175], %[bb] offset:576\n\ts_waitcnt lgkmcnt(8)\n\ts_nop 0\n\tv_mfma_f32_16x16x32_bf16 v[132:135], v[28:31], v[164:167], v[132:135]\n\tds_read_b128 v[176:179], %[bb] offset:640\n\tv_mfma_f32_16x16x32_bf16 v[136:139], v[60:63], v[164:167], v[136:139]\n\ts_waitcnt lgkmcnt(5)\n\ts_nop 0\n\tv_mfma_f32_16x16x32_bf16 v[132:135], v[24:27], v[168:171], v[132:135]\n\tds_write_b16 %[ta], %[t78]\n\tds_read_b128 v[180:183], %[bb] offset:704\n\tv_mfma_f32_16x16x32_bf16 v[136:139], v[56:59], v[168:171], v[136:139]\n\ts_waitcnt lgkmcnt(3)\n\ts_nop 0\n\tv_mfma_f32_16x16x32_bf16 v[140:143], v[88:91], v[168:171], 0\n\tds_write_b16_d16_hi %[ta], %[t78] offset:64\n\tds_read_b128 v[184:187], %[bb] offset:768\n\tv_mfma_f32_16x16x32_bf16 v[204:207], v[188:191], v[4:7], 0\n\tds_write_b128 %[yb], v[132:135] offset:0\n\tv_mfma_f32_16x16x32_bf16 v[218:221], v[168:171], v[4:7], 0\n\tds_write_b16 %[ta], %[t78] offset:388\n\tds_read_b128 v[164:167], %[bb] offset:832\n\tv_mfma_f32_16x16x32_bf16 v[208:211], v[188:191], v[8:11], 0\n\tds_write_b8 %[ctc], %[one] offset:0\n\tds_write_b16_d16_hi %[ta], %[t78] offset:452\n\tv_mfma_f32_16x16x32_bf16 v[222:225], v[168:171], v[8:11], 0\n\tds_write_b16 %[tb], %[t9a]\n\tv_mfma_f32_16x16x32_bf16 v[212:215], v[188:191], v[12:15], 0\n\tds_write_b16_d16_hi %[tb], %[t9a] offset:128\n\tv_cvt_pk_bf16_f32 v192, v221, v207\n\tv_mfma_f32_16x16x32_bf16 v[226:229], v[168:171], v[16:19], 0\n\tds_write_b16 %[tb], %[t9a] offset:388\n\tv_cvt_pk_bf16_f32 v193, v220, v206\n\tv_mfma_f32_16x16x32_bf16 v[148:151], v[20:23], v[168:171], 0\n\tds_write_b16_d16_hi %[tb], %[t9a] offset:516\n\tds_read_b128 v[168:171], %[bb] offset:896\n\tv_mfma_f32_16x16x32_bf16 v[136:139], v[52:55], v[172:175], v[136:139]\n\tds_read_b64 v[92:93], %[tp] offset:0\n\tds_read_b64 v[94:95], %[tp] offset:8\n\tds_read_b64 v[96:97], %[tp] offset:64\n\tv_mfma_f32_16x16x32_bf16 v[140:143], v[84:87], v[172:175], v[140:143]\n\tds_read_b128 v[172:175], %[bb] offset:960\n\ts_waitcnt lgkmcnt(7)\n\ts_nop 0\n\tv_mfma_f32_16x16x32_bf16 v[136:139], v[48:51], v[176:179], v[136:139]\n\tds_read_b64 v[98:99], %[tp] offset:72\n\tds_read_b64 v[100:101], %[tp] offset:128\n\tds_read_b64 v[102:103], %[tp] offset:136\n\tv_mfma_f32_16x16x32_bf16 v[140:143], v[80:83], v[176:179], v[140:143]\n\tds_read_b128 v[176:179], %[bb] offset:1024\n\tds_read_b64 v[104:105], %[tp] offset:768\n\tds_read_b64 v[106:107], %[tp] offset:776\n\tv_mfma_f32_16x16x32_bf16 v[136:139], v[44:47], v[180:183], v[136:139]\n\tds_read_b64 v[108:109], %[tp] offset:832\n\tds_read_b64 v[110:111], %[tp] offset:840\n\tds_read_b64 v[112:113], %[tp] offset:896\n\tv_mfma_f32_16x16x32_bf16 v[140:143], v[76:79], v[180:183], v[140:143]\n\tds_read_b64 v[114:115], %[tp] offset:904\n\tds_read_b64 v[116:117], %[tp] offset:1536\n\tds_read_b64 v[118:119], %[tp] offset:1544\n\tv_mfma_f32_16x16x32_bf16 v[136:139], v[40:43], v[184:187], v[136:139]\n\tds_read_b64 v[120:121], %[tp] offset:1600\n\tds_read_b64 v[122:123], %[tp] offset:1608\n\tv_cvt_pk_bf16_f32 v194, v219, v205\n\tv_mfma_f32_16x16x32_bf16 v[140:143], v[72:75], v[184:187], v[140:143]\n\tv_cvt_pk_bf16_f32 v195, v218, v204\n\tds_read_b64 v[124:125], %[tp] offset:1664\n\tds_read_b64 v[126:127], %[tp] offset:1672\n\tv_mfma_f32_16x16x32_bf16 v[136:139], v[36:39], v[164:167], v[136:139]\n\tds_read_b64 v[128:129], %[tp] offset:1728\n\tds_read_b64 v[130:131], %[tp] offset:1736\n\tv_cvt_pk_bf16_f32 v196, v225, v211\n\tv_mfma_f32_16x16x32_bf16 v[140:143], v[68:71], v[164:167], v[140:143]\n\tv_cvt_pk_bf16_f32 v197, v224, v210\n\ts_waitcnt lgkmcnt(6)\n\ts_nop 0\n\tv_mfma_f32_16x16x32_bf16 v[136:139], v[32:35], v[168:171], v[136:139]\n\tv_cvt_pk_bf16_f32 v198, v223, v209\n\tv_mfma_f32_16x16x32_bf16 v[140:143], v[64:67], v[168:171], v[140:143]\n\tv_cvt_pk_bf16_f32 v199, v222, v208\n\tv_mfma_f32_16x16x32_bf16 v[136:139], v[28:31], v[172:175], v[136:139]\n\tv_cvt_pk_bf16_f32 v200, v229, v215\n\tv_mfma_f32_16x16x32_bf16 v[140:143], v[60:63], v[172:175], v[140:143]\n\tds_read_b128 v[188:191], %[bb] offset:1008\n\tv_cvt_pk_bf16_f32 v201, v228, v214\n\tv_mfma_f32_16x16x32_bf16 v[136:139], v[24:27], v[176:179], v[136:139]\n\tv_cvt_pk_bf16_f32 v202, v227, v213\n\tv_mfma_f32_16x16x32_bf16 v[140:143], v[56:59], v[176:179], v[140:143]\n\tv_cvt_pk_bf16_f32 v203, v226, v212\n\tv_mfma_f32_16x16x32_bf16 v[144:147], v[88:91], v[176:179], 0\n\tv_mfma_f32_16x16x32_bf16 v[140:143], v[192:195], v[92:95], v[140:143]\n\tv_mfma_f32_16x16x32_bf16 v[144:147], v[192:195], v[96:99], v[144:147]\n\tv_mfma_f32_16x16x32_bf16 v[148:151], v[192:195], v[100:103], v[148:151]\n\tv_mfma_f32_16x16x32_bf16 v[140:143], v[196:199], v[104:107], v[140:143]\n\tds_write_b128 %[yb], v[136:139] offset:1152\n\tds_read_b128 v[180:183], %[bb] offset:1088\n\tv_mfma_f32_16x16x32_bf16 v[144:147], v[196:199], v[108:111], v[144:147]\n\tv_mfma_f32_16x16x32_bf16 v[148:151], v[196:199], v[112:115], v[148:151]\n\tds_write_b8 %[ctc], %[one] offset:64\n\tds_read_b128 v[184:187], %[bb] offset:1152\n\tv_mfma_f32_16x16x32_bf16 v[148:151], v[200:203], v[116:119], v[148:151]\n\ts_waitcnt lgkmcnt(4)\n\ts_nop 0\n\tv_mfma_f32_16x16x32_bf16 v[152:155], v[200:203], v[120:123], 0\n\tds_read_b128 v[164:167], %[bb] offset:1216\n\tv_mfma_f32_16x16x32_bf16 v[156:159], v[200:203], v[124:127], 0\n\tv_mfma_f32_16x16x32_bf16 v[160:163], v[200:203], v[128:131], 0\n\tds_read_b128 v[168:171], %[bb] offset:1280\n\tv_mfma_f32_16x16x32_bf16 v[204:207], v[188:191], v[4:7], 0\n\tv_mfma_f32_16x16x32_bf16 v[218:221], v[176:179], v[4:7], 0\n\tds_read_b128 v[172:175], %[bb] offset:1344\n\tv_mfma_f32_16x16x32_bf16 v[208:211], v[188:191], v[8:11], 0\n\tv_mfma_f32_16x16x32_bf16 v[222:225], v[176:179], v[8:11], 0\n\tv_mfma_f32_16x16x32_bf16 v[212:215], v[188:191], v[12:15], 0\n\tv_mfma_f32_16x16x32_bf16 v[226:229], v[176:179], v[16:19], 0\n\tv_mfma_f32_16x16x32_bf16 v[152:155], v[20:23], v[176:179], v[152:155]\n\ts_waitcnt lgkmcnt(0)\n\ts_nop 0\n\tv_mfma_f32_16x16x32_bf16 v[140:143], v[52:55], v[180:183], v[140:143]\n\tds_read_b128 v[176:179], %[bb] offset:1408\n\tv_cvt_pk_bf16_f32 v192, v221, v207\n\tv_mfma_f32_16x16x32_bf16 v[144:147], v[84:87], v[180:183], v[144:147]\n\tds_read_b128 v[180:183], %[bb] offset:1472\n\tv_cvt_pk_bf16_f32 v193, v220, v206\n\tv_mfma_f32_16x16x32_bf16 v[140:143], v[48:51], v[184:187], v[140:143]\n\tv_cvt_pk_bf16_f32 v194, v219, v205\n\tv_mfma_f32_16x16x32_bf16 v[144:147], v[80:83], v[184:187], v[144:147]\n\tds_read_b128 v[184:187], %[bb] offset:1536\n\tv_cvt_pk_bf16_f32 v195, v218, v204\n\tv_mfma_f32_16x16x32_bf16 v[140:143], v[44:47], v[164:167], v[140:143]\n\tv_cvt_pk_bf16_f32 v196, v225, v211\n\tv_mfma_f32_16x16x32_bf16 v[144:147], v[76:79], v[164:167], v[144:147]\n\tv_cvt_pk_bf16_f32 v197, v224, v210\n\tv_mfma_f32_16x16x32_bf16 v[140:143], v[40:43], v[168:171], v[140:143]\n\tv_cvt_pk_bf16_f32 v198, v223, v209\n\tv_mfma_f32_16x16x32_bf16 v[144:147], v[72:75], v[168:171], v[144:147]\n\tv_cvt_pk_bf16_f32 v199, v222, v208\n\tv_mfma_f32_16x16x32_bf16 v[140:143], v[36:39], v[172:175], v[140:143]\n\tv_cvt_pk_bf16_f32 v200, v229, v215\n\tv_mfma_f32_16x16x32_bf16 v[144:147], v[68:71], v[172:175], v[144:147]\n\ts_waitcnt lgkmcnt(0)\n\ts_nop 0\n\tv_mfma_f32_16x16x32_bf16 v[140:143], v[32:35], v[176:179], v[140:143]\n\tv_cvt_pk_bf16_f32 v201, v228, v214\n\tv_mfma_f32_16x16x32_bf16 v[144:147], v[64:67], v[176:179], v[144:147]\n\tv_cvt_pk_bf16_f32 v202, v227, v213\n\tv_mfma_f32_16x16x32_bf16 v[140:143], v[28:31], v[180:183], v[140:143]\n\tds_read_b128 v[188:191], %[bb] offset:1520\n\tv_cvt_pk_bf16_f32 v203, v226, v212\n\tv_mfma_f32_16x16x32_bf16 v[144:147], v[60:63], v[180:183], v[144:147]\n\tv_mfma_f32_16x16x32_bf16 v[140:143], v[24:27], v[184:187], v[140:143]\n\tv_mfma_f32_16x16x32_bf16 v[144:147], v[56:59], v[184:187], v[144:147]\n\tv_mfma_f32_16x16x32_bf16 v[148:151], v[88:91], v[184:187], v[148:151]\n\tv_mfma_f32_16x16x32_bf16 v[144:147], v[192:195], v[92:95], v[144:147]\n\tv_mfma_f32_16x16x32_bf16 v[148:151], v[192:195], v[96:99], v[148:151]\n\tv_mfma_f32_16x16x32_bf16 v[152:155], v[192:195], v[100:103], v[152:155]\n\tds_read_b128 v[164:167], %[bb] offset:1600\n\tv_mfma_f32_16x16x32_bf16 v[144:147], v[196:199], v[104:107], v[144:147]\n\tv_mfma_f32_16x16x32_bf16 v[148:151], v[196:199], v[108:111], v[148:151]\n\tds_write_b128 %[yb], v[140:143] offset:2304\n\tv_mfma_f32_16x16x32_bf16 v[152:155], v[196:199], v[112:115], v[152:155]\n\tds_read_b128 v[168:171], %[bb] offset:1664\n\tv_mfma_f32_16x16x32_bf16 v[152:155], v[200:203], v[116:119], v[152:155]\n\tds_write_b8 %[ctc], %[one] offset:128\n\tds_read_b128 v[172:175], %[bb] offset:1728\n\tv_mfma_f32_16x16x32_bf16 v[156:159], v[200:203], v[120:123], v[156:159]\n\tv_mfma_f32_16x16x32_bf16 v[160:163], v[200:203], v[124:127], v[160:163]\n\ts_waitcnt lgkmcnt(3)\n\ts_nop 0\n\tv_mfma_f32_16x16x32_bf16 v[204:207], v[188:191], v[4:7], 0\n\tds_read_b128 v[176:179], %[bb] offset:1792\n\tv_mfma_f32_16x16x32_bf16 v[218:221], v[184:187], v[4:7], 0\n\tds_read_b128 v[180:183], %[bb] offset:1856\n\tv_mfma_f32_16x16x32_bf16 v[208:211], v[188:191], v[8:11], 0\n\tv_mfma_f32_16x16x32_bf16 v[222:225], v[184:187], v[8:11], 0\n\tv_mfma_f32_16x16x32_bf16 v[212:215], v[188:191], v[12:15], 0\n\tv_mfma_f32_16x16x32_bf16 v[226:229], v[184:187], v[16:19], 0\n\tv_mfma_f32_16x16x32_bf16 v[156:159], v[20:23], v[184:187], v[156:159]\n\tds_read_b128 v[184:187], %[bb] offset:1920\n\tv_mfma_f32_16x16x32_bf16 v[144:147], v[52:55], v[164:167], v[144:147]\n\tv_cvt_pk_bf16_f32 v192, v221, v207\n\tv_mfma_f32_16x16x32_bf16 v[148:151], v[84:87], v[164:167], v[148:151]\n\ts_waitcnt lgkmcnt(1)\n\ts_nop 0\n\tv_mfma_f32_16x16x32_bf16 v[144:147], v[48:51], v[168:171], v[144:147]\n\tds_read_b128 v[164:167], %[bb] offset:1984\n\tv_cvt_pk_bf16_f32 v193, v220, v206\n\tv_mfma_f32_16x16x32_bf16 v[148:151], v[80:83], v[168:171], v[148:151]\n\tds_read_b128 v[168:171], %[bb] offset:2048\n\tv_cvt_pk_bf16_f32 v194, v219, v205\n\tv_mfma_f32_16x16x32_bf16 v[144:147], v[44:47], v[172:175], v[144:147]\n\tds_read_b128 v[188:191], %[bb] offset:2032\n\tv_cvt_pk_bf16_f32 v195, v218, v204\n\tv_mfma_f32_16x16x32_bf16 v[148:151], v[76:79], v[172:175], v[148:151]\n\tv_cvt_pk_bf16_f32 v196, v225, v211\n\tv_mfma_f32_16x16x32_bf16 v[144:147], v[40:43], v[176:179], v[144:147]\n\tv_cvt_pk_bf16_f32 v197, v224, v210\n\tv_mfma_f32_16x16x32_bf16 v[148:151], v[72:75], v[176:179], v[148:151]\n\tv_cvt_pk_bf16_f32 v198, v223, v209\n\tv_mfma_f32_16x16x32_bf16 v[144:147], v[36:39], v[180:183], v[144:147]\n\tv_cvt_pk_bf16_f32 v199, v222, v208\n\tv_mfma_f32_16x16x32_bf16 v[148:151], v[68:71], v[180:183], v[148:151]\n\ts_waitcnt lgkmcnt(0)\n\ts_nop 0\n\tv_mfma_f32_16x16x32_bf16 v[144:147], v[32:35], v[184:187], v[144:147]\n\tv_cvt_pk_bf16_f32 v200, v229, v215\n\tv_mfma_f32_16x16x32_bf16 v[148:151], v[64:67], v[184:187], v[148:151]\n\tv_cvt_pk_bf16_f32 v201, v228, v214\n\tv_mfma_f32_16x16x32_bf16 v[144:147], v[28:31], v[164:167], v[144:147]\n\tv_cvt_pk_bf16_f32 v202, v227, v213\n\tv_mfma_f32_16x16x32_bf16 v[148:151], v[60:63], v[164:167], v[148:151]\n\tv_cvt_pk_bf16_f32 v203, v226, v212\n\tv_mfma_f32_16x16x32_bf16 v[144:147], v[24:27], v[168:171], v[144:147]\n\tv_mfma_f32_16x16x32_bf16 v[148:151], v[56:59], v[168:171], v[148:151]\n\tv_mfma_f32_16x16x32_bf16 v[152:155], v[88:91], v[168:171], v[152:155]\n\tv_mfma_f32_16x16x32_bf16 v[204:207], v[188:191], v[4:7], 0\n\tv_mfma_f32_16x16x32_bf16 v[218:221], v[168:171], v[4:7], 0\n\tv_mfma_f32_16x16x32_bf16 v[208:211], v[188:191], v[8:11], 0\n\tv_mfma_f32_16x16x32_bf16 v[222:225], v[168:171], v[8:11], 0\n\tv_mfma_f32_16x16x32_bf16 v[212:215], v[188:191], v[12:15], 0\n\tv_mfma_f32_16x16x32_bf16 v[226:229], v[168:171], v[16:19], 0\n\tds_write_b128 %[yb], v[144:147] offset:3456\n\tv_mfma_f32_16x16x32_bf16 v[160:163], v[20:23], v[168:171], v[160:163]\n\tds_read_b32 v216, %[ctl] offset:20\n\tv_mfma_f32_16x16x32_bf16 v[148:151], v[192:195], v[92:95], v[148:151]\n\tds_write_b8 %[ctc], %[one] offset:192\n\tv_mfma_f32_16x16x32_bf16 v[152:155], v[192:195], v[96:99], v[152:155]\n\tv_mfma_f32_16x16x32_bf16 v[156:159], v[192:195], v[100:103], v[156:159]\n\tv_mfma_f32_16x16x32_bf16 v[148:151], v[196:199], v[104:107], v[148:151]\n\tv_cvt_pk_bf16_f32 v192, v221, v207\n\tv_mfma_f32_16x16x32_bf16 v[152:155], v[196:199], v[108:111], v[152:155]\n\tv_cvt_pk_bf16_f32 v193, v220, v206\n\tv_mfma_f32_16x16x32_bf16 v[156:159], v[196:199], v[112:115], v[156:159]\n\tv_cvt_pk_bf16_f32 v194, v219, v205\n\tv_mfma_f32_16x16x32_bf16 v[156:159], v[200:203], v[116:119], v[156:159]\n\tv_cvt_pk_bf16_f32 v195, v218, v204\n\tv_mfma_f32_16x16x32_bf16 v[160:163], v[200:203], v[120:123], v[160:163]\n\ts_waitcnt lgkmcnt(0)\n\ts_nop 0\n\tv_readfirstlane_b32 %[s], v216\n\ts_mov_b32 %[c], 0x400000\n\ts_cmp_ge_u32 %[s], 2\n\ts_cbranch_scc1 2f\n\t1:\n\tds_read_b32 v216, %[ctl] offset:20\n\ts_waitcnt lgkmcnt(0)\n\tv_readfirstlane_b32 %[s], v216\n\ts_cmp_ge_u32 %[s], 2\n\ts_cbranch_scc1 2f\n\ts_sleep 1\n\ts_sub_u32 %[c], %[c], 1\n\ts_cbranch_scc0 1b\n\t.p2align 3\n\t2:\n\tv_mfma_f32_16x16x32_bf16 v[152:155], v[192:195], v[92:95], v[152:155]\n\tds_read_b128 v[172:175], %[bb] offset:2112\n\tv_cvt_pk_bf16_f32 v196, v225, v211\n\tv_mfma_f32_16x16x32_bf16 v[156:159], v[192:195], v[96:99], v[156:159]\n\tds_read_b128 v[176:179], %[bb] offset:2176\n\tv_cvt_pk_bf16_f32 v197, v224, v210\n\tv_mfma_f32_16x16x32_bf16 v[160:163], v[192:195], v[100:103], v[160:163]\n\tv_cvt_pk_bf16_f32 v198, v223, v209\n\tds_read_b128 v[180:183], %[bb] offset:2240\n\tv_cvt_pk_bf16_f32 v199, v222, v208\n\ts_nop 1\n\ts_nop 0\n\tv_mfma_f32_16x16x32_bf16 v[152:155], v[196:199], v[104:107], v[152:155]\n\tds_read_b128 v[184:187], %[bb] offset:2304\n\tv_cvt_pk_bf16_f32 v200, v229, v215\n\tv_mfma_f32_16x16x32_bf16 v[156:159], v[196:199], v[108:111], v[156:159]\n\tv_cvt_pk_bf16_f32 v201, v228, v214\n\tds_read_b128 v[164:167], %[bb] offset:2368\n\tv_mfma_f32_16x16x32_bf16 v[160:163], v[196:199], v[112:115], v[160:163]\n\tv_cvt_pk_bf16_f32 v202, v227, v213\n\tds_read_b128 v[168:171], %[bb] offset:2432\n\tv_cvt_pk_bf16_f32 v203, v226, v212\n\ts_nop 1\n\ts_nop 0\n\tv_mfma_f32_16x16x32_bf16 v[160:163], v[200:203], v[116:119], v[160:163]\n\ts_waitcnt lgkmcnt(3)\n\ts_nop 0\n\tv_mfma_f32_16x16x32_bf16 v[148:151], v[52:55], v[172:175], v[148:151]\n\tv_mfma_f32_16x16x32_bf16 v[152:155], v[84:87], v[172:175], v[152:155]\n\tds_read_b128 v[172:175], %[bb] offset:2496\n\tv_mfma_f32_16x16x32_bf16 v[148:151], v[48:51], v[176:179], v[148:151]\n\tv_mfma_f32_16x16x32_bf16 v[152:155], v[80:83], v[176:179], v[152:155]\n\tds_read_b128 v[176:179], %[bb] offset:2560\n\tv_mfma_f32_16x16x32_bf16 v[148:151], v[44:47], v[180:183], v[148:151]\n\tds_read_b128 v[188:191], %[bb] offset:2544\n\tv_mfma_f32_16x16x32_bf16 v[152:155], v[76:79], v[180:183], v[152:155]\n\ts_waitcnt lgkmcnt(2)\n\ts_nop 0\n\tv_mfma_f32_16x16x32_bf16 v[148:151], v[40:43], v[184:187], v[148:151]\n\tv_mfma_f32_16x16x32_bf16 v[152:155], v[72:75], v[184:187], v[152:155]\n\tds_read_b128 v[180:183], %[bb] offset:2624\n\tv_mfma_f32_16x16x32_bf16 v[148:151], v[36:39], v[164:167], v[148:151]\n\tv_mfma_f32_16x16x32_bf16 v[152:155], v[68:71], v[164:167], v[152:155]\n\tds_read_b128 v[184:187], %[bb] offset:2688\n\tv_mfma_f32_16x16x32_bf16 v[148:151], v[32:35], v[168:171], v[148:151]\n\tv_mfma_f32_16x16x32_bf16 v[152:155], v[64:67], v[168:171], v[152:155]\n\tds_read_b128 v[164:167], %[bb] offset:2752\n\tv_mfma_f32_16x16x32_bf16 v[148:151], v[28:31], v[172:175], v[148:151]\n\tv_mfma_f32_16x16x32_bf16 v[152:155], v[60:63], v[172:175], v[152:155]\n\ts_waitcnt lgkmcnt(1)\n\ts_nop 0\n\tv_mfma_f32_16x16x32_bf16 v[148:151], v[24:27], v[176:179], v[148:151]\n\tds_read_b128 v[168:171], %[bb] offset:2816\n\tv_mfma_f32_16x16x32_bf16 v[152:155], v[56:59], v[176:179], v[152:155]\n\tds_read_b128 v[172:175], %[bb] offset:2880\n\tv_mfma_f32_16x16x32_bf16 v[156:159], v[88:91], v[176:179], v[156:159]\n\tv_mfma_f32_16x16x32_bf16 v[204:207], v[188:191], v[4:7], 0\n\tv_mfma_f32_16x16x32_bf16 v[218:221], v[176:179], v[4:7], 0\n\tv_mfma_f32_16x16x32_bf16 v[208:211], v[188:191], v[8:11], 0\n\tv_mfma_f32_16x16x32_bf16 v[222:225], v[176:179], v[8:11], 0\n\tds_write_b128 %[yb], v[148:151] offset:4608\n\tv_mfma_f32_16x16x32_bf16 v[152:155], v[52:55], v[180:183], v[152:155]\n\tds_read_b128 v[176:179], %[bb] offset:2944\n\tv_mfma_f32_16x16x32_bf16 v[156:159], v[84:87], v[180:183], v[156:159]\n\tds_write_b8 %[ctc], %[one] offset:256\n\tds_read_b128 v[180:183], %[bb] offset:3008\n\tv_mfma_f32_16x16x32_bf16 v[152:155], v[48:51], v[184:187], v[152:155]\n\tds_read_b128 v[116:119], %[bb] offset:3072\n\tv_cvt_pk_bf16_f32 v192, v221, v207\n\tv_mfma_f32_16x16x32_bf16 v[156:159], v[80:83], v[184:187], v[156:159]\n\ts_waitcnt lgkmcnt(4)\n\ts_nop 0\n\tv_mfma_f32_16x16x32_bf16 v[152:155], v[44:47], v[164:167], v[152:155]\n\tds_read_b32 v216, %[ctl] offset:24\n\tv_cvt_pk_bf16_f32 v193, v220, v206\n\tv_mfma_f32_16x16x32_bf16 v[156:159], v[76:79], v[164:167], v[156:159]\n\tv_cvt_pk_bf16_f32 v194, v219, v205\n\tv_mfma_f32_16x16x32_bf16 v[152:155], v[40:43], v[168:171], v[152:155]\n\tv_cvt_pk_bf16_f32 v195, v218, v204\n\tv_mfma_f32_16x16x32_bf16 v[156:159], v[72:75], v[168:171], v[156:159]\n\tv_cvt_pk_bf16_f32 v196, v225, v211\n\tv_mfma_f32_16x16x32_bf16 v[152:155], v[36:39], v[172:175], v[152:155]\n\tv_cvt_pk_bf16_f32 v197, v224, v210\n\tv_mfma_f32_16x16x32_bf16 v[156:159], v[68:71], v[172:175], v[156:159]\n\ts_waitcnt lgkmcnt(0)\n\ts_nop 0\n\tv_mfma_f32_16x16x32_bf16 v[152:155], v[32:35], v[176:179], v[152:155]\n\tv_cvt_pk_bf16_f32 v198, v223, v209\n\tv_mfma_f32_16x16x32_bf16 v[156:159], v[64:67], v[176:179], v[156:159]\n\tv_readfirstlane_b32 %[s], v216\n\ts_mov_b32 %[c], 0x400000\n\ts_cmp_ge_u32 %[s], 1\n\ts_cbranch_scc1 2f\n\t1:\n\tds_read_b32 v216, %[ctl] offset:24\n\ts_waitcnt lgkmcnt(0)\n\tv_readfirstlane_b32 %[s], v216\n\ts_cmp_ge_u32 %[s], 1\n\ts_cbranch_scc1 2f\n\ts_sleep 1\n\ts_sub_u32 %[c], %[c], 1\n\ts_cbranch_scc0 1b\n\t.p2align 3\n\t2:\n\tv_mfma_f32_16x16x32_bf16 v[152:155], v[28:31], v[180:183], v[152:155]\n\tds_read_b128 v[120:123], %[bb] offset:3136\n\tv_cvt_pk_bf16_f32 v199, v222, v208\n\tv_mfma_f32_16x16x32_bf16 v[156:159], v[60:63], v[180:183], v[156:159]\n\tds_read_b128 v[124:127], %[bb] offset:3200\n\tv_mfma_f32_16x16x32_bf16 v[152:155], v[24:27], v[116:119], v[152:155]\n\tds_read_b128 v[128:131], %[bb] offset:3264\n\tv_mfma_f32_16x16x32_bf16 v[156:159], v[56:59], v[116:119], v[156:159]\n\tds_read_b128 v[132:135], %[bb] offset:3328\n\tv_mfma_f32_16x16x32_bf16 v[160:163], v[88:91], v[116:119], v[160:163]\n\tds_read_b128 v[136:139], %[bb] offset:3392\n\tv_mfma_f32_16x16x32_bf16 v[156:159], v[192:195], v[92:95], v[156:159]\n\tds_read_b128 v[140:143], %[bb] offset:3456\n\tv_mfma_f32_16x16x32_bf16 v[156:159], v[196:199], v[104:107], v[156:159]\n\ts_waitcnt lgkmcnt(3)\n\ts_nop 0\n\tv_mfma_f32_16x16x32_bf16 v[156:159], v[52:55], v[120:123], v[156:159]\n\tds_write_b128 %[yb], v[152:155] offset:5760\n\tv_mfma_f32_16x16x32_bf16 v[156:159], v[48:51], v[124:127], v[156:159]\n\tds_read_b128 v[144:147], %[bb] offset:3520\n\tv_mfma_f32_16x16x32_bf16 v[156:159], v[44:47], v[128:131], v[156:159]\n\ts_waitcnt lgkmcnt(2)\n\ts_nop 0\n\tv_mfma_f32_16x16x32_bf16 v[156:159], v[40:43], v[132:135], v[156:159]\n\tds_write_b8 %[ctc], %[one] offset:320\n\tds_read_b128 v[200:203], %[bb] offset:3584\n\tv_mfma_f32_16x16x32_bf16 v[156:159], v[36:39], v[136:139], v[156:159]\n\tds_read_b128 v[188:191], %[bb] offset:3056\n\tglobal_load_dwordx2 v[240:241], %[so], %[ws] sc1\n\tv_mfma_f32_16x16x32_bf16 v[156:159], v[32:35], v[140:143], v[156:159]\n\ts_waitcnt lgkmcnt(3)\n\ts_nop 0\n\tv_mfma_f32_16x16x32_bf16 v[156:159], v[28:31], v[144:147], v[156:159]\n\ts_waitcnt lgkmcnt(1)\n\ts_nop 0\n\tv_mfma_f32_16x16x32_bf16 v[156:159], v[24:27], v[200:203], v[156:159]\n\ts_waitcnt lgkmcnt(0)\n\ts_nop 0\n\tv_mfma_f32_16x16x32_bf16 v[204:207], v[188:191], v[4:7], 0\n\tds_read_b32 v216, %[ctl] offset:28\n\tv_mfma_f32_16x16x32_bf16 v[218:221], v[116:119], v[4:7], 0\n\tv_mfma_f32_16x16x32_bf16 v[208:211], v[188:191], v[8:11], 0\n\tv_mfma_f32_16x16x32_bf16 v[222:225], v[116:119], v[8:11], 0\n\tv_mfma_f32_16x16x32_bf16 v[160:163], v[192:195], v[96:99], v[160:163]\n\tds_write_b128 %[yb], v[156:159] offset:6912\n\tv_mfma_f32_16x16x32_bf16 v[160:163], v[196:199], v[108:111], v[160:163]\n\tv_mfma_f32_16x16x32_bf16 v[160:163], v[84:87], v[120:123], v[160:163]\n\tds_write_b8 %[ctc], %[one] offset:384\n\tv_mfma_f32_16x16x32_bf16 v[160:163], v[80:83], v[124:127], v[160:163]\n\tv_cvt_pk_bf16_f32 v192, v221, v207\n\tv_mfma_f32_16x16x32_bf16 v[160:163], v[76:79], v[128:131], v[160:163]\n\ts_waitcnt lgkmcnt(2)\n\ts_nop 0\n\tv_readfirstlane_b32 %[s], v216\n\ts_mov_b32 %[c], 0x400000\n\ts_cmp_ge_u32 %[s], 1\n\ts_cbranch_scc1 2f\n\t1:\n\tds_read_b32 v216, %[ctl] offset:28\n\ts_waitcnt lgkmcnt(0)\n\tv_readfirstlane_b32 %[s], v216\n\ts_cmp_ge_u32 %[s], 1\n\ts_cbranch_scc1 2f\n\ts_sleep 1\n\ts_sub_u32 %[c], %[c], 1\n\ts_cbranch_scc0 1b\n\t.p2align 3\n\t2:\n\tv_mfma_f32_16x16x32_bf16 v[160:163], v[72:75], v[132:135], v[160:163]\n\tds_read_b128 v[184:187], %[bb] offset:3648\n\tv_cvt_pk_bf16_f32 v193, v220, v206\n\tv_mfma_f32_16x16x32_bf16 v[160:163], v[68:71], v[136:139], v[160:163]\n\tds_read_b128 v[164:167], %[bb] offset:3712\n\tv_cvt_pk_bf16_f32 v194, v219, v205\n\tv_mfma_f32_16x16x32_bf16 v[160:163], v[64:67], v[140:143], v[160:163]\n\tds_read_b128 v[168:171], %[bb] offset:3776\n\tv_cvt_pk_bf16_f32 v195, v218, v204\n\tv_mfma_f32_16x16x32_bf16 v[160:163], v[60:63], v[144:147], v[160:163]\n\tds_read_b128 v[172:175], %[bb] offset:3840\n\tv_cvt_pk_bf16_f32 v196, v225, v211\n\tv_mfma_f32_16x16x32_bf16 v[160:163], v[56:59], v[200:203], v[160:163]\n\tds_read_b128 v[176:179], %[bb] offset:3904\n\tv_cvt_pk_bf16_f32 v197, v224, v210\n\tv_mfma_f32_16x16x32_bf16 v[160:163], v[192:195], v[92:95], v[160:163]\n\tds_read_b128 v[180:183], %[bb] offset:3968\n\tv_cvt_pk_bf16_f32 v198, v223, v209\n\tv_cvt_pk_bf16_f32 v199, v222, v208\n\ts_nop 1\n\ts_nop 0\n\tv_mfma_f32_16x16x32_bf16 v[160:163], v[196:199], v[104:107], v[160:163]\n\ts_waitcnt lgkmcnt(3)\n\ts_nop 0\n\tv_mfma_f32_16x16x32_bf16 v[160:163], v[52:55], v[184:187], v[160:163]\n\tds_read_b128 v[184:187], %[bb] offset:4032\n\tv_mfma_f32_16x16x32_bf16 v[160:163], v[48:51], v[164:167], v[160:163]\n\tds_read_b128 v[164:167], %[bb] offset:4096\n\tv_mfma_f32_16x16x32_bf16 v[160:163], v[44:47], v[168:171], v[160:163]\n\ts_waitcnt lgkmcnt(2)\n\ts_nop 0\n\tv_mfma_f32_16x16x32_bf16 v[160:163], v[40:43], v[172:175], v[160:163]\n\tv_mfma_f32_16x16x32_bf16 v[160:163], v[36:39], v[176:179], v[160:163]\n\tv_mfma_f32_16x16x32_bf16 v[160:163], v[32:35], v[180:183], v[160:163]\n\ts_waitcnt lgkmcnt(0)\n\ts_nop 0\n\tv_mfma_f32_16x16x32_bf16 v[160:163], v[28:31], v[184:187], v[160:163]\n\tv_mfma_f32_16x16x32_bf16 v[160:163], v[24:27], v[164:167], v[160:163]\n\ts_nop 7\n\tds_write_b128 %[yb], v[160:163] offset:8064\n\tds_write_b8 %[ctc], %[one] offset:448"
#define CONV_CLOBBER "v24", "v25", "v26", "v27", "v28", "v29", "v30", "v31", "v32", "v33", "v34", "v35", "v36", "v37", "v38", "v39", "v40", "v41", "v42", "v43", "v44", "v45", "v46", "v47", "v48", "v49", "v50", "v51", "v52", "v53", "v54", "v55", "v56", "v57", "v58", "v59", "v60", "v61", "v62", "v63", "v64", "v65", "v66", "v67", "v68", "v69", "v70", "v71", "v72", "v73", "v74", "v75", "v76", "v77", "v78", "v79", "v80", "v81", "v82", "v83", "v84", "v85", "v86", "v87", "v88", "v89", "v90", "v91", "v92", "v93", "v94", "v95", "v96", "v97", "v98", "v99", "v100", "v101", "v102", "v103", "v104", "v105", "v106", "v107", "v108", "v109", "v110", "v111", "v112", "v113", "v114", "v115", "v116", "v117", "v118", "v119", "v120", "v121", "v122", "v123", "v124", "v125", "v126", "v127", "v128", "v129", "v130", "v131", "v132", "v133", "v134", "v135", "v136", "v137", "v138", "v139", "v140", "v141", "v142", "v143", "v144", "v145", "v146", "v147", "v148", "v149", "v150", "v151", "v152", "v153", "v154", "v155", "v156", "v157", "v158", "v159", "v160", "v161", "v162", "v163", "v164", "v165", "v166", "v167", "v168", "v169", "v170", "v171", "v172", "v173", "v174", "v175", "v176", "v177", "v178", "v179", "v180", "v181", "v182", "v183", "v184", "v185", "v186", "v187", "v188", "v189", "v190", "v191", "v192", "v193", "v194", "v195", "v196", "v197", "v198", "v199", "v200", "v201", "v202", "v203", "v204", "v205", "v206", "v207", "v208", "v209", "v210", "v211", "v212", "v213", "v214", "v215", "v216", "v217", "v218", "v219", "v220", "v221", "v222", "v223", "v224", "v225", "v226", "v227", "v228", "v229"

typedef __attribute__((address_space(4))) const Params* KArg;
#define ROLE_BEGIN(N) int b, tid; \
    asm volatile("fk_role" #N ":\n\tv_mov_b32 %0, v0" : "=&v"(tid) : "{s2}"(b0), "{v0}"(tid0) : "memory"); \
    asm volatile("s_mov_b32 %0, s2" : "=&s"(b) : "{s2}"(b0)); \
    const int lane = tid & 63, wave = __builtin_amdgcn_readfirstlane(tid >> 6), xcd = b & 7, slot = b >> 3; \
    const Xcd xs = xcd_shares(xcd); \
    const int cw = xcd * 16 + slot + ((112 + xcd * (CB - 32)) & ((15 - slot) >> 31)), chbase = cw * 4, w = xs.w0 + slot - xs.nconv; (void)chbase; (void)w; (void)lane
#define ROLE_ARGS() Params p; SBAR(); \
    asm volatile("s_waitcnt lgkmcnt(0)" : "={s[56:57]}"(p.x), "={s[58:59]}"(p.Are), "={s[60:61]}"(p.Aim), "={s[62:63]}"(p.Bre), "={s[64:65]}"(p.Bim), "={s[66:67]}"(p.Cre), "={s[68:69]}"(p.Cim), "={s[70:71]}"(p.logdt), \
                 "={s[72:73]}"(p.dskip), "={s[74:75]}"(p.wr), "={s[76:77]}"(p.wg), "={s[78:79]}"(p.wu), "={s[80:81]}"(p.wd), "={s[82:83]}"(p.gw), "={s[84:85]}"(p.out), "={s[86:87]}"(p.ws))
#define ROLE_END() do { __builtin_amdgcn_s_waitcnt(0x0070); asm volatile("s_endpgm" ::: "memory"); } while (0)
#define MF(A_, B_, C_) __builtin_amdgcn_mfma_f32_16x16x32_bf16(A_, B_, C_, 0, 0, 0)

#define FFN_PRE() u32 gof = w * 128 + ((wave >> 1) * 8 + (lane >> 3)) * (F * 2) + ((lane & 7) ^ (((lane >> 3) & 2) | ((wave & 2) << 1))) * 16; asm volatile("" : "+v"(gof)); ROLE_ARGS()
#define FFN_FILL() \
    { const char* gsrc = (const char*)((wave & 1) ? p.wu : p.wg); \
      _Pragma("unroll") for (int i = 0; i < 18; ++i) { \
          asm volatile("s_mov_b32 m0, %2\n\ts_nop 0\n\tglobal_load_lds_dwordx4 %0, %1" WNT :: "v"(gof), "s"(gsrc + i * (32 * F * 2)), "s"((8 * i + wave) * 1024) : "memory"); \
          if (i == 5 || i == 11) __builtin_amdgcn_s_barrier(); } }
#define FFN_WDFRAG(k, pr) do { LAS char* t_ = ta + (pr) * 64; \
    _Pragma("unroll") for (int e_ = 0; e_ < 8; ++e_) { const int e = (e_ & 1) | ((e_ & 2) << 1) | ((e_ & 4) >> 1); a[k][e] = __builtin_amdgcn_ds_read_tr16_b64_v4i16((LAS s16x4*)(t_ + (e >> 2) * 32 + (e & 3) * 16 * WDP)); } SBAR(); } while (0)
#define FFN_DOWN(k, pr) do { v4f y_ = MF(__builtin_shufflevector(a[k][0], a[k][1], 0, 1, 2, 3, 4, 5, 6, 7), hf[0], ((v4f){0.f, 0.f, 0.f, 0.f})); \
    y_ = MF(__builtin_shufflevector(a[k][4], a[k][5], 0, 1, 2, 3, 4, 5, 6, 7), hf[1], y_); \
    y_ = MF(__builtin_shufflevector(a[k][2], a[k][3], 0, 1, 2, 3, 4, 5, 6, 7), hf[2], y_); \
    ya[k] = MF(__builtin_shufflevector(a[k][6], a[k][7], 0, 1, 2, 3, 4, 5, 6, 7), hf[3], y_); } while (0)
#define FFN_ADDR() const int n = lane & 15, q = lane >> 4; \
    LAS char* ta = smem + (lane >> 2) * WDP + (lane & 3) * 8; \
    LAS char* hrow = smem + FL_H + (n & 7) * 144 + q * 16; \
    LAS char* ha = n < 8 ? hrow : smem + FL_Z + q * 16; \
    LAS char* hb = n < 8 ? smem + FL_Z + q * 16 : hrow; \
    u32 obase = OFF_PART + (4 * (n >> 3) + q) * PP + hline(p.ws, w) + (n & 7) * 16; \
    asm volatile("" : "+v"(ta), "+v"(ha), "+v"(hb), "+v"(obase))
#define FFN_HFRAG() const bf16x8 hf[4] = {*(LAS bf16x8*)(ha), *(LAS bf16x8*)(hb), *(LAS bf16x8*)(ha + 64), *(LAS bf16x8*)(hb + 64)}
#define FFN_PUB(k, pr) __builtin_amdgcn_raw_buffer_store_b128((v4u){cvtpk(ya[k][0], ya[k][1]), tag, cvtpk(ya[k][2], ya[k][3]), tag}, WSR(), obase + (pr) * (8 * PP), 0, 16)

__global__ __launch_bounds__(512, 2) void fused_kernel(Params p_unused) {
    extern __shared__ __attribute__((aligned(16))) char smem_[];
    LAS char* smem = (LAS char*)smem_;
    const KArg kp0 = (KArg)__builtin_amdgcn_kernarg_segment_ptr();
    const int tid0 = threadIdx.x, b0 = blockIdx.x;
    asm volatile("s_load_dwordx16 s[56:71], s[0:1], 0x0\n\ts_load_dwordx16 s[72:87], s[0:1], 0x40\n\tv_readfirstlane_b32 s92, v0\n\t"
                 "s_cmp_ge_u32 s2, 32\n\ts_cbranch_scc1 1f\n\t"
                 "s_getpc_b64 s[90:91]\n\ts_and_b32 s90, s90, 0xffffff00\n\ts_and_b32 s93, s2, -8\n\ts_lshr_b32 s89, s92, 6\n\ts_add_u32 s93, s93, s89\n\ts_min_u32 s93, s93, %3\n\ts_lshl_b32 s93, s93, 10\n\t"
                 "v_and_b32 v1, 63, v0\n\tv_lshlrev_b32 v1, 4, v1\n\tv_add_u32 v1, s93, v1\n\ts_mov_b32 m0, %4\n\ts_nop 0\n\tglobal_load_lds_dwordx4 v1, s[90:91]\n"
                 "1:\n\ts_and_b32 s93, s2, 7\n\ts_cmp_lt_u32 s93, %5\n\ts_cselect_b32 s93, %6, 16\n\ts_cselect_b32 s89, %7, %8\n\ts_lshr_b32 s90, s2, 3\n\t"
                 "s_cmp_lt_u32 s90, s93\n\ts_cbranch_scc0 2f\n\ts_cmp_lt_u32 s92, 256\n\ts_cbranch_scc1 fk_role0\n\ts_branch fk_role1\n"
                 "2:\n\ts_cmp_lt_u32 s90, s89\n\ts_cbranch_scc0 3f\n\ts_cmp_lt_u32 s92, 256\n\ts_cbranch_scc1 fk_role2\n\ts_branch fk_role3\n"
                 "3:\n\ts_cmp_eq_u32 s90, s89\n\ts_cbranch_scc1 fk_role4\n\ts_branch fk_role5"
                 :: "{s[0:1]}"(kp0), "{s2}"(b0), "{v0}"(tid0), "n"(CODE_KB - 1), "n"(LDS_DUMMY), "n"(NBX), "n"(CB), "n"(CB + FB), "n"(16 + FA) : "memory", "scc", "s89", "s90", "s91", "s92", "s93", "v1");
    const int slot = b0 >> 3, wave = __builtin_amdgcn_readfirstlane(tid0 >> 6);
    if (slot >= 18) {
    if (slot >= 27 || wave >= 4) {
    if (slot < 28) {
    if (slot == 27)
    {
        ROLE_BEGIN(4);
        ROLE_ARGS();
        u32 tagv;
        asm volatile("v_mov_b32 %0, 0\n\tglobal_load_dword %0, %0, %1 sc1\n\ts_waitcnt vmcnt(0)" : "=&v"(tagv) : "s"(p.ws) : "memory");
        const u32 tag = __builtin_amdgcn_readfirstlane(tagv) + 1u;
        if (tid < 128) ((LAS u32*)smem)[tid] = 0;
        u32 ga = (u32)(uintptr_t)(smem + (lane & 15) * 4), gv = (u32)(uintptr_t)(smem + (lane >> 2) * 4), gq_ = (u32)(uintptr_t)(smem + (lane & 3) * 16), zero = (u32)(uintptr_t)smem;
        u32 ro = OFF_RG + hline(p.ws, tid >> 3) + (tid & 7) * 16, ro2 = OFF_RG + hline(p.ws, 64 + (tid >> 3)) + (tid & 7) * 16, so = OFF_SEL + ((lane & 1) * 8 + xcd) * 256, xo = (lane & 3) * 128;
        asm volatile("" : "+v"(ga), "+v"(gv), "+v"(gq_), "+v"(zero), "+v"(ro), "+v"(ro2), "+v"(so), "+v"(xo));
        BAR();
        __builtin_amdgcn_s_sleep(SELWAIT);
        v4u qa, qb;
        u32 c_;
#define POLL_A "global_load_dwordx4 v[20:23], %[ro], %[ws] sc1\n\tglobal_load_dwordx4 v[24:27], %[r2], %[ws] sc1\n\t"
#define POLL_B "global_load_dwordx4 v[28:31], %[ro], %[ws] sc1\n\tglobal_load_dwordx4 v[32:35], %[r2], %[ws] sc1\n\t"
        asm volatile("s_mov_b32 %[c], 0x40000\n\t" POLL_A "s_sleep 8\n\t" POLL_B
                     "1:\n\ts_waitcnt vmcnt(2)\n\tv_cmpx_eq_u32 %[tag], v21\n\tv_cmpx_eq_u32 %[tag], v23\n\tv_cmpx_eq_u32 %[tag], v25\n\tv_cmpx_eq_u32 %[tag], v27\n\t"
                     "s_cmp_eq_u64 exec, -1\n\ts_mov_b64 exec, -1\n\ts_cbranch_scc1 3f\n\t" POLL_A
                     "s_waitcnt vmcnt(2)\n\tv_cmpx_eq_u32 %[tag], v29\n\tv_cmpx_eq_u32 %[tag], v31\n\tv_cmpx_eq_u32 %[tag], v33\n\tv_cmpx_eq_u32 %[tag], v35\n\t"
                     "s_cmp_eq_u64 exec, -1\n\ts_mov_b64 exec, -1\n\ts_cbranch_scc1 2f\n\t" POLL_B
                     "s_sub_u32 %[c], %[c], 1\n\ts_cbranch_scc0 1b\n\ts_waitcnt vmcnt(0)\n"
                     "2:\n\tv_mov_b32 v36, v28\n\tv_mov_b32 v37, v30\n\tv_mov_b32 v38, v32\n\tv_mov_b32 v39, v34\n\ts_branch 4f\n"
                     "3:\n\tv_mov_b32 v36, v20\n\tv_mov_b32 v37, v22\n\tv_mov_b32 v38, v24\n\tv_mov_b32 v39, v26\n"
                     "4:"
                     : "=&{v[36:39]}"(qa), [c] "=&s"(c_) : [ro] "v"(ro), [r2] "v"(ro2), [ws] "s"(p.ws), [tag] "s"(tag)
                     : "memory", "scc", "vcc", "v20", "v21", "v22", "v23", "v24", "v25", "v26", "v27", "v28", "v29", "v30", "v31", "v32", "v33", "v34", "v35");
        asm volatile("" : "=&{v[20:23]}"(qb));
        v4u qc, qd, qe;
        asm volatile("" : "=&{v[24:27]}"(qc), "=&{v[28:31]}"(qd), "=&{v[32:35]}"(qe));
        u32 rank, tau, cnt;
        u64 m0_, m1_, m2_, m3_;
        {
            u32 v, nv; v4u q4;
            asm volatile("ds_max_u32 %0, %1\n\ts_waitcnt lgkmcnt(0)\n\ts_barrier" :: "v"(ga), "v"(max(max(qa.x, qa.y), max(qa.z, qa.w))) : "memory");
#define RANK16(off) asm volatile("ds_read_b32 %[v], %[gv] offset:" #off "\n\tds_read_b128 v[40:43], %[gq] offset:" #off "\n\tds_read_b32 %[n], %[z] offset:64\n\tv_mov_b32 %[r], 0\n\ts_waitcnt lgkmcnt(0)\n\t" \
                         "v_cmp_gt_u32 %[a], v40, %[v]\n\tv_cmp_gt_u32 %[b], v41, %[v]\n\tv_cmp_gt_u32 %[c], v42, %[v]\n\tv_cmp_gt_u32 %[d], v43, %[v]\n\t" \
                         "v_addc_co_u32 %[r], vcc, 0, %[r], %[a]\n\tv_addc_co_u32 %[r], vcc, 0, %[r], %[b]\n\tv_addc_co_u32 %[r], vcc, 0, %[r], %[c]\n\tv_addc_co_u32 %[r], vcc, 0, %[r], %[d]\n\ts_nop 1\n\t" \
                         "v_add_u32_dpp %[r], %[r], %[r] quad_perm:[1,0,3,2] row_mask:0xf bank_mask:0xf\n\ts_nop 1\n\tv_add_u32_dpp %[r], %[r], %[r] quad_perm:[2,3,0,1] row_mask:0xf bank_mask:0xf\n\ts_nop 0" \
                         : [v] "=&v"(v), [r] "=&v"(rank), [n] "=&v"(nv), "=&{v[40:43]}"(q4), [a] "=&s"(m0_), [b] "=&s"(m1_), [c] "=&s"(m2_), [d] "=&s"(m3_) : [gv] "v"(gv), [gq] "v"(gq_), [z] "v"(zero) : "memory", "vcc")
            RANK16(0);
            tau = (u32)__builtin_amdgcn_readlane((int)v, (int)__builtin_ctzll(__ballot(rank == 7)));
            u32 pos;
            asm volatile("s_nop 1\n\tv_mov_b32 %[n], 0\n\tv_cmp_le_u32 %[a], %[t], v36\n\tv_cmp_le_u32 %[b], %[t], v37\n\tv_cmp_le_u32 %[c], %[t], v38\n\tv_cmp_le_u32 %[d], %[t], v39\n\t"
                         "v_addc_co_u32 %[n], vcc, 0, %[n], %[a]\n\tv_addc_co_u32 %[n], vcc, 0, %[n], %[b]\n\tv_addc_co_u32 %[n], vcc, 0, %[n], %[c]\n\tv_addc_co_u32 %[n], vcc, 0, %[n], %[d]\n\t"
                         "v_lshlrev_b32 %[n], 2, %[n]\n\tv_cmpx_ne_u32 0, %[n]\n\tds_add_rtn_u32 %[p], %[z], %[n] offset:64\n\ts_waitcnt lgkmcnt(0)\n\t"
                         "s_mov_b64 exec, %[a]\n\tds_write_b32 %[p], v36 offset:256\n\tv_add_u32 %[p], 4, %[p]\n\t"
                         "s_mov_b64 exec, %[b]\n\tds_write_b32 %[p], v37 offset:256\n\tv_add_u32 %[p], 4, %[p]\n\t"
                         "s_mov_b64 exec, %[c]\n\tds_write_b32 %[p], v38 offset:256\n\tv_add_u32 %[p], 4, %[p]\n\t"
                         "s_mov_b64 exec, %[d]\n\tds_write_b32 %[p], v39 offset:256\n\t"
                         "s_mov_b64 exec, -1\n\ts_waitcnt lgkmcnt(0)\n\ts_barrier"
                         : [n] "=&v"(cnt), [p] "=&v"(pos), [a] "=&s"(m0_), [b] "=&s"(m1_), [c] "=&s"(m2_), [d] "=&s"(m3_) : [t] "s"(tau), [z] "v"(zero), "{v[36:39]}"(qa) : "memory", "vcc");
            if (wave == 0) {
                RANK16(256);
                const int nc = __builtin_amdgcn_readfirstlane((int)nv) >> 2;
                if (__builtin_expect(nc > 16, 0)) {
                    v = (u32)__shfl((int)top8_d(smem, lane, nc), (lane >> 2) & 7);
                    rank = lane >> 2;
                }
                const u64 m8 = __ballot(rank < 8);
                asm volatile("s_and_b64 exec, %[m], %[k2]\n\tglobal_store_dwordx2 %[so], %[d], %[ws] sc1\n\ts_mov_b64 exec, %[m]\n\ts_mov_b32 m0, 0x8000\n\ts_nop 0\n\t"
                             "global_load_lds_dword %[xo], %[x]\n\tglobal_load_lds_dword %[xo], %[x] offset:512\n\ts_and_b64 exec, %[m], %[k1]\n\tglobal_load_lds_dword %[xo], %[x] offset:1024\n\ts_mov_b64 exec, -1"
                             :: [m] "s"(m8), [k2] "s"(0x3333333333333333ull), [k1] "s"(0x1111111111111111ull), [so] "v"(so + rank * 8), [d] "v"((v2u){v, tag}), [ws] "s"(p.ws),
                                [xo] "v"(xo + (~v & 2047u) * 1152u), [x] "s"(p.x) : "memory", "scc");
                asm volatile("s_mov_b64 exec, 1\n\tglobal_store_dwordx2 %0, %1, %2 sc1\n\ts_mov_b64 exec, -1" :: "v"(OFF_CI + (416 + xcd) * 8), "v"((v2u){tag, tag}), "s"(p.ws) : "memory");
            }
        }
        asm volatile("s_waitcnt vmcnt(0)" :: "v"(qb), "v"(qc), "v"(qd), "v"(qe) : "memory");
        if (xcd == 0) {
            const u32 cio = OFF_CI + (tid < 212 ? tid : 0) * 16;
            for (u32 spins = 0;; ++spins) {
                REREAD();
                const v4u q2 = __builtin_amdgcn_raw_buffer_load_b128(WSR(), cio, 0, 16);
                if (__all((q2.y == tag) & (q2.w == tag)) || spins > SPIN_CAP) break;
                __builtin_amdgcn_s_sleep(16);
            }
            BAR();
            if (tid == 0) __hip_atomic_store((GAS u32*)p.ws, tag, RLX_AGENT);
        }
        ROLE_END();
    }
    else
        {
            ROLE_BEGIN(3);
            FFN_PRE();
            const int pw = wave - 4;
            u32 tagv;
            asm volatile("v_mov_b32 %0, 0\n\tglobal_load_dword %0, %0, %1 sc1" : "=&v"(tagv) : "s"(p.ws) : "memory");
            FFN_FILL();
            v2u sel;
            const u32 so = OFF_SEL + xcd * 256 + (2 * pw + (lane & 1)) * 8, l16 = lane * 16;
#define POLL_S "global_load_dwordx2 v[10:11], %[so], %[ws] sc1\n\t"
            asm volatile("s_waitcnt vmcnt(18)" : "+v"(tagv) :: "memory");
            const u32 tag = __builtin_amdgcn_readfirstlane(tagv) + 1u;
            *(LAS v4u*)(smem + FL_Z + min(l16, 256u)) = (v4u){lane >= 16 ? tag : 0u, 0u, 0u, 0u};
            FFN_ADDR();
            const char* dsrc = (const char*)(p.wd + (size_t)(64 * w + 32 * (pw >> 1)) * D);
            const u32 ddst = (pw >> 1) * 32 * WDP + (pw & 1) * 1024;
            u32 dof[19];
#pragma unroll
            for (int i = 0; i < 19; ++i) {
                const u32 id = min(128u * i + 64u * (pw & 1) + lane, 2367u), r0 = id / 74u;
                dof[i] = r0 * 1152u + min(id - 74u * r0, 71u) * 16u;
                asm volatile("" : "+v"(dof[i]));
            }
#define LD_WD(i) asm volatile("s_mov_b32 m0, %2\n\ts_nop 0\n\tglobal_load_lds_dwordx4 %0, %1" :: "v"(dof[i]), "s"(dsrc), "s"(ddst + (i) * 2048) : "memory")
            asm volatile("s_waitcnt vmcnt(12) lgkmcnt(0)\n\ts_barrier\n\ts_waitcnt vmcnt(6)\n\ts_barrier\n\ts_waitcnt vmcnt(2)\n\t" POLL_S "s_waitcnt vmcnt(1)\n\ts_barrier" : "=&{v[10:11]}"(sel) : [so] "v"(so), [ws] "s"(p.ws) : "memory");
            u32 c_, early, mw = ddst;
            asm volatile("s_mov_b32 %[e], 0\n\ts_waitcnt vmcnt(0)\n\tv_cmp_ne_u32 vcc, %[tag], v11\n\ts_cmp_eq_u64 vcc, 0\n\ts_cbranch_scc1 2f\n\t"
                         "s_mov_b32 %[e], 1\n\ts_mov_b32 %[c], 0x80000\n\ts_mov_b32 m0, %[m]\n\ts_add_u32 %[m], %[m], 0x800\n\tglobal_load_lds_dwordx4 %[d0], %[src]\n\ts_mov_b32 m0, %[m]\n\ts_add_u32 %[m], %[m], 0x800\n\tglobal_load_lds_dwordx4 %[d1], %[src]\n\ts_mov_b32 m0, %[m]\n\ts_add_u32 %[m], %[m], 0x800\n\tglobal_load_lds_dwordx4 %[d2], %[src]\n\ts_mov_b32 m0, %[m]\n\ts_add_u32 %[m], %[m], 0x800\n\tglobal_load_lds_dwordx4 %[d3], %[src]\n\ts_mov_b32 m0, %[m]\n\ts_add_u32 %[m], %[m], 0x800\n\tglobal_load_lds_dwordx4 %[d4], %[src]\n\ts_mov_b32 m0, %[m]\n\ts_add_u32 %[m], %[m], 0x800\n\tglobal_load_lds_dwordx4 %[d5], %[src]\n\t"
                         "1:\n\t" POLL_S "s_waitcnt vmcnt(0)\n\tv_cmp_ne_u32 vcc, %[tag], v11\n\ts_cmp_eq_u64 vcc, 0\n\ts_cbranch_scc1 2f\n\ts_sub_u32 %[c], %[c], 1\n\ts_cbranch_scc0 1b\n2:"
                         : "+{v[10:11]}"(sel), [c] "=&s"(c_), [e] "=&s"(early), [m] "+s"(mw) : [so] "v"(so), [ws] "s"(p.ws), [tag] "s"(tag), [src] "s"(dsrc), [d0] "v"(dof[0]), [d1] "v"(dof[1]), [d2] "v"(dof[2]), [d3] "v"(dof[3]), [d4] "v"(dof[4]), [d5] "v"(dof[5]) : "memory", "scc", "vcc");
#pragma unroll
            for (int k = 0; k < 2; ++k) {
                const u32 t = ~(u32)__builtin_amdgcn_readlane((int)sel.x, k) & 2047u;
                const char* xr = (const char*)p.x + t * (D * 2);
                asm volatile("s_mov_b32 m0, %2\n\ts_nop 0\n\tglobal_load_lds_dwordx4 %0, %1\n\ts_mov_b64 exec, 0xff\n\tglobal_load_lds_dwordx4 %0, %1 offset:1024\n\ts_mov_b64 exec, -1"
                             :: "v"(l16), "s"(xr), "s"(FL_XS + (2 * pw + k) * XSP) : "memory");
            }
            asm volatile("s_barrier\n\ts_cmp_eq_u32 %[e], 0\n\ts_cbranch_scc0 1f\n\ts_mov_b32 m0, %[m]\n\ts_add_u32 %[m], %[m], 0x800\n\tglobal_load_lds_dwordx4 %[d0], %[src]\n\ts_mov_b32 m0, %[m]\n\ts_add_u32 %[m], %[m], 0x800\n\tglobal_load_lds_dwordx4 %[d1], %[src]\n\ts_mov_b32 m0, %[m]\n\ts_add_u32 %[m], %[m], 0x800\n\tglobal_load_lds_dwordx4 %[d2], %[src]\n\ts_mov_b32 m0, %[m]\n\ts_add_u32 %[m], %[m], 0x800\n\tglobal_load_lds_dwordx4 %[d3], %[src]\n\ts_mov_b32 m0, %[m]\n\ts_add_u32 %[m], %[m], 0x800\n\tglobal_load_lds_dwordx4 %[d4], %[src]\n\ts_mov_b32 m0, %[m]\n\ts_add_u32 %[m], %[m], 0x800\n\tglobal_load_lds_dwordx4 %[d5], %[src]\n\ts_mov_b32 m0, %[m]\n\ts_add_u32 %[m], %[m], 0x800\n\tglobal_load_lds_dwordx4 %[d6], %[src]\n\ts_mov_b32 m0, %[m]\n\ts_add_u32 %[m], %[m], 0x800\n\tglobal_load_lds_dwordx4 %[d7], %[src]\n\ts_mov_b32 m0, %[m]\n\ts_add_u32 %[m], %[m], 0x800\n\tglobal_load_lds_dwordx4 %[d8], %[src]\n\ts_mov_b32 m0, %[m]\n\ts_add_u32 %[m], %[m], 0x800\n\tglobal_load_lds_dwordx4 %[d9], %[src]\n\ts_mov_b32 m0, %[m]\n\ts_add_u32 %[m], %[m], 0x800\n\tglobal_load_lds_dwordx4 %[d10], %[src]\n\ts_mov_b32 m0, %[m]\n\ts_add_u32 %[m], %[m], 0x800\n\tglobal_load_lds_dwordx4 %[d11], %[src]\n\ts_waitcnt vmcnt(12)\n\ts_branch 2f\n1:\n\ts_mov_b32 m0, %[m]\n\ts_add_u32 %[m], %[m], 0x800\n\tglobal_load_lds_dwordx4 %[d6], %[src]\n\ts_mov_b32 m0, %[m]\n\ts_add_u32 %[m], %[m], 0x800\n\tglobal_load_lds_dwordx4 %[d7], %[src]\n\ts_mov_b32 m0, %[m]\n\ts_add_u32 %[m], %[m], 0x800\n\tglobal_load_lds_dwordx4 %[d8], %[src]\n\ts_mov_b32 m0, %[m]\n\ts_add_u32 %[m], %[m], 0x800\n\tglobal_load_lds_dwordx4 %[d9], %[src]\n\ts_mov_b32 m0, %[m]\n\ts_add_u32 %[m], %[m], 0x800\n\tglobal_load_lds_dwordx4 %[d10], %[src]\n\ts_mov_b32 m0, %[m]\n\ts_add_u32 %[m], %[m], 0x800\n\tglobal_load_lds_dwordx4 %[d11], %[src]\n\ts_waitcnt vmcnt(6)\n2:\n\ts_barrier"
                         : [m] "+s"(mw) : [e] "s"(early), [src] "s"(dsrc), [d0] "v"(dof[0]), [d1] "v"(dof[1]), [d2] "v"(dof[2]), [d3] "v"(dof[3]), [d4] "v"(dof[4]), [d5] "v"(dof[5]), [d6] "v"(dof[6]), [d7] "v"(dof[7]), [d8] "v"(dof[8]), [d9] "v"(dof[9]), [d10] "v"(dof[10]), [d11] "v"(dof[11]) : "memory", "scc");
#pragma unroll
            for (int i = 12; i < 18; ++i) LD_WD(i);
            if (!(pw & 1)) LD_WD(18);
            asm volatile("s_mov_b64 exec, 1\n\tglobal_store_dwordx2 %0, %1, %2 sc1\n\ts_mov_b64 exec, -1\n\ts_barrier" :: "v"(OFF_CI + (128 + 4 * w + pw) * 8), "v"((v2u){tag, tag}), "s"(p.ws) : "memory");
            FFN_HFRAG();
            asm volatile("s_waitcnt vmcnt(1)\n\ts_barrier" ::: "memory");
            s16x4 a[3][8];
            FFN_WDFRAG(0, 8 + pw); FFN_WDFRAG(1, 12 + pw); FFN_WDFRAG(2, 16 + (pw & 1));
            v4f ya[3];
            FFN_DOWN(0, 8 + pw); FFN_DOWN(1, 12 + pw);
            if (pw < 2) { FFN_DOWN(2, 16 + pw); FFN_PUB(2, 16 + pw); }
            FFN_PUB(0, 8 + pw); FFN_PUB(1, 12 + pw);
            ROLE_END();
        }
    } else
    {
        ROLE_BEGIN(5);
        ROLE_ARGS();
        _Pragma("unroll 1") for (int np = 31 - xs.nconv - xs.nffn, e = (31 - slot) + np * wave; touch(p, xs, e, lane, (u32)(uintptr_t)(smem + wave * 256)); e += 8 * np) {}
        asm volatile("s_waitcnt vmcnt(0)" ::: "memory");
        ROLE_END();
    }
    } else
        {
            ROLE_BEGIN(2);
            FFN_PRE();
            FFN_FILL();
            FFN_ADDR();
            LAS char* gp = smem + q * 2048 + ((lane >> 2) & 3) * 128 + ((2 * wave + ((lane >> 1) & 1)) ^ (((lane >> 2) & 2) | ((q & 1) << 2))) * 16 + (lane & 1) * 8;
            LAS char* xp = smem + FL_XS + (n & 7) * XSP + q * 16;
            const int hc = 16 * wave + n;
            LAS char* hw_ = smem + FL_H + (4 * (q & 1) + 2 * (q >> 1)) * 144 + ((hc & 35) | ((hc & 12) << 1) | ((hc & 16) >> 2)) * 2;
            asm volatile("" : "+v"(gp), "+v"(xp), "+v"(hw_));
            bf16x8 gf[18], uf[18];
#pragma unroll
            for (int sl = 0; sl < 3; ++sl) {
                if (sl == 0) asm volatile("s_waitcnt vmcnt(12)\n\ts_barrier" ::: "memory");
                if (sl == 1) asm volatile("s_waitcnt vmcnt(6)\n\ts_barrier" ::: "memory");
                if (sl == 2) asm volatile("s_waitcnt vmcnt(0) lgkmcnt(0)\n\ts_barrier" ::: "memory");
                LAS char* g_ = gp + sl * 49152;
                asm volatile("" : "+v"(g_));
#pragma unroll
                for (int i = 0; i < 6; ++i) {
                    gf[6 * sl + i] = __builtin_shufflevector(__builtin_amdgcn_ds_read_tr16_b64_v4i16((LAS s16x4*)(g_ + i * 8192)), __builtin_amdgcn_ds_read_tr16_b64_v4i16((LAS s16x4*)(g_ + i * 8192 + 512)), 0, 1, 2, 3, 4, 5, 6, 7);
                    uf[6 * sl + i] = __builtin_shufflevector(__builtin_amdgcn_ds_read_tr16_b64_v4i16((LAS s16x4*)(g_ + i * 8192 + 1024)), __builtin_amdgcn_ds_read_tr16_b64_v4i16((LAS s16x4*)(g_ + i * 8192 + 1536)), 0, 1, 2, 3, 4, 5, 6, 7);
                }
#pragma unroll
                for (int i = 0; i < 6; ++i) asm volatile("" : "+v"(gf[6 * sl + i]), "+v"(uf[6 * sl + i]));
            }
            u32 tag = *(LAS u32*)(smem + FL_TAG);
            asm volatile("s_barrier\n\ts_barrier" : "+v"(tag) :: "memory");
            bf16x8 xf[18];
            v4f ag = {0.f, 0.f, 0.f, 0.f}, au = ag;
#pragma unroll
            for (int s = 0; s < 4; ++s) xf[s] = *(LAS bf16x8*)(xp + s * 64);
            SBAR();
#pragma unroll
            for (int s = 0; s < 18; ++s) {
                ag = MF(xf[s], gf[s], ag);
                if (s + 4 < 18) xf[s + 4] = *(LAS bf16x8*)(xp + (s + 4) * 64);
                au = MF(xf[s], uf[s], au);
                SBAR();
            }
            {
                const u32 ab = cvtpk(q < 2 ? ag[0] : ag[2], q < 2 ? ag[1] : ag[3]), ub = cvtpk(q < 2 ? au[0] : au[2], q < 2 ? au[1] : au[3]);
                const u32 sl = cvtpk(bflo(ab) * sigmoidf_(bflo(ab)), bfhi(ab) * sigmoidf_(bfhi(ab)));
                const u32 hh = cvtpk(bflo(sl) * bflo(ub), bfhi(sl) * bfhi(ub));
                *(LAS u16*)(hw_) = (u16)hh; *(LAS u16*)(hw_ + 144) = (u16)(hh >> 16);
            }
            BAR();
            FFN_HFRAG();
            asm volatile("s_barrier" ::: "memory");
            s16x4 a[2][8];
            FFN_WDFRAG(0, 2 * wave); FFN_WDFRAG(1, 2 * wave + 1);
            v4f ya[2];
            FFN_DOWN(0, 2 * wave); FFN_DOWN(1, 2 * wave + 1);
            FFN_PUB(0, 2 * wave); FFN_PUB(1, 2 * wave + 1);
            ROLE_END();
        }
    } else {
    if (wave >= 4)
    {
        ROLE_BEGIN(1);
        const int hw = wave - 4, ht = tid - 256;
        const v4u z4 = {0, 0, 0, 0};
        const int sub = lane & 15;
        v4u xr[5];
        u32 tagv;
        const int rrow = 16 * (cw & 127) + 4 * hw;
        u32 dst = (u32)(uintptr_t)(smem + LDS_RW) + hw * 3072, l16 = lane * 16;
        u32 ro = (u32)(((rrow + (lane >> 4)) & ~((127 - cw) >> 31)) * D + sub * 8) * 2, ro4 = ro - (sub & 8) * 16;
        u32 uo0 = (u32)((256 * hw + 4 * lane) * D + chbase) * 2;
        asm volatile("" : "+s"(dst), "+v"(l16), "+v"(ro), "+v"(ro4), "+v"(uo0));
        ROLE_ARGS();
        {
            asm volatile("s_mov_b32 m0, %5\n\tv_mov_b32 %0, 0\n\tglobal_load_dword %0, %0, %2 sc1\n\tglobal_load_lds_dwordx4 %3, %4\n\ts_mov_b64 exec, 0xff\n\tglobal_load_lds_dwordx4 %3, %4 offset:1024\n\t"
                         "s_add_u32 m0, m0, 1280\n\ts_mov_b64 exec, -1\n\tglobal_load_lds_dwordx4 %3, %6\n\ts_mov_b64 exec, 0xff\n\tglobal_load_lds_dwordx4 %3, %6 offset:1024\n\ts_mov_b64 exec, -1"
                         : "=&v"(tagv) : "n"(0), "s"(p.ws), "v"(l16), "s"(p.wr), "s"(dst), "s"(p.gw) : "memory", "scc");
#pragma unroll
            for (int j = 0; j < 4; ++j) asm volatile("global_load_dwordx4 %0, %1, %2 offset:%3" : "=v"(xr[j]) : "v"(ro), "s"(p.x), "n"(j * 256));
            asm volatile("global_load_dwordx4 %0, %1, %2 offset:1024" : "=v"(xr[4]) : "v"(ro4), "s"(p.x));
        }
        __builtin_amdgcn_s_barrier();
        v2u uu[8];
        const u32 uo1 = uo0 + 1024 * D * 2;
#define LD_U(i) asm volatile("global_load_dwordx2 %0, %1, %2 offset:%3" : "=v"(uu[i]) : "v"((i) < 4 ? uo0 : uo1), "s"(p.x), "n"(((i) & 3) * D * 2))
#pragma unroll
        for (int i = 0; i < NB1; ++i) LD_U(i);
        *(LAS v4u*)(smem + LDS_TAP + ht * 16) = z4; *(LAS v4u*)(smem + LDS_TAP + 4096 + ht * 16) = z4;
        *(LAS v4u*)(smem + LDS_TAP + 8192 + ht * 16 + (ht >> 7) * (LDS_U - LDS_TAP - 8192 + ((ht >> 5) & 3) * 4608 + (ht & 31) * 16 - ht * 16)) = z4;
        u32 ctl = (u32)(uintptr_t)(smem + LDS_CTL), one = 1u, cty = ctl + 64 * hw + (lane & 3) * 16;
        const u32 late = (u32)((127 - cw) >> 31), pa_ = OFF_RG + hline(p.ws, cw & 127) + hw * 32, pk_ = pa_ ^ ((pa_ ^ (OFF_CI + ((cw - 128) * 4 + hw) * 16)) & late);
        u32 po = pk_ + (lane >> 1) + (sub != 0) * (OFF_G + hline(p.ws, ((lane - 1) & 3) * 64 + ((cw & 127) >> 1)) + ((4 * cw + hw) & 7) * 16 - pk_ - (lane >> 1));
        const u64 pmask = 1ull | ((u64)(~late & 1u) << 32) | (~late & ((1u << (1 + REPG)) - 1));
        u32 nrow = 0xffffu - (rrow + (lane >> 4));
        u32 go = OFF_G + hline(p.ws, (cw % REPG) * 64 + 8 * hw + (lane >> 3)) + (lane & 7) * 16, go2 = OFF_G + hline(p.ws, (cw % REPG) * 64 + 32 + 8 * hw + (lane >> 3)) + (lane & 7) * 16;
        LAS char* up = smem + LDS_U + (256 + 256 * hw + 4 * lane) * 2;
        LAS char* yp = smem + CH_Y + hw * 1152 + (lane + (lane >> 3)) * 16;
        LAS char* rw = smem + LDS_RW + hw * 3072 + sub * 16;
        LAS char* rw4 = rw - (sub & 8) * 16;
        asm volatile("" : "+v"(ctl), "+v"(one), "+v"(cty), "+v"(po), "+v"(go), "+v"(go2), "+v"(up), "+v"(yp), "+v"(rw), "+v"(rw4), "+v"(nrow));
        u32 wt[3];
        {
            const u32 l0 = xs.wpf * 192 * xs.nffn, nl = 576 * xs.nffn - l0, per = (nl + 6 * xs.nconv - 1) / (6 * xs.nconv);
#pragma unroll
            for (int k = 0; k < 3; ++k) {
                wt[k] = wd_line(xs, min(l0 + ((2 * k * xs.nconv + slot * 2 + (hw & 1)) * per + min((u32)lane, per - 1)), 576 * xs.nffn - 1));
                asm volatile("" : "+v"(wt[k]));
            }
        }
        BAR();
        __builtin_amdgcn_s_setprio(3);
        u32 tag;
        {
            asm volatile("s_waitcnt vmcnt(%1)" : "+v"(tagv) : "n"(NB1 + 5) : "memory");
            tag = __builtin_amdgcn_readfirstlane(tagv) + 1u;
            v4u wa[5], wg_[5];
#pragma unroll
            for (int j = 0; j < 5; ++j) { wa[j] = *(LAS v4u*)((j < 4 ? rw : rw4) + j * 256); wg_[j] = *(LAS v4u*)((j < 4 ? rw : rw4) + 1280 + j * 256); }
#pragma unroll
            for (int j = 0; j < 5; ++j) asm volatile("" : "+v"(wa[j]), "+v"(wg_[j]));
            asm volatile("s_waitcnt vmcnt(%5)" : "+v"(xr[0]), "+v"(xr[1]), "+v"(xr[2]), "+v"(xr[3]), "+v"(xr[4]) : "n"(NB1) : "memory");
            if (sub >= 8) xr[4] = z4;
            float r = 0.f, s = 0.f;
#pragma unroll
            for (int j = 0; j < 5; ++j) {
                const v4u a = wa[j], g = wg_[j];
#pragma unroll
                for (int e = 0; e < 4; ++e) {
                    const u32 xe = xr[j][e], ae = a[e], ge = g[e];
                    r = __builtin_amdgcn_fdot2_f32_bf16(__builtin_bit_cast(bf16x2_t, xe), __builtin_bit_cast(bf16x2_t, ae), r, false);
                    s = __builtin_amdgcn_fdot2_f32_bf16(__builtin_bit_cast(bf16x2_t, xe), __builtin_bit_cast(bf16x2_t, ge), s, false);
                }
            }
            r = dpp_add<0x128, 0xf>(r); s = dpp_add<0x128, 0xf>(s); r = dpp_add<0x124, 0xf>(r); s = dpp_add<0x124, 0xf>(s);
            r = dpp_add<0x122, 0xf>(r); s = dpp_add<0x122, 0xf>(s); r = dpp_add<0x121, 0xf>(r); s = dpp_add<0x121, 0xf>(s);
            const u32 pay = make_payload(r, sigmoidf_(rbf(s))), kk = (pay & 0xffff0000u) | nrow;
            const u32 p0 = (u32)__builtin_amdgcn_readlane((int)pay, 0), p1 = (u32)__builtin_amdgcn_readlane((int)pay, 16), p2 = (u32)__builtin_amdgcn_readlane((int)pay, 32), p3 = (u32)__builtin_amdgcn_readlane((int)pay, 48);
            const auto sw = __builtin_amdgcn_permlane16_swap(kk, kk, false, false);
            const u32 g01 = (p0 & 0xffffu) | (p1 << 16), g23 = (p2 & 0xffffu) | (p3 << 16);
            const v4u pv = {sub ? g01 : sw[0], tag, sub ? g23 : sw[1], tag};
            asm volatile("s_mov_b64 exec, %3\n\tglobal_store_dwordx4 %0, %1, %2 sc1\n\ts_mov_b64 exec, -1\n\ts_nop 0" :: "v"(po), "v"(pv), "s"(p.ws), "s"(pmask) : "memory");
        }
        __builtin_amdgcn_s_setprio(0);
        *(LAS u32*)(smem + LDS_CTL + C_TAG) = tag;
        LDS_ADD1(ctl, one, C_PUB);
        LWAIT(ctl, C_PUB, 4);
#pragma unroll
        for (int i = NB1; i < 4; ++i) LD_U(i);
        LDS_ADD1(ctl, one, C_PUB);
        LWAIT(ctl, C_PUB, 8);
        v4u gq[2];
#define POLL_G "global_load_dwordx4 v[100:103], %[go], %[ws] sc1\n\tglobal_load_dwordx4 v[104:107], %[go2], %[ws] sc1\n\t"
#define LD_U4 "global_load_dwordx2 %[u4], %[uo], %[x]\n\tglobal_load_dwordx2 %[u5], %[uo], %[x] offset:1152\n\tglobal_load_dwordx2 %[u6], %[uo], %[x] offset:2304\n\tglobal_load_dwordx2 %[u7], %[uo], %[x] offset:3456\n\t"
        asm volatile("" : "=&{v[100:103]}"(gq[0]), "=&{v[104:107]}"(gq[1]), "=&v"(uu[4]), "=&v"(uu[5]), "=&v"(uu[6]), "=&v"(uu[7]));
        asm volatile("s_cmp_ge_u32 %[hw], 2\n\ts_cbranch_scc1 1f\n\t" LD_U4 POLL_G "s_waitcnt vmcnt(6)\n\ts_branch 2f\n1:\n\ts_waitcnt vmcnt(0)\n2:"
                     : "+{v[100:103]}"(gq[0]), "+{v[104:107]}"(gq[1]), [u4] "+v"(uu[4]), [u5] "+v"(uu[5]), [u6] "+v"(uu[6]), [u7] "+v"(uu[7]), "+v"(uu[0]), "+v"(uu[1]), "+v"(uu[2]), "+v"(uu[3])
                     : [go] "v"(go), [go2] "v"(go2), [ws] "s"(p.ws), [uo] "v"(uo1), [x] "s"(p.x), [hw] "s"(hw) : "memory", "scc");
        u32 ct2 = ctl + max(hw - 1, 0) * 4;
        auto stage = [&](int g) __attribute__((always_inline)) {
            const v2u* u4 = uu + 4 * g;
            LAS char* d = up + g * 2048;
            *(LAS v2u*)(d) = (v2u){__builtin_amdgcn_perm(u4[1].x, u4[0].x, 0x05040100u), __builtin_amdgcn_perm(u4[3].x, u4[2].x, 0x05040100u)};
            *(LAS v2u*)(d + 4608) = (v2u){__builtin_amdgcn_perm(u4[1].x, u4[0].x, 0x07060302u), __builtin_amdgcn_perm(u4[3].x, u4[2].x, 0x07060302u)};
            *(LAS v2u*)(d + 2 * 4608) = (v2u){__builtin_amdgcn_perm(u4[1].y, u4[0].y, 0x05040100u), __builtin_amdgcn_perm(u4[3].y, u4[2].y, 0x05040100u)};
            *(LAS v2u*)(d + 3 * 4608) = (v2u){__builtin_amdgcn_perm(u4[1].y, u4[0].y, 0x07060302u), __builtin_amdgcn_perm(u4[3].y, u4[2].y, 0x07060302u)};
            if (g) LDS_ADD1(ct2, one, C_ST2); else LDS_ADD1(ctl, one, C_STG);
        };
        stage(0);
        asm volatile("s_cmp_lt_u32 %[hw], 2\n\ts_cbranch_scc1 1f\n\t" POLL_G LD_U4 "\n1:"
                     : "+{v[100:103]}"(gq[0]), "+{v[104:107]}"(gq[1]), [u4] "+v"(uu[4]), [u5] "+v"(uu[5]), [u6] "+v"(uu[6]), [u7] "+v"(uu[7])
                     : [go] "v"(go), [go2] "v"(go2), [ws] "s"(p.ws), [uo] "v"(uo1), [x] "s"(p.x), [hw] "s"(hw) : "memory", "scc");
        if (hw < 2) {
            asm volatile("s_waitcnt vmcnt(2)" : "+v"(uu[4]), "+v"(uu[5]), "+v"(uu[6]), "+v"(uu[7]));
            stage(1);
        }
        {
            u32 c_;
            asm volatile("s_mov_b32 %[c], 0x80000\n\ts_waitcnt vmcnt(4)\n\ts_cmp_ge_u32 %[hw], 2\n\ts_cbranch_scc1 1f\n\ts_waitcnt vmcnt(0)\n1:\n\tv_cmpx_eq_u32 %[tag], v101\n\tv_cmpx_eq_u32 %[tag], v103\n\tv_cmpx_eq_u32 %[tag], v105\n\tv_cmpx_eq_u32 %[tag], v107\n\t"
                         "s_cmp_eq_u64 exec, -1\n\ts_mov_b64 exec, -1\n\ts_cbranch_scc1 2f\n\ts_sleep 4\n\t" POLL_G "s_waitcnt vmcnt(0)\n\ts_sub_u32 %[c], %[c], 1\n\ts_cbranch_scc0 1b\n2:\n\ts_nop 4"
                         : "+{v[100:103]}"(gq[0]), "+{v[104:107]}"(gq[1]), [c] "=&s"(c_) : [go] "v"(go), [go2] "v"(go2), [ws] "s"(p.ws), [tag] "s"(tag), [hw] "s"(hw) : "memory", "scc", "vcc");
        }
        *(LAS v2u*)(smem + LDS_G + hw * 3072 + lane * 8) = (v2u){gq[0].x, gq[0].z}; *(LAS v2u*)(smem + LDS_G + hw * 3072 + 512 + lane * 8) = (v2u){gq[1].x, gq[1].z};
        LDS_ADD1(ctl, one, C_GOK);
        if (hw < 2) {
#pragma unroll
            for (int k = 0; k < 3; ++k) asm volatile("s_mov_b32 m0, %2\n\ts_nop 0\n\tglobal_load_lds_dword %0, %1" :: "v"(wt[k]), "s"(p.wd), "n"(LDS_DUMMY) : "memory");
        }
#pragma unroll
        for (int g = 0; g < 2; ++g) {
            if (g == 1 && hw >= 2) {
                asm volatile("s_waitcnt vmcnt(4)" : "+v"(uu[4]), "+v"(uu[5]), "+v"(uu[6]), "+v"(uu[7]));
                stage(1);
            }
            const v2u* u4 = uu + 4 * g;
            const float gr[4] = {bflo(gq[g].x), bfhi(gq[g].x), bflo(gq[g].z), bfhi(gq[g].z)};
            float o[4][4];
#pragma unroll
            for (int r = 0; r < 4; ++r) {
                const float xv[4] = {bflo(u4[r].x), bfhi(u4[r].x), bflo(u4[r].y), bfhi(u4[r].y)};
#pragma unroll
                for (int c = 0; c < 4; ++c) asm("v_fma_f32 %0, -%1, %2, %2" : "=v"(o[r][c]) : "v"(gr[r]), "v"(xv[c]));
                asm volatile("" : "+v"(o[r][0]), "+v"(o[r][1]), "+v"(o[r][2]), "+v"(o[r][3]));
            }
            {
                u32 t_, c_;
                asm volatile("s_mov_b32 %1, 0x400000\n1:\n\tds_read_b32 %0, %2 offset:%3\n\ts_waitcnt lgkmcnt(0)\n\tv_cmp_eq_u32 vcc, 0, %0\n\ts_cmp_eq_u64 vcc, 0\n\ts_cbranch_scc1 2f\n\ts_sleep 1\n\ts_sub_u32 %1, %1, 1\n\ts_cbranch_scc0 1b\n2:"
                             : "=&v"(t_), "=&s"(c_) : "v"(cty), "n"(C_Y + 256 * g) : "memory", "scc", "vcc");
            }
            v4f y[4];
#pragma unroll
            for (int c = 0; c < 4; ++c) y[c] = *(LAS v4f*)(yp + c * KCH + g * 4608);
            SBAR();
#define ST_O(i, v) asm volatile("global_store_dwordx2 %0, %1, %2 offset:%3" :: "v"(g ? uo1 : uo0), "v"(v), "s"(p.out), "n"((i) * D * 2) : "memory")
#pragma unroll
            for (int r = 0; r < 4; ++r) {
#pragma unroll
                for (int c = 0; c < 4; ++c) { const float yv = y[c][r]; asm("v_fmac_f32 %0, %1, %2" : "+v"(o[r][c]) : "v"(gr[r]), "v"(yv)); }
                ST_O(r, ((v2u){cvtpk(o[r][0], o[r][1]), cvtpk(o[r][2], o[r][3])}));
            }
        }
        asm volatile("s_waitcnt vmcnt(0)" ::: "memory");
        LDS_ADD1(ctl, one, C_ACK);
        ROLE_END();
    }
    else
    {
        ROLE_BEGIN(0);
        const int ch = wave;
        u32 pi = ((chbase + ch) * NST + lane * 2) * 2, dl = (chbase + ch) * 2;
        asm volatile("" : "+v"(pi), "+v"(dl));
        ROLE_ARGS();
#define GLD(T, ptr, idx) __builtin_nontemporal_load((const GAS T*)((const GAS char*)(ptr) + (idx)))
        const u32 are = GLD(u32, p.Are, pi), aim = GLD(u32, p.Aim, pi), bre = GLD(u32, p.Bre, pi), bim = GLD(u32, p.Bim, pi), cre = GLD(u32, p.Cre, pi), cim = GLD(u32, p.Cim, pi);
        const u16 ldt = *(const GAS u16*)((const GAS char*)p.logdt + dl), dsk = *(const GAS u16*)((const GAS char*)p.dskip + dl);
        SBAR();
        __builtin_amdgcn_s_barrier();
        SBAR();
        LAS char* kb = smem + ch * KCH;
        const int r32 = lane & 31, h = lane >> 5, q = lane >> 4, n = lane & 15;
        const int ai = 4 * (r32 >> 3) + (r32 & 3), hh = (r32 >> 2) & 1;
        const u64 tsel = ai < 8 ? (hh ? KROW_1LO : KROW_0LO) : (hh ? KROW_1HI : KROW_0HI);
        const int o = (int)((tsel >> (8 * (ai & 7))) & 0xff);
        LAS char* ap = kb + o * STG_ROW + h * 16;
        LAS char* bp = kb + r32 * STG_ROW + h * 16;
        u32 sg[14];
#pragma unroll
        for (int k = 0; k < 14; ++k) { sg[k] = (u32)(uintptr_t)(kb + lane * 8 + k * 4 * STG_ROW); asm volatile("" : "+v"(sg[k])); }
        bf16x8 b5 = band(((0x1fu << 6) << n) >> (8 * q), 0x3f803f80u), b3 = band(((0x7u << 11) << n) >> (8 * q), 0x3f803f80u);
        bf16x8 b9 = band(((0x1ffu << 4) << n) >> (8 * q), 0x3f803f80u), b7 = band(((0x7fu << 5) << n) >> (8 * q), 0x3f803f80u);
        bf16x8 pat = band(((0x3u << 12) << n) >> (8 * q), 0xffffffffu);
        LAS char* k0 = kb + 32 + lane * 16;
        LAS char* kt = kb + (lane & 3) * KCOPY + (lane < 16 ? 32 + 1024 + (lane >> 2) * 16 : ((lane >> 2) & 1) * 16);
        LAS char* a0 = kb + ((lane >> 2) & 3) * KCOPY + (28 - 8 * q + 4 * (lane & 3)) * 2;
        LAS char* bb = smem + LDS_U + ch * 4608 + (16 * n + 8 * q - 16) * 2;
        LAS char* yb = kb + CH_Y + (4 * n + q + (n >> 1)) * 16;
        LAS char* tp = smem + LDS_TAP + ch * 2304 + ((n & 1) ? 13 + n - 4 * q : 96 + 14 + n - 4 * q) * 4;
        LAS char* ta = smem + LDS_TAP + ch * 2304 + (h ? 0 : 768) + 64 + 2 * r32;
        LAS char* tb = smem + LDS_TAP + ch * 2304 + 1600 + 64 * h + 2 * r32;
        u32 ctl = (u32)(uintptr_t)(smem + LDS_CTL), ctc = ctl + C_Y + ch * 16 + (lane & 15), one = 1u, so = OFF_SEL + (8 + xcd) * 256 + (lane & 7) * 8;
        u32 m01 = lane < 2 ? ~0u : 0u, m2 = lane == 2 ? ~0u : 0u, m3 = lane == 3 ? ~0u : 0u, m0_ = lane == 0 ? ~0u : 0u;
        asm volatile("" : "+v"(b5), "+v"(b3), "+v"(b9), "+v"(b7), "+v"(pat), "+v"(ap), "+v"(bp), "+v"(m01), "+v"(m2), "+v"(m3), "+v"(m0_));
        asm volatile("" : "+v"(k0), "+v"(kt), "+v"(a0), "+v"(bb), "+v"(yb), "+v"(tp), "+v"(ta), "+v"(tb), "+v"(ctl), "+v"(ctc), "+v"(one), "+v"(so));
        const char* hint = p.ws + (OFF_PART + cw * PP + hline(p.ws, cw >> 1));
        asm volatile("" : "+s"(hint));
        __builtin_amdgcn_s_barrier();
        SBAR();
        const float dt = rbf(expf(bf2f(ldt)));
        v16f acc;
        u32 stg;
        float kscale;
        {
            const v2f a2 = (v2f){bflo(are), bfhi(are)} * dt * 1.44269504088896341f;
            const v2f brev = (v2f){bflo(aim), bfhi(aim)} * dt * 0.15915494309189535f;
            const cf2 Bc = {{dt * bflo(bre), dt * bflo(bim)}, {dt * bfhi(bre), dt * bfhi(bim)}};
            const cf2 Cc = {{bflo(cre), bflo(cim)}, {bfhi(cre), bfhi(cim)}};
            cf2 W = cmul2(Cc, Bc);
            u32 mx = max(max(__float_as_uint(W.a.x) & 0x7f800000u, __float_as_uint(W.a.y) & 0x7f800000u), max(__float_as_uint(W.b.x) & 0x7f800000u, __float_as_uint(W.b.y) & 0x7f800000u));
            mx = dpp_umax<0x111, 0xf>(mx); mx = dpp_umax<0x112, 0xf>(mx); mx = dpp_umax<0x114, 0xf>(mx); mx = dpp_umax<0x118, 0xf>(mx);
            mx = dpp_umax<0x142, 0xa>(mx); mx = dpp_umax<0x143, 0xc>(mx);
            mx = min(max((u32)__builtin_amdgcn_readlane((int)mx, 63), 0x00800000u), 0x7e800000u);
            kscale = __uint_as_float(mx);
            const float inv = __uint_as_float(0x7f000000u - mx);
            W.a *= inv; W.b *= inv;
            const cf2 E1 = cexp2(a2, brev, 1.f), E8 = cexp2(a2, brev, 8.f), E256 = cexp2(a2, brev, 256.f);
            asm volatile("s_nop 0\n\t" KGEN_ASM :: "{v[48:49]}"(W.a), "{v[50:51]}"(W.b), "{v[52:53]}"(E1.a), "{v[54:55]}"(E1.b), "{v[56:57]}"(E8.a), "{v[58:59]}"(E8.b), "{v[60:61]}"(E256.a), "{v[62:63]}"(E256.b),
                         [b0] "v"(sg[0]), [b1] "v"(sg[1]), [b2] "v"(sg[2]), [b3] "v"(sg[3]), [b4] "v"(sg[4]), [b5] "v"(sg[5]), [b6] "v"(sg[6]), [b7] "v"(sg[7]), [b8] "v"(sg[8]), [b9] "v"(sg[9]),
                         [b10] "v"(sg[10]), [b11] "v"(sg[11]), [b12] "v"(sg[12]), [b13] "v"(sg[13]) : "memory", KGEN_CLOBBER);
            asm volatile(KMFMA_ASM : "=&{v[48:63]}"(acc), [stg] "=&v"(stg) : [ap] "v"(ap), [bp] "v"(bp), [ctl] "v"(ctl) : "memory", KMFMA_CLOBBER);
            acc *= kscale;
        }
        acc[0] += __uint_as_float(((u32)dsk << 16) & m0_);
        u32 t78 = cvtpk(acc[7], acc[8]), t9a = cvtpk(acc[9], acc[10]);
        {
            const auto s0 = __builtin_amdgcn_permlane32_swap(cvtpk(acc[0], acc[1]), cvtpk(acc[4], acc[4]), false, false);
            const auto s1 = __builtin_amdgcn_permlane32_swap(cvtpk(acc[2], acc[3]), cvtpk(acc[5], acc[6]), false, false);
            const v4u ww = {s0[0], s1[0], s0[1], s1[1]};
            const u32 pz = (u32)__builtin_amdgcn_update_dpp(0, (int)ww.z, 0x138, 0xf, 0xf, true), pw = (u32)__builtin_amdgcn_update_dpp(0, (int)ww.w, 0x138, 0xf, 0xf, true);
            const v4u c1 = {__builtin_amdgcn_alignbit(ww.x, pw, 16), __builtin_amdgcn_alignbit(ww.y, ww.x, 16), __builtin_amdgcn_alignbit(ww.z, ww.y, 16), __builtin_amdgcn_alignbit(ww.w, ww.z, 16)};
            *(LAS v4u*)(k0) = ww;
            *(LAS v4u*)(k0 + KCOPY) = c1;
            *(LAS v4u*)(k0 + 2 * KCOPY) = (v4u){pw, ww.x, ww.y, ww.z};
            *(LAS v4u*)(k0 + 3 * KCOPY) = (v4u){__builtin_amdgcn_alignbit(pw, pz, 16), c1.x, c1.y, c1.z};
            const u32 w0 = (u32)__builtin_amdgcn_readlane((int)ww.z, 63), w1 = (u32)__builtin_amdgcn_readlane((int)ww.w, 63);
            const u32 w3 = w1 >> 16, w2 = w3 * 0x10001u, w10 = (w0 >> 16) | (w1 << 16);
            *(LAS v4u*)kt = (v4u){(w2 & m01) | (w1 & m2) | (w10 & m3), (w3 & m0_) | (w2 & ((m01 ^ m0_) | m2 | m3)), (w3 & m2) | (w2 & m3), 0u};
        }
        asm volatile("" ::: "memory");
        pat = __builtin_bit_cast(bf16x8, __builtin_bit_cast(v4u, pat) & (((u32)__builtin_amdgcn_readlane((int)t78, 63) >> 16) * 0x10001u));
#define POLL_SEL "global_load_dwordx2 v[240:241], %[so], %[ws] sc1\n\t"
        v2u sq;
        const v4f zero4 = {0.f, 0.f, 0.f, 0.f};
        {
            u32 s_, c_;
            asm volatile(CONV_ASM : "=&{v[240:241]}"(sq), [s] "=&s"(s_), [c] "=&s"(c_)
                         : "{v[4:7]}"(b5), "{v[8:11]}"(b3), "{v[12:15]}"(b9), "{v[16:19]}"(b7), "{v[20:23]}"(pat), [a0] "v"(a0), [bb] "v"(bb), [yb] "v"(yb), [tp] "v"(tp), [ta] "v"(ta), [tb] "v"(tb),
                           [ctl] "v"(ctl), [ctc] "v"(ctc), [one] "v"(one), [so] "v"(so), [t78] "v"(t78), [t9a] "v"(t9a), [stg] "v"(stg), [ws] "s"(p.ws) : "memory", "scc", CONV_CLOBBER);
        }
        asm volatile("s_cmp_eq_u32 %0, 0\n\ts_cbranch_scc1 1f\n\ts_endpgm\n1:" :: "s"(wave) : "memory", "scc");
        {
            u32 h_;
            asm volatile("s_load_dword %0, %1, 0x4 glc" : "=&s"(h_) : "s"(hint) : "memory");
            const u32 tag = __builtin_amdgcn_readfirstlane(*(LAS u32*)(smem + LDS_CTL + C_TAG));
            u32 c_;
            u32 pb[9];
#pragma unroll
            for (int k = 0; k < 9; ++k) { pb[k] = OFF_PART + cw * PP + hline(p.ws, 8 * k + (lane >> 3)) + (lane & 7) * 16; asm volatile("" : "+v"(pb[k])); }
            {
                asm volatile("s_mov_b32 %[c], 64\n1:\n\ts_waitcnt lgkmcnt(0)\n\ts_cmp_eq_u32 %[h], %[tag]\n\ts_cbranch_scc1 2f\n\ts_load_dword %[h], %[a], 0x4 glc\n\ts_sub_u32 %[c], %[c], 1\n\ts_cbranch_scc0 1b\n\ts_waitcnt lgkmcnt(0)\n2:"
                             : [h] "+s"(h_), [c] "=&s"(c_) : [a] "s"(hint), [tag] "s"(tag) : "memory", "scc");
            }
            typedef __attribute__((ext_vector_type(32))) u32 v32u;
            v32u ra, rb; v4u rc, rd;
            asm volatile("global_load_dwordx4 v[64:67], %[pb0], %[ws] sc1\n\tglobal_load_dwordx4 v[68:71], %[pb1], %[ws] sc1\n\tglobal_load_dwordx4 v[72:75], %[pb2], %[ws] sc1\n\tglobal_load_dwordx4 v[76:79], %[pb3], %[ws] sc1\n\tglobal_load_dwordx4 v[80:83], %[pb4], %[ws] sc1\n\tglobal_load_dwordx4 v[84:87], %[pb5], %[ws] sc1\n\tglobal_load_dwordx4 v[88:91], %[pb6], %[ws] sc1\n\tglobal_load_dwordx4 v[92:95], %[pb7], %[ws] sc1\n\tglobal_load_dwordx4 v[96:99], %[pb8], %[ws] sc1\n\t" : "=&{v[64:95]}"(ra), "=&{v[96:99]}"(rc) : [pb0] "v"(pb[0]), [pb1] "v"(pb[1]), [pb2] "v"(pb[2]), [pb3] "v"(pb[3]), [pb4] "v"(pb[4]), [pb5] "v"(pb[5]), [pb6] "v"(pb[6]), [pb7] "v"(pb[7]), [pb8] "v"(pb[8]), [ws] "s"(p.ws) : "memory");
            asm volatile("s_mov_b32 %[c], 0x80000\n\ts_waitcnt vmcnt(9)\n1:\n\tv_cmpx_eq_u32 %[tag], v241\n\t"
                         "s_cmp_eq_u64 exec, -1\n\ts_mov_b64 exec, -1\n\ts_cbranch_scc1 2f\n\t" POLL_SEL "s_waitcnt vmcnt(0)\n\ts_sub_u32 %[c], %[c], 1\n\ts_cbranch_scc0 1b\n2:\n\ts_nop 4"
                         : "+{v[240:241]}"(sq), [c] "=&s"(c_) : [so] "v"(so), [ws] "s"(p.ws), [tag] "s"(tag) : "memory", "scc", "vcc");
            const int t = (int)(0xffffu - (sq.x & 0xffffu)) & 2047;
            const float rs = rbf(sigmoidf_(key_to_r(sq.x)));
            LWAIT(ctl, C_GOK, 4);
            {
                u32 t_;
                asm volatile("s_mov_b32 %1, 0x400000\n1:\n\tds_read_b32 %0, %2 offset:%3\n\ts_waitcnt lgkmcnt(0)\n\tv_cmp_eq_u32 vcc, 0, %0\n\ts_cmp_eq_u64 vcc, 0\n\ts_cbranch_scc1 2f\n\ts_sub_u32 %1, %1, 1\n\ts_cbranch_scc0 1b\n2:"
                             : "=&v"(t_), "=&s"(c_) : "v"(ctl + (lane & 3) * 16), "n"(C_Y + 64 * 7) : "memory", "scc", "vcc");
            }
            const float g = bf2f(*(LAS u16*)(smem + LDS_G + ((t >> 8) & 3) * 3072 + (t >> 10) * 512 + (t & 255) * 2));
            const int uq = (t & 255) >> 2;
            float xv[4], av[4];
#pragma unroll
            for (int c = 0; c < 4; ++c) {
                xv[c] = bf2f(*(LAS u16*)(smem + LDS_U + c * 4608 + (256 + t) * 2));
                av[c] = g * *(LAS float*)(smem + c * KCH + CH_Y + (t >> 8) * 1152 + (uq + (uq >> 3)) * 16 + (t & 3) * 4);
            }
            const u32 e1 = n < 4 ? ((n & 1) ? 0x3f800000u : 0x00003f80u) : 0u;
            bf16x8 sel4 = __builtin_bit_cast(bf16x8, (n & 2) ? (v4u){0u, 0u, e1, e1} : (v4u){e1, e1, 0u, 0u});
            u32 oo = (u32)(t * D + chbase) * 2;
            float omg = 1.f - g;
            asm volatile("" : "+v"(sel4), "+v"(oo), "+v"(omg), "+v"(xv[0]), "+v"(xv[1]), "+v"(xv[2]), "+v"(xv[3]), "+v"(av[0]), "+v"(av[1]), "+v"(av[2]), "+v"(av[3]));
            v4f s0, s1;
            asm volatile("s_mov_b32 %[c], 0x40000\n\ts_waitcnt vmcnt(0)\n\tv_cmpx_eq_u32 %[tag], v65\n\tv_cmpx_eq_u32 %[tag], v67\n\tv_cmpx_eq_u32 %[tag], v69\n\tv_cmpx_eq_u32 %[tag], v71\n\tv_cmpx_eq_u32 %[tag], v73\n\tv_cmpx_eq_u32 %[tag], v75\n\tv_cmpx_eq_u32 %[tag], v77\n\tv_cmpx_eq_u32 %[tag], v79\n\tv_cmpx_eq_u32 %[tag], v81\n\tv_cmpx_eq_u32 %[tag], v83\n\tv_cmpx_eq_u32 %[tag], v85\n\tv_cmpx_eq_u32 %[tag], v87\n\tv_cmpx_eq_u32 %[tag], v89\n\tv_cmpx_eq_u32 %[tag], v91\n\tv_cmpx_eq_u32 %[tag], v93\n\tv_cmpx_eq_u32 %[tag], v95\n\tv_cmpx_eq_u32 %[tag], v97\n\tv_cmpx_eq_u32 %[tag], v99\n\ts_cmp_eq_u64 exec, -1\n\ts_mov_b64 exec, -1\n\ts_cbranch_scc1 3f\n\tglobal_load_dwordx4 v[64:67], %[pb0], %[ws] sc1\n\tglobal_load_dwordx4 v[68:71], %[pb1], %[ws] sc1\n\tglobal_load_dwordx4 v[72:75], %[pb2], %[ws] sc1\n\tglobal_load_dwordx4 v[76:79], %[pb3], %[ws] sc1\n\tglobal_load_dwordx4 v[80:83], %[pb4], %[ws] sc1\n\tglobal_load_dwordx4 v[84:87], %[pb5], %[ws] sc1\n\tglobal_load_dwordx4 v[88:91], %[pb6], %[ws] sc1\n\tglobal_load_dwordx4 v[92:95], %[pb7], %[ws] sc1\n\tglobal_load_dwordx4 v[96:99], %[pb8], %[ws] sc1\n\ts_sleep 6\n\tglobal_load_dwordx4 v[100:103], %[pb0], %[ws] sc1\n\tglobal_load_dwordx4 v[104:107], %[pb1], %[ws] sc1\n\tglobal_load_dwordx4 v[108:111], %[pb2], %[ws] sc1\n\tglobal_load_dwordx4 v[112:115], %[pb3], %[ws] sc1\n\tglobal_load_dwordx4 v[116:119], %[pb4], %[ws] sc1\n\tglobal_load_dwordx4 v[120:123], %[pb5], %[ws] sc1\n\tglobal_load_dwordx4 v[124:127], %[pb6], %[ws] sc1\n\tglobal_load_dwordx4 v[128:131], %[pb7], %[ws] sc1\n\tglobal_load_dwordx4 v[132:135], %[pb8], %[ws] sc1\n\t"
                         "1:\n\ts_waitcnt vmcnt(9)\n\tv_cmpx_eq_u32 %[tag], v65\n\tv_cmpx_eq_u32 %[tag], v67\n\tv_cmpx_eq_u32 %[tag], v69\n\tv_cmpx_eq_u32 %[tag], v71\n\tv_cmpx_eq_u32 %[tag], v73\n\tv_cmpx_eq_u32 %[tag], v75\n\tv_cmpx_eq_u32 %[tag], v77\n\tv_cmpx_eq_u32 %[tag], v79\n\tv_cmpx_eq_u32 %[tag], v81\n\tv_cmpx_eq_u32 %[tag], v83\n\tv_cmpx_eq_u32 %[tag], v85\n\tv_cmpx_eq_u32 %[tag], v87\n\tv_cmpx_eq_u32 %[tag], v89\n\tv_cmpx_eq_u32 %[tag], v91\n\tv_cmpx_eq_u32 %[tag], v93\n\tv_cmpx_eq_u32 %[tag], v95\n\tv_cmpx_eq_u32 %[tag], v97\n\tv_cmpx_eq_u32 %[tag], v99\n\ts_cmp_eq_u64 exec, -1\n\ts_mov_b64 exec, -1\n\ts_cbranch_scc1 3f\n\tglobal_load_dwordx4 v[64:67], %[pb0], %[ws] sc1\n\tglobal_load_dwordx4 v[68:71], %[pb1], %[ws] sc1\n\tglobal_load_dwordx4 v[72:75], %[pb2], %[ws] sc1\n\tglobal_load_dwordx4 v[76:79], %[pb3], %[ws] sc1\n\tglobal_load_dwordx4 v[80:83], %[pb4], %[ws] sc1\n\tglobal_load_dwordx4 v[84:87], %[pb5], %[ws] sc1\n\tglobal_load_dwordx4 v[88:91], %[pb6], %[ws] sc1\n\tglobal_load_dwordx4 v[92:95], %[pb7], %[ws] sc1\n\tglobal_load_dwordx4 v[96:99], %[pb8], %[ws] sc1\n\t"
                         "s_waitcnt vmcnt(9)\n\tv_cmpx_eq_u32 %[tag], v101\n\tv_cmpx_eq_u32 %[tag], v103\n\tv_cmpx_eq_u32 %[tag], v105\n\tv_cmpx_eq_u32 %[tag], v107\n\tv_cmpx_eq_u32 %[tag], v109\n\tv_cmpx_eq_u32 %[tag], v111\n\tv_cmpx_eq_u32 %[tag], v113\n\tv_cmpx_eq_u32 %[tag], v115\n\tv_cmpx_eq_u32 %[tag], v117\n\tv_cmpx_eq_u32 %[tag], v119\n\tv_cmpx_eq_u32 %[tag], v121\n\tv_cmpx_eq_u32 %[tag], v123\n\tv_cmpx_eq_u32 %[tag], v125\n\tv_cmpx_eq_u32 %[tag], v127\n\tv_cmpx_eq_u32 %[tag], v129\n\tv_cmpx_eq_u32 %[tag], v131\n\tv_cmpx_eq_u32 %[tag], v133\n\tv_cmpx_eq_u32 %[tag], v135\n\ts_cmp_eq_u64 exec, -1\n\ts_mov_b64 exec, -1\n\ts_cbranch_scc1 2f\n\tglobal_load_dwordx4 v[100:103], %[pb0], %[ws] sc1\n\tglobal_load_dwordx4 v[104:107], %[pb1], %[ws] sc1\n\tglobal_load_dwordx4 v[108:111], %[pb2], %[ws] sc1\n\tglobal_load_dwordx4 v[112:115], %[pb3], %[ws] sc1\n\tglobal_load_dwordx4 v[116:119], %[pb4], %[ws] sc1\n\tglobal_load_dwordx4 v[120:123], %[pb5], %[ws] sc1\n\tglobal_load_dwordx4 v[124:127], %[pb6], %[ws] sc1\n\tglobal_load_dwordx4 v[128:131], %[pb7], %[ws] sc1\n\tglobal_load_dwordx4 v[132:135], %[pb8], %[ws] sc1\n\t"
                         "s_sub_u32 %[c], %[c], 1\n\ts_cbranch_scc0 1b\n\ts_waitcnt vmcnt(0)\n"
                         "2:\n\tv_mov_b32 v101, v104\n\tv_mov_b32 v103, v106\n\tv_mov_b32 v109, v112\n\tv_mov_b32 v111, v114\n\tv_mov_b32 v117, v120\n\tv_mov_b32 v119, v122\n\tv_mov_b32 v125, v128\n\tv_mov_b32 v127, v130\n\tv_mov_b32 v133, 0\n\tv_mov_b32 v135, 0\n\tv_mfma_f32_16x16x32_bf16 v[136:139], %[sel], v[100:103], 0\n\tv_mfma_f32_16x16x32_bf16 v[140:143], %[sel], v[108:111], 0\n\tv_mfma_f32_16x16x32_bf16 v[136:139], %[sel], v[116:119], v[136:139]\n\tv_mfma_f32_16x16x32_bf16 v[140:143], %[sel], v[124:127], v[140:143]\n\tv_mfma_f32_16x16x32_bf16 v[136:139], %[sel], v[132:135], v[136:139]\n\ts_branch 4f\n"
                         "3:\n\tv_mov_b32 v65, v68\n\tv_mov_b32 v67, v70\n\tv_mov_b32 v73, v76\n\tv_mov_b32 v75, v78\n\tv_mov_b32 v81, v84\n\tv_mov_b32 v83, v86\n\tv_mov_b32 v89, v92\n\tv_mov_b32 v91, v94\n\tv_mov_b32 v97, 0\n\tv_mov_b32 v99, 0\n\tv_mfma_f32_16x16x32_bf16 v[136:139], %[sel], v[64:67], 0\n\tv_mfma_f32_16x16x32_bf16 v[140:143], %[sel], v[72:75], 0\n\tv_mfma_f32_16x16x32_bf16 v[136:139], %[sel], v[80:83], v[136:139]\n\tv_mfma_f32_16x16x32_bf16 v[140:143], %[sel], v[88:91], v[140:143]\n\tv_mfma_f32_16x16x32_bf16 v[136:139], %[sel], v[96:99], v[136:139]\n\t\n4:\n\ts_nop 7"
                         : "+{v[64:95]}"(ra), "+{v[96:99]}"(rc), "=&{v[100:131]}"(rb), "=&{v[132:135]}"(rd), "=&{v[136:139]}"(s0), "=&{v[140:143]}"(s1), [c] "=&s"(c_)
                         : [pb0] "v"(pb[0]), [pb1] "v"(pb[1]), [pb2] "v"(pb[2]), [pb3] "v"(pb[3]), [pb4] "v"(pb[4]), [pb5] "v"(pb[5]), [pb6] "v"(pb[6]), [pb7] "v"(pb[7]), [pb8] "v"(pb[8]), [ws] "s"(p.ws), [tag] "s"(tag), [sel] "v"(sel4) : "memory", "scc", "vcc");
            float o[4];
#pragma unroll
            for (int c = 0; c < 4; ++c) o[c] = fmaf(omg, xv[c] + rbf(rbf(dpp_add<0x128, 0xf>(s0[c] + s1[c])) * rs), av[c]);
            v2u ov = {cvtpk(o[0], o[1]), cvtpk(o[2], o[3])};
            asm volatile("" : "+v"(ov));
            LWAIT(ctl, C_ACK, 4);
            asm volatile("s_mov_b64 exec, 0xff\n\tglobal_store_dwordx2 %0, %1, %2\n\ts_mov_b64 exec, -1" :: "v"(oo), "v"(ov), "s"(p.out) : "memory");
            asm volatile("s_waitcnt vmcnt(0)" :: "v"(ra), "v"(rb), "v"(rc), "v"(rd) : "memory");
        }
        ROLE_END();
    }
    }
}

extern "C" void kernel_launch(void* const* d_in, const int* in_sizes, int n_in,
                              void* d_out, int out_size, void* d_ws, size_t ws_size,
                              hipStream_t stream) {
    Params p;
    p.x = (const u16*)d_in[0]; p.Are = (const u16*)d_in[1]; p.Aim = (const u16*)d_in[2]; p.Bre = (const u16*)d_in[3];
    p.Bim = (const u16*)d_in[4]; p.Cre = (const u16*)d_in[5]; p.Cim = (const u16*)d_in[6]; p.logdt = (const u16*)d_in[7];
    p.dskip = (const u16*)d_in[8]; p.wr = (const u16*)d_in[10]; p.wg = (const u16*)d_in[11]; p.wu = (const u16*)d_in[12];
    p.wd = (const u16*)d_in[13]; p.gw = (const u16*)d_in[14]; p.out = (u16*)d_out; p.ws = (char*)d_ws;
    (void)hipFuncSetAttribute((const void*)fused_kernel, hipFuncAttributeMaxDynamicSharedMemorySize, LDS_ALLOC);
    fused_kernel<<<256, 512, LDS_ALLOC, stream>>>(p);
}
```

```cpp
#include <hip/hip_runtime.h>
#include <stdint.h>

typedef unsigned short u16;
typedef unsigned int u32;
typedef unsigned long long u64;
typedef __attribute__((ext_vector_type(4))) u32 v4u;
typedef __attribute__((ext_vector_type(2))) u32 v2u;
typedef __attribute__((ext_vector_type(2))) float v2f;
typedef __attribute__((ext_vector_type(4))) float v4f;
typedef __attribute__((ext_vector_type(16))) float v16f;
typedef __attribute__((ext_vector_type(8))) short bf16x8;
typedef __attribute__((ext_vector_type(4))) short s16x4;
typedef __attribute__((ext_vector_type(2))) __bf16 bf16x2_t;
#define LAS __attribute__((address_space(3)))
#define GAS __attribute__((address_space(1)))
#define RLX_AGENT __ATOMIC_RELAXED, __HIP_MEMORY_SCOPE_AGENT
#define SBAR() __builtin_amdgcn_sched_barrier(0)
#define BAR() asm volatile("s_waitcnt lgkmcnt(0)\n\ts_barrier" ::: "memory")
#define WSR() __builtin_amdgcn_make_buffer_rsrc(p.ws, 0, 0x7fffffff, 0x00020000)
#define REREAD() asm volatile("" ::: "memory")

constexpr int NBX = 2, CB = 16 + 16 / NBX, FA = 11, FB = (72 - (8 - NBX) * FA) / NBX, WPFB = 3;
static_assert(CB + FB == 27 && 16 + FA == 27, "slot 27 selects, 28..31 prefetch");
#define WNT " nt"
constexpr int CODE_KB = 25;
constexpr int NB1 = 2;
constexpr int REPG = 2;
constexpr int PFG0 = 528;
constexpr int WPF = 1;
constexpr int SELWAIT = 50;

constexpr int L = 2048, D = 576, NST = 128, F = 4608;
constexpr int NFFN = 72;
constexpr u32 OFF_RG = 4096;
constexpr u32 OFF_G = OFF_RG + 20480;
constexpr u32 OFF_SEL = OFF_G + REPG * 10240;
constexpr u32 OFF_CI = OFF_SEL + 16 * 256;
constexpr u32 OFF_PART = OFF_CI + 4096;
constexpr u32 PP = 11 * 1024;
constexpr u32 SPIN_CAP = 1u << 19;


struct Params {
    const u16 *x, *Are, *Aim, *Bre, *Bim, *Cre, *Cim, *logdt, *dskip, *wr, *wg, *wu, *wd, *gw;
    u16* out;
    char* ws;
};

__device__ __forceinline__ float bflo(u32 w) { return __uint_as_float(w << 16); }
__device__ __forceinline__ float bfhi(u32 w) { return __uint_as_float(w & 0xffff0000u); }
__device__ __forceinline__ float bf2f(u16 h) { return __uint_as_float((u32)h << 16); }
__device__ __forceinline__ u32 cvtpk(float lo, float hi) { v2f v = {lo, hi}; bf16x2_t r = __builtin_convertvector(v, bf16x2_t); return __builtin_bit_cast(u32, r); }
__device__ __forceinline__ float rbf(float f) { return bflo(cvtpk(f, f)); }
__device__ __forceinline__ float sigmoidf_(float s) { return __builtin_amdgcn_rcpf(1.f + __expf(-s)); }

template <int CTRL, int RM> __device__ __forceinline__ float dpp_add(float v) {
    return v + __builtin_bit_cast(float, __builtin_amdgcn_update_dpp(0, __builtin_bit_cast(int, v), CTRL, RM, 0xf, true));
}
template <int CTRL, int RM> __device__ __forceinline__ u32 dpp_umax(u32 v) {
    u32 o = (u32)__builtin_amdgcn_update_dpp(0, (int)v, CTRL, RM, 0xf, true);
    return v > o ? v : o;
}

typedef v2f cf;
__device__ __forceinline__ cf cmul(cf a, cf b) { cf t = a.yy * (cf){-b.y, b.x}; return __builtin_elementwise_fma(a.xx, b, t); }
__device__ __forceinline__ cf cexp_(float a2, float brev, float rho) {
    float m = __builtin_amdgcn_exp2f(a2 * rho);
    float ph = __builtin_amdgcn_fractf(brev * rho);
    cf c; c.x = m * __builtin_amdgcn_cosf(ph); c.y = m * __builtin_amdgcn_sinf(ph); return c;
}

struct cf2 { cf a, b; };
__device__ __forceinline__ cf2 cmul2(cf2 x, cf2 y) { return {cmul(x.a, y.a), cmul(x.b, y.b)}; }
__device__ __forceinline__ cf2 cexp2(v2f a2, v2f brev, float rho) { return {cexp_(a2.x, brev.x, rho), cexp_(a2.y, brev.y, rho)}; }

__device__ __forceinline__ u32 make_payload(float r, float g) {
    const u32 w = cvtpk(g, r);
    return w ^ (((u32)((int)w >> 31) & 0x7fff0000u) | 0x80000000u);
}
__device__ __forceinline__ float key_to_r(u32 k) {
    u32 hi = k & 0xffff0000u;
    return __uint_as_float((hi & 0x80000000u) ? (hi ^ 0x80000000u) : (~hi & 0xffff0000u));
}


constexpr int STG_ROW = 528;
constexpr int NA = 21;
constexpr int KCH = (NA + 32) * STG_ROW;
constexpr int KCOPY = 1344;
constexpr int CH_Y = 4 * KCOPY;
constexpr int LDS_U = 4 * KCH;
constexpr int LDS_TAP = LDS_U + 4 * 4608;
constexpr int LDS_CTL = LDS_TAP + 4 * 2304;
constexpr int C_TAG = 0, C_STG = 4, C_PUB = 8, C_ACK = 12, C_GOK = 16, C_ST2 = 20, C_Y = 64;
constexpr int LDS_RW = LDS_CTL + 1024;
constexpr int LDS_G = LDS_RW;
constexpr int LDS_DUMMY = LDS_RW + 4 * 3072;
constexpr int LDS_TOTAL = LDS_DUMMY + 1024;
static_assert(CH_Y + 8 * 1152 <= KCH && LDS_TOTAL <= 160 * 1024, "lds");
constexpr int WDP = 1184;
constexpr int XSP = 1168;
constexpr int FL_XS = 144 * 1024;
constexpr int FL_H = FL_XS + 8 * XSP;
constexpr int FL_Z = FL_H + 8 * 144;
constexpr int FL_TAG = FL_Z + 256;
constexpr int LDS_ALLOC = FL_TAG + 64 > LDS_TOTAL ? FL_TAG + 64 : LDS_TOTAL;
static_assert(LDS_ALLOC <= 160 * 1024, "lds");

__device__ __forceinline__ u32 top8_d(LAS char* t8, int lane, int n) {
    LAS u32* cand = (LAS u32*)(t8 + 256);
    u32 res = 0;
    _Pragma("unroll 1") for (int i0 = 0; i0 < n; i0 += 64) {
        const int i = i0 + lane;
        const u32 k = i < n ? cand[i] : 0u;
        int rk = 0;
        _Pragma("unroll 1") for (int j0 = 0; j0 < n; j0 += 16) {
            v4u c[4];
#pragma unroll
            for (int e = 0; e < 4; ++e) c[e] = *(LAS v4u*)(cand + j0 + 4 * e);
#pragma unroll
            for (int e = 0; e < 16; ++e) rk += (c[e >> 2][e & 3] > k) & (j0 + e < n);
        }
        const bool hit = i < n && rk < 8;
        const u32 got = (u32)__builtin_amdgcn_ds_permute((hit ? rk : 63) * 4, (int)(hit ? k : 0u));
        if (lane < 8) res |= got;
    }
    return res;
}


#define LWAIT_BODY "1:\n\tds_read_b32 %0, %3 offset:%4\n\ts_waitcnt lgkmcnt(0)\n\tv_readfirstlane_b32 %1, %0\n\ts_cmp_ge_u32 %1, %5\n\ts_cbranch_scc1 2f\n\ts_sleep 1\n\ts_sub_u32 %2, %2, 1\n\ts_cbranch_scc0 1b\n2:"
#define LWAIT(addr, off, want) do { u32 t_, s_, c_; asm volatile("s_mov_b32 %2, 0x400000\n" LWAIT_BODY : "=&v"(t_), "=&s"(s_), "=&s"(c_) : "v"(addr), "n"(off), "n"(want) : "memory", "scc"); } while (0)
#define LDS_ADD1(addr, one, off) asm volatile("s_mov_b64 exec, 1\n\tds_add_u32 %0, %1 offset:%2\n\ts_mov_b64 exec, -1" :: "v"(addr), "v"(one), "n"(off) : "memory")
__device__ __forceinline__ u32 hline(const char* ws, u32 i) { const u32 q = __umul24(i, 9363u) >> 16, r = i - 7u * q; return (8u * q + r + (r >= ((3u - (u32)((uintptr_t)ws >> 7)) & 7u))) * 128u; }
struct Xcd { u32 nconv, nffn, w0, inv, wpf; };
__device__ __forceinline__ Xcd xcd_shares(int xcd) {
    const u32 m = (u32)((xcd - NBX) >> 31);
    return {16u + ((CB - 16) & m), FA - ((FA - FB) & m), (u32)(FA * xcd - (FA - FB) * min(xcd, NBX)), 65535u / FA + 1 + ((65535u / FB - 65535u / FA) & m), WPF - ((NBX < 8 ? WPF - WPFB : 0) & m)};
}
__device__ __forceinline__ u32 wd_line(const Xcd& x, u32 i) {
    const u32 k = __umul24(i, 7282u) >> 16, r = __umul24(k, x.inv) >> 16;
    return __umul24((x.w0 + k - __umul24(x.nffn, r)) * 64u + (r >> 1) + 32u * (r & 1), 1152u) + (i - 9u * k) * 128u;
}
__device__ __forceinline__ bool touch(const Params& p, const Xcd& x, int e, int lane, u32 dst) {
    const u32 wa = x.w0 + ((3 - (u32)(((uintptr_t)p.wg >> 7) & 7) - x.w0) & 7), two = wa + 8 < x.w0 + x.nffn;
    const u32 g0 = two ? 576 : PFG0, na = wa < x.w0 + x.nffn ? (g0 * 2) << two : 0, nb = (576 - g0) * 2 * x.nffn, nc = x.wpf * 192 * x.nffn;
    if ((u32)e * 64 >= na + nb + nc) return false;
    const u32 i = min((u32)(e * 64 + lane), na + nb + nc - 1);
    const u32 j = i - na, k = (j * x.inv) >> 16;
    const bool first = i < na, last = i >= na + nb;
    const u32 seg = first ? i >> two : k + 2 * g0, col = first ? (wa + 8 * (i & two)) * 128u : (x.w0 + j - x.nffn * k) * 128u;
    const char* a = (const char*)(last ? p.wd : ((seg & 1) ? p.wu : p.wg)) + (last ? wd_line(x, j - nb) : (seg >> 1) * (F * 2) + col);
    asm volatile("s_mov_b32 m0, %1\n\ts_nop 0\n\tglobal_load_lds_dword %0, off" :: "v"(a), "s"(dst) : "memory");
    return true;
}

#define KGEN_ASM ".p2align 3\n\tv_mov_b32 v64, 0x3c00\n\tv_mov_b32 v65, 0x3c00\n\tv_pk_mul_f32 v[66:67], v[56:57], v[56:57] op_sel:[1,1] op_sel_hi:[1,0] neg_lo:[0,1]\n\tv_pk_mul_f32 v[68:69], v[58:59], v[58:59] op_sel:[1,1] op_sel_hi:[1,0] neg_lo:[0,1]\n\tv_pk_mul_f32 v[70:71], v[52:53], v[52:53] op_sel:[1,1] op_sel_hi:[1,0] neg_lo:[0,1]\n\tv_pk_mul_f32 v[72:73], v[54:55], v[54:55] op_sel:[1,1] op_sel_hi:[1,0] neg_lo:[0,1]\n\tv_pk_fma_f32 v[66:67], v[56:57], v[56:57], v[66:67] op_sel:[0,0,0] op_sel_hi:[0,1,1]\n\tv_pk_fma_f32 v[68:69], v[58:59], v[58:59], v[68:69] op_sel:[0,0,0] op_sel_hi:[0,1,1]\n\tv_pk_fma_f32 v[70:71], v[52:53], v[52:53], v[70:71] op_sel:[0,0,0] op_sel_hi:[0,1,1]\n\tv_pk_fma_f32 v[72:73], v[54:55], v[54:55], v[72:73] op_sel:[0,0,0] op_sel_hi:[0,1,1]\n\tv_cvt_pk_f16_f32 v74, v56, -v57\n\tv_cvt_pk_f16_f32 v75, v58, -v59\n\tv_cvt_pk_f16_f32 v76, v48, v49\n\tv_cvt_pk_f16_f32 v77, v50, v51\n\tv_pk_mul_f32 v[78:79], v[48:49], v[60:61] op_sel:[1,1] op_sel_hi:[1,0] neg_lo:[0,1]\n\tv_pk_mul_f32 v[80:81], v[50:51], v[62:63] op_sel:[1,1] op_sel_hi:[1,0] neg_lo:[0,1]\n\tv_pk_mul_f32 v[82:83], v[48:49], v[52:53] op_sel:[1,1] op_sel_hi:[1,0] neg_lo:[0,1]\n\tv_pk_mul_f32 v[84:85], v[50:51], v[54:55] op_sel:[1,1] op_sel_hi:[1,0] neg_lo:[0,1]\n\tds_write2_b64 %[b0], v[64:65], v[74:75] offset0:0 offset1:66\n\tv_pk_fma_f32 v[78:79], v[48:49], v[60:61], v[78:79] op_sel:[0,0,0] op_sel_hi:[0,1,1]\n\tv_pk_fma_f32 v[80:81], v[50:51], v[62:63], v[80:81] op_sel:[0,0,0] op_sel_hi:[0,1,1]\n\tv_pk_fma_f32 v[82:83], v[48:49], v[52:53], v[82:83] op_sel:[0,0,0] op_sel_hi:[0,1,1]\n\tv_pk_fma_f32 v[84:85], v[50:51], v[54:55], v[84:85] op_sel:[0,0,0] op_sel_hi:[0,1,1]\n\tv_cvt_pk_f16_f32 v86, v66, -v67\n\tv_cvt_pk_f16_f32 v87, v68, -v69\n\tv_pk_mul_f32 v[88:89], v[66:67], v[66:67] op_sel:[1,1] op_sel_hi:[1,0] neg_lo:[0,1]\n\tv_pk_mul_f32 v[90:91], v[68:69], v[68:69] op_sel:[1,1] op_sel_hi:[1,0] neg_lo:[0,1]\n\tv_pk_mul_f32 v[92:93], v[66:67], v[56:57] op_sel:[1,1] op_sel_hi:[1,0] neg_lo:[0,1]\n\tv_pk_mul_f32 v[94:95], v[68:69], v[58:59] op_sel:[1,1] op_sel_hi:[1,0] neg_lo:[0,1]\n\tv_pk_fma_f32 v[88:89], v[66:67], v[66:67], v[88:89] op_sel:[0,0,0] op_sel_hi:[0,1,1]\n\tv_pk_fma_f32 v[90:91], v[68:69], v[68:69], v[90:91] op_sel:[0,0,0] op_sel_hi:[0,1,1]\n\tv_pk_fma_f32 v[92:93], v[66:67], v[56:57], v[92:93] op_sel:[0,0,0] op_sel_hi:[0,1,1]\n\tv_pk_fma_f32 v[94:95], v[68:69], v[58:59], v[94:95] op_sel:[0,0,0] op_sel_hi:[0,1,1]\n\tv_cvt_pk_f16_f32 v64, v78, v79\n\tv_cvt_pk_f16_f32 v65, v80, v81\n\tv_cvt_pk_f16_f32 v96, v82, v83\n\tv_cvt_pk_f16_f32 v97, v84, v85\n\tv_pk_mul_f32 v[98:99], v[70:71], v[70:71] op_sel:[1,1] op_sel_hi:[1,0] neg_lo:[0,1]\n\tv_pk_mul_f32 v[100:101], v[72:73], v[72:73] op_sel:[1,1] op_sel_hi:[1,0] neg_lo:[0,1]\n\tv_pk_mul_f32 v[102:103], v[78:79], v[56:57] op_sel:[1,1] op_sel_hi:[1,0] neg_lo:[0,1]\n\tv_pk_mul_f32 v[104:105], v[80:81], v[58:59] op_sel:[1,1] op_sel_hi:[1,0] neg_lo:[0,1]\n\tds_write2_b64 %[b8], v[76:77], v[96:97] offset0:0 offset1:66\n\tv_pk_fma_f32 v[98:99], v[70:71], v[70:71], v[98:99] op_sel:[0,0,0] op_sel_hi:[0,1,1]\n\tv_pk_fma_f32 v[100:101], v[72:73], v[72:73], v[100:101] op_sel:[0,0,0] op_sel_hi:[0,1,1]\n\tv_pk_fma_f32 v[102:103], v[78:79], v[56:57], v[102:103] op_sel:[0,0,0] op_sel_hi:[0,1,1]\n\tv_pk_fma_f32 v[104:105], v[80:81], v[58:59], v[104:105] op_sel:[0,0,0] op_sel_hi:[0,1,1]\n\tv_cvt_pk_f16_f32 v106, v88, -v89\n\tv_cvt_pk_f16_f32 v107, v90, -v91\n\tv_cvt_pk_f16_f32 v108, v92, -v93\n\tv_cvt_pk_f16_f32 v109, v94, -v95\n\tv_pk_mul_f32 v[110:111], v[78:79], v[70:71] op_sel:[1,1] op_sel_hi:[1,0] neg_lo:[0,1]\n\tv_pk_mul_f32 v[112:113], v[80:81], v[72:73] op_sel:[1,1] op_sel_hi:[1,0] neg_lo:[0,1]\n\tv_pk_mul_f32 v[114:115], v[48:49], v[70:71] op_sel:[1,1] op_sel_hi:[1,0] neg_lo:[0,1]\n\tv_pk_mul_f32 v[116:117], v[50:51], v[72:73] op_sel:[1,1] op_sel_hi:[1,0] neg_lo:[0,1]\n\tds_write2_b64 %[b0], v[86:87], v[108:109] offset0:132 offset1:198\n\tv_pk_fma_f32 v[110:111], v[78:79], v[70:71], v[110:111] op_sel:[0,0,0] op_sel_hi:[0,1,1]\n\tv_pk_fma_f32 v[112:113], v[80:81], v[72:73], v[112:113] op_sel:[0,0,0] op_sel_hi:[0,1,1]\n\tv_pk_fma_f32 v[114:115], v[48:49], v[70:71], v[114:115] op_sel:[0,0,0] op_sel_hi:[0,1,1]\n\tv_pk_fma_f32 v[116:117], v[50:51], v[72:73], v[116:117] op_sel:[0,0,0] op_sel_hi:[0,1,1]\n\tv_cvt_pk_f16_f32 v76, v102, v103\n\tv_cvt_pk_f16_f32 v77, v104, v105\n\tv_pk_mul_f32 v[96:97], v[82:83], v[70:71] op_sel:[1,1] op_sel_hi:[1,0] neg_lo:[0,1]\n\tv_pk_mul_f32 v[118:119], v[84:85], v[72:73] op_sel:[1,1] op_sel_hi:[1,0] neg_lo:[0,1]\n\tv_pk_mul_f32 v[120:121], v[88:89], v[88:89] op_sel:[1,1] op_sel_hi:[1,0] neg_lo:[0,1]\n\tv_pk_mul_f32 v[122:123], v[90:91], v[90:91] op_sel:[1,1] op_sel_hi:[1,0] neg_lo:[0,1]\n\tv_pk_fma_f32 v[96:97], v[82:83], v[70:71], v[96:97] op_sel:[0,0,0] op_sel_hi:[0,1,1]\n\tv_pk_fma_f32 v[118:119], v[84:85], v[72:73], v[118:119] op_sel:[0,0,0] op_sel_hi:[0,1,1]\n\tv_pk_fma_f32 v[120:121], v[88:89], v[88:89], v[120:121] op_sel:[0,0,0] op_sel_hi:[0,1,1]\n\tv_pk_fma_f32 v[122:123], v[90:91], v[90:91], v[122:123] op_sel:[0,0,0] op_sel_hi:[0,1,1]\n\tv_cvt_pk_f16_f32 v124, v110, v111\n\tv_cvt_pk_f16_f32 v125, v112, v113\n\tv_cvt_pk_f16_f32 v126, v114, v115\n\tv_cvt_pk_f16_f32 v127, v116, v117\n\tv_pk_mul_f32 v[70:71], v[88:89], v[56:57] op_sel:[1,1] op_sel_hi:[1,0] neg_lo:[0,1]\n\tv_pk_mul_f32 v[72:73], v[90:91], v[58:59] op_sel:[1,1] op_sel_hi:[1,0] neg_lo:[0,1]\n\tv_pk_mul_f32 v[128:129], v[102:103], v[60:61] op_sel:[1,1] op_sel_hi:[1,0] neg_lo:[0,1]\n\tv_pk_mul_f32 v[130:131], v[104:105], v[62:63] op_sel:[1,1] op_sel_hi:[1,0] neg_lo:[0,1]\n\tds_write2_b64 %[b11], v[64:65], v[124:125] offset0:0 offset1:66\n\tv_pk_fma_f32 v[70:71], v[88:89], v[56:57], v[70:71] op_sel:[0,0,0] op_sel_hi:[0,1,1]\n\tv_pk_fma_f32 v[72:73], v[90:91], v[58:59], v[72:73] op_sel:[0,0,0] op_sel_hi:[0,1,1]\n\tv_pk_fma_f32 v[128:129], v[102:103], v[60:61], v[128:129] op_sel:[0,0,0] op_sel_hi:[0,1,1]\n\tv_pk_fma_f32 v[130:131], v[104:105], v[62:63], v[130:131] op_sel:[0,0,0] op_sel_hi:[0,1,1]\n\tv_cvt_pk_f16_f32 v132, v96, v97\n\tv_cvt_pk_f16_f32 v133, v118, v119\n\tv_cvt_pk_f16_f32 v134, v120, -v121\n\tv_cvt_pk_f16_f32 v135, v122, -v123\n\tv_pk_mul_f32 v[102:103], v[78:79], v[98:99] op_sel:[1,1] op_sel_hi:[1,0] neg_lo:[0,1]\n\tv_pk_mul_f32 v[104:105], v[80:81], v[100:101] op_sel:[1,1] op_sel_hi:[1,0] neg_lo:[0,1]\n\tv_pk_mul_f32 v[136:137], v[88:89], v[66:67] op_sel:[1,1] op_sel_hi:[1,0] neg_lo:[0,1]\n\tv_pk_mul_f32 v[138:139], v[90:91], v[68:69] op_sel:[1,1] op_sel_hi:[1,0] neg_lo:[0,1]\n\tds_write2_b64 %[b8], v[126:127], v[132:133] offset0:132 offset1:198\n\tv_pk_fma_f32 v[102:103], v[78:79], v[98:99], v[102:103] op_sel:[0,0,0] op_sel_hi:[0,1,1]\n\tv_pk_fma_f32 v[104:105], v[80:81], v[100:101], v[104:105] op_sel:[0,0,0] op_sel_hi:[0,1,1]\n\tv_pk_fma_f32 v[136:137], v[88:89], v[66:67], v[136:137] op_sel:[0,0,0] op_sel_hi:[0,1,1]\n\tv_pk_fma_f32 v[138:139], v[90:91], v[68:69], v[138:139] op_sel:[0,0,0] op_sel_hi:[0,1,1]\n\tv_cvt_pk_f16_f32 v64, v70, -v71\n\tv_cvt_pk_f16_f32 v65, v72, -v73\n\tv_cvt_pk_f16_f32 v124, v128, v129\n\tv_cvt_pk_f16_f32 v125, v130, v131\n\tv_pk_mul_f32 v[78:79], v[88:89], v[92:93] op_sel:[1,1] op_sel_hi:[1,0] neg_lo:[0,1]\n\tv_pk_mul_f32 v[80:81], v[90:91], v[94:95] op_sel:[1,1] op_sel_hi:[1,0] neg_lo:[0,1]\n\tv_pk_mul_f32 v[140:141], v[120:121], v[120:121] op_sel:[1,1] op_sel_hi:[1,0] neg_lo:[0,1]\n\tv_pk_mul_f32 v[142:143], v[122:123], v[122:123] op_sel:[1,1] op_sel_hi:[1,0] neg_lo:[0,1]\n\tds_write2_b64 %[b1], v[106:107], v[64:65] offset0:0 offset1:66\n\tv_pk_fma_f32 v[78:79], v[88:89], v[92:93], v[78:79] op_sel:[0,0,0] op_sel_hi:[0,1,1]\n\tv_pk_fma_f32 v[80:81], v[90:91], v[94:95], v[80:81] op_sel:[0,0,0] op_sel_hi:[0,1,1]\n\tv_pk_fma_f32 v[140:141], v[120:121], v[120:121], v[140:141] op_sel:[0,0,0] op_sel_hi:[0,1,1]\n\tv_pk_fma_f32 v[142:143], v[122:123], v[122:123], v[142:143] op_sel:[0,0,0] op_sel_hi:[0,1,1]\n\tv_cvt_pk_f16_f32 v126, v102, v103\n\tv_cvt_pk_f16_f32 v127, v104, v105\n\tv_cvt_pk_f16_f32 v132, v136, -v137\n\tv_cvt_pk_f16_f32 v133, v138, -v139\n\tv_pk_mul_f32 v[144:145], v[120:121], v[56:57] op_sel:[1,1] op_sel_hi:[1,0] neg_lo:[0,1]\n\tv_pk_mul_f32 v[146:147], v[122:123], v[58:59] op_sel:[1,1] op_sel_hi:[1,0] neg_lo:[0,1]\n\tv_pk_mul_f32 v[148:149], v[128:129], v[60:61] op_sel:[1,1] op_sel_hi:[1,0] neg_lo:[0,1]\n\tv_pk_mul_f32 v[150:151], v[130:131], v[62:63] op_sel:[1,1] op_sel_hi:[1,0] neg_lo:[0,1]\n\tds_write2_b64 %[b9], v[76:77], v[124:125] offset0:0 offset1:66\n\tv_pk_fma_f32 v[144:145], v[120:121], v[56:57], v[144:145] op_sel:[0,0,0] op_sel_hi:[0,1,1]\n\tv_pk_fma_f32 v[146:147], v[122:123], v[58:59], v[146:147] op_sel:[0,0,0] op_sel_hi:[0,1,1]\n\tv_pk_fma_f32 v[148:149], v[128:129], v[60:61], v[148:149] op_sel:[0,0,0] op_sel_hi:[0,1,1]\n\tv_pk_fma_f32 v[150:151], v[130:131], v[62:63], v[150:151] op_sel:[0,0,0] op_sel_hi:[0,1,1]\n\tv_cvt_pk_f16_f32 v152, v78, -v79\n\tv_cvt_pk_f16_f32 v153, v80, -v81\n\tv_cvt_pk_f16_f32 v154, v140, -v141\n\tv_cvt_pk_f16_f32 v155, v142, -v143\n\tv_pk_mul_f32 v[128:129], v[102:103], v[60:61] op_sel:[1,1] op_sel_hi:[1,0] neg_lo:[0,1]\n\tv_pk_mul_f32 v[130:131], v[104:105], v[62:63] op_sel:[1,1] op_sel_hi:[1,0] neg_lo:[0,1]\n\tv_pk_mul_f32 v[140:141], v[110:111], v[98:99] op_sel:[1,1] op_sel_hi:[1,0] neg_lo:[0,1]\n\tv_pk_mul_f32 v[142:143], v[112:113], v[100:101] op_sel:[1,1] op_sel_hi:[1,0] neg_lo:[0,1]\n\tds_write2_b64 %[b1], v[132:133], v[152:153] offset0:132 offset1:198\n\tv_pk_fma_f32 v[128:129], v[102:103], v[60:61], v[128:129] op_sel:[0,0,0] op_sel_hi:[0,1,1]\n\tv_pk_fma_f32 v[130:131], v[104:105], v[62:63], v[130:131] op_sel:[0,0,0] op_sel_hi:[0,1,1]\n\tv_pk_fma_f32 v[140:141], v[110:111], v[98:99], v[140:141] op_sel:[0,0,0] op_sel_hi:[0,1,1]\n\tv_pk_fma_f32 v[142:143], v[112:113], v[100:101], v[142:143] op_sel:[0,0,0] op_sel_hi:[0,1,1]\n\tv_cvt_pk_f16_f32 v76, v144, -v145\n\tv_cvt_pk_f16_f32 v77, v146, -v147\n\tv_cvt_pk_f16_f32 v124, v148, v149\n\tv_cvt_pk_f16_f32 v125, v150, v151\n\tv_pk_mul_f32 v[102:103], v[120:121], v[66:67] op_sel:[1,1] op_sel_hi:[1,0] neg_lo:[0,1]\n\tv_pk_mul_f32 v[104:105], v[122:123], v[68:69] op_sel:[1,1] op_sel_hi:[1,0] neg_lo:[0,1]\n\tv_pk_mul_f32 v[110:111], v[120:121], v[92:93] op_sel:[1,1] op_sel_hi:[1,0] neg_lo:[0,1]\n\tv_pk_mul_f32 v[112:113], v[122:123], v[94:95] op_sel:[1,1] op_sel_hi:[1,0] neg_lo:[0,1]\n\tds_write2_b64 %[b2], v[134:135], v[76:77] offset0:0 offset1:66\n\tv_pk_fma_f32 v[102:103], v[120:121], v[66:67], v[102:103] op_sel:[0,0,0] op_sel_hi:[0,1,1]\n\tv_pk_fma_f32 v[104:105], v[122:123], v[68:69], v[104:105] op_sel:[0,0,0] op_sel_hi:[0,1,1]\n\tv_pk_fma_f32 v[110:111], v[120:121], v[92:93], v[110:111] op_sel:[0,0,0] op_sel_hi:[0,1,1]\n\tv_pk_fma_f32 v[112:113], v[122:123], v[94:95], v[112:113] op_sel:[0,0,0] op_sel_hi:[0,1,1]\n\tv_cvt_pk_f16_f32 v144, v128, v129\n\tv_cvt_pk_f16_f32 v145, v130, v131\n\tv_cvt_pk_f16_f32 v146, v140, v141\n\tv_cvt_pk_f16_f32 v147, v142, v143\n\tv_pk_mul_f32 v[66:67], v[148:149], v[60:61] op_sel:[1,1] op_sel_hi:[1,0] neg_lo:[0,1]\n\tv_pk_mul_f32 v[68:69], v[150:151], v[62:63] op_sel:[1,1] op_sel_hi:[1,0] neg_lo:[0,1]\n\tv_pk_mul_f32 v[92:93], v[128:129], v[60:61] op_sel:[1,1] op_sel_hi:[1,0] neg_lo:[0,1]\n\tv_pk_mul_f32 v[94:95], v[130:131], v[62:63] op_sel:[1,1] op_sel_hi:[1,0] neg_lo:[0,1]\n\tds_write2_b64 %[b9], v[124:125], v[146:147] offset0:132 offset1:198\n\tv_pk_fma_f32 v[66:67], v[148:149], v[60:61], v[66:67] op_sel:[0,0,0] op_sel_hi:[0,1,1]\n\tv_pk_fma_f32 v[68:69], v[150:151], v[62:63], v[68:69] op_sel:[0,0,0] op_sel_hi:[0,1,1]\n\tv_pk_fma_f32 v[92:93], v[128:129], v[60:61], v[92:93] op_sel:[0,0,0] op_sel_hi:[0,1,1]\n\tv_pk_fma_f32 v[94:95], v[130:131], v[62:63], v[94:95] op_sel:[0,0,0] op_sel_hi:[0,1,1]\n\tv_cvt_pk_f16_f32 v140, v102, -v103\n\tv_cvt_pk_f16_f32 v141, v104, -v105\n\tv_cvt_pk_f16_f32 v142, v110, -v111\n\tv_cvt_pk_f16_f32 v143, v112, -v113\n\tv_pk_mul_f32 v[102:103], v[120:121], v[88:89] op_sel:[1,1] op_sel_hi:[1,0] neg_lo:[0,1]\n\tv_pk_mul_f32 v[104:105], v[122:123], v[90:91] op_sel:[1,1] op_sel_hi:[1,0] neg_lo:[0,1]\n\tv_pk_mul_f32 v[110:111], v[120:121], v[70:71] op_sel:[1,1] op_sel_hi:[1,0] neg_lo:[0,1]\n\tv_pk_mul_f32 v[112:113], v[122:123], v[72:73] op_sel:[1,1] op_sel_hi:[1,0] neg_lo:[0,1]\n\tds_write2_b64 %[b2], v[140:141], v[142:143] offset0:132 offset1:198\n\tv_pk_fma_f32 v[102:103], v[120:121], v[88:89], v[102:103] op_sel:[0,0,0] op_sel_hi:[0,1,1]\n\tv_pk_fma_f32 v[104:105], v[122:123], v[90:91], v[104:105] op_sel:[0,0,0] op_sel_hi:[0,1,1]\n\tv_pk_fma_f32 v[110:111], v[120:121], v[70:71], v[110:111] op_sel:[0,0,0] op_sel_hi:[0,1,1]\n\tv_pk_fma_f32 v[112:113], v[122:123], v[72:73], v[112:113] op_sel:[0,0,0] op_sel_hi:[0,1,1]\n\tv_cvt_pk_f16_f32 v124, v66, v67\n\tv_cvt_pk_f16_f32 v125, v68, v69\n\tv_cvt_pk_f16_f32 v128, v92, v93\n\tv_cvt_pk_f16_f32 v129, v94, v95\n\tv_pk_mul_f32 v[70:71], v[48:49], v[98:99] op_sel:[1,1] op_sel_hi:[1,0] neg_lo:[0,1]\n\tv_pk_mul_f32 v[72:73], v[50:51], v[100:101] op_sel:[1,1] op_sel_hi:[1,0] neg_lo:[0,1]\n\tv_pk_mul_f32 v[88:89], v[82:83], v[98:99] op_sel:[1,1] op_sel_hi:[1,0] neg_lo:[0,1]\n\tv_pk_mul_f32 v[90:91], v[84:85], v[100:101] op_sel:[1,1] op_sel_hi:[1,0] neg_lo:[0,1]\n\tds_write2_b64 %[b10], v[126:127], v[144:145] offset0:132 offset1:198\n\tv_pk_fma_f32 v[70:71], v[48:49], v[98:99], v[70:71] op_sel:[0,0,0] op_sel_hi:[0,1,1]\n\tv_pk_fma_f32 v[72:73], v[50:51], v[100:101], v[72:73] op_sel:[0,0,0] op_sel_hi:[0,1,1]\n\tv_pk_fma_f32 v[88:89], v[82:83], v[98:99], v[88:89] op_sel:[0,0,0] op_sel_hi:[0,1,1]\n\tv_pk_fma_f32 v[90:91], v[84:85], v[100:101], v[90:91] op_sel:[0,0,0] op_sel_hi:[0,1,1]\n\tv_cvt_pk_f16_f32 v92, v102, -v103\n\tv_cvt_pk_f16_f32 v93, v104, -v105\n\tv_cvt_pk_f16_f32 v94, v110, -v111\n\tv_cvt_pk_f16_f32 v95, v112, -v113\n\tv_pk_mul_f32 v[82:83], v[66:67], v[60:61] op_sel:[1,1] op_sel_hi:[1,0] neg_lo:[0,1]\n\tv_pk_mul_f32 v[84:85], v[68:69], v[62:63] op_sel:[1,1] op_sel_hi:[1,0] neg_lo:[0,1]\n\tv_pk_mul_f32 v[102:103], v[120:121], v[136:137] op_sel:[1,1] op_sel_hi:[1,0] neg_lo:[0,1]\n\tv_pk_mul_f32 v[104:105], v[122:123], v[138:139] op_sel:[1,1] op_sel_hi:[1,0] neg_lo:[0,1]\n\tds_write2_b64 %[b3], v[92:93], v[94:95] offset0:0 offset1:66\n\tv_pk_fma_f32 v[82:83], v[66:67], v[60:61], v[82:83] op_sel:[0,0,0] op_sel_hi:[0,1,1]\n\tv_pk_fma_f32 v[84:85], v[68:69], v[62:63], v[84:85] op_sel:[0,0,0] op_sel_hi:[0,1,1]\n\tv_pk_fma_f32 v[102:103], v[120:121], v[136:137], v[102:103] op_sel:[0,0,0] op_sel_hi:[0,1,1]\n\tv_pk_fma_f32 v[104:105], v[122:123], v[138:139], v[104:105] op_sel:[0,0,0] op_sel_hi:[0,1,1]\n\tv_cvt_pk_f16_f32 v110, v70, v71\n\tv_cvt_pk_f16_f32 v111, v72, v73\n\tv_cvt_pk_f16_f32 v112, v88, v89\n\tv_cvt_pk_f16_f32 v113, v90, v91\n\tv_pk_mul_f32 v[66:67], v[120:121], v[78:79] op_sel:[1,1] op_sel_hi:[1,0] neg_lo:[0,1]\n\tv_pk_mul_f32 v[68:69], v[122:123], v[80:81] op_sel:[1,1] op_sel_hi:[1,0] neg_lo:[0,1]\n\tv_pk_mul_f32 v[70:71], v[114:115], v[98:99] op_sel:[1,1] op_sel_hi:[1,0] neg_lo:[0,1]\n\tv_pk_mul_f32 v[72:73], v[116:117], v[100:101] op_sel:[1,1] op_sel_hi:[1,0] neg_lo:[0,1]\n\tds_write2_b64 %[b11], v[110:111], v[112:113] offset0:132 offset1:198\n\tv_pk_fma_f32 v[66:67], v[120:121], v[78:79], v[66:67] op_sel:[0,0,0] op_sel_hi:[0,1,1]\n\tv_pk_fma_f32 v[68:69], v[122:123], v[80:81], v[68:69] op_sel:[0,0,0] op_sel_hi:[0,1,1]\n\tv_pk_fma_f32 v[70:71], v[114:115], v[98:99], v[70:71] op_sel:[0,0,0] op_sel_hi:[0,1,1]\n\tv_pk_fma_f32 v[72:73], v[116:117], v[100:101], v[72:73] op_sel:[0,0,0] op_sel_hi:[0,1,1]\n\tv_cvt_pk_f16_f32 v88, v82, v83\n\tv_cvt_pk_f16_f32 v89, v84, v85\n\tv_cvt_pk_f16_f32 v90, v102, -v103\n\tv_cvt_pk_f16_f32 v91, v104, -v105\n\tv_pk_mul_f32 v[78:79], v[96:97], v[98:99] op_sel:[1,1] op_sel_hi:[1,0] neg_lo:[0,1]\n\tv_pk_mul_f32 v[80:81], v[118:119], v[100:101] op_sel:[1,1] op_sel_hi:[1,0] neg_lo:[0,1]\n\tv_pk_mul_f32 v[102:103], v[82:83], v[60:61] op_sel:[1,1] op_sel_hi:[1,0] neg_lo:[0,1]\n\tv_pk_mul_f32 v[104:105], v[84:85], v[62:63] op_sel:[1,1] op_sel_hi:[1,0] neg_lo:[0,1]\n\tds_write2_b64 %[b12], v[128:129], v[124:125] offset0:132 offset1:198\n\tv_pk_fma_f32 v[78:79], v[96:97], v[98:99], v[78:79] op_sel:[0,0,0] op_sel_hi:[0,1,1]\n\tv_pk_fma_f32 v[80:81], v[118:119], v[100:101], v[80:81] op_sel:[0,0,0] op_sel_hi:[0,1,1]\n\tv_pk_fma_f32 v[102:103], v[82:83], v[60:61], v[102:103] op_sel:[0,0,0] op_sel_hi:[0,1,1]\n\tv_pk_fma_f32 v[104:105], v[84:85], v[62:63], v[104:105] op_sel:[0,0,0] op_sel_hi:[0,1,1]\n\tv_cvt_pk_f16_f32 v110, v66, -v67\n\tv_cvt_pk_f16_f32 v111, v68, -v69\n\tv_cvt_pk_f16_f32 v112, v70, v71\n\tv_cvt_pk_f16_f32 v113, v72, v73\n\tv_pk_mul_f16 v66, v154, v74 op_sel:[1,1] op_sel_hi:[1,0] neg_lo:[0,1]\n\tv_pk_mul_f16 v67, v155, v75 op_sel:[1,1] op_sel_hi:[1,0] neg_lo:[0,1]\n\tv_pk_mul_f16 v68, v154, v86 op_sel:[1,1] op_sel_hi:[1,0] neg_lo:[0,1]\n\tv_pk_mul_f16 v69, v155, v87 op_sel:[1,1] op_sel_hi:[1,0] neg_lo:[0,1]\n\tds_write2_b64 %[b3], v[90:91], v[110:111] offset0:132 offset1:198\n\tv_pk_fma_f16 v66, v154, v74, v66 op_sel:[0,0,0] op_sel_hi:[0,1,1]\n\tv_pk_fma_f16 v67, v155, v75, v67 op_sel:[0,0,0] op_sel_hi:[0,1,1]\n\tv_pk_fma_f16 v68, v154, v86, v68 op_sel:[0,0,0] op_sel_hi:[0,1,1]\n\tv_pk_fma_f16 v69, v155, v87, v69 op_sel:[0,0,0] op_sel_hi:[0,1,1]\n\tv_cvt_pk_f16_f32 v70, v78, v79\n\tv_cvt_pk_f16_f32 v71, v80, v81\n\tv_cvt_pk_f16_f32 v72, v102, v103\n\tv_cvt_pk_f16_f32 v73, v104, v105\n\tv_pk_mul_f16 v78, v154, v108 op_sel:[1,1] op_sel_hi:[1,0] neg_lo:[0,1]\n\tv_pk_mul_f16 v79, v155, v109 op_sel:[1,1] op_sel_hi:[1,0] neg_lo:[0,1]\n\tv_pk_mul_f32 v[80:81], v[102:103], v[60:61] op_sel:[1,1] op_sel_hi:[1,0] neg_lo:[0,1]\n\tv_pk_mul_f32 v[82:83], v[104:105], v[62:63] op_sel:[1,1] op_sel_hi:[1,0] neg_lo:[0,1]\n\tds_write2_b64 %[b4], v[154:155], v[66:67] offset0:0 offset1:66\n\tv_pk_fma_f16 v78, v154, v108, v78 op_sel:[0,0,0] op_sel_hi:[0,1,1]\n\tv_pk_fma_f16 v79, v155, v109, v79 op_sel:[0,0,0] op_sel_hi:[0,1,1]\n\tv_pk_fma_f32 v[80:81], v[102:103], v[60:61], v[80:81] op_sel:[0,0,0] op_sel_hi:[0,1,1]\n\tv_pk_fma_f32 v[82:83], v[104:105], v[62:63], v[82:83] op_sel:[0,0,0] op_sel_hi:[0,1,1]\n\tv_pk_mul_f16 v74, v154, v106 op_sel:[1,1] op_sel_hi:[1,0] neg_lo:[0,1]\n\tv_pk_mul_f16 v75, v155, v107 op_sel:[1,1] op_sel_hi:[1,0] neg_lo:[0,1]\n\tv_pk_mul_f16 v84, v154, v64 op_sel:[1,1] op_sel_hi:[1,0] neg_lo:[0,1]\n\tv_pk_mul_f16 v85, v155, v65 op_sel:[1,1] op_sel_hi:[1,0] neg_lo:[0,1]\n\tds_write2_b64 %[b4], v[68:69], v[78:79] offset0:132 offset1:198\n\tv_pk_fma_f16 v74, v154, v106, v74 op_sel:[0,0,0] op_sel_hi:[0,1,1]\n\tv_pk_fma_f16 v75, v155, v107, v75 op_sel:[0,0,0] op_sel_hi:[0,1,1]\n\tv_pk_fma_f16 v84, v154, v64, v84 op_sel:[0,0,0] op_sel_hi:[0,1,1]\n\tv_pk_fma_f16 v85, v155, v65, v85 op_sel:[0,0,0] op_sel_hi:[0,1,1]\n\tv_cvt_pk_f16_f32 v66, v80, v81\n\tv_cvt_pk_f16_f32 v67, v82, v83\n\tv_pk_mul_f16 v80, v154, v132 op_sel:[1,1] op_sel_hi:[1,0] neg_lo:[0,1]\n\tv_pk_mul_f16 v81, v155, v133 op_sel:[1,1] op_sel_hi:[1,0] neg_lo:[0,1]\n\tv_pk_mul_f16 v82, v154, v152 op_sel:[1,1] op_sel_hi:[1,0] neg_lo:[0,1]\n\tv_pk_mul_f16 v83, v155, v153 op_sel:[1,1] op_sel_hi:[1,0] neg_lo:[0,1]\n\tds_write2_b64 %[b5], v[74:75], v[84:85] offset0:0 offset1:66\n\tv_pk_fma_f16 v80, v154, v132, v80 op_sel:[0,0,0] op_sel_hi:[0,1,1]\n\tv_pk_fma_f16 v81, v155, v133, v81 op_sel:[0,0,0] op_sel_hi:[0,1,1]\n\tv_pk_fma_f16 v82, v154, v152, v82 op_sel:[0,0,0] op_sel_hi:[0,1,1]\n\tv_pk_fma_f16 v83, v155, v153, v83 op_sel:[0,0,0] op_sel_hi:[0,1,1]\n\tv_pk_mul_f16 v64, v154, v134 op_sel:[1,1] op_sel_hi:[1,0] neg_lo:[0,1]\n\tv_pk_mul_f16 v65, v155, v135 op_sel:[1,1] op_sel_hi:[1,0] neg_lo:[0,1]\n\tv_pk_mul_f16 v68, v154, v76 op_sel:[1,1] op_sel_hi:[1,0] neg_lo:[0,1]\n\tv_pk_mul_f16 v69, v155, v77 op_sel:[1,1] op_sel_hi:[1,0] neg_lo:[0,1]\n\tds_write2_b64 %[b5], v[80:81], v[82:83] offset0:132 offset1:198\n\tv_pk_fma_f16 v64, v154, v134, v64 op_sel:[0,0,0] op_sel_hi:[0,1,1]\n\tv_pk_fma_f16 v65, v155, v135, v65 op_sel:[0,0,0] op_sel_hi:[0,1,1]\n\tv_pk_fma_f16 v68, v154, v76, v68 op_sel:[0,0,0] op_sel_hi:[0,1,1]\n\tv_pk_fma_f16 v69, v155, v77, v69 op_sel:[0,0,0] op_sel_hi:[0,1,1]\n\tv_pk_mul_f16 v74, v154, v140 op_sel:[1,1] op_sel_hi:[1,0] neg_lo:[0,1]\n\tv_pk_mul_f16 v75, v155, v141 op_sel:[1,1] op_sel_hi:[1,0] neg_lo:[0,1]\n\tv_pk_mul_f16 v78, v154, v142 op_sel:[1,1] op_sel_hi:[1,0] neg_lo:[0,1]\n\tv_pk_mul_f16 v79, v155, v143 op_sel:[1,1] op_sel_hi:[1,0] neg_lo:[0,1]\n\tds_write2_b64 %[b6], v[64:65], v[68:69] offset0:0 offset1:66\n\tv_pk_fma_f16 v74, v154, v140, v74 op_sel:[0,0,0] op_sel_hi:[0,1,1]\n\tv_pk_fma_f16 v75, v155, v141, v75 op_sel:[0,0,0] op_sel_hi:[0,1,1]\n\tv_pk_fma_f16 v78, v154, v142, v78 op_sel:[0,0,0] op_sel_hi:[0,1,1]\n\tv_pk_fma_f16 v79, v155, v143, v79 op_sel:[0,0,0] op_sel_hi:[0,1,1]\n\tv_pk_mul_f16 v76, v154, v92 op_sel:[1,1] op_sel_hi:[1,0] neg_lo:[0,1]\n\tv_pk_mul_f16 v77, v155, v93 op_sel:[1,1] op_sel_hi:[1,0] neg_lo:[0,1]\n\tv_pk_mul_f16 v80, v154, v94 op_sel:[1,1] op_sel_hi:[1,0] neg_lo:[0,1]\n\tv_pk_mul_f16 v81, v155, v95 op_sel:[1,1] op_sel_hi:[1,0] neg_lo:[0,1]\n\tds_write2_b64 %[b6], v[74:75], v[78:79] offset0:132 offset1:198\n\tv_pk_fma_f16 v76, v154, v92, v76 op_sel:[0,0,0] op_sel_hi:[0,1,1]\n\tv_pk_fma_f16 v77, v155, v93, v77 op_sel:[0,0,0] op_sel_hi:[0,1,1]\n\tv_pk_fma_f16 v80, v154, v94, v80 op_sel:[0,0,0] op_sel_hi:[0,1,1]\n\tv_pk_fma_f16 v81, v155, v95, v81 op_sel:[0,0,0] op_sel_hi:[0,1,1]\n\tv_pk_mul_f16 v64, v154, v90 op_sel:[1,1] op_sel_hi:[1,0] neg_lo:[0,1]\n\tv_pk_mul_f16 v65, v155, v91 op_sel:[1,1] op_sel_hi:[1,0] neg_lo:[0,1]\n\tv_pk_mul_f16 v68, v154, v110 op_sel:[1,1] op_sel_hi:[1,0] neg_lo:[0,1]\n\tv_pk_mul_f16 v69, v155, v111 op_sel:[1,1] op_sel_hi:[1,0] neg_lo:[0,1]\n\tds_write2_b64 %[b7], v[76:77], v[80:81] offset0:0 offset1:66\n\tv_pk_fma_f16 v64, v154, v90, v64 op_sel:[0,0,0] op_sel_hi:[0,1,1]\n\tv_pk_fma_f16 v65, v155, v91, v65 op_sel:[0,0,0] op_sel_hi:[0,1,1]\n\tv_pk_fma_f16 v68, v154, v110, v68 op_sel:[0,0,0] op_sel_hi:[0,1,1]\n\tv_pk_fma_f16 v69, v155, v111, v69 op_sel:[0,0,0] op_sel_hi:[0,1,1]\n\ts_nop 0\n\tds_write2_b64 %[b7], v[64:65], v[68:69] offset0:132 offset1:198\n\tds_write2_b64 %[b10], v[88:89], v[66:67] offset0:0 offset1:66\n\tds_write2_b64 %[b12], v[112:113], v[70:71] offset0:0 offset1:66\n\tds_write_b64 %[b13], v[72:73] offset:0"
#define KGEN_CLOBBER "v64", "v65", "v66", "v67", "v68", "v69", "v70", "v71", "v72", "v73", "v74", "v75", "v76", "v77", "v78", "v79", "v80", "v81", "v82", "v83", "v84", "v85", "v86", "v87", "v88", "v89", "v90", "v91", "v92", "v93", "v94", "v95", "v96", "v97", "v98", "v99", "v100", "v101", "v102", "v103", "v104", "v105", "v106", "v107", "v108", "v109", "v110", "v111", "v112", "v113", "v114", "v115", "v116", "v117", "v118", "v119", "v120", "v121", "v122", "v123", "v124", "v125", "v126", "v127", "v128", "v129", "v130", "v131", "v132", "v133", "v134", "v135", "v136", "v137", "v138", "v139", "v140", "v141", "v142", "v143", "v144", "v145", "v146", "v147", "v148", "v149", "v150", "v151", "v152", "v153", "v154", "v155"
#define KROW_0LO 0x2b2a2d2c23222120ull
#define KROW_0HI 0x202020202b343332ull
#define KROW_1LO 0x2524272a31302f2eull
#define KROW_1HI 0x2b2b2b2b20292826ull
#define KMFMA_ASM "ds_read_b128 v[64:67], %[ap] offset:0\n\tds_read_b128 v[68:71], %[bp] offset:0\n\tds_read_b128 v[72:75], %[ap] offset:32\n\tds_read_b128 v[76:79], %[bp] offset:32\n\tds_read_b128 v[80:83], %[ap] offset:64\n\tds_read_b128 v[84:87], %[bp] offset:64\n\tds_read_b128 v[88:91], %[ap] offset:96\n\tds_read_b128 v[92:95], %[bp] offset:96\n\tv_mov_b32 v112, 0x7c0\n\tv_lshlrev_b32_e64 v112, %[n], v112\n\tv_lshrrev_b32_e64 v112, %[q8], v112\n\tv_bfe_i32 v113, v112, 0, 1\n\tv_bfe_i32 v114, v112, 1, 1\n\tv_perm_b32 v4, v114, v113, %[ps]\n\tv_and_b32 v4, 0x3f803f80, v4\n\tv_bfe_i32 v113, v112, 2, 1\n\tv_bfe_i32 v114, v112, 3, 1\n\tv_perm_b32 v5, v114, v113, %[ps]\n\ts_waitcnt lgkmcnt(6)\n\tv_mfma_f32_32x32x16_f16 v[48:63], v[64:67], v[68:71], 0\n\tds_read_b128 v[96:99], %[ap] offset:128\n\tds_read_b128 v[100:103], %[bp] offset:128\n\tv_and_b32 v5, 0x3f803f80, v5\n\tv_bfe_i32 v113, v112, 4, 1\n\tv_bfe_i32 v114, v112, 5, 1\n\tv_perm_b32 v6, v114, v113, %[ps]\n\ts_waitcnt lgkmcnt(6)\n\tv_mfma_f32_32x32x16_f16 v[48:63], v[72:75], v[76:79], v[48:63]\n\tds_read_b128 v[104:107], %[ap] offset:160\n\tds_read_b128 v[108:111], %[bp] offset:160\n\tv_and_b32 v6, 0x3f803f80, v6\n\tv_bfe_i32 v113, v112, 6, 1\n\tv_bfe_i32 v114, v112, 7, 1\n\tv_perm_b32 v7, v114, v113, %[ps]\n\ts_waitcnt lgkmcnt(6)\n\tv_mfma_f32_32x32x16_f16 v[48:63], v[80:83], v[84:87], v[48:63]\n\tds_read_b128 v[64:67], %[ap] offset:192\n\tds_read_b128 v[68:71], %[bp] offset:192\n\tv_and_b32 v7, 0x3f803f80, v7\n\tv_mov_b32 v112, 0x3800\n\tv_lshlrev_b32_e64 v112, %[n], v112\n\tv_lshrrev_b32_e64 v112, %[q8], v112\n\ts_waitcnt lgkmcnt(6)\n\tv_mfma_f32_32x32x16_f16 v[48:63], v[88:91], v[92:95], v[48:63]\n\tds_read_b128 v[72:75], %[ap] offset:224\n\tds_read_b128 v[76:79], %[bp] offset:224\n\tv_bfe_i32 v113, v112, 0, 1\n\tv_bfe_i32 v114, v112, 1, 1\n\tv_perm_b32 v8, v114, v113, %[ps]\n\tv_and_b32 v8, 0x3f803f80, v8\n\ts_waitcnt lgkmcnt(6)\n\tv_mfma_f32_32x32x16_f16 v[48:63], v[96:99], v[100:103], v[48:63]\n\tds_read_b128 v[80:83], %[ap] offset:256\n\tds_read_b128 v[84:87], %[bp] offset:256\n\tv_bfe_i32 v113, v112, 2, 1\n\tv_bfe_i32 v114, v112, 3, 1\n\tv_perm_b32 v9, v114, v113, %[ps]\n\tv_and_b32 v9, 0x3f803f80, v9\n\ts_waitcnt lgkmcnt(6)\n\tv_mfma_f32_32x32x16_f16 v[48:63], v[104:107], v[108:111], v[48:63]\n\tds_read_b128 v[88:91], %[ap] offset:288\n\tds_read_b128 v[92:95], %[bp] offset:288\n\tv_bfe_i32 v113, v112, 4, 1\n\tv_bfe_i32 v114, v112, 5, 1\n\tv_perm_b32 v10, v114, v113, %[ps]\n\tv_and_b32 v10, 0x3f803f80, v10\n\ts_waitcnt lgkmcnt(6)\n\tv_mfma_f32_32x32x16_f16 v[48:63], v[64:67], v[68:71], v[48:63]\n\tds_read_b128 v[96:99], %[ap] offset:320\n\tds_read_b128 v[100:103], %[bp] offset:320\n\tv_bfe_i32 v113, v112, 6, 1\n\tv_bfe_i32 v114, v112, 7, 1\n\tv_perm_b32 v11, v114, v113, %[ps]\n\tv_and_b32 v11, 0x3f803f80, v11\n\ts_waitcnt lgkmcnt(6)\n\tv_mfma_f32_32x32x16_f16 v[48:63], v[72:75], v[76:79], v[48:63]\n\tds_read_b128 v[104:107], %[ap] offset:352\n\tds_read_b128 v[108:111], %[bp] offset:352\n\tv_mov_b32 v112, 0x1ff0\n\tv_lshlrev_b32_e64 v112, %[n], v112\n\tv_lshrrev_b32_e64 v112, %[q8], v112\n\tv_bfe_i32 v113, v112, 0, 1\n\ts_waitcnt lgkmcnt(6)\n\tv_mfma_f32_32x32x16_f16 v[48:63], v[80:83], v[84:87], v[48:63]\n\tds_read_b128 v[64:67], %[ap] offset:384\n\tds_read_b128 v[68:71], %[bp] offset:384\n\tv_bfe_i32 v114, v112, 1, 1\n\tv_perm_b32 v12, v114, v113, %[ps]\n\tv_and_b32 v12, 0x3f803f80, v12\n\tv_bfe_i32 v113, v112, 2, 1\n\ts_waitcnt lgkmcnt(6)\n\tv_mfma_f32_32x32x16_f16 v[48:63], v[88:91], v[92:95], v[48:63]\n\tds_read_b128 v[72:75], %[ap] offset:416\n\tds_read_b128 v[76:79], %[bp] offset:416\n\tv_bfe_i32 v114, v112, 3, 1\n\tv_perm_b32 v13, v114, v113, %[ps]\n\tv_and_b32 v13, 0x3f803f80, v13\n\tv_bfe_i32 v113, v112, 4, 1\n\ts_waitcnt lgkmcnt(6)\n\tv_mfma_f32_32x32x16_f16 v[48:63], v[96:99], v[100:103], v[48:63]\n\tds_read_b128 v[80:83], %[ap] offset:448\n\tds_read_b128 v[84:87], %[bp] offset:448\n\tv_bfe_i32 v114, v112, 5, 1\n\tv_perm_b32 v14, v114, v113, %[ps]\n\tv_and_b32 v14, 0x3f803f80, v14\n\tv_bfe_i32 v113, v112, 6, 1\n\ts_waitcnt lgkmcnt(6)\n\tv_mfma_f32_32x32x16_f16 v[48:63], v[104:107], v[108:111], v[48:63]\n\tds_read_b128 v[88:91], %[ap] offset:480\n\tds_read_b128 v[92:95], %[bp] offset:480\n\tv_bfe_i32 v114, v112, 7, 1\n\tv_perm_b32 v15, v114, v113, %[ps]\n\tv_and_b32 v15, 0x3f803f80, v15\n\tv_mov_b32 v112, 0xfe0\n\ts_waitcnt lgkmcnt(6)\n\tv_mfma_f32_32x32x16_f16 v[48:63], v[64:67], v[68:71], v[48:63]\n\tds_read_b32 %[stg], %[ctl] offset:4\n\tv_lshlrev_b32_e64 v112, %[n], v112\n\tv_lshrrev_b32_e64 v112, %[q8], v112\n\tv_bfe_i32 v113, v112, 0, 1\n\tv_bfe_i32 v114, v112, 1, 1\n\tv_perm_b32 v16, v114, v113, %[ps]\n\ts_waitcnt lgkmcnt(4)\n\tv_mfma_f32_32x32x16_f16 v[48:63], v[72:75], v[76:79], v[48:63]\n\tv_and_b32 v16, 0x3f803f80, v16\n\tv_bfe_i32 v113, v112, 2, 1\n\tv_bfe_i32 v114, v112, 3, 1\n\tv_perm_b32 v17, v114, v113, %[ps]\n\tv_and_b32 v17, 0x3f803f80, v17\n\ts_waitcnt lgkmcnt(2)\n\tv_mfma_f32_32x32x16_f16 v[48:63], v[80:83], v[84:87], v[48:63]\n\tv_bfe_i32 v113, v112, 4, 1\n\tv_bfe_i32 v114, v112, 5, 1\n\tv_perm_b32 v18, v114, v113, %[ps]\n\tv_and_b32 v18, 0x3f803f80, v18\n\tv_bfe_i32 v113, v112, 6, 1\n\ts_waitcnt lgkmcnt(0)\n\tv_mfma_f32_32x32x16_f16 v[48:63], v[88:91], v[92:95], v[48:63]\n\tv_bfe_i32 v114, v112, 7, 1\n\tv_perm_b32 v19, v114, v113, %[ps]\n\tv_and_b32 v19, 0x3f803f80, v19\n\tv_mov_b32 v112, 0x3000\n\tv_lshlrev_b32_e64 v112, %[n], v112\n\tv_lshrrev_b32_e64 v112, %[q8], v112\n\tv_bfe_i32 v113, v112, 0, 1\n\tv_bfe_i32 v114, v112, 1, 1\n\tv_perm_b32 v20, v114, v113, %[ps]\n\tv_bfe_i32 v113, v112, 2, 1\n\tv_bfe_i32 v114, v112, 3, 1\n\tv_perm_b32 v21, v114, v113, %[ps]\n\tv_bfe_i32 v113, v112, 4, 1\n\tv_bfe_i32 v114, v112, 5, 1\n\tv_perm_b32 v22, v114, v113, %[ps]\n\tv_bfe_i32 v113, v112, 6, 1\n\tv_bfe_i32 v114, v112, 7, 1\n\tv_perm_b32 v23, v114, v113, %[ps]\n\ts_waitcnt lgkmcnt(0)"
#define KMFMA_CLOBBER "v64", "v65", "v66", "v67", "v68", "v69", "v70", "v71", "v72", "v73", "v74", "v75", "v76", "v77", "v78", "v79", "v80", "v81", "v82", "v83", "v84", "v85", "v86", "v87", "v88", "v89", "v90", "v91", "v92", "v93", "v94", "v95", "v96", "v97", "v98", "v99", "v100", "v101", "v102", "v103", "v104", "v105", "v106", "v107", "v108", "v109", "v110", "v111", "v112", "v113", "v114"
#define CONV_ASM ".p2align 3\n\tds_read_b64_tr_b16 v[52:53], %[a0] offset:456\n\tds_read_b64_tr_b16 v[54:55], %[a0] offset:448\n\tds_read_b64_tr_b16 v[84:85], %[a0] offset:968\n\tds_read_b64_tr_b16 v[86:87], %[a0] offset:960\n\tds_read_b64_tr_b16 v[48:49], %[a0] offset:392\n\tds_read_b64_tr_b16 v[50:51], %[a0] offset:384\n\tds_read_b64_tr_b16 v[80:81], %[a0] offset:904\n\tds_read_b64_tr_b16 v[82:83], %[a0] offset:896\n\tds_read_b64_tr_b16 v[44:45], %[a0] offset:328\n\tds_read_b64_tr_b16 v[46:47], %[a0] offset:320\n\tds_read_b64_tr_b16 v[76:77], %[a0] offset:840\n\tds_read_b64_tr_b16 v[78:79], %[a0] offset:832\n\tv_readfirstlane_b32 %[s], %[stg]\n\ts_mov_b32 %[c], 0x400000\n\ts_cmp_ge_u32 %[s], 4\n\ts_cbranch_scc1 2f\n\t1:\n\tds_read_b32 v216, %[ctl] offset:4\n\ts_waitcnt lgkmcnt(0)\n\tv_readfirstlane_b32 %[s], v216\n\ts_cmp_ge_u32 %[s], 4\n\ts_cbranch_scc1 2f\n\ts_sleep 1\n\ts_sub_u32 %[c], %[c], 1\n\ts_cbranch_scc0 1b\n\t.p2align 3\n\t2:\n\tds_read_b128 v[164:167], %[bb] offset:64\n\tds_read_b128 v[168:171], %[bb] offset:128\n\tds_read_b128 v[172:175], %[bb] offset:192\n\tds_read_b64_tr_b16 v[40:41], %[a0] offset:264\n\tds_read_b64_tr_b16 v[42:43], %[a0] offset:256\n\tds_read_b128 v[176:179], %[bb] offset:256\n\ts_waitcnt lgkmcnt(5)\n\ts_nop 0\n\tv_mfma_f32_16x16x32_bf16 v[132:135], v[52:55], v[164:167], 0\n\tds_read_b64_tr_b16 v[72:73], %[a0] offset:776\n\tds_read_b64_tr_b16 v[74:75], %[a0] offset:768\n\tds_read_b64_tr_b16 v[36:37], %[a0] offset:200\n\tv_mfma_f32_16x16x32_bf16 v[136:139], v[84:87], v[164:167], 0\n\tds_read_b64_tr_b16 v[38:39], %[a0] offset:192\n\ts_waitcnt lgkmcnt(8)\n\ts_nop 0\n\tv_mfma_f32_16x16x32_bf16 v[132:135], v[48:51], v[168:171], v[132:135]\n\tds_read_b128 v[180:183], %[bb] offset:320\n\tds_read_b64_tr_b16 v[68:69], %[a0] offset:712\n\tds_read_b64_tr_b16 v[70:71], %[a0] offset:704\n\tv_mfma_f32_16x16x32_bf16 v[136:139], v[80:83], v[168:171], v[136:139]\n\tds_read_b64_tr_b16 v[32:33], %[a0] offset:136\n\ts_waitcnt lgkmcnt(8)\n\ts_nop 0\n\tv_mfma_f32_16x16x32_bf16 v[132:135], v[44:47], v[172:175], v[132:135]\n\tds_read_b64_tr_b16 v[34:35], %[a0] offset:128\n\tds_read_b128 v[184:187], %[bb] offset:384\n\tds_read_b64_tr_b16 v[64:65], %[a0] offset:648\n\tv_mfma_f32_16x16x32_bf16 v[136:139], v[76:79], v[172:175], v[136:139]\n\tds_read_b64_tr_b16 v[66:67], %[a0] offset:640\n\tds_read_b64_tr_b16 v[28:29], %[a0] offset:72\n\tds_read_b64_tr_b16 v[30:31], %[a0] offset:64\n\tv_mfma_f32_16x16x32_bf16 v[132:135], v[40:43], v[176:179], v[132:135]\n\tds_read_b128 v[164:167], %[bb] offset:448\n\ts_waitcnt lgkmcnt(8)\n\ts_nop 0\n\tv_mfma_f32_16x16x32_bf16 v[136:139], v[72:75], v[176:179], v[136:139]\n\tds_read_b64_tr_b16 v[60:61], %[a0] offset:584\n\tds_read_b64_tr_b16 v[62:63], %[a0] offset:576\n\tds_read_b64_tr_b16 v[24:25], %[a0] offset:8\n\tv_mfma_f32_16x16x32_bf16 v[132:135], v[36:39], v[180:183], v[132:135]\n\tds_read_b64_tr_b16 v[26:27], %[a0] offset:0\n\tds_read_b128 v[168:171], %[bb] offset:512\n\tds_read_b64_tr_b16 v[56:57], %[a0] offset:520\n\tv_mfma_f32_16x16x32_bf16 v[136:139], v[68:71], v[180:183], v[136:139]\n\tds_read_b64_tr_b16 v[58:59], %[a0] offset:512\n\ts_waitcnt lgkmcnt(8)\n\ts_nop 0\n\tv_mfma_f32_16x16x32_bf16 v[132:135], v[32:35], v[184:187], v[132:135]\n\tds_read_b64_tr_b16 v[88:89], %[a0] offset:1032\n\tds_read_b64_tr_b16 v[90:91], %[a0] offset:1024\n\tds_read_b128 v[188:191], %[bb] offset:496\n\tv_mfma_f32_16x16x32_bf16 v[136:139], v[64:67], v[184:187], v[136:139]\n\tds_read_b128 v[172:175], %[bb] offset:576\n\ts_waitcnt lgkmcnt(8)\n\ts_nop 0\n\tv_mfma_f32_16x16x32_bf16 v[132:135], v[28:31], v[164:167], v[132:135]\n\tds_read_b128 v[176:179], %[bb] offset:640\n\tv_mfma_f32_16x16x32_bf16 v[136:139], v[60:63], v[164:167], v[136:139]\n\ts_waitcnt lgkmcnt(5)\n\ts_nop 0\n\tv_mfma_f32_16x16x32_bf16 v[132:135], v[24:27], v[168:171], v[132:135]\n\tds_write_b16 %[ta], %[t78]\n\tds_read_b128 v[180:183], %[bb] offset:704\n\tv_mfma_f32_16x16x32_bf16 v[136:139], v[56:59], v[168:171], v[136:139]\n\ts_waitcnt lgkmcnt(3)\n\ts_nop 0\n\tv_mfma_f32_16x16x32_bf16 v[140:143], v[88:91], v[168:171], 0\n\tds_write_b16_d16_hi %[ta], %[t78] offset:64\n\tds_read_b128 v[184:187], %[bb] offset:768\n\tv_mfma_f32_16x16x32_bf16 v[204:207], v[188:191], v[4:7], 0\n\tds_write_b128 %[yb], v[132:135] offset:0\n\tv_mfma_f32_16x16x32_bf16 v[218:221], v[168:171], v[4:7], 0\n\tds_write_b16 %[ta], %[t78] offset:388\n\tds_read_b128 v[164:167], %[bb] offset:832\n\tv_mfma_f32_16x16x32_bf16 v[208:211], v[188:191], v[8:11], 0\n\tds_write_b8 %[ctc], %[one] offset:0\n\tds_write_b16_d16_hi %[ta], %[t78] offset:452\n\tv_mfma_f32_16x16x32_bf16 v[222:225], v[168:171], v[8:11], 0\n\tds_write_b16 %[tb], %[t9a]\n\tv_mfma_f32_16x16x32_bf16 v[212:215], v[188:191], v[12:15], 0\n\tds_write_b16_d16_hi %[tb], %[t9a] offset:128\n\tv_cvt_pk_bf16_f32 v192, v221, v207\n\tv_mfma_f32_16x16x32_bf16 v[226:229], v[168:171], v[16:19], 0\n\tds_write_b16 %[tb], %[t9a] offset:388\n\tv_cvt_pk_bf16_f32 v193, v220, v206\n\tv_mfma_f32_16x16x32_bf16 v[148:151], v[20:23], v[168:171], 0\n\tds_write_b16_d16_hi %[tb], %[t9a] offset:516\n\tds_read_b128 v[168:171], %[bb] offset:896\n\tv_mfma_f32_16x16x32_bf16 v[136:139], v[52:55], v[172:175], v[136:139]\n\tds_read_b64 v[92:93], %[tp] offset:0\n\tds_read_b64 v[94:95], %[tp] offset:8\n\tds_read_b64 v[96:97], %[tp] offset:64\n\tv_mfma_f32_16x16x32_bf16 v[140:143], v[84:87], v[172:175], v[140:143]\n\tds_read_b128 v[172:175], %[bb] offset:960\n\ts_waitcnt lgkmcnt(7)\n\ts_nop 0\n\tv_mfma_f32_16x16x32_bf16 v[136:139], v[48:51], v[176:179], v[136:139]\n\tds_read_b64 v[98:99], %[tp] offset:72\n\tds_read_b64 v[100:101], %[tp] offset:128\n\tds_read_b64 v[102:103], %[tp] offset:136\n\tv_mfma_f32_16x16x32_bf16 v[140:143], v[80:83], v[176:179], v[140:143]\n\tds_read_b128 v[176:179], %[bb] offset:1024\n\tds_read_b64 v[104:105], %[tp] offset:768\n\tds_read_b64 v[106:107], %[tp] offset:776\n\tv_mfma_f32_16x16x32_bf16 v[136:139], v[44:47], v[180:183], v[136:139]\n\tds_read_b64 v[108:109], %[tp] offset:832\n\tds_read_b64 v[110:111], %[tp] offset:840\n\tds_read_b64 v[112:113], %[tp] offset:896\n\tv_mfma_f32_16x16x32_bf16 v[140:143], v[76:79], v[180:183], v[140:143]\n\tds_read_b64 v[114:115], %[tp] offset:904\n\tds_read_b64 v[116:117], %[tp] offset:1536\n\tds_read_b64 v[118:119], %[tp] offset:1544\n\tv_mfma_f32_16x16x32_bf16 v[136:139], v[40:43], v[184:187], v[136:139]\n\tds_read_b64 v[120:121], %[tp] offset:1600\n\tds_read_b64 v[122:123], %[tp] offset:1608\n\tv_cvt_pk_bf16_f32 v194, v219, v205\n\tv_mfma_f32_16x16x32_bf16 v[140:143], v[72:75], v[184:187], v[140:143]\n\tv_cvt_pk_bf16_f32 v195, v218, v204\n\tds_read_b64 v[124:125], %[tp] offset:1664\n\tds_read_b64 v[126:127], %[tp] offset:1672\n\tv_mfma_f32_16x16x32_bf16 v[136:139], v[36:39], v[164:167], v[136:139]\n\tds_read_b64 v[128:129], %[tp] offset:1728\n\tds_read_b64 v[130:131], %[tp] offset:1736\n\tv_cvt_pk_bf16_f32 v196, v225, v211\n\tv_mfma_f32_16x16x32_bf16 v[140:143], v[68:71], v[164:167], v[140:143]\n\tv_cvt_pk_bf16_f32 v197, v224, v210\n\ts_waitcnt lgkmcnt(6)\n\ts_nop 0\n\tv_mfma_f32_16x16x32_bf16 v[136:139], v[32:35], v[168:171], v[136:139]\n\tv_cvt_pk_bf16_f32 v198, v223, v209\n\tv_mfma_f32_16x16x32_bf16 v[140:143], v[64:67], v[168:171], v[140:143]\n\tv_cvt_pk_bf16_f32 v199, v222, v208\n\tv_mfma_f32_16x16x32_bf16 v[136:139], v[28:31], v[172:175], v[136:139]\n\tv_cvt_pk_bf16_f32 v200, v229, v215\n\tv_mfma_f32_16x16x32_bf16 v[140:143], v[60:63], v[172:175], v[140:143]\n\tds_read_b128 v[188:191], %[bb] offset:1008\n\tv_cvt_pk_bf16_f32 v201, v228, v214\n\tv_mfma_f32_16x16x32_bf16 v[136:139], v[24:27], v[176:179], v[136:139]\n\tv_cvt_pk_bf16_f32 v202, v227, v213\n\tv_mfma_f32_16x16x32_bf16 v[140:143], v[56:59], v[176:179], v[140:143]\n\tv_cvt_pk_bf16_f32 v203, v226, v212\n\tv_mfma_f32_16x16x32_bf16 v[144:147], v[88:91], v[176:179], 0\n\tv_mfma_f32_16x16x32_bf16 v[140:143], v[192:195], v[92:95], v[140:143]\n\tv_mfma_f32_16x16x32_bf16 v[144:147], v[192:195], v[96:99], v[144:147]\n\tv_mfma_f32_16x16x32_bf16 v[148:151], v[192:195], v[100:103], v[148:151]\n\tv_mfma_f32_16x16x32_bf16 v[140:143], v[196:199], v[104:107], v[140:143]\n\tds_write_b128 %[yb], v[136:139] offset:1152\n\tds_read_b128 v[180:183], %[bb] offset:1088\n\tv_mfma_f32_16x16x32_bf16 v[144:147], v[196:199], v[108:111], v[144:147]\n\tv_mfma_f32_16x16x32_bf16 v[148:151], v[196:199], v[112:115], v[148:151]\n\tds_write_b8 %[ctc], %[one] offset:64\n\tds_read_b128 v[184:187], %[bb] offset:1152\n\tv_mfma_f32_16x16x32_bf16 v[148:151], v[200:203], v[116:119], v[148:151]\n\ts_waitcnt lgkmcnt(4)\n\ts_nop 0\n\tv_mfma_f32_16x16x32_bf16 v[152:155], v[200:203], v[120:123], 0\n\tds_read_b128 v[164:167], %[bb] offset:1216\n\tv_mfma_f32_16x16x32_bf16 v[156:159], v[200:203], v[124:127], 0\n\tv_mfma_f32_16x16x32_bf16 v[160:163], v[200:203], v[128:131], 0\n\tds_read_b128 v[168:171], %[bb] offset:1280\n\tv_mfma_f32_16x16x32_bf16 v[204:207], v[188:191], v[4:7], 0\n\tv_mfma_f32_16x16x32_bf16 v[218:221], v[176:179], v[4:7], 0\n\tds_read_b128 v[172:175], %[bb] offset:1344\n\tv_mfma_f32_16x16x32_bf16 v[208:211], v[188:191], v[8:11], 0\n\tv_mfma_f32_16x16x32_bf16 v[222:225], v[176:179], v[8:11], 0\n\tv_mfma_f32_16x16x32_bf16 v[212:215], v[188:191], v[12:15], 0\n\tv_mfma_f32_16x16x32_bf16 v[226:229], v[176:179], v[16:19], 0\n\tv_mfma_f32_16x16x32_bf16 v[152:155], v[20:23], v[176:179], v[152:155]\n\ts_waitcnt lgkmcnt(0)\n\ts_nop 0\n\tv_mfma_f32_16x16x32_bf16 v[140:143], v[52:55], v[180:183], v[140:143]\n\tds_read_b128 v[176:179], %[bb] offset:1408\n\tv_cvt_pk_bf16_f32 v192, v221, v207\n\tv_mfma_f32_16x16x32_bf16 v[144:147], v[84:87], v[180:183], v[144:147]\n\tds_read_b128 v[180:183], %[bb] offset:1472\n\tv_cvt_pk_bf16_f32 v193, v220, v206\n\tv_mfma_f32_16x16x32_bf16 v[140:143], v[48:51], v[184:187], v[140:143]\n\tv_cvt_pk_bf16_f32 v194, v219, v205\n\tv_mfma_f32_16x16x32_bf16 v[144:147], v[80:83], v[184:187], v[144:147]\n\tds_read_b128 v[184:187], %[bb] offset:1536\n\tv_cvt_pk_bf16_f32 v195, v218, v204\n\tv_mfma_f32_16x16x32_bf16 v[140:143], v[44:47], v[164:167], v[140:143]\n\tv_cvt_pk_bf16_f32 v196, v225, v211\n\tv_mfma_f32_16x16x32_bf16 v[144:147], v[76:79], v[164:167], v[144:147]\n\tv_cvt_pk_bf16_f32 v197, v224, v210\n\tv_mfma_f32_16x16x32_bf16 v[140:143], v[40:43], v[168:171], v[140:143]\n\tv_cvt_pk_bf16_f32 v198, v223, v209\n\tv_mfma_f32_16x16x32_bf16 v[144:147], v[72:75], v[168:171], v[144:147]\n\tv_cvt_pk_bf16_f32 v199, v222, v208\n\tv_mfma_f32_16x16x32_bf16 v[140:143], v[36:39], v[172:175], v[140:143]\n\tv_cvt_pk_bf16_f32 v200, v229, v215\n\tv_mfma_f32_16x16x32_bf16 v[144:147], v[68:71], v[172:175], v[144:147]\n\ts_waitcnt lgkmcnt(0)\n\ts_nop 0\n\tv_mfma_f32_16x16x32_bf16 v[140:143], v[32:35], v[176:179], v[140:143]\n\tv_cvt_pk_bf16_f32 v201, v228, v214\n\tv_mfma_f32_16x16x32_bf16 v[144:147], v[64:67], v[176:179], v[144:147]\n\tv_cvt_pk_bf16_f32 v202, v227, v213\n\tv_mfma_f32_16x16x32_bf16 v[140:143], v[28:31], v[180:183], v[140:143]\n\tds_read_b128 v[188:191], %[bb] offset:1520\n\tv_cvt_pk_bf16_f32 v203, v226, v212\n\tv_mfma_f32_16x16x32_bf16 v[144:147], v[60:63], v[180:183], v[144:147]\n\tv_mfma_f32_16x16x32_bf16 v[140:143], v[24:27], v[184:187], v[140:143]\n\tv_mfma_f32_16x16x32_bf16 v[144:147], v[56:59], v[184:187], v[144:147]\n\tv_mfma_f32_16x16x32_bf16 v[148:151], v[88:91], v[184:187], v[148:151]\n\tv_mfma_f32_16x16x32_bf16 v[144:147], v[192:195], v[92:95], v[144:147]\n\tv_mfma_f32_16x16x32_bf16 v[148:151], v[192:195], v[96:99], v[148:151]\n\tv_mfma_f32_16x16x32_bf16 v[152:155], v[192:195], v[100:103], v[152:155]\n\tds_read_b128 v[164:167], %[bb] offset:1600\n\tv_mfma_f32_16x16x32_bf16 v[144:147], v[196:199], v[104:107], v[144:147]\n\tv_mfma_f32_16x16x32_bf16 v[148:151], v[196:199], v[108:111], v[148:151]\n\tds_write_b128 %[yb], v[140:143] offset:2304\n\tv_mfma_f32_16x16x32_bf16 v[152:155], v[196:199], v[112:115], v[152:155]\n\tds_read_b128 v[168:171], %[bb] offset:1664\n\tv_mfma_f32_16x16x32_bf16 v[152:155], v[200:203], v[116:119], v[152:155]\n\tds_write_b8 %[ctc], %[one] offset:128\n\tds_read_b128 v[172:175], %[bb] offset:1728\n\tv_mfma_f32_16x16x32_bf16 v[156:159], v[200:203], v[120:123], v[156:159]\n\tv_mfma_f32_16x16x32_bf16 v[160:163], v[200:203], v[124:127], v[160:163]\n\ts_waitcnt lgkmcnt(3)\n\ts_nop 0\n\tv_mfma_f32_16x16x32_bf16 v[204:207], v[188:191], v[4:7], 0\n\tds_read_b128 v[176:179], %[bb] offset:1792\n\tv_mfma_f32_16x16x32_bf16 v[218:221], v[184:187], v[4:7], 0\n\tds_read_b128 v[180:183], %[bb] offset:1856\n\tv_mfma_f32_16x16x32_bf16 v[208:211], v[188:191], v[8:11], 0\n\tv_mfma_f32_16x16x32_bf16 v[222:225], v[184:187], v[8:11], 0\n\tv_mfma_f32_16x16x32_bf16 v[212:215], v[188:191], v[12:15], 0\n\tv_mfma_f32_16x16x32_bf16 v[226:229], v[184:187], v[16:19], 0\n\tv_mfma_f32_16x16x32_bf16 v[156:159], v[20:23], v[184:187], v[156:159]\n\tds_read_b128 v[184:187], %[bb] offset:1920\n\tv_mfma_f32_16x16x32_bf16 v[144:147], v[52:55], v[164:167], v[144:147]\n\tv_cvt_pk_bf16_f32 v192, v221, v207\n\tv_mfma_f32_16x16x32_bf16 v[148:151], v[84:87], v[164:167], v[148:151]\n\ts_waitcnt lgkmcnt(1)\n\ts_nop 0\n\tv_mfma_f32_16x16x32_bf16 v[144:147], v[48:51], v[168:171], v[144:147]\n\tds_read_b128 v[164:167], %[bb] offset:1984\n\tv_cvt_pk_bf16_f32 v193, v220, v206\n\tv_mfma_f32_16x16x32_bf16 v[148:151], v[80:83], v[168:171], v[148:151]\n\tds_read_b128 v[168:171], %[bb] offset:2048\n\tv_cvt_pk_bf16_f32 v194, v219, v205\n\tv_mfma_f32_16x16x32_bf16 v[144:147], v[44:47], v[172:175], v[144:147]\n\tds_read_b128 v[188:191], %[bb] offset:2032\n\tv_cvt_pk_bf16_f32 v195, v218, v204\n\tv_mfma_f32_16x16x32_bf16 v[148:151], v[76:79], v[172:175], v[148:151]\n\tv_cvt_pk_bf16_f32 v196, v225, v211\n\tv_mfma_f32_16x16x32_bf16 v[144:147], v[40:43], v[176:179], v[144:147]\n\tv_cvt_pk_bf16_f32 v197, v224, v210\n\tv_mfma_f32_16x16x32_bf16 v[148:151], v[72:75], v[176:179], v[148:151]\n\tv_cvt_pk_bf16_f32 v198, v223, v209\n\tv_mfma_f32_16x16x32_bf16 v[144:147], v[36:39], v[180:183], v[144:147]\n\tv_cvt_pk_bf16_f32 v199, v222, v208\n\tv_mfma_f32_16x16x32_bf16 v[148:151], v[68:71], v[180:183], v[148:151]\n\ts_waitcnt lgkmcnt(0)\n\ts_nop 0\n\tv_mfma_f32_16x16x32_bf16 v[144:147], v[32:35], v[184:187], v[144:147]\n\tv_cvt_pk_bf16_f32 v200, v229, v215\n\tv_mfma_f32_16x16x32_bf16 v[148:151], v[64:67], v[184:187], v[148:151]\n\tv_cvt_pk_bf16_f32 v201, v228, v214\n\tv_mfma_f32_16x16x32_bf16 v[144:147], v[28:31], v[164:167], v[144:147]\n\tv_cvt_pk_bf16_f32 v202, v227, v213\n\tv_mfma_f32_16x16x32_bf16 v[148:151], v[60:63], v[164:167], v[148:151]\n\tv_cvt_pk_bf16_f32 v203, v226, v212\n\tv_mfma_f32_16x16x32_bf16 v[144:147], v[24:27], v[168:171], v[144:147]\n\tv_mfma_f32_16x16x32_bf16 v[148:151], v[56:59], v[168:171], v[148:151]\n\tv_mfma_f32_16x16x32_bf16 v[152:155], v[88:91], v[168:171], v[152:155]\n\tv_mfma_f32_16x16x32_bf16 v[204:207], v[188:191], v[4:7], 0\n\tv_mfma_f32_16x16x32_bf16 v[218:221], v[168:171], v[4:7], 0\n\tv_mfma_f32_16x16x32_bf16 v[208:211], v[188:191], v[8:11], 0\n\tv_mfma_f32_16x16x32_bf16 v[222:225], v[168:171], v[8:11], 0\n\tv_mfma_f32_16x16x32_bf16 v[212:215], v[188:191], v[12:15], 0\n\tv_mfma_f32_16x16x32_bf16 v[226:229], v[168:171], v[16:19], 0\n\tds_write_b128 %[yb], v[144:147] offset:3456\n\tv_mfma_f32_16x16x32_bf16 v[160:163], v[20:23], v[168:171], v[160:163]\n\tds_read_b32 v216, %[ctl] offset:20\n\tv_mfma_f32_16x16x32_bf16 v[148:151], v[192:195], v[92:95], v[148:151]\n\tds_write_b8 %[ctc], %[one] offset:192\n\tv_mfma_f32_16x16x32_bf16 v[152:155], v[192:195], v[96:99], v[152:155]\n\tv_mfma_f32_16x16x32_bf16 v[156:159], v[192:195], v[100:103], v[156:159]\n\tv_mfma_f32_16x16x32_bf16 v[148:151], v[196:199], v[104:107], v[148:151]\n\tv_cvt_pk_bf16_f32 v192, v221, v207\n\tv_mfma_f32_16x16x32_bf16 v[152:155], v[196:199], v[108:111], v[152:155]\n\tv_cvt_pk_bf16_f32 v193, v220, v206\n\tv_mfma_f32_16x16x32_bf16 v[156:159], v[196:199], v[112:115], v[156:159]\n\tv_cvt_pk_bf16_f32 v194, v219, v205\n\tv_mfma_f32_16x16x32_bf16 v[156:159], v[200:203], v[116:119], v[156:159]\n\tv_cvt_pk_bf16_f32 v195, v218, v204\n\tv_mfma_f32_16x16x32_bf16 v[160:163], v[200:203], v[120:123], v[160:163]\n\ts_waitcnt lgkmcnt(0)\n\ts_nop 0\n\tv_readfirstlane_b32 %[s], v216\n\ts_mov_b32 %[c], 0x400000\n\ts_cmp_ge_u32 %[s], 2\n\ts_cbranch_scc1 2f\n\t1:\n\tds_read_b32 v216, %[ctl] offset:20\n\ts_waitcnt lgkmcnt(0)\n\tv_readfirstlane_b32 %[s], v216\n\ts_cmp_ge_u32 %[s], 2\n\ts_cbranch_scc1 2f\n\ts_sleep 1\n\ts_sub_u32 %[c], %[c], 1\n\ts_cbranch_scc0 1b\n\t.p2align 3\n\t2:\n\tv_mfma_f32_16x16x32_bf16 v[152:155], v[192:195], v[92:95], v[152:155]\n\tds_read_b128 v[172:175], %[bb] offset:2112\n\tv_cvt_pk_bf16_f32 v196, v225, v211\n\tv_mfma_f32_16x16x32_bf16 v[156:159], v[192:195], v[96:99], v[156:159]\n\tds_read_b128 v[176:179], %[bb] offset:2176\n\tv_cvt_pk_bf16_f32 v197, v224, v210\n\tv_mfma_f32_16x16x32_bf16 v[160:163], v[192:195], v[100:103], v[160:163]\n\tv_cvt_pk_bf16_f32 v198, v223, v209\n\tds_read_b128 v[180:183], %[bb] offset:2240\n\tv_cvt_pk_bf16_f32 v199, v222, v208\n\ts_nop 1\n\ts_nop 0\n\tv_mfma_f32_16x16x32_bf16 v[152:155], v[196:199], v[104:107], v[152:155]\n\tds_read_b128 v[184:187], %[bb] offset:2304\n\tv_cvt_pk_bf16_f32 v200, v229, v215\n\tv_mfma_f32_16x16x32_bf16 v[156:159], v[196:199], v[108:111], v[156:159]\n\tv_cvt_pk_bf16_f32 v201, v228, v214\n\tds_read_b128 v[164:167], %[bb] offset:2368\n\tv_mfma_f32_16x16x32_bf16 v[160:163], v[196:199], v[112:115], v[160:163]\n\tv_cvt_pk_bf16_f32 v202, v227, v213\n\tds_read_b128 v[168:171], %[bb] offset:2432\n\tv_cvt_pk_bf16_f32 v203, v226, v212\n\ts_nop 1\n\ts_nop 0\n\tv_mfma_f32_16x16x32_bf16 v[160:163], v[200:203], v[116:119], v[160:163]\n\ts_waitcnt lgkmcnt(3)\n\ts_nop 0\n\tv_mfma_f32_16x16x32_bf16 v[148:151], v[52:55], v[172:175], v[148:151]\n\tv_mfma_f32_16x16x32_bf16 v[152:155], v[84:87], v[172:175], v[152:155]\n\tds_read_b128 v[172:175], %[bb] offset:2496\n\tv_mfma_f32_16x16x32_bf16 v[148:151], v[48:51], v[176:179], v[148:151]\n\tv_mfma_f32_16x16x32_bf16 v[152:155], v[80:83], v[176:179], v[152:155]\n\tds_read_b128 v[176:179], %[bb] offset:2560\n\tv_mfma_f32_16x16x32_bf16 v[148:151], v[44:47], v[180:183], v[148:151]\n\tds_read_b128 v[188:191], %[bb] offset:2544\n\tv_mfma_f32_16x16x32_bf16 v[152:155], v[76:79], v[180:183], v[152:155]\n\ts_waitcnt lgkmcnt(2)\n\ts_nop 0\n\tv_mfma_f32_16x16x32_bf16 v[148:151], v[40:43], v[184:187], v[148:151]\n\tv_mfma_f32_16x16x32_bf16 v[152:155], v[72:75], v[184:187], v[152:155]\n\tds_read_b128 v[180:183], %[bb] offset:2624\n\tv_mfma_f32_16x16x32_bf16 v[148:151], v[36:39], v[164:167], v[148:151]\n\tv_mfma_f32_16x16x32_bf16 v[152:155], v[68:71], v[164:167], v[152:155]\n\tds_read_b128 v[184:187], %[bb] offset:2688\n\tv_mfma_f32_16x16x32_bf16 v[148:151], v[32:35], v[168:171], v[148:151]\n\tv_mfma_f32_16x16x32_bf16 v[152:155], v[64:67], v[168:171], v[152:155]\n\tds_read_b128 v[164:167], %[bb] offset:2752\n\tv_mfma_f32_16x16x32_bf16 v[148:151], v[28:31], v[172:175], v[148:151]\n\tv_mfma_f32_16x16x32_bf16 v[152:155], v[60:63], v[172:175], v[152:155]\n\ts_waitcnt lgkmcnt(1)\n\ts_nop 0\n\tv_mfma_f32_16x16x32_bf16 v[148:151], v[24:27], v[176:179], v[148:151]\n\tds_read_b128 v[168:171], %[bb] offset:2816\n\tv_mfma_f32_16x16x32_bf16 v[152:155], v[56:59], v[176:179], v[152:155]\n\tds_read_b128 v[172:175], %[bb] offset:2880\n\tv_mfma_f32_16x16x32_bf16 v[156:159], v[88:91], v[176:179], v[156:159]\n\tv_mfma_f32_16x16x32_bf16 v[204:207], v[188:191], v[4:7], 0\n\tv_mfma_f32_16x16x32_bf16 v[218:221], v[176:179], v[4:7], 0\n\tv_mfma_f32_16x16x32_bf16 v[208:211], v[188:191], v[8:11], 0\n\tv_mfma_f32_16x16x32_bf16 v[222:225], v[176:179], v[8:11], 0\n\tds_write_b128 %[yb], v[148:151] offset:4608\n\tv_mfma_f32_16x16x32_bf16 v[152:155], v[52:55], v[180:183], v[152:155]\n\tds_read_b128 v[176:179], %[bb] offset:2944\n\tv_mfma_f32_16x16x32_bf16 v[156:159], v[84:87], v[180:183], v[156:159]\n\tds_write_b8 %[ctc], %[one] offset:256\n\tds_read_b128 v[180:183], %[bb] offset:3008\n\tv_mfma_f32_16x16x32_bf16 v[152:155], v[48:51], v[184:187], v[152:155]\n\tds_read_b128 v[116:119], %[bb] offset:3072\n\tv_cvt_pk_bf16_f32 v192, v221, v207\n\tv_mfma_f32_16x16x32_bf16 v[156:159], v[80:83], v[184:187], v[156:159]\n\ts_waitcnt lgkmcnt(4)\n\ts_nop 0\n\tv_mfma_f32_16x16x32_bf16 v[152:155], v[44:47], v[164:167], v[152:155]\n\tds_read_b32 v216, %[ctl] offset:24\n\tv_cvt_pk_bf16_f32 v193, v220, v206\n\tv_mfma_f32_16x16x32_bf16 v[156:159], v[76:79], v[164:167], v[156:159]\n\tv_cvt_pk_bf16_f32 v194, v219, v205\n\tv_mfma_f32_16x16x32_bf16 v[152:155], v[40:43], v[168:171], v[152:155]\n\tv_cvt_pk_bf16_f32 v195, v218, v204\n\tv_mfma_f32_16x16x32_bf16 v[156:159], v[72:75], v[168:171], v[156:159]\n\tv_cvt_pk_bf16_f32 v196, v225, v211\n\tv_mfma_f32_16x16x32_bf16 v[152:155], v[36:39], v[172:175], v[152:155]\n\tv_cvt_pk_bf16_f32 v197, v224, v210\n\tv_mfma_f32_16x16x32_bf16 v[156:159], v[68:71], v[172:175], v[156:159]\n\ts_waitcnt lgkmcnt(0)\n\ts_nop 0\n\tv_mfma_f32_16x16x32_bf16 v[152:155], v[32:35], v[176:179], v[152:155]\n\tv_cvt_pk_bf16_f32 v198, v223, v209\n\tv_mfma_f32_16x16x32_bf16 v[156:159], v[64:67], v[176:179], v[156:159]\n\tv_readfirstlane_b32 %[s], v216\n\ts_mov_b32 %[c], 0x400000\n\ts_cmp_ge_u32 %[s], 1\n\ts_cbranch_scc1 2f\n\t1:\n\tds_read_b32 v216, %[ctl] offset:24\n\ts_waitcnt lgkmcnt(0)\n\tv_readfirstlane_b32 %[s], v216\n\ts_cmp_ge_u32 %[s], 1\n\ts_cbranch_scc1 2f\n\ts_sleep 1\n\ts_sub_u32 %[c], %[c], 1\n\ts_cbranch_scc0 1b\n\t.p2align 3\n\t2:\n\tv_mfma_f32_16x16x32_bf16 v[152:155], v[28:31], v[180:183], v[152:155]\n\tds_read_b128 v[120:123], %[bb] offset:3136\n\tv_cvt_pk_bf16_f32 v199, v222, v208\n\tv_mfma_f32_16x16x32_bf16 v[156:159], v[60:63], v[180:183], v[156:159]\n\tds_read_b128 v[124:127], %[bb] offset:3200\n\tv_mfma_f32_16x16x32_bf16 v[152:155], v[24:27], v[116:119], v[152:155]\n\tds_read_b128 v[128:131], %[bb] offset:3264\n\tv_mfma_f32_16x16x32_bf16 v[156:159], v[56:59], v[116:119], v[156:159]\n\tds_read_b128 v[132:135], %[bb] offset:3328\n\tv_mfma_f32_16x16x32_bf16 v[160:163], v[88:91], v[116:119], v[160:163]\n\tds_read_b128 v[136:139], %[bb] offset:3392\n\tv_mfma_f32_16x16x32_bf16 v[156:159], v[192:195], v[92:95], v[156:159]\n\tds_read_b128 v[140:143], %[bb] offset:3456\n\tv_mfma_f32_16x16x32_bf16 v[156:159], v[196:199], v[104:107], v[156:159]\n\ts_waitcnt lgkmcnt(3)\n\ts_nop 0\n\tv_mfma_f32_16x16x32_bf16 v[156:159], v[52:55], v[120:123], v[156:159]\n\tds_write_b128 %[yb], v[152:155] offset:5760\n\tv_mfma_f32_16x16x32_bf16 v[156:159], v[48:51], v[124:127], v[156:159]\n\tds_read_b128 v[144:147], %[bb] offset:3520\n\tv_mfma_f32_16x16x32_bf16 v[156:159], v[44:47], v[128:131], v[156:159]\n\ts_waitcnt lgkmcnt(2)\n\ts_nop 0\n\tv_mfma_f32_16x16x32_bf16 v[156:159], v[40:43], v[132:135], v[156:159]\n\tds_write_b8 %[ctc], %[one] offset:320\n\tds_read_b128 v[200:203], %[bb] offset:3584\n\tv_mfma_f32_16x16x32_bf16 v[156:159], v[36:39], v[136:139], v[156:159]\n\tds_read_b128 v[188:191], %[bb] offset:3056\n\tglobal_load_dwordx2 v[240:241], %[so], %[ws] sc1\n\tv_mfma_f32_16x16x32_bf16 v[156:159], v[32:35], v[140:143], v[156:159]\n\ts_waitcnt lgkmcnt(3)\n\ts_nop 0\n\tv_mfma_f32_16x16x32_bf16 v[156:159], v[28:31], v[144:147], v[156:159]\n\ts_waitcnt lgkmcnt(1)\n\ts_nop 0\n\tv_mfma_f32_16x16x32_bf16 v[156:159], v[24:27], v[200:203], v[156:159]\n\ts_waitcnt lgkmcnt(0)\n\ts_nop 0\n\tv_mfma_f32_16x16x32_bf16 v[204:207], v[188:191], v[4:7], 0\n\tds_read_b32 v216, %[ctl] offset:28\n\tv_mfma_f32_16x16x32_bf16 v[218:221], v[116:119], v[4:7], 0\n\tv_mfma_f32_16x16x32_bf16 v[208:211], v[188:191], v[8:11], 0\n\tv_mfma_f32_16x16x32_bf16 v[222:225], v[116:119], v[8:11], 0\n\tv_mfma_f32_16x16x32_bf16 v[160:163], v[192:195], v[96:99], v[160:163]\n\tds_write_b128 %[yb], v[156:159] offset:6912\n\tv_mfma_f32_16x16x32_bf16 v[160:163], v[196:199], v[108:111], v[160:163]\n\tv_mfma_f32_16x16x32_bf16 v[160:163], v[84:87], v[120:123], v[160:163]\n\tds_write_b8 %[ctc], %[one] offset:384\n\tv_mfma_f32_16x16x32_bf16 v[160:163], v[80:83], v[124:127], v[160:163]\n\tv_cvt_pk_bf16_f32 v192, v221, v207\n\tv_mfma_f32_16x16x32_bf16 v[160:163], v[76:79], v[128:131], v[160:163]\n\ts_waitcnt lgkmcnt(2)\n\ts_nop 0\n\tv_readfirstlane_b32 %[s], v216\n\ts_mov_b32 %[c], 0x400000\n\ts_cmp_ge_u32 %[s], 1\n\ts_cbranch_scc1 2f\n\t1:\n\tds_read_b32 v216, %[ctl] offset:28\n\ts_waitcnt lgkmcnt(0)\n\tv_readfirstlane_b32 %[s], v216\n\ts_cmp_ge_u32 %[s], 1\n\ts_cbranch_scc1 2f\n\ts_sleep 1\n\ts_sub_u32 %[c], %[c], 1\n\ts_cbranch_scc0 1b\n\t.p2align 3\n\t2:\n\tv_mfma_f32_16x16x32_bf16 v[160:163], v[72:75], v[132:135], v[160:163]\n\tds_read_b128 v[184:187], %[bb] offset:3648\n\tv_cvt_pk_bf16_f32 v193, v220, v206\n\tv_mfma_f32_16x16x32_bf16 v[160:163], v[68:71], v[136:139], v[160:163]\n\tds_read_b128 v[164:167], %[bb] offset:3712\n\tv_cvt_pk_bf16_f32 v194, v219, v205\n\tv_mfma_f32_16x16x32_bf16 v[160:163], v[64:67], v[140:143], v[160:163]\n\tds_read_b128 v[168:171], %[bb] offset:3776\n\tv_cvt_pk_bf16_f32 v195, v218, v204\n\tv_mfma_f32_16x16x32_bf16 v[160:163], v[60:63], v[144:147], v[160:163]\n\tds_read_b128 v[172:175], %[bb] offset:3840\n\tv_cvt_pk_bf16_f32 v196, v225, v211\n\tv_mfma_f32_16x16x32_bf16 v[160:163], v[56:59], v[200:203], v[160:163]\n\tds_read_b128 v[176:179], %[bb] offset:3904\n\tv_cvt_pk_bf16_f32 v197, v224, v210\n\tv_mfma_f32_16x16x32_bf16 v[160:163], v[192:195], v[92:95], v[160:163]\n\tds_read_b128 v[180:183], %[bb] offset:3968\n\tv_cvt_pk_bf16_f32 v198, v223, v209\n\tv_cvt_pk_bf16_f32 v199, v222, v208\n\ts_nop 1\n\ts_nop 0\n\tv_mfma_f32_16x16x32_bf16 v[160:163], v[196:199], v[104:107], v[160:163]\n\ts_waitcnt lgkmcnt(3)\n\ts_nop 0\n\tv_mfma_f32_16x16x32_bf16 v[160:163], v[52:55], v[184:187], v[160:163]\n\tds_read_b128 v[184:187], %[bb] offset:4032\n\tv_mfma_f32_16x16x32_bf16 v[160:163], v[48:51], v[164:167], v[160:163]\n\tds_read_b128 v[164:167], %[bb] offset:4096\n\tv_mfma_f32_16x16x32_bf16 v[160:163], v[44:47], v[168:171], v[160:163]\n\ts_waitcnt lgkmcnt(2)\n\ts_nop 0\n\tv_mfma_f32_16x16x32_bf16 v[160:163], v[40:43], v[172:175], v[160:163]\n\tv_mfma_f32_16x16x32_bf16 v[160:163], v[36:39], v[176:179], v[160:163]\n\tv_mfma_f32_16x16x32_bf16 v[160:163], v[32:35], v[180:183], v[160:163]\n\ts_waitcnt lgkmcnt(0)\n\ts_nop 0\n\tv_mfma_f32_16x16x32_bf16 v[160:163], v[28:31], v[184:187], v[160:163]\n\tv_mfma_f32_16x16x32_bf16 v[160:163], v[24:27], v[164:167], v[160:163]\n\ts_nop 7\n\tds_write_b128 %[yb], v[160:163] offset:8064\n\tds_write_b8 %[ctc], %[one] offset:448"
#define CONV_CLOBBER "v24", "v25", "v26", "v27", "v28", "v29", "v30", "v31", "v32", "v33", "v34", "v35", "v36", "v37", "v38", "v39", "v40", "v41", "v42", "v43", "v44", "v45", "v46", "v47", "v48", "v49", "v50", "v51", "v52", "v53", "v54", "v55", "v56", "v57", "v58", "v59", "v60", "v61", "v62", "v63", "v64", "v65", "v66", "v67", "v68", "v69", "v70", "v71", "v72", "v73", "v74", "v75", "v76", "v77", "v78", "v79", "v80", "v81", "v82", "v83", "v84", "v85", "v86", "v87", "v88", "v89", "v90", "v91", "v92", "v93", "v94", "v95", "v96", "v97", "v98", "v99", "v100", "v101", "v102", "v103", "v104", "v105", "v106", "v107", "v108", "v109", "v110", "v111", "v112", "v113", "v114", "v115", "v116", "v117", "v118", "v119", "v120", "v121", "v122", "v123", "v124", "v125", "v126", "v127", "v128", "v129", "v130", "v131", "v132", "v133", "v134", "v135", "v136", "v137", "v138", "v139", "v140", "v141", "v142", "v143", "v144", "v145", "v146", "v147", "v148", "v149", "v150", "v151", "v152", "v153", "v154", "v155", "v156", "v157", "v158", "v159", "v160", "v161", "v162", "v163", "v164", "v165", "v166", "v167", "v168", "v169", "v170", "v171", "v172", "v173", "v174", "v175", "v176", "v177", "v178", "v179", "v180", "v181", "v182", "v183", "v184", "v185", "v186", "v187", "v188", "v189", "v190", "v191", "v192", "v193", "v194", "v195", "v196", "v197", "v198", "v199", "v200", "v201", "v202", "v203", "v204", "v205", "v206", "v207", "v208", "v209", "v210", "v211", "v212", "v213", "v214", "v215", "v216", "v217", "v218", "v219", "v220", "v221", "v222", "v223", "v224", "v225", "v226", "v227", "v228", "v229"

typedef __attribute__((address_space(4))) const Params* KArg;
#define ROLE_BEGIN(N) int b, tid; \
    asm volatile("fk_role" #N ":\n\tv_mov_b32 %0, v0" : "=&v"(tid) : "{s2}"(b0), "{v0}"(tid0) : "memory"); \
    asm volatile("s_mov_b32 %0, s2" : "=&s"(b) : "{s2}"(b0)); \
    const int lane = tid & 63, wave = __builtin_amdgcn_readfirstlane(tid >> 6), xcd = b & 7, slot = b >> 3; \
    const Xcd xs = xcd_shares(xcd); \
    const int cw = xcd * 16 + slot + ((112 + xcd * (CB - 32)) & ((15 - slot) >> 31)), chbase = cw * 4, w = xs.w0 + slot - xs.nconv; (void)chbase; (void)w; (void)lane
#define ROLE_ARGS() Params p; SBAR(); \
    asm volatile("s_waitcnt lgkmcnt(0)" : "={s[56:57]}"(p.x), "={s[58:59]}"(p.Are), "={s[60:61]}"(p.Aim), "={s[62:63]}"(p.Bre), "={s[64:65]}"(p.Bim), "={s[66:67]}"(p.Cre), "={s[68:69]}"(p.Cim), "={s[70:71]}"(p.logdt), \
                 "={s[72:73]}"(p.dskip), "={s[74:75]}"(p.wr), "={s[76:77]}"(p.wg), "={s[78:79]}"(p.wu), "={s[80:81]}"(p.wd), "={s[82:83]}"(p.gw), "={s[84:85]}"(p.out), "={s[86:87]}"(p.ws))
#define ROLE_END() do { __builtin_amdgcn_s_waitcnt(0x0070); asm volatile("s_endpgm" ::: "memory"); } while (0)
#define MF(A_, B_, C_) __builtin_amdgcn_mfma_f32_16x16x32_bf16(A_, B_, C_, 0, 0, 0)

#define FFN_PRE() u32 gof = w * 128 + ((wave >> 1) * 8 + (lane >> 3)) * (F * 2) + ((lane & 7) ^ (((lane >> 3) & 2) | ((wave & 2) << 1))) * 16; asm volatile("" : "+v"(gof)); ROLE_ARGS()
#define FFN_FILL() \
    { const char* gsrc = (const char*)((wave & 1) ? p.wu : p.wg); \
      _Pragma("unroll") for (int i = 0; i < 18; ++i) { \
          asm volatile("s_mov_b32 m0, %2\n\ts_nop 0\n\tglobal_load_lds_dwordx4 %0, %1" WNT :: "v"(gof), "s"(gsrc + i * (32 * F * 2)), "s"((8 * i + wave) * 1024) : "memory"); \
          if (i == 5 || i == 11) __builtin_amdgcn_s_barrier(); } }
#define FFN_WDFRAG(k, pr) do { LAS char* t_ = ta + (pr) * 64; \
    _Pragma("unroll") for (int e_ = 0; e_ < 8; ++e_) { const int e = (e_ & 1) | ((e_ & 2) << 1) | ((e_ & 4) >> 1); a[k][e] = __builtin_amdgcn_ds_read_tr16_b64_v4i16((LAS s16x4*)(t_ + (e >> 2) * 32 + (e & 3) * 16 * WDP)); } SBAR(); } while (0)
#define FFN_DOWN(k, pr) do { v4f y_ = MF(__builtin_shufflevector(a[k][0], a[k][1], 0, 1, 2, 3, 4, 5, 6, 7), hf[0], ((v4f){0.f, 0.f, 0.f, 0.f})); \
    y_ = MF(__builtin_shufflevector(a[k][4], a[k][5], 0, 1, 2, 3, 4, 5, 6, 7), hf[1], y_); \
    y_ = MF(__builtin_shufflevector(a[k][2], a[k][3], 0, 1, 2, 3, 4, 5, 6, 7), hf[2], y_); \
    ya[k] = MF(__builtin_shufflevector(a[k][6], a[k][7], 0, 1, 2, 3, 4, 5, 6, 7), hf[3], y_); } while (0)
#define FFN_ADDR() const int n = lane & 15, q = lane >> 4; \
    LAS char* ta = smem + (lane >> 2) * WDP + (lane & 3) * 8; \
    LAS char* hrow = smem + FL_H + (n & 7) * 144 + q * 16; \
    LAS char* ha = n < 8 ? hrow : smem + FL_Z + q * 16; \
    LAS char* hb = n < 8 ? smem + FL_Z + q * 16 : hrow; \
    u32 obase = OFF_PART + (4 * (n >> 3) + q) * PP + hline(p.ws, w) + (n & 7) * 16; \
    asm volatile("" : "+v"(ta), "+v"(ha), "+v"(hb), "+v"(obase))
#define FFN_HFRAG() const bf16x8 hf[4] = {*(LAS bf16x8*)(ha), *(LAS bf16x8*)(hb), *(LAS bf16x8*)(ha + 64), *(LAS bf16x8*)(hb + 64)}
#define FFN_PUB(k, pr) __builtin_amdgcn_raw_buffer_store_b128((v4u){cvtpk(ya[k][0], ya[k][1]), tag, cvtpk(ya[k][2], ya[k][3]), tag}, WSR(), obase + (pr) * (8 * PP), 0, 16)

__global__ __launch_bounds__(512, 2) void fused_kernel(Params p_unused) {
    extern __shared__ __attribute__((aligned(16))) char smem_[];
    LAS char* smem = (LAS char*)smem_;
    const KArg kp0 = (KArg)__builtin_amdgcn_kernarg_segment_ptr();
    const int tid0 = threadIdx.x, b0 = blockIdx.x;
    asm volatile("s_load_dwordx16 s[56:71], s[0:1], 0x0\n\ts_load_dwordx16 s[72:87], s[0:1], 0x40\n\tv_readfirstlane_b32 s92, v0\n\t"
                 "s_cmp_ge_u32 s2, 32\n\ts_cbranch_scc1 1f\n\t"
                 "s_getpc_b64 s[90:91]\n\ts_and_b32 s90, s90, 0xffffff00\n\ts_and_b32 s93, s2, -8\n\ts_lshr_b32 s89, s92, 6\n\ts_add_u32 s93, s93, s89\n\ts_min_u32 s93, s93, %3\n\ts_lshl_b32 s93, s93, 10\n\t"
                 "v_and_b32 v1, 63, v0\n\tv_lshlrev_b32 v1, 4, v1\n\tv_add_u32 v1, s93, v1\n\ts_mov_b32 m0, %4\n\ts_nop 0\n\tglobal_load_lds_dwordx4 v1, s[90:91]\n"
                 "1:\n\ts_and_b32 s93, s2, 7\n\ts_cmp_lt_u32 s93, %5\n\ts_cselect_b32 s93, %6, 16\n\ts_lshr_b32 s90, s2, 3\n\t"
                 "s_cmp_lt_u32 s90, s93\n\ts_cbranch_scc0 2f\n\ts_cmp_lt_u32 s92, 256\n\ts_cbranch_scc1 fk_role0\n\ts_branch fk_role1\n"
                 "2:\n\ts_cmp_lt_u32 s90, 27\n\ts_cbranch_scc0 3f\n\ts_cmp_lt_u32 s92, 256\n\ts_cbranch_scc1 fk_role2\n\ts_branch fk_role3\n"
                 "3:\n\ts_cmp_eq_u32 s90, 27\n\ts_cbranch_scc1 fk_role4\n\ts_branch fk_role5"
                 :: "{s[0:1]}"(kp0), "{s2}"(b0), "{v0}"(tid0), "n"(CODE_KB - 1), "n"(LDS_DUMMY), "n"(NBX), "n"(CB) : "memory", "scc", "s89", "s90", "s91", "s92", "s93", "v1");
    const int slot = b0 >> 3, wave = __builtin_amdgcn_readfirstlane(tid0 >> 6);
    if (slot >= 18) {
    if (slot >= 27 || wave >= 4) {
    if (slot < 28) {
    if (slot == 27)
    {
        ROLE_BEGIN(4);
        ROLE_ARGS();
        u32 tagv;
        asm volatile("v_mov_b32 %0, 0\n\tglobal_load_dword %0, %0, %1 sc1\n\ts_waitcnt vmcnt(0)" : "=&v"(tagv) : "s"(p.ws) : "memory");
        const u32 tag = __builtin_amdgcn_readfirstlane(tagv) + 1u;
        if (tid < 128) ((LAS u32*)smem)[tid] = 0;
        u32 ga = (u32)(uintptr_t)(smem + (lane & 15) * 4), gv = (u32)(uintptr_t)(smem + (lane >> 2) * 4), gq_ = (u32)(uintptr_t)(smem + (lane & 3) * 16), zero = (u32)(uintptr_t)smem;
        u32 ro = OFF_RG + hline(p.ws, tid >> 3) + (tid & 7) * 16, ro2 = OFF_RG + hline(p.ws, 64 + (tid >> 3)) + (tid & 7) * 16, so = OFF_SEL + ((lane & 1) * 8 + xcd) * 256, xo = (lane & 3) * 128;
        asm volatile("" : "+v"(ga), "+v"(gv), "+v"(gq_), "+v"(zero), "+v"(ro), "+v"(ro2), "+v"(so), "+v"(xo));
        BAR();
        __builtin_amdgcn_s_sleep(SELWAIT);
        v4u qa, qb;
        u32 c_;
#define POLL_A "global_load_dwordx4 v[20:23], %[ro], %[ws] sc1\n\tglobal_load_dwordx4 v[24:27], %[r2], %[ws] sc1\n\t"
#define POLL_B "global_load_dwordx4 v[28:31], %[ro], %[ws] sc1\n\tglobal_load_dwordx4 v[32:35], %[r2], %[ws] sc1\n\t"
        asm volatile("s_mov_b32 %[c], 0x40000\n\t" POLL_A "s_sleep 8\n\t" POLL_B
                     "1:\n\ts_waitcnt vmcnt(2)\n\tv_cmpx_eq_u32 %[tag], v21\n\tv_cmpx_eq_u32 %[tag], v23\n\tv_cmpx_eq_u32 %[tag], v25\n\tv_cmpx_eq_u32 %[tag], v27\n\t"
                     "s_cmp_eq_u64 exec, -1\n\ts_mov_b64 exec, -1\n\ts_cbranch_scc1 3f\n\t" POLL_A
                     "s_waitcnt vmcnt(2)\n\tv_cmpx_eq_u32 %[tag], v29\n\tv_cmpx_eq_u32 %[tag], v31\n\tv_cmpx_eq_u32 %[tag], v33\n\tv_cmpx_eq_u32 %[tag], v35\n\t"
                     "s_cmp_eq_u64 exec, -1\n\ts_mov_b64 exec, -1\n\ts_cbranch_scc1 2f\n\t" POLL_B
                     "s_sub_u32 %[c], %[c], 1\n\ts_cbranch_scc0 1b\n\ts_waitcnt vmcnt(0)\n"
                     "2:\n\tv_mov_b32 v36, v28\n\tv_mov_b32 v37, v30\n\tv_mov_b32 v38, v32\n\tv_mov_b32 v39, v34\n\ts_branch 4f\n"
                     "3:\n\tv_mov_b32 v36, v20\n\tv_mov_b32 v37, v22\n\tv_mov_b32 v38, v24\n\tv_mov_b32 v39, v26\n"
                     "4:"
                     : "=&{v[36:39]}"(qa), [c] "=&s"(c_) : [ro] "v"(ro), [r2] "v"(ro2), [ws] "s"(p.ws), [tag] "s"(tag)
                     : "memory", "scc", "vcc", "v20", "v21", "v22", "v23", "v24", "v25", "v26", "v27", "v28", "v29", "v30", "v31", "v32", "v33", "v34", "v35");
        asm volatile("" : "=&{v[20:23]}"(qb));
        v4u qc, qd, qe;
        asm volatile("" : "=&{v[24:27]}"(qc), "=&{v[28:31]}"(qd), "=&{v[32:35]}"(qe));
        u32 rank, tau, cnt;
        u64 m0_, m1_, m2_, m3_;
        {
            u32 v, nv; v4u q4;
            asm volatile("ds_max_u32 %0, %1\n\ts_waitcnt lgkmcnt(0)\n\ts_barrier" :: "v"(ga), "v"(max(max(qa.x, qa.y), max(qa.z, qa.w))) : "memory");
#define RANK16(off) asm volatile("ds_read_b32 %[v], %[gv] offset:" #off "\n\tds_read_b128 v[40:43], %[gq] offset:" #off "\n\tds_read_b32 %[n], %[z] offset:64\n\tv_mov_b32 %[r], 0\n\ts_waitcnt lgkmcnt(0)\n\t" \
                         "v_cmp_gt_u32 %[a], v40, %[v]\n\tv_cmp_gt_u32 %[b], v41, %[v]\n\tv_cmp_gt_u32 %[c], v42, %[v]\n\tv_cmp_gt_u32 %[d], v43, %[v]\n\t" \
                         "v_addc_co_u32 %[r], vcc, 0, %[r], %[a]\n\tv_addc_co_u32 %[r], vcc, 0, %[r], %[b]\n\tv_addc_co_u32 %[r], vcc, 0, %[r], %[c]\n\tv_addc_co_u32 %[r], vcc, 0, %[r], %[d]\n\ts_nop 1\n\t" \
                         "v_add_u32_dpp %[r], %[r], %[r] quad_perm:[1,0,3,2] row_mask:0xf bank_mask:0xf\n\ts_nop 1\n\tv_add_u32_dpp %[r], %[r], %[r] quad_perm:[2,3,0,1] row_mask:0xf bank_mask:0xf\n\ts_nop 0" \
                         : [v] "=&v"(v), [r] "=&v"(rank), [n] "=&v"(nv), "=&{v[40:43]}"(q4), [a] "=&s"(m0_), [b] "=&s"(m1_), [c] "=&s"(m2_), [d] "=&s"(m3_) : [gv] "v"(gv), [gq] "v"(gq_), [z] "v"(zero) : "memory", "vcc")
            RANK16(0);
            tau = (u32)__builtin_amdgcn_readlane((int)v, (int)__builtin_ctzll(__ballot(rank == 7)));
            u32 pos;
            asm volatile("s_nop 1\n\tv_mov_b32 %[n], 0\n\tv_cmp_le_u32 %[a], %[t], v36\n\tv_cmp_le_u32 %[b], %[t], v37\n\tv_cmp_le_u32 %[c], %[t], v38\n\tv_cmp_le_u32 %[d], %[t], v39\n\t"
                         "v_addc_co_u32 %[n], vcc, 0, %[n], %[a]\n\tv_addc_co_u32 %[n], vcc, 0, %[n], %[b]\n\tv_addc_co_u32 %[n], vcc, 0, %[n], %[c]\n\tv_addc_co_u32 %[n], vcc, 0, %[n], %[d]\n\t"
                         "v_lshlrev_b32 %[n], 2, %[n]\n\tv_cmpx_ne_u32 0, %[n]\n\tds_add_rtn_u32 %[p], %[z], %[n] offset:64\n\ts_waitcnt lgkmcnt(0)\n\t"
                         "s_mov_b64 exec, %[a]\n\tds_write_b32 %[p], v36 offset:256\n\tv_add_u32 %[p], 4, %[p]\n\t"
                         "s_mov_b64 exec, %[b]\n\tds_write_b32 %[p], v37 offset:256\n\tv_add_u32 %[p], 4, %[p]\n\t"
                         "s_mov_b64 exec, %[c]\n\tds_write_b32 %[p], v38 offset:256\n\tv_add_u32 %[p], 4, %[p]\n\t"
                         "s_mov_b64 exec, %[d]\n\tds_write_b32 %[p], v39 offset:256\n\t"
                         "s_mov_b64 exec, -1\n\ts_waitcnt lgkmcnt(0)\n\ts_barrier"
                         : [n] "=&v"(cnt), [p] "=&v"(pos), [a] "=&s"(m0_), [b] "=&s"(m1_), [c] "=&s"(m2_), [d] "=&s"(m3_) : [t] "s"(tau), [z] "v"(zero), "{v[36:39]}"(qa) : "memory", "vcc");
            if (wave == 0) {
                RANK16(256);
                const int nc = __builtin_amdgcn_readfirstlane((int)nv) >> 2;
                if (__builtin_expect(nc > 16, 0)) {
                    v = (u32)__shfl((int)top8_d(smem, lane, nc), (lane >> 2) & 7);
                    rank = lane >> 2;
                }
                const u64 m8 = __ballot(rank < 8);
                asm volatile("s_and_b64 exec, %[m], %[k2]\n\tglobal_store_dwordx2 %[so], %[d], %[ws] sc1\n\ts_mov_b64 exec, %[m]\n\ts_mov_b32 m0, 0x8000\n\ts_nop 0\n\t"
                             "global_load_lds_dword %[xo], %[x]\n\tglobal_load_lds_dword %[xo], %[x] offset:512\n\ts_and_b64 exec, %[m], %[k1]\n\tglobal_load_lds_dword %[xo], %[x] offset:1024\n\ts_mov_b64 exec, -1"
                             :: [m] "s"(m8), [k2] "s"(0x3333333333333333ull), [k1] "s"(0x1111111111111111ull), [so] "v"(so + rank * 8), [d] "v"((v2u){v, tag}), [ws] "s"(p.ws),
                                [xo] "v"(xo + (~v & 2047u) * 1152u), [x] "s"(p.x) : "memory", "scc");
                asm volatile("s_mov_b64 exec, 1\n\tglobal_store_dwordx2 %0, %1, %2 sc1\n\ts_mov_b64 exec, -1" :: "v"(OFF_CI + (416 + xcd) * 8), "v"((v2u){tag, tag}), "s"(p.ws) : "memory");
            }
        }
        asm volatile("s_waitcnt vmcnt(0)" :: "v"(qb), "v"(qc), "v"(qd), "v"(qe) : "memory");
        if (xcd == 0) {
            const u32 cio = OFF_CI + (tid < 212 ? tid : 0) * 16;
            for (u32 spins = 0;; ++spins) {
                REREAD();
                const v4u q2 = __builtin_amdgcn_raw_buffer_load_b128(WSR(), cio, 0, 16);
                if (__all((q2.y == tag) & (q2.w == tag)) || spins > SPIN_CAP) break;
                __builtin_amdgcn_s_sleep(16);
            }
            BAR();
            if (tid == 0) __hip_atomic_store((GAS u32*)p.ws, tag, RLX_AGENT);
        }
        ROLE_END();
    }
    else
        {
            ROLE_BEGIN(3);
            FFN_PRE();
            const int pw = wave - 4;
            u32 tagv;
            asm volatile("v_mov_b32 %0, 0\n\tglobal_load_dword %0, %0, %1 sc1" : "=&v"(tagv) : "s"(p.ws) : "memory");
            FFN_FILL();
            v2u sel;
            const u32 so = OFF_SEL + xcd * 256 + (2 * pw + (lane & 1)) * 8, l16 = lane * 16;
#define POLL_S "global_load_dwordx2 v[10:11], %[so], %[ws] sc1\n\t"
            asm volatile("s_waitcnt vmcnt(18)" : "+v"(tagv) :: "memory");
            const u32 tag = __builtin_amdgcn_readfirstlane(tagv) + 1u;
            *(LAS v4u*)(smem + FL_Z + min(l16, 256u)) = (v4u){lane >= 16 ? tag : 0u, 0u, 0u, 0u};
            FFN_ADDR();
            const char* dsrc = (const char*)(p.wd + (size_t)(64 * w + 32 * (pw >> 1)) * D);
            const u32 ddst = (pw >> 1) * 32 * WDP + (pw & 1) * 1024;
            u32 dof[19];
#pragma unroll
            for (int i = 0; i < 19; ++i) {
                const u32 id = min(128u * i + 64u * (pw & 1) + lane, 2367u), r0 = id / 74u;
                dof[i] = r0 * 1152u + min(id - 74u * r0, 71u) * 16u;
                asm volatile("" : "+v"(dof[i]));
            }
#define LD_WD(i) asm volatile("s_mov_b32 m0, %2\n\ts_nop 0\n\tglobal_load_lds_dwordx4 %0, %1" WNT :: "v"(dof[i]), "s"(dsrc), "s"(ddst + (i) * 2048) : "memory")
            asm volatile("s_waitcnt vmcnt(12) lgkmcnt(0)\n\ts_barrier\n\ts_waitcnt vmcnt(6)\n\ts_barrier\n\ts_waitcnt vmcnt(2)\n\t" POLL_S "s_waitcnt vmcnt(1)\n\ts_barrier" : "=&{v[10:11]}"(sel) : [so] "v"(so), [ws] "s"(p.ws) : "memory");
            u32 c_, early, mw = ddst;
            asm volatile("s_mov_b32 %[e], 0\n\ts_waitcnt vmcnt(0)\n\tv_cmp_ne_u32 vcc, %[tag], v11\n\ts_cmp_eq_u64 vcc, 0\n\ts_cbranch_scc1 2f\n\t"
                         "s_mov_b32 %[e], 1\n\ts_mov_b32 %[c], 0x80000\n\ts_mov_b32 m0, %[m]\n\ts_add_u32 %[m], %[m], 0x800\n\tglobal_load_lds_dwordx4 %[d0], %[src]" WNT "\n\ts_mov_b32 m0, %[m]\n\ts_add_u32 %[m], %[m], 0x800\n\tglobal_load_lds_dwordx4 %[d1], %[src]" WNT "\n\ts_mov_b32 m0, %[m]\n\ts_add_u32 %[m], %[m], 0x800\n\tglobal_load_lds_dwordx4 %[d2], %[src]" WNT "\n\ts_mov_b32 m0, %[m]\n\ts_add_u32 %[m], %[m], 0x800\n\tglobal_load_lds_dwordx4 %[d3], %[src]" WNT "\n\ts_mov_b32 m0, %[m]\n\ts_add_u32 %[m], %[m], 0x800\n\tglobal_load_lds_dwordx4 %[d4], %[src]" WNT "\n\ts_mov_b32 m0, %[m]\n\ts_add_u32 %[m], %[m], 0x800\n\tglobal_load_lds_dwordx4 %[d5], %[src]" WNT "\n\t"
                         "1:\n\t" POLL_S "s_waitcnt vmcnt(0)\n\tv_cmp_ne_u32 vcc, %[tag], v11\n\ts_cmp_eq_u64 vcc, 0\n\ts_cbranch_scc1 2f\n\ts_sub_u32 %[c], %[c], 1\n\ts_cbranch_scc0 1b\n2:"
                         : "+{v[10:11]}"(sel), [c] "=&s"(c_), [e] "=&s"(early), [m] "+s"(mw) : [so] "v"(so), [ws] "s"(p.ws), [tag] "s"(tag), [src] "s"(dsrc), [d0] "v"(dof[0]), [d1] "v"(dof[1]), [d2] "v"(dof[2]), [d3] "v"(dof[3]), [d4] "v"(dof[4]), [d5] "v"(dof[5]) : "memory", "scc", "vcc");
#pragma unroll
            for (int k = 0; k < 2; ++k) {
                const u32 t = ~(u32)__builtin_amdgcn_readlane((int)sel.x, k) & 2047u;
                const char* xr = (const char*)p.x + t * (D * 2);
                asm volatile("s_mov_b32 m0, %2\n\ts_nop 0\n\tglobal_load_lds_dwordx4 %0, %1\n\ts_mov_b64 exec, 0xff\n\tglobal_load_lds_dwordx4 %0, %1 offset:1024\n\ts_mov_b64 exec, -1"
                             :: "v"(l16), "s"(xr), "s"(FL_XS + (2 * pw + k) * XSP) : "memory");
            }
            asm volatile("s_barrier\n\ts_cmp_eq_u32 %[e], 0\n\ts_cbranch_scc0 1f\n\ts_mov_b32 m0, %[m]\n\ts_add_u32 %[m], %[m], 0x800\n\tglobal_load_lds_dwordx4 %[d0], %[src]" WNT "\n\ts_mov_b32 m0, %[m]\n\ts_add_u32 %[m], %[m], 0x800\n\tglobal_load_lds_dwordx4 %[d1], %[src]" WNT "\n\ts_mov_b32 m0, %[m]\n\ts_add_u32 %[m], %[m], 0x800\n\tglobal_load_lds_dwordx4 %[d2], %[src]" WNT "\n\ts_mov_b32 m0, %[m]\n\ts_add_u32 %[m], %[m], 0x800\n\tglobal_load_lds_dwordx4 %[d3], %[src]" WNT "\n\ts_mov_b32 m0, %[m]\n\ts_add_u32 %[m], %[m], 0x800\n\tglobal_load_lds_dwordx4 %[d4], %[src]" WNT "\n\ts_mov_b32 m0, %[m]\n\ts_add_u32 %[m], %[m], 0x800\n\tglobal_load_lds_dwordx4 %[d5], %[src]" WNT "\n\ts_mov_b32 m0, %[m]\n\ts_add_u32 %[m], %[m], 0x800\n\tglobal_load_lds_dwordx4 %[d6], %[src]" WNT "\n\ts_mov_b32 m0, %[m]\n\ts_add_u32 %[m], %[m], 0x800\n\tglobal_load_lds_dwordx4 %[d7], %[src]" WNT "\n\ts_mov_b32 m0, %[m]\n\ts_add_u32 %[m], %[m], 0x800\n\tglobal_load_lds_dwordx4 %[d8], %[src]" WNT "\n\ts_mov_b32 m0, %[m]\n\ts_add_u32 %[m], %[m], 0x800\n\tglobal_load_lds_dwordx4 %[d9], %[src]" WNT "\n\ts_mov_b32 m0, %[m]\n\ts_add_u32 %[m], %[m], 0x800\n\tglobal_load_lds_dwordx4 %[d10], %[src]" WNT "\n\ts_mov_b32 m0, %[m]\n\ts_add_u32 %[m], %[m], 0x800\n\tglobal_load_lds_dwordx4 %[d11], %[src]" WNT "\n\ts_waitcnt vmcnt(12)\n\ts_branch 2f\n1:\n\ts_mov_b32 m0, %[m]\n\ts_add_u32 %[m], %[m], 0x800\n\tglobal_load_lds_dwordx4 %[d6], %[src]" WNT "\n\ts_mov_b32 m0, %[m]\n\ts_add_u32 %[m], %[m], 0x800\n\tglobal_load_lds_dwordx4 %[d7], %[src]" WNT "\n\ts_mov_b32 m0, %[m]\n\ts_add_u32 %[m], %[m], 0x800\n\tglobal_load_lds_dwordx4 %[d8], %[src]" WNT "\n\ts_mov_b32 m0, %[m]\n\ts_add_u32 %[m], %[m], 0x800\n\tglobal_load_lds_dwordx4 %[d9], %[src]" WNT "\n\ts_mov_b32 m0, %[m]\n\ts_add_u32 %[m], %[m], 0x800\n\tglobal_load_lds_dwordx4 %[d10], %[src]" WNT "\n\ts_mov_b32 m0, %[m]\n\ts_add_u32 %[m], %[m], 0x800\n\tglobal_load_lds_dwordx4 %[d11], %[src]" WNT "\n\ts_waitcnt vmcnt(6)\n2:\n\ts_barrier"
                         : [m] "+s"(mw) : [e] "s"(early), [src] "s"(dsrc), [d0] "v"(dof[0]), [d1] "v"(dof[1]), [d2] "v"(dof[2]), [d3] "v"(dof[3]), [d4] "v"(dof[4]), [d5] "v"(dof[5]), [d6] "v"(dof[6]), [d7] "v"(dof[7]), [d8] "v"(dof[8]), [d9] "v"(dof[9]), [d10] "v"(dof[10]), [d11] "v"(dof[11]) : "memory", "scc");
#pragma unroll
            for (int i = 12; i < 18; ++i) LD_WD(i);
            if (!(pw & 1)) LD_WD(18);
            asm volatile("s_mov_b64 exec, 1\n\tglobal_store_dwordx2 %0, %1, %2 sc1\n\ts_mov_b64 exec, -1\n\ts_barrier" :: "v"(OFF_CI + (128 + 4 * w + pw) * 8), "v"((v2u){tag, tag}), "s"(p.ws) : "memory");
            FFN_HFRAG();
            asm volatile("s_waitcnt vmcnt(1)\n\ts_barrier" ::: "memory");
            s16x4 a[3][8];
            FFN_WDFRAG(0, 8 + pw); FFN_WDFRAG(1, 12 + pw); FFN_WDFRAG(2, 16 + (pw & 1));
            v4f ya[3];
            FFN_DOWN(0, 8 + pw); FFN_DOWN(1, 12 + pw);
            if (pw < 2) { FFN_DOWN(2, 16 + pw); FFN_PUB(2, 16 + pw); }
            FFN_PUB(0, 8 + pw); FFN_PUB(1, 12 + pw);
            ROLE_END();
        }
    } else
    {
        ROLE_BEGIN(5);
        ROLE_ARGS();
        _Pragma("unroll 1") for (int e = (slot - 28) + 4 * wave; touch(p, xs, e, lane, (u32)(uintptr_t)(smem + wave * 256)); e += 32) {}
        asm volatile("s_waitcnt vmcnt(0)" ::: "memory");
        ROLE_END();
    }
    } else
        {
            ROLE_BEGIN(2);
            FFN_PRE();
            FFN_FILL();
            FFN_ADDR();
            LAS char* gp = smem + q * 2048 + ((lane >> 2) & 3) * 128 + ((2 * wave + ((lane >> 1) & 1)) ^ (((lane >> 2) & 2) | ((q & 1) << 2))) * 16 + (lane & 1) * 8;
            LAS char* xp = smem + FL_XS + (n & 7) * XSP + q * 16;
            const int hc = 16 * wave + n;
            LAS char* hw_ = smem + FL_H + (4 * (q & 1) + 2 * (q >> 1)) * 144 + ((hc & 35) | ((hc & 12) << 1) | ((hc & 16) >> 2)) * 2;
            asm volatile("" : "+v"(gp), "+v"(xp), "+v"(hw_));
            bf16x8 gf[18], uf[18];
#pragma unroll
            for (int sl = 0; sl < 3; ++sl) {
                if (sl == 0) asm volatile("s_waitcnt vmcnt(12)\n\ts_barrier" ::: "memory");
                if (sl == 1) asm volatile("s_waitcnt vmcnt(6)\n\ts_barrier" ::: "memory");
                if (sl == 2) asm volatile("s_waitcnt vmcnt(0) lgkmcnt(0)\n\ts_barrier" ::: "memory");
                LAS char* g_ = gp + sl * 49152;
                asm volatile("" : "+v"(g_));
#pragma unroll
                for (int i = 0; i < 6; ++i) {
                    gf[6 * sl + i] = __builtin_shufflevector(__builtin_amdgcn_ds_read_tr16_b64_v4i16((LAS s16x4*)(g_ + i * 8192)), __builtin_amdgcn_ds_read_tr16_b64_v4i16((LAS s16x4*)(g_ + i * 8192 + 512)), 0, 1, 2, 3, 4, 5, 6, 7);
                    uf[6 * sl + i] = __builtin_shufflevector(__builtin_amdgcn_ds_read_tr16_b64_v4i16((LAS s16x4*)(g_ + i * 8192 + 1024)), __builtin_amdgcn_ds_read_tr16_b64_v4i16((LAS s16x4*)(g_ + i * 8192 + 1536)), 0, 1, 2, 3, 4, 5, 6, 7);
                }
#pragma unroll
                for (int i = 0; i < 6; ++i) asm volatile("" : "+v"(gf[6 * sl + i]), "+v"(uf[6 * sl + i]));
            }
            u32 tag = *(LAS u32*)(smem + FL_TAG);
            asm volatile("s_barrier\n\ts_barrier" : "+v"(tag) :: "memory");
            bf16x8 xf[18];
            v4f ag = {0.f, 0.f, 0.f, 0.f}, au = ag;
#pragma unroll
            for (int s = 0; s < 4; ++s) xf[s] = *(LAS bf16x8*)(xp + s * 64);
            SBAR();
#pragma unroll
            for (int s = 0; s < 18; ++s) {
                ag = MF(xf[s], gf[s], ag);
                if (s + 4 < 18) xf[s + 4] = *(LAS bf16x8*)(xp + (s + 4) * 64);
                au = MF(xf[s], uf[s], au);
                SBAR();
            }
            {
                const u32 ab = cvtpk(q < 2 ? ag[0] : ag[2], q < 2 ? ag[1] : ag[3]), ub = cvtpk(q < 2 ? au[0] : au[2], q < 2 ? au[1] : au[3]);
                const u32 sl = cvtpk(bflo(ab) * sigmoidf_(bflo(ab)), bfhi(ab) * sigmoidf_(bfhi(ab)));
                const u32 hh = cvtpk(bflo(sl) * bflo(ub), bfhi(sl) * bfhi(ub));
                *(LAS u16*)(hw_) = (u16)hh; *(LAS u16*)(hw_ + 144) = (u16)(hh >> 16);
            }
            BAR();
            FFN_HFRAG();
            asm volatile("s_barrier" ::: "memory");
            s16x4 a[2][8];
            FFN_WDFRAG(0, 2 * wave); FFN_WDFRAG(1, 2 * wave + 1);
            v4f ya[2];
            FFN_DOWN(0, 2 * wave); FFN_DOWN(1, 2 * wave + 1);
            FFN_PUB(0, 2 * wave); FFN_PUB(1, 2 * wave + 1);
            ROLE_END();
        }
    } else {
    if (wave >= 4)
    {
        ROLE_BEGIN(1);
        const int hw = wave - 4, ht = tid - 256;
        const v4u z4 = {0, 0, 0, 0};
        const int sub = lane & 15;
        v4u xr[5];
        u32 tagv;
        const int rrow = 16 * (cw & 127) + 4 * hw;
        u32 dst = (u32)(uintptr_t)(smem + LDS_RW) + hw * 3072, l16 = lane * 16;
        u32 ro = (u32)(((rrow + (lane >> 4)) & ~((127 - cw) >> 31)) * D + sub * 8) * 2, ro4 = ro - (sub & 8) * 16;
        u32 uo0 = (u32)((256 * hw + 4 * lane) * D + chbase) * 2;
        asm volatile("" : "+s"(dst), "+v"(l16), "+v"(ro), "+v"(ro4), "+v"(uo0));
        ROLE_ARGS();
        {
            asm volatile("s_mov_b32 m0, %5\n\tv_mov_b32 %0, 0\n\tglobal_load_dword %0, %0, %2 sc1\n\tglobal_load_lds_dwordx4 %3, %4\n\ts_mov_b64 exec, 0xff\n\tglobal_load_lds_dwordx4 %3, %4 offset:1024\n\t"
                         "s_add_u32 m0, m0, 1280\n\ts_mov_b64 exec, -1\n\tglobal_load_lds_dwordx4 %3, %6\n\ts_mov_b64 exec, 0xff\n\tglobal_load_lds_dwordx4 %3, %6 offset:1024\n\ts_mov_b64 exec, -1"
                         : "=&v"(tagv) : "n"(0), "s"(p.ws), "v"(l16), "s"(p.wr), "s"(dst), "s"(p.gw) : "memory", "scc");
#pragma unroll
            for (int j = 0; j < 4; ++j) asm volatile("global_load_dwordx4 %0, %1, %2 offset:%3" : "=v"(xr[j]) : "v"(ro), "s"(p.x), "n"(j * 256));
            asm volatile("global_load_dwordx4 %0, %1, %2 offset:1024" : "=v"(xr[4]) : "v"(ro4), "s"(p.x));
        }
        __builtin_amdgcn_s_barrier();
        v2u uu[8];
        const u32 uo1 = uo0 + 1024 * D * 2;
#define LD_U(i) asm volatile("global_load_dwordx2 %0, %1, %2 offset:%3" : "=v"(uu[i]) : "v"((i) < 4 ? uo0 : uo1), "s"(p.x), "n"(((i) & 3) * D * 2))
#pragma unroll
        for (int i = 0; i < NB1; ++i) LD_U(i);
        *(LAS v4u*)(smem + LDS_TAP + ht * 16) = z4; *(LAS v4u*)(smem + LDS_TAP + 4096 + ht * 16) = z4;
        *(LAS v4u*)(smem + LDS_TAP + 8192 + ht * 16 + (ht >> 7) * (LDS_U - LDS_TAP - 8192 + ((ht >> 5) & 3) * 4608 + (ht & 31) * 16 - ht * 16)) = z4;
        BAR();
        __builtin_amdgcn_s_setprio(3);
        SBAR();
        u32 ctl = (u32)(uintptr_t)(smem + LDS_CTL), one = 1u, cty = ctl + 64 * hw + (lane & 3) * 16;
        const u32 late = (u32)((127 - cw) >> 31), pa_ = OFF_RG + hline(p.ws, cw & 127) + hw * 32, pk_ = pa_ ^ ((pa_ ^ (OFF_CI + ((cw - 128) * 4 + hw) * 16)) & late);
        u32 po = pk_ + (lane >> 1) + (sub != 0) * (OFF_G + hline(p.ws, ((lane - 1) & 3) * 64 + ((cw & 127) >> 1)) + ((4 * cw + hw) & 7) * 16 - pk_ - (lane >> 1));
        const u64 pmask = 1ull | ((u64)(~late & 1u) << 32) | (~late & ((1u << (1 + REPG)) - 1));
        u32 nrow = 0xffffu - (rrow + (lane >> 4));
        u32 go = OFF_G + hline(p.ws, (cw % REPG) * 64 + 8 * hw + (lane >> 3)) + (lane & 7) * 16, go2 = OFF_G + hline(p.ws, (cw % REPG) * 64 + 32 + 8 * hw + (lane >> 3)) + (lane & 7) * 16;
        LAS char* up = smem + LDS_U + (256 + 256 * hw + 4 * lane) * 2;
        LAS char* yp = smem + CH_Y + hw * 1152 + (lane + (lane >> 3)) * 16;
        LAS char* rw = smem + LDS_RW + hw * 3072 + sub * 16;
        LAS char* rw4 = rw - (sub & 8) * 16;
        asm volatile("" : "+v"(ctl), "+v"(one), "+v"(cty), "+v"(po), "+v"(go), "+v"(go2), "+v"(up), "+v"(yp), "+v"(rw), "+v"(rw4), "+v"(nrow));
        u32 tag;
        {
            asm volatile("s_waitcnt vmcnt(%1)" : "+v"(tagv) : "n"(NB1 + 5) : "memory");
            tag = __builtin_amdgcn_readfirstlane(tagv) + 1u;
            v4u wa[5], wg_[5];
#pragma unroll
            for (int j = 0; j < 5; ++j) { wa[j] = *(LAS v4u*)((j < 4 ? rw : rw4) + j * 256); wg_[j] = *(LAS v4u*)((j < 4 ? rw : rw4) + 1280 + j * 256); }
#pragma unroll
            for (int j = 0; j < 5; ++j) asm volatile("" : "+v"(wa[j]), "+v"(wg_[j]));
            asm volatile("s_waitcnt vmcnt(%5)" : "+v"(xr[0]), "+v"(xr[1]), "+v"(xr[2]), "+v"(xr[3]), "+v"(xr[4]) : "n"(NB1) : "memory");
            if (sub >= 8) xr[4] = z4;
            float r = 0.f, s = 0.f;
#pragma unroll
            for (int j = 0; j < 5; ++j) {
                const v4u a = wa[j], g = wg_[j];
#pragma unroll
                for (int e = 0; e < 4; ++e) {
                    const u32 xe = xr[j][e], ae = a[e], ge = g[e];
                    r = __builtin_amdgcn_fdot2_f32_bf16(__builtin_bit_cast(bf16x2_t, xe), __builtin_bit_cast(bf16x2_t, ae), r, false);
                    s = __builtin_amdgcn_fdot2_f32_bf16(__builtin_bit_cast(bf16x2_t, xe), __builtin_bit_cast(bf16x2_t, ge), s, false);
                }
            }
            r = dpp_add<0x128, 0xf>(r); s = dpp_add<0x128, 0xf>(s); r = dpp_add<0x124, 0xf>(r); s = dpp_add<0x124, 0xf>(s);
            r = dpp_add<0x122, 0xf>(r); s = dpp_add<0x122, 0xf>(s); r = dpp_add<0x121, 0xf>(r); s = dpp_add<0x121, 0xf>(s);
            const u32 pay = make_payload(r, sigmoidf_(rbf(s))), kk = (pay & 0xffff0000u) | nrow;
            const u32 p0 = (u32)__builtin_amdgcn_readlane((int)pay, 0), p1 = (u32)__builtin_amdgcn_readlane((int)pay, 16), p2 = (u32)__builtin_amdgcn_readlane((int)pay, 32), p3 = (u32)__builtin_amdgcn_readlane((int)pay, 48);
            const auto sw = __builtin_amdgcn_permlane16_swap(kk, kk, false, false);
            const u32 g01 = (p0 & 0xffffu) | (p1 << 16), g23 = (p2 & 0xffffu) | (p3 << 16);
            const v4u pv = {sub ? g01 : sw[0], tag, sub ? g23 : sw[1], tag};
            asm volatile("s_mov_b64 exec, %3\n\tglobal_store_dwordx4 %0, %1, %2 sc1\n\ts_mov_b64 exec, -1\n\ts_nop 0" :: "v"(po), "v"(pv), "s"(p.ws), "s"(pmask) : "memory");
        }
        __builtin_amdgcn_s_setprio(0);
        *(LAS u32*)(smem + LDS_CTL + C_TAG) = tag;
        LDS_ADD1(ctl, one, C_PUB);
        LWAIT(ctl, C_PUB, 4);
#pragma unroll
        for (int i = NB1; i < 4; ++i) LD_U(i);
        LDS_ADD1(ctl, one, C_PUB);
        LWAIT(ctl, C_PUB, 8);
        v4u gq[2];
#define POLL_G "global_load_dwordx4 v[100:103], %[go], %[ws] sc1\n\tglobal_load_dwordx4 v[104:107], %[go2], %[ws] sc1\n\t"
#define LD_U4 "global_load_dwordx2 %[u4], %[uo], %[x]\n\tglobal_load_dwordx2 %[u5], %[uo], %[x] offset:1152\n\tglobal_load_dwordx2 %[u6], %[uo], %[x] offset:2304\n\tglobal_load_dwordx2 %[u7], %[uo], %[x] offset:3456\n\t"
        asm volatile("" : "=&{v[100:103]}"(gq[0]), "=&{v[104:107]}"(gq[1]), "=&v"(uu[4]), "=&v"(uu[5]), "=&v"(uu[6]), "=&v"(uu[7]));
        asm volatile("s_cmp_ge_u32 %[hw], 2\n\ts_cbranch_scc1 1f\n\t" LD_U4 POLL_G "s_waitcnt vmcnt(6)\n\ts_branch 2f\n1:\n\ts_waitcnt vmcnt(0)\n2:"
                     : "+{v[100:103]}"(gq[0]), "+{v[104:107]}"(gq[1]), [u4] "+v"(uu[4]), [u5] "+v"(uu[5]), [u6] "+v"(uu[6]), [u7] "+v"(uu[7]), "+v"(uu[0]), "+v"(uu[1]), "+v"(uu[2]), "+v"(uu[3])
                     : [go] "v"(go), [go2] "v"(go2), [ws] "s"(p.ws), [uo] "v"(uo1), [x] "s"(p.x), [hw] "s"(hw) : "memory", "scc");
        u32 ct2 = ctl + max(hw - 1, 0) * 4;
        auto stage = [&](int g) __attribute__((always_inline)) {
            const v2u* u4 = uu + 4 * g;
            LAS char* d = up + g * 2048;
            *(LAS v2u*)(d) = (v2u){__builtin_amdgcn_perm(u4[1].x, u4[0].x, 0x05040100u), __builtin_amdgcn_perm(u4[3].x, u4[2].x, 0x05040100u)};
            *(LAS v2u*)(d + 4608) = (v2u){__builtin_amdgcn_perm(u4[1].x, u4[0].x, 0x07060302u), __builtin_amdgcn_perm(u4[3].x, u4[2].x, 0x07060302u)};
            *(LAS v2u*)(d + 2 * 4608) = (v2u){__builtin_amdgcn_perm(u4[1].y, u4[0].y, 0x05040100u), __builtin_amdgcn_perm(u4[3].y, u4[2].y, 0x05040100u)};
            *(LAS v2u*)(d + 3 * 4608) = (v2u){__builtin_amdgcn_perm(u4[1].y, u4[0].y, 0x07060302u), __builtin_amdgcn_perm(u4[3].y, u4[2].y, 0x07060302u)};
            if (g) LDS_ADD1(ct2, one, C_ST2); else LDS_ADD1(ctl, one, C_STG);
        };
        stage(0);
        asm volatile("s_cmp_lt_u32 %[hw], 2\n\ts_cbranch_scc1 1f\n\t" POLL_G LD_U4 "\n1:"
                     : "+{v[100:103]}"(gq[0]), "+{v[104:107]}"(gq[1]), [u4] "+v"(uu[4]), [u5] "+v"(uu[5]), [u6] "+v"(uu[6]), [u7] "+v"(uu[7])
                     : [go] "v"(go), [go2] "v"(go2), [ws] "s"(p.ws), [uo] "v"(uo1), [x] "s"(p.x), [hw] "s"(hw) : "memory", "scc");
        if (hw < 2) {
            asm volatile("s_waitcnt vmcnt(2)" : "+v"(uu[4]), "+v"(uu[5]), "+v"(uu[6]), "+v"(uu[7]));
            stage(1);
        }
        {
            u32 c_;
            asm volatile("s_mov_b32 %[c], 0x80000\n\ts_waitcnt vmcnt(4)\n\ts_cmp_ge_u32 %[hw], 2\n\ts_cbranch_scc1 1f\n\ts_waitcnt vmcnt(0)\n1:\n\tv_cmpx_eq_u32 %[tag], v101\n\tv_cmpx_eq_u32 %[tag], v103\n\tv_cmpx_eq_u32 %[tag], v105\n\tv_cmpx_eq_u32 %[tag], v107\n\t"
                         "s_cmp_eq_u64 exec, -1\n\ts_mov_b64 exec, -1\n\ts_cbranch_scc1 2f\n\ts_sleep 4\n\t" POLL_G "s_waitcnt vmcnt(0)\n\ts_sub_u32 %[c], %[c], 1\n\ts_cbranch_scc0 1b\n2:\n\ts_nop 4"
                         : "+{v[100:103]}"(gq[0]), "+{v[104:107]}"(gq[1]), [c] "=&s"(c_) : [go] "v"(go), [go2] "v"(go2), [ws] "s"(p.ws), [tag] "s"(tag), [hw] "s"(hw) : "memory", "scc", "vcc");
        }
        *(LAS v2u*)(smem + LDS_G + hw * 3072 + lane * 8) = (v2u){gq[0].x, gq[0].z}; *(LAS v2u*)(smem + LDS_G + hw * 3072 + 512 + lane * 8) = (v2u){gq[1].x, gq[1].z};
        LDS_ADD1(ctl, one, C_GOK);
        if (hw < 2) {
            u32 wt[3];
            {
                const u32 l0 = xs.wpf * 192 * xs.nffn, per = xcd < NBX ? ((3 - WPFB) * 192 * FB + 6 * CB - 1) / (6 * CB) + (WPFB == 3) : ((3 - WPF) * 192 * FA + 95) / 96;
#pragma unroll
                for (int k = 0; k < 3; ++k) {
                    wt[k] = wd_line(xs, min(l0 + ((2 * k * xs.nconv + slot * 2 + (hw & 1)) * per + min((u32)lane, per - 1)), 576 * xs.nffn - 1));
                    asm volatile("" : "+v"(wt[k]));
                }
            }
#pragma unroll
            for (int k = 0; k < 3; ++k) asm volatile("s_mov_b32 m0, %2\n\ts_nop 0\n\tglobal_load_lds_dword %0, %1" :: "v"(wt[k]), "s"(p.wd), "n"(LDS_DUMMY) : "memory");
        }
#pragma unroll
        for (int g = 0; g < 2; ++g) {
            if (g == 1 && hw >= 2) {
                asm volatile("s_waitcnt vmcnt(4)" : "+v"(uu[4]), "+v"(uu[5]), "+v"(uu[6]), "+v"(uu[7]));
                stage(1);
            }
            const v2u* u4 = uu + 4 * g;
            const float gr[4] = {bflo(gq[g].x), bfhi(gq[g].x), bflo(gq[g].z), bfhi(gq[g].z)};
            float o[4][4];
#pragma unroll
            for (int r = 0; r < 4; ++r) {
                const float xv[4] = {bflo(u4[r].x), bfhi(u4[r].x), bflo(u4[r].y), bfhi(u4[r].y)};
#pragma unroll
                for (int c = 0; c < 4; ++c) asm("v_fma_f32 %0, -%1, %2, %2" : "=v"(o[r][c]) : "v"(gr[r]), "v"(xv[c]));
                asm volatile("" : "+v"(o[r][0]), "+v"(o[r][1]), "+v"(o[r][2]), "+v"(o[r][3]));
            }
            {
                u32 t_, c_;
                asm volatile("s_mov_b32 %1, 0x400000\n1:\n\tds_read_b32 %0, %2 offset:%3\n\ts_waitcnt lgkmcnt(0)\n\tv_cmp_eq_u32 vcc, 0, %0\n\ts_cmp_eq_u64 vcc, 0\n\ts_cbranch_scc1 2f\n\ts_sleep 1\n\ts_sub_u32 %1, %1, 1\n\ts_cbranch_scc0 1b\n2:"
                             : "=&v"(t_), "=&s"(c_) : "v"(cty), "n"(C_Y + 256 * g) : "memory", "scc", "vcc");
            }
            v4f y[4];
#pragma unroll
            for (int c = 0; c < 4; ++c) y[c] = *(LAS v4f*)(yp + c * KCH + g * 4608);
            SBAR();
#define ST_O(i, v) asm volatile("global_store_dwordx2 %0, %1, %2 offset:%3" :: "v"(g ? uo1 : uo0), "v"(v), "s"(p.out), "n"((i) * D * 2) : "memory")
#pragma unroll
            for (int r = 0; r < 4; ++r) {
#pragma unroll
                for (int c = 0; c < 4; ++c) { const float yv = y[c][r]; asm("v_fmac_f32 %0, %1, %2" : "+v"(o[r][c]) : "v"(gr[r]), "v"(yv)); }
                ST_O(r, ((v2u){cvtpk(o[r][0], o[r][1]), cvtpk(o[r][2], o[r][3])}));
            }
        }
        asm volatile("s_waitcnt vmcnt(0)" ::: "memory");
        LDS_ADD1(ctl, one, C_ACK);
        ROLE_END();
    }
    else
    {
        ROLE_BEGIN(0);
        const int ch = wave;
        u32 pi = ((chbase + ch) * NST + lane * 2) * 2, dl = (chbase + ch) * 2;
        asm volatile("" : "+v"(pi), "+v"(dl));
        ROLE_ARGS();
#define GLD(T, ptr, idx) __builtin_nontemporal_load((const GAS T*)((const GAS char*)(ptr) + (idx)))
        const u32 are = GLD(u32, p.Are, pi), aim = GLD(u32, p.Aim, pi), bre = GLD(u32, p.Bre, pi), bim = GLD(u32, p.Bim, pi), cre = GLD(u32, p.Cre, pi), cim = GLD(u32, p.Cim, pi);
        const u16 ldt = *(const GAS u16*)((const GAS char*)p.logdt + dl), dsk = *(const GAS u16*)((const GAS char*)p.dskip + dl);
        SBAR();
        __builtin_amdgcn_s_barrier();
        SBAR();
        LAS char* kb = smem + ch * KCH;
        const int r32 = lane & 31, h = lane >> 5, q = lane >> 4, n = lane & 15;
        const int ai = 4 * (r32 >> 3) + (r32 & 3), hh = (r32 >> 2) & 1;
        const u64 tsel = ai < 8 ? (hh ? KROW_1LO : KROW_0LO) : (hh ? KROW_1HI : KROW_0HI);
        const int o = (int)((tsel >> (8 * (ai & 7))) & 0xff);
        LAS char* ap = kb + o * STG_ROW + h * 16;
        LAS char* bp = kb + r32 * STG_ROW + h * 16;
        u32 sg[14];
#pragma unroll
        for (int k = 0; k < 14; ++k) { sg[k] = (u32)(uintptr_t)(kb + lane * 8 + k * 4 * STG_ROW); asm volatile("" : "+v"(sg[k])); }
        bf16x8 b5, b3, b9, b7, pat;
        u32 q8 = 8 * q, nn = n;
        LAS char* k0 = kb + 32 + lane * 16;
        LAS char* kt = kb + (lane & 3) * KCOPY + (lane < 16 ? 32 + 1024 + (lane >> 2) * 16 : ((lane >> 2) & 1) * 16);
        LAS char* a0 = kb + ((lane >> 2) & 3) * KCOPY + (28 - 8 * q + 4 * (lane & 3)) * 2;
        LAS char* bb = smem + LDS_U + ch * 4608 + (16 * n + 8 * q - 16) * 2;
        LAS char* yb = kb + CH_Y + (4 * n + q + (n >> 1)) * 16;
        LAS char* tp = smem + LDS_TAP + ch * 2304 + ((n & 1) ? 13 + n - 4 * q : 96 + 14 + n - 4 * q) * 4;
        LAS char* ta = smem + LDS_TAP + ch * 2304 + (h ? 0 : 768) + 64 + 2 * r32;
        LAS char* tb = smem + LDS_TAP + ch * 2304 + 1600 + 64 * h + 2 * r32;
        u32 ctl = (u32)(uintptr_t)(smem + LDS_CTL), ctc = ctl + C_Y + ch * 16 + (lane & 15), one = 1u, so = OFF_SEL + (8 + xcd) * 256 + (lane & 7) * 8;
        u32 m01 = lane < 2 ? ~0u : 0u, m2 = lane == 2 ? ~0u : 0u, m3 = lane == 3 ? ~0u : 0u, m0_ = lane == 0 ? ~0u : 0u;
        asm volatile("" : "+v"(q8), "+v"(nn), "+v"(ap), "+v"(bp), "+v"(m01), "+v"(m2), "+v"(m3), "+v"(m0_));
        asm volatile("" : "+v"(k0), "+v"(kt), "+v"(a0), "+v"(bb), "+v"(yb), "+v"(tp), "+v"(ta), "+v"(tb), "+v"(ctl), "+v"(ctc), "+v"(one), "+v"(so));
        const char* hint = p.ws + (OFF_PART + cw * PP + hline(p.ws, cw >> 1));
        asm volatile("" : "+s"(hint));
        __builtin_amdgcn_s_barrier();
        SBAR();
        const float dt = rbf(expf(bf2f(ldt)));
        v16f acc;
        u32 stg;
        float kscale;
        {
            const v2f a2 = (v2f){bflo(are), bfhi(are)} * dt * 1.44269504088896341f;
            const v2f brev = (v2f){bflo(aim), bfhi(aim)} * dt * 0.15915494309189535f;
            const cf2 Bc = {{dt * bflo(bre), dt * bflo(bim)}, {dt * bfhi(bre), dt * bfhi(bim)}};
            const cf2 Cc = {{bflo(cre), bflo(cim)}, {bfhi(cre), bfhi(cim)}};
            cf2 W = cmul2(Cc, Bc);
            u32 mx = max(max(__float_as_uint(W.a.x) & 0x7f800000u, __float_as_uint(W.a.y) & 0x7f800000u), max(__float_as_uint(W.b.x) & 0x7f800000u, __float_as_uint(W.b.y) & 0x7f800000u));
            mx = dpp_umax<0x111, 0xf>(mx); mx = dpp_umax<0x112, 0xf>(mx); mx = dpp_umax<0x114, 0xf>(mx); mx = dpp_umax<0x118, 0xf>(mx);
            mx = dpp_umax<0x142, 0xa>(mx); mx = dpp_umax<0x143, 0xc>(mx);
            mx = min(max((u32)__builtin_amdgcn_readlane((int)mx, 63), 0x00800000u), 0x7e800000u);
            kscale = __uint_as_float(mx);
            const float inv = __uint_as_float(0x7f000000u - mx);
            W.a *= inv; W.b *= inv;
            const cf2 E1 = cexp2(a2, brev, 1.f), E8 = cexp2(a2, brev, 8.f), E256 = cexp2(a2, brev, 256.f);
            asm volatile("s_nop 0\n\t" KGEN_ASM :: "{v[48:49]}"(W.a), "{v[50:51]}"(W.b), "{v[52:53]}"(E1.a), "{v[54:55]}"(E1.b), "{v[56:57]}"(E8.a), "{v[58:59]}"(E8.b), "{v[60:61]}"(E256.a), "{v[62:63]}"(E256.b),
                         [b0] "v"(sg[0]), [b1] "v"(sg[1]), [b2] "v"(sg[2]), [b3] "v"(sg[3]), [b4] "v"(sg[4]), [b5] "v"(sg[5]), [b6] "v"(sg[6]), [b7] "v"(sg[7]), [b8] "v"(sg[8]), [b9] "v"(sg[9]),
                         [b10] "v"(sg[10]), [b11] "v"(sg[11]), [b12] "v"(sg[12]), [b13] "v"(sg[13]) : "memory", KGEN_CLOBBER);
            asm volatile(KMFMA_ASM : "=&{v[48:63]}"(acc), [stg] "=&v"(stg), "=&{v[4:7]}"(b5), "=&{v[8:11]}"(b3), "=&{v[12:15]}"(b9), "=&{v[16:19]}"(b7), "=&{v[20:23]}"(pat)
                         : [ap] "v"(ap), [bp] "v"(bp), [ctl] "v"(ctl), [n] "v"(nn), [q8] "v"(q8), [ps] "s"(0x05040100u) : "memory", KMFMA_CLOBBER);
            acc *= kscale;
        }
        acc[0] += __uint_as_float(((u32)dsk << 16) & m0_);
        u32 t78 = cvtpk(acc[7], acc[8]), t9a = cvtpk(acc[9], acc[10]);
        {
            const auto s0 = __builtin_amdgcn_permlane32_swap(cvtpk(acc[0], acc[1]), cvtpk(acc[4], acc[4]), false, false);
            const auto s1 = __builtin_amdgcn_permlane32_swap(cvtpk(acc[2], acc[3]), cvtpk(acc[5], acc[6]), false, false);
            const v4u ww = {s0[0], s1[0], s0[1], s1[1]};
            const u32 pz = (u32)__builtin_amdgcn_update_dpp(0, (int)ww.z, 0x138, 0xf, 0xf, true), pw = (u32)__builtin_amdgcn_update_dpp(0, (int)ww.w, 0x138, 0xf, 0xf, true);
            const v4u c1 = {__builtin_amdgcn_alignbit(ww.x, pw, 16), __builtin_amdgcn_alignbit(ww.y, ww.x, 16), __builtin_amdgcn_alignbit(ww.z, ww.y, 16), __builtin_amdgcn_alignbit(ww.w, ww.z, 16)};
            *(LAS v4u*)(k0) = ww;
            *(LAS v4u*)(k0 + KCOPY) = c1;
            *(LAS v4u*)(k0 + 2 * KCOPY) = (v4u){pw, ww.x, ww.y, ww.z};
            *(LAS v4u*)(k0 + 3 * KCOPY) = (v4u){__builtin_amdgcn_alignbit(pw, pz, 16), c1.x, c1.y, c1.z};
            const u32 w0 = (u32)__builtin_amdgcn_readlane((int)ww.z, 63), w1 = (u32)__builtin_amdgcn_readlane((int)ww.w, 63);
            const u32 w3 = w1 >> 16, w2 = w3 * 0x10001u, w10 = (w0 >> 16) | (w1 << 16);
            *(LAS v4u*)kt = (v4u){(w2 & m01) | (w1 & m2) | (w10 & m3), (w3 & m0_) | (w2 & ((m01 ^ m0_) | m2 | m3)), (w3 & m2) | (w2 & m3), 0u};
        }
        asm volatile("" ::: "memory");
        pat = __builtin_bit_cast(bf16x8, __builtin_bit_cast(v4u, pat) & (((u32)__builtin_amdgcn_readlane((int)t78, 63) >> 16) * 0x10001u));
#define POLL_SEL "global_load_dwordx2 v[240:241], %[so], %[ws] sc1\n\t"
        v2u sq;
        const v4f zero4 = {0.f, 0.f, 0.f, 0.f};
        {
            u32 s_, c_;
            asm volatile(CONV_ASM : "=&{v[240:241]}"(sq), [s] "=&s"(s_), [c] "=&s"(c_)
                         : "{v[4:7]}"(b5), "{v[8:11]}"(b3), "{v[12:15]}"(b9), "{v[16:19]}"(b7), "{v[20:23]}"(pat), [a0] "v"(a0), [bb] "v"(bb), [yb] "v"(yb), [tp] "v"(tp), [ta] "v"(ta), [tb] "v"(tb),
                           [ctl] "v"(ctl), [ctc] "v"(ctc), [one] "v"(one), [so] "v"(so), [t78] "v"(t78), [t9a] "v"(t9a), [stg] "v"(stg), [ws] "s"(p.ws) : "memory", "scc", CONV_CLOBBER);
        }
        asm volatile("s_cmp_eq_u32 %0, 0\n\ts_cbranch_scc1 1f\n\ts_endpgm\n1:" :: "s"(wave) : "memory", "scc");
        {
            u32 h_;
            asm volatile("s_load_dword %0, %1, 0x4 glc" : "=&s"(h_) : "s"(hint) : "memory");
            const u32 tag = __builtin_amdgcn_readfirstlane(*(LAS u32*)(smem + LDS_CTL + C_TAG));
            u32 c_;
            u32 pb[9];
#pragma unroll
            for (int k = 0; k < 9; ++k) { pb[k] = OFF_PART + cw * PP + hline(p.ws, 8 * k + (lane >> 3)) + (lane & 7) * 16; asm volatile("" : "+v"(pb[k])); }
            {
                asm volatile("s_mov_b32 %[c], 64\n1:\n\ts_waitcnt lgkmcnt(0)\n\ts_cmp_eq_u32 %[h], %[tag]\n\ts_cbranch_scc1 2f\n\ts_load_dword %[h], %[a], 0x4 glc\n\ts_sub_u32 %[c], %[c], 1\n\ts_cbranch_scc0 1b\n\ts_waitcnt lgkmcnt(0)\n2:"
                             : [h] "+s"(h_), [c] "=&s"(c_) : [a] "s"(hint), [tag] "s"(tag) : "memory", "scc");
            }
            typedef __attribute__((ext_vector_type(32))) u32 v32u;
            v32u ra, rb; v4u rc, rd;
            asm volatile("global_load_dwordx4 v[64:67], %[pb0], %[ws] sc1\n\tglobal_load_dwordx4 v[68:71], %[pb1], %[ws] sc1\n\tglobal_load_dwordx4 v[72:75], %[pb2], %[ws] sc1\n\tglobal_load_dwordx4 v[76:79], %[pb3], %[ws] sc1\n\tglobal_load_dwordx4 v[80:83], %[pb4], %[ws] sc1\n\tglobal_load_dwordx4 v[84:87], %[pb5], %[ws] sc1\n\tglobal_load_dwordx4 v[88:91], %[pb6], %[ws] sc1\n\tglobal_load_dwordx4 v[92:95], %[pb7], %[ws] sc1\n\tglobal_load_dwordx4 v[96:99], %[pb8], %[ws] sc1\n\t" : "=&{v[64:95]}"(ra), "=&{v[96:99]}"(rc) : [pb0] "v"(pb[0]), [pb1] "v"(pb[1]), [pb2] "v"(pb[2]), [pb3] "v"(pb[3]), [pb4] "v"(pb[4]), [pb5] "v"(pb[5]), [pb6] "v"(pb[6]), [pb7] "v"(pb[7]), [pb8] "v"(pb[8]), [ws] "s"(p.ws) : "memory");
            asm volatile("s_mov_b32 %[c], 0x80000\n\ts_waitcnt vmcnt(9)\n1:\n\tv_cmpx_eq_u32 %[tag], v241\n\t"
                         "s_cmp_eq_u64 exec, -1\n\ts_mov_b64 exec, -1\n\ts_cbranch_scc1 2f\n\t" POLL_SEL "s_waitcnt vmcnt(0)\n\ts_sub_u32 %[c], %[c], 1\n\ts_cbranch_scc0 1b\n2:\n\ts_nop 4"
                         : "+{v[240:241]}"(sq), [c] "=&s"(c_) : [so] "v"(so), [ws] "s"(p.ws), [tag] "s"(tag) : "memory", "scc", "vcc");
            const int t = (int)(0xffffu - (sq.x & 0xffffu)) & 2047;
            const float rs = rbf(sigmoidf_(key_to_r(sq.x)));
            LWAIT(ctl, C_GOK, 4);
            {
                u32 t_;
                asm volatile("s_mov_b32 %1, 0x400000\n1:\n\tds_read_b32 %0, %2 offset:%3\n\ts_waitcnt lgkmcnt(0)\n\tv_cmp_eq_u32 vcc, 0, %0\n\ts_cmp_eq_u64 vcc, 0\n\ts_cbranch_scc1 2f\n\ts_sub_u32 %1, %1, 1\n\ts_cbranch_scc0 1b\n2:"
                             : "=&v"(t_), "=&s"(c_) : "v"(ctl + (lane & 3) * 16), "n"(C_Y + 64 * 7) : "memory", "scc", "vcc");
            }
            const float g = bf2f(*(LAS u16*)(smem + LDS_G + ((t >> 8) & 3) * 3072 + (t >> 10) * 512 + (t & 255) * 2));
            const int uq = (t & 255) >> 2;
            float xv[4], av[4];
#pragma unroll
            for (int c = 0; c < 4; ++c) {
                xv[c] = bf2f(*(LAS u16*)(smem + LDS_U + c * 4608 + (256 + t) * 2));
                av[c] = g * *(LAS float*)(smem + c * KCH + CH_Y + (t >> 8) * 1152 + (uq + (uq >> 3)) * 16 + (t & 3) * 4);
            }
            const u32 e1 = n < 4 ? ((n & 1) ? 0x3f800000u : 0x00003f80u) : 0u;
            bf16x8 sel4 = __builtin_bit_cast(bf16x8, (n & 2) ? (v4u){0u, 0u, e1, e1} : (v4u){e1, e1, 0u, 0u});
            u32 oo = (u32)(t * D + chbase) * 2;
            float omg = 1.f - g;
            asm volatile("" : "+v"(sel4), "+v"(oo), "+v"(omg), "+v"(xv[0]), "+v"(xv[1]), "+v"(xv[2]), "+v"(xv[3]), "+v"(av[0]), "+v"(av[1]), "+v"(av[2]), "+v"(av[3]));
            v4f s0, s1;
            asm volatile("s_mov_b32 %[c], 0x40000\n\ts_waitcnt vmcnt(0)\n\tv_cmpx_eq_u32 %[tag], v65\n\tv_cmpx_eq_u32 %[tag], v67\n\tv_cmpx_eq_u32 %[tag], v69\n\tv_cmpx_eq_u32 %[tag], v71\n\tv_cmpx_eq_u32 %[tag], v73\n\tv_cmpx_eq_u32 %[tag], v75\n\tv_cmpx_eq_u32 %[tag], v77\n\tv_cmpx_eq_u32 %[tag], v79\n\tv_cmpx_eq_u32 %[tag], v81\n\tv_cmpx_eq_u32 %[tag], v83\n\tv_cmpx_eq_u32 %[tag], v85\n\tv_cmpx_eq_u32 %[tag], v87\n\tv_cmpx_eq_u32 %[tag], v89\n\tv_cmpx_eq_u32 %[tag], v91\n\tv_cmpx_eq_u32 %[tag], v93\n\tv_cmpx_eq_u32 %[tag], v95\n\tv_cmpx_eq_u32 %[tag], v97\n\tv_cmpx_eq_u32 %[tag], v99\n\ts_cmp_eq_u64 exec, -1\n\ts_mov_b64 exec, -1\n\ts_cbranch_scc1 3f\n\tglobal_load_dwordx4 v[64:67], %[pb0], %[ws] sc1\n\tglobal_load_dwordx4 v[68:71], %[pb1], %[ws] sc1\n\tglobal_load_dwordx4 v[72:75], %[pb2], %[ws] sc1\n\tglobal_load_dwordx4 v[76:79], %[pb3], %[ws] sc1\n\tglobal_load_dwordx4 v[80:83], %[pb4], %[ws] sc1\n\tglobal_load_dwordx4 v[84:87], %[pb5], %[ws] sc1\n\tglobal_load_dwordx4 v[88:91], %[pb6], %[ws] sc1\n\tglobal_load_dwordx4 v[92:95], %[pb7], %[ws] sc1\n\tglobal_load_dwordx4 v[96:99], %[pb8], %[ws] sc1\n\ts_sleep 6\n\tglobal_load_dwordx4 v[100:103], %[pb0], %[ws] sc1\n\tglobal_load_dwordx4 v[104:107], %[pb1], %[ws] sc1\n\tglobal_load_dwordx4 v[108:111], %[pb2], %[ws] sc1\n\tglobal_load_dwordx4 v[112:115], %[pb3], %[ws] sc1\n\tglobal_load_dwordx4 v[116:119], %[pb4], %[ws] sc1\n\tglobal_load_dwordx4 v[120:123], %[pb5], %[ws] sc1\n\tglobal_load_dwordx4 v[124:127], %[pb6], %[ws] sc1\n\tglobal_load_dwordx4 v[128:131], %[pb7], %[ws] sc1\n\tglobal_load_dwordx4 v[132:135], %[pb8], %[ws] sc1\n\t"
                         "1:\n\ts_waitcnt vmcnt(9)\n\tv_cmpx_eq_u32 %[tag], v65\n\tv_cmpx_eq_u32 %[tag], v67\n\tv_cmpx_eq_u32 %[tag], v69\n\tv_cmpx_eq_u32 %[tag], v71\n\tv_cmpx_eq_u32 %[tag], v73\n\tv_cmpx_eq_u32 %[tag], v75\n\tv_cmpx_eq_u32 %[tag], v77\n\tv_cmpx_eq_u32 %[tag], v79\n\tv_cmpx_eq_u32 %[tag], v81\n\tv_cmpx_eq_u32 %[tag], v83\n\tv_cmpx_eq_u32 %[tag], v85\n\tv_cmpx_eq_u32 %[tag], v87\n\tv_cmpx_eq_u32 %[tag], v89\n\tv_cmpx_eq_u32 %[tag], v91\n\tv_cmpx_eq_u32 %[tag], v93\n\tv_cmpx_eq_u32 %[tag], v95\n\tv_cmpx_eq_u32 %[tag], v97\n\tv_cmpx_eq_u32 %[tag], v99\n\ts_cmp_eq_u64 exec, -1\n\ts_mov_b64 exec, -1\n\ts_cbranch_scc1 3f\n\tglobal_load_dwordx4 v[64:67], %[pb0], %[ws] sc1\n\tglobal_load_dwordx4 v[68:71], %[pb1], %[ws] sc1\n\tglobal_load_dwordx4 v[72:75], %[pb2], %[ws] sc1\n\tglobal_load_dwordx4 v[76:79], %[pb3], %[ws] sc1\n\tglobal_load_dwordx4 v[80:83], %[pb4], %[ws] sc1\n\tglobal_load_dwordx4 v[84:87], %[pb5], %[ws] sc1\n\tglobal_load_dwordx4 v[88:91], %[pb6], %[ws] sc1\n\tglobal_load_dwordx4 v[92:95], %[pb7], %[ws] sc1\n\tglobal_load_dwordx4 v[96:99], %[pb8], %[ws] sc1\n\t"
                         "s_waitcnt vmcnt(9)\n\tv_cmpx_eq_u32 %[tag], v101\n\tv_cmpx_eq_u32 %[tag], v103\n\tv_cmpx_eq_u32 %[tag], v105\n\tv_cmpx_eq_u32 %[tag], v107\n\tv_cmpx_eq_u32 %[tag], v109\n\tv_cmpx_eq_u32 %[tag], v111\n\tv_cmpx_eq_u32 %[tag], v113\n\tv_cmpx_eq_u32 %[tag], v115\n\tv_cmpx_eq_u32 %[tag], v117\n\tv_cmpx_eq_u32 %[tag], v119\n\tv_cmpx_eq_u32 %[tag], v121\n\tv_cmpx_eq_u32 %[tag], v123\n\tv_cmpx_eq_u32 %[tag], v125\n\tv_cmpx_eq_u32 %[tag], v127\n\tv_cmpx_eq_u32 %[tag], v129\n\tv_cmpx_eq_u32 %[tag], v131\n\tv_cmpx_eq_u32 %[tag], v133\n\tv_cmpx_eq_u32 %[tag], v135\n\ts_cmp_eq_u64 exec, -1\n\ts_mov_b64 exec, -1\n\ts_cbranch_scc1 2f\n\tglobal_load_dwordx4 v[100:103], %[pb0], %[ws] sc1\n\tglobal_load_dwordx4 v[104:107], %[pb1], %[ws] sc1\n\tglobal_load_dwordx4 v[108:111], %[pb2], %[ws] sc1\n\tglobal_load_dwordx4 v[112:115], %[pb3], %[ws] sc1\n\tglobal_load_dwordx4 v[116:119], %[pb4], %[ws] sc1\n\tglobal_load_dwordx4 v[120:123], %[pb5], %[ws] sc1\n\tglobal_load_dwordx4 v[124:127], %[pb6], %[ws] sc1\n\tglobal_load_dwordx4 v[128:131], %[pb7], %[ws] sc1\n\tglobal_load_dwordx4 v[132:135], %[pb8], %[ws] sc1\n\t"
                         "s_sub_u32 %[c], %[c], 1\n\ts_cbranch_scc0 1b\n\ts_waitcnt vmcnt(0)\n"
                         "2:\n\tv_mov_b32 v101, v104\n\tv_mov_b32 v103, v106\n\tv_mov_b32 v109, v112\n\tv_mov_b32 v111, v114\n\tv_mov_b32 v117, v120\n\tv_mov_b32 v119, v122\n\tv_mov_b32 v125, v128\n\tv_mov_b32 v127, v130\n\tv_mov_b32 v133, 0\n\tv_mov_b32 v135, 0\n\tv_mfma_f32_16x16x32_bf16 v[136:139], %[sel], v[100:103], 0\n\tv_mfma_f32_16x16x32_bf16 v[140:143], %[sel], v[108:111], 0\n\tv_mfma_f32_16x16x32_bf16 v[136:139], %[sel], v[116:119], v[136:139]\n\tv_mfma_f32_16x16x32_bf16 v[140:143], %[sel], v[124:127], v[140:143]\n\tv_mfma_f32_16x16x32_bf16 v[136:139], %[sel], v[132:135], v[136:139]\n\ts_branch 4f\n"
                         "3:\n\tv_mov_b32 v65, v68\n\tv_mov_b32 v67, v70\n\tv_mov_b32 v73, v76\n\tv_mov_b32 v75, v78\n\tv_mov_b32 v81, v84\n\tv_mov_b32 v83, v86\n\tv_mov_b32 v89, v92\n\tv_mov_b32 v91, v94\n\tv_mov_b32 v97, 0\n\tv_mov_b32 v99, 0\n\tv_mfma_f32_16x16x32_bf16 v[136:139], %[sel], v[64:67], 0\n\tv_mfma_f32_16x16x32_bf16 v[140:143], %[sel], v[72:75], 0\n\tv_mfma_f32_16x16x32_bf16 v[136:139], %[sel], v[80:83], v[136:139]\n\tv_mfma_f32_16x16x32_bf16 v[140:143], %[sel], v[88:91], v[140:143]\n\tv_mfma_f32_16x16x32_bf16 v[136:139], %[sel], v[96:99], v[136:139]\n\t\n4:\n\ts_nop 7"
                         : "+{v[64:95]}"(ra), "+{v[96:99]}"(rc), "=&{v[100:131]}"(rb), "=&{v[132:135]}"(rd), "=&{v[136:139]}"(s0), "=&{v[140:143]}"(s1), [c] "=&s"(c_)
                         : [pb0] "v"(pb[0]), [pb1] "v"(pb[1]), [pb2] "v"(pb[2]), [pb3] "v"(pb[3]), [pb4] "v"(pb[4]), [pb5] "v"(pb[5]), [pb6] "v"(pb[6]), [pb7] "v"(pb[7]), [pb8] "v"(pb[8]), [ws] "s"(p.ws), [tag] "s"(tag), [sel] "v"(sel4) : "memory", "scc", "vcc");
            float o[4];
#pragma unroll
            for (int c = 0; c < 4; ++c) o[c] = fmaf(omg, xv[c] + rbf(rbf(dpp_add<0x128, 0xf>(s0[c] + s1[c])) * rs), av[c]);
            v2u ov = {cvtpk(o[0], o[1]), cvtpk(o[2], o[3])};
            asm volatile("" : "+v"(ov));
            LWAIT(ctl, C_ACK, 4);
            asm volatile("s_mov_b64 exec, 0xff\n\tglobal_store_dwordx2 %0, %1, %2\n\ts_mov_b64 exec, -1" :: "v"(oo), "v"(ov), "s"(p.out) : "memory");
            asm volatile("s_waitcnt vmcnt(0)" :: "v"(ra), "v"(rb), "v"(rc), "v"(rd) : "memory");
        }
        ROLE_END();
    }
    }
}

extern "C" void kernel_launch(void* const* d_in, const int* in_sizes, int n_in,
                              void* d_out, int out_size, void* d_ws, size_t ws_size,
                              hipStream_t stream) {
    Params p;
    p.x = (const u16*)d_in[0]; p.Are = (const u16*)d_in[1]; p.Aim = (const u16*)d_in[2]; p.Bre = (const u16*)d_in[3];
    p.Bim = (const u16*)d_in[4]; p.Cre = (const u16*)d_in[5]; p.Cim = (const u16*)d_in[6]; p.logdt = (const u16*)d_in[7];
    p.dskip = (const u16*)d_in[8]; p.wr = (const u16*)d_in[10]; p.wg = (const u16*)d_in[11]; p.wu = (const u16*)d_in[12];
    p.wd = (const u16*)d_in[13]; p.gw = (const u16*)d_in[14]; p.out = (u16*)d_out; p.ws = (char*)d_ws;
    (void)hipFuncSetAttribute((const void*)fused_kernel, hipFuncAttributeMaxDynamicSharedMemorySize, LDS_ALLOC);
    fused_kernel<<<256, 512, LDS_ALLOC, stream>>>(p);
}
```
